```python
import torch

def _softmax(x, axis=-1):
    return torch.softmax(x, dim=axis)

def _gelu(x):
    return torch.nn.functional.gelu(x, approximate='tanh')

def _silu(x):
    return torch.nn.functional.silu(x)

def _sigmoid(x):
    return torch.sigmoid(x)

def _layernorm(x, axis=-1, eps=1e-05):
    m = x.mean(dim=axis, keepdim=True)
    v = x.var(dim=axis, keepdim=True, unbiased=False)
    return (x - m) / torch.sqrt(v + eps)

def _scatter_add(tgt, idx, src, axis=0):
    return tgt.clone().index_add_(axis, idx, src)

def _segment_sum(src, seg, n):
    out = torch.zeros((n,) + src.shape[1:], dtype=src.dtype)
    return out.index_add_(0, seg, src)

def _pad(x, pw):
    flat = tuple((v for pair in reversed(pw) for v in pair))
    return torch.nn.functional.pad(x, flat)
kernel_name = 'interleave_layers__sliding_local_dilated__mixer_gmlp__prompt'

def setup_inputs():

    _w = lambda a: (lambda t: t.to(torch.bfloat16) if t.is_floating_point() else t)(torch.as_tensor(a))

    def _A_setup():
        B, S, D = (1, 2048, 576)
        return {'x': _w((lambda *s: torch.randn(*s, dtype=torch.bfloat16) * 0.05)(B, S, D)), 'wqkv': _w((lambda *s: torch.randn(*s, dtype=torch.bfloat16) * 0.05)(D, 3 * D)), 'wo': _w((lambda *s: torch.randn(*s, dtype=torch.bfloat16) * 0.05)(D, D))}
    _A_in = _A_setup()

    def _B_setup():
        B, S, D, F = (1, 2048, 576, 4608)
        Fh = F // 2
        return {'x': _w((lambda *s: torch.randn(*s, dtype=torch.bfloat16) * 0.05)(B, S, D)), 'w_in': _w((lambda *s: torch.randn(*s, dtype=torch.bfloat16) * 0.2)(D, 2 * Fh)), 'w_sp': _w(0.01 * (lambda *s: torch.randn(*s, dtype=torch.bfloat16) * 0.2)(S, S)), 'b_sp': _w(1.0 + 0.01 * (lambda *s: torch.randn(*s, dtype=torch.bfloat16) * 0.05)(S)), 'w_out': _w((lambda *s: torch.randn(*s, dtype=torch.bfloat16) * 0.05)(Fh, D))}
    _B_in = _B_setup()
    return {'A_x': _A_in['x'], 'A_wqkv': _A_in['wqkv'], 'A_wo': _A_in['wo'], 'B_x': _B_in['x'], 'B_w_in': _B_in['w_in'], 'B_w_sp': _B_in['w_sp'], 'B_b_sp': _B_in['b_sp'], 'B_w_out': _B_in['w_out']}

def reference(A_x, A_wqkv, A_wo, B_x, B_w_in, B_w_sp, B_b_sp, B_w_out):

    def _A_ref(x, wqkv, wo):
        B, S, D = x.shape
        H = 32
        Dh = D // H
        W, dil = (8, 2)
        qkv = (lambda x, *d: x.permute(*d))((x @ wqkv).reshape(B, S, 3, H, Dh), 2, 0, 3, 1, 4)
        q, k, v = (qkv[0], qkv[1], qkv[2])
        scale = Dh ** (-0.5)
        pos = torch.arange(S)
        off = torch.arange(W) * dil
        idx = pos[:, None] - off[None, :]
        mask = idx >= 0
        idx_c = torch.where(mask, idx, 0).reshape(-1)
        kg = k[:, :, idx_c, :].reshape(B, H, S, W, Dh)
        vg = v[:, :, idx_c, :].reshape(B, H, S, W, Dh)
        logits = torch.einsum('bhsd,bhswd->bhsw', q, kg) * scale
        logits = torch.where(mask[None, None, :, :], logits, -1000000000.0)
        attn = _softmax(logits, axis=-1)
        o = torch.einsum('bhsw,bhswd->bhsd', attn, vg)
        return (lambda x, *d: x.permute(*d))(o, 0, 2, 1, 3).reshape(B, S, D) @ wo

    def _B_ref(x, w_in, w_sp, b_sp, w_out):
        B, S, D = x.shape
        F = w_in.shape[-1]
        Fh = F // 2
        z = _gelu(torch.einsum('bsd,df->bsf', x, w_in))
        z1 = z[..., :Fh].clone()
        z2 = _layernorm(z[..., Fh:], axis=-1)
        g = torch.einsum('ts,bsf->btf', w_sp, z2) + b_sp[None, :, None]
        return torch.einsum('bsf,fd->bsd', z1 * g, w_out)
    x = A_x
    x = x + _A_ref(_layernorm(x, axis=-1), A_wqkv, A_wo)
    x = x + _B_ref(_layernorm(x, axis=-1), B_w_in, B_w_sp, B_b_sp, B_w_out)
    x = x + _A_ref(_layernorm(x, axis=-1), A_wqkv, A_wo)
    x = x + _B_ref(_layernorm(x, axis=-1), B_w_in, B_w_sp, B_b_sp, B_w_out)
    return x
```

```cpp
#include <hip/hip_runtime.h>
#include <stdint.h>

namespace {

typedef unsigned short u16;
typedef short bf16x8 __attribute__((ext_vector_type(8)));
typedef float f32x4 __attribute__((ext_vector_type(4)));
typedef float f32x2 __attribute__((ext_vector_type(2)));
typedef unsigned u32x4 __attribute__((ext_vector_type(4)));
typedef unsigned u32x2 __attribute__((ext_vector_type(2)));
typedef __bf16 bf16x2_t __attribute__((ext_vector_type(2)));
#define LAS __attribute__((address_space(3)))
#define DEVI __device__ __forceinline__

constexpr int S_ = 2048, D_ = 576, FH = 2304;

constexpr size_t SZ_TWQKV = (size_t)16 * 18 * 7 * 1024;
constexpr size_t SZ_TWO = (size_t)8 * 18 * 4608;
constexpr size_t SZ_TWIN = (size_t)16 * 18 * 18 * 1024;
constexpr size_t SZ_TWOUT = (size_t)8 * 72 * 4608;
constexpr size_t SZ_X = (size_t)S_ * D_ * 2;
constexpr size_t SZ_Z = (size_t)S_ * FH * 2;
constexpr size_t SZ_ST = (size_t)S_ * 64;
constexpr size_t SZ_ZST = (size_t)16 * 16 * 128 * 8;
constexpr size_t OFF_TWQKV = 0;
constexpr size_t OFF_TWO = OFF_TWQKV + SZ_TWQKV;
constexpr size_t OFF_TWIN = OFF_TWO + SZ_TWO;
constexpr size_t OFF_TWOUT = OFF_TWIN + SZ_TWIN;
constexpr size_t OFF_CNT = OFF_TWOUT + SZ_TWOUT;
constexpr size_t SZ_CNT = 10 * 65536;
constexpr size_t OFF_ZST = OFF_CNT + SZ_CNT;
constexpr size_t OFF_L0 = OFF_ZST + 2 * SZ_ZST;
constexpr size_t L_XA = 0, L_STA = L_XA + SZ_X, L_O = L_STA + SZ_ST, L_XB = L_O + SZ_X, L_STB = L_XB + SZ_X, L_Z2T = L_STB + SZ_ST, L_Y = L_Z2T + SZ_Z;
constexpr size_t SZ_L = 0;
static_assert(OFF_L0 + L_Y + SZ_Z <= ((size_t)256 << 20), "workspace");
constexpr unsigned F_O = OFF_CNT, F_X1 = F_O + 65536, F_Z2 = F_O + 131072, F_Y = F_O + 196608, F_LAYER = 262144, F_X2 = F_O + 2 * F_LAYER;

DEVI unsigned pack2(float a, float b) { f32x2 v = {a, b}; bf16x2_t r = __builtin_convertvector(v, bf16x2_t); return __builtin_bit_cast(unsigned, r); }
DEVI float bflo(unsigned u) { return __uint_as_float(u << 16); }
DEVI float bfhi(unsigned u) { return __uint_as_float(u & 0xffff0000u); }
DEVI int slot2row(int sg) { return (sg & ~31) + 2 * (sg & 15) + ((sg >> 4) & 1); }
DEVI float fmax2(float a, float b) { return __builtin_amdgcn_fmed3f(a, b, __builtin_inff()); }
#define XQ4(OP, MID) \
    asm volatile("s_nop 1\n\tv_permlane32_swap_b32 %0, %1\n\tv_permlane32_swap_b32 %2, %3\n\t" OP " %0, %0, %1\n\t" OP " %2, %2, %3\n\t" \
                 "s_nop 1\n\tv_permlane16_swap_b32 %0, %2\n\t" OP " %0, %0, %2\n\t" MID \
                 "v_mov_b32 %2, %0\n\ts_nop 1\n\tv_permlane16_swap_b32 %0, %2\n\tv_mov_b32 %1, %0\n\tv_mov_b32 %3, %2\n\ts_nop 0\n\t" \
                 "v_permlane32_swap_b32 %0, %1\n\tv_permlane32_swap_b32 %2, %3" : "+v"(x[0]), "+v"(x[1]), "+v"(x[2]), "+v"(x[3]))
DEVI void xq4_max(float (&x)[4]) { XQ4("v_max_f32", ""); }
DEVI void xq4_sum_rcp(float (&x)[4]) { XQ4("v_add_f32", "v_rcp_f32 %0, %0\n\ts_nop 0\n\t"); }
template <class T> DEVI T pin(T v) { asm volatile("" : "+v"(v)); return v; }
template <int V> struct IC { static constexpr int value = V; };
template <int N, int I = 0, class F> DEVI void static_for(F&& f) { if constexpr (I < N) { f(IC<I>{}); static_for<N, I + 1>(f); } }
DEVI int pin_s(int v) { asm volatile("" : "+s"(v)); return v; }
DEVI float xq_sum(float v) {
    auto a = __builtin_amdgcn_permlane32_swap(__float_as_uint(v), __float_as_uint(v), false, false);
    v = __uint_as_float(a[0]) + __uint_as_float(a[1]);
    auto b = __builtin_amdgcn_permlane16_swap(__float_as_uint(v), __float_as_uint(v), false, false);
    return __uint_as_float(b[0]) + __uint_as_float(b[1]);
}
#define BLDS(rs, l, vo, so, off) __builtin_amdgcn_raw_ptr_buffer_load_lds((rs), (LAS void*)(l), 16, (vo), (so), (off), 0)
#define BLDS1(rs, l, vo, so, off) __builtin_amdgcn_raw_ptr_buffer_load_lds((rs), (LAS void*)(l), 16, (vo), (so), (off), 16)
#define RSRC(p) __builtin_amdgcn_make_buffer_rsrc((void*)(p), 0, 0x7fffffff, 0x00020000)
#define ST16(rs, vo, so, v, aux) __builtin_amdgcn_raw_buffer_store_b128((v), (rs), (vo) + (so), 0, aux)
#define ST8(rs, vo, so, v, aux) __builtin_amdgcn_raw_buffer_store_b64((v), (rs), (vo), (so), aux)
#define WAIT_V(n) asm volatile("s_waitcnt vmcnt(%0)" ::"i"(n) : "memory")
#define WAIT_L0() asm volatile("s_waitcnt lgkmcnt(0)" ::: "memory")
#define BAR() do { asm volatile("" ::: "memory"); __builtin_amdgcn_s_barrier(); asm volatile("" ::: "memory"); } while (0)
#define BARX() do { __builtin_amdgcn_sched_barrier(0); BAR(); __builtin_amdgcn_sched_barrier(0); } while (0)
#define LOOP_ALIGN() asm volatile(".p2align 6")
#define SCHED0() __builtin_amdgcn_sched_barrier(0)
#define MFMA(a, b, c) __builtin_amdgcn_mfma_f32_16x16x32_bf16((a), (b), (c), 0, 0, 0)
#define MFMA_T(a, b, c) asm volatile("v_mfma_f32_16x16x32_bf16 %0, %1, %2, %0" : "+v"(c) : "v"(a), "v"(b))
#define MFMA_0(a, b, c) asm volatile("v_mfma_f32_16x16x32_bf16 %0, %1, %2, 0" : "=&v"(c) : "v"(a), "v"(b))
#define WAIT_L(n) __builtin_amdgcn_s_waitcnt(0xC07F | ((n) << 8))
#define LDS_FRAG(p) (*(const LAS bf16x8*)(p))
DEVI int lane_id_fresh() { int z = 0; asm volatile("" : "+v"(z)); return __builtin_amdgcn_mbcnt_hi(~0u, __builtin_amdgcn_mbcnt_lo(~0u, z)); }
DEVI const LAS char* opaque(const LAS char* p) { asm volatile("" : "+v"(p)); return p; }
#define DBG_ARG
#define DBG_LAUNCH(n)
#define STAMP(i)
#define STAMP_DECL
#define STAMP_BEGIN
#define STAMP_END(n)
#define STAMP_PARAM
#define STAMP_FWD

DEVI float ln_rstd(const f32x4 p) {
    float s = p[0] + p[2], q = p[1] + p[3];
    s += __builtin_amdgcn_update_dpp(0.f, s, 0xB1, 0xf, 0xf, true); q += __builtin_amdgcn_update_dpp(0.f, q, 0xB1, 0xf, 0xf, true);
    s += __builtin_amdgcn_update_dpp(0.f, s, 0x4E, 0xf, 0xf, true); q += __builtin_amdgcn_update_dpp(0.f, q, 0x4E, 0xf, 0xf, true);
    const float mean = s * (1.0f / 576.0f);
    return __builtin_amdgcn_rsqf(fmax2(q * (1.0f / 576.0f) - mean * mean, 0.f) + 1e-5f);
}

struct Res { u32x4 a, c; u32x2 e; };
DEVI Res proj_residual_load(const char* xin, int rt, int cg, int lane) {
    const auto rs = RSRC(xin + (size_t)rt * 18432 + cg * 2304);
    Res x;
    x.a = __builtin_amdgcn_raw_buffer_load_b128(rs, lane * 16, 0, 16); x.c = __builtin_amdgcn_raw_buffer_load_b128(rs, lane * 16 + 1024, 0, 16);
    x.e = __builtin_amdgcn_raw_buffer_load_b64(rs, 2048 + (lane & 15) * 16 + ((lane >> 4) & 1) * 8, 0, 16);
    return x;
}
DEVI void proj_residual(f32x4 (&v)[5], const Res& x) {
    v[0] = (f32x4){bflo(x.a.x), bfhi(x.a.x), bflo(x.a.y), bfhi(x.a.y)}; v[1] = (f32x4){bflo(x.a.z), bfhi(x.a.z), bflo(x.a.w), bfhi(x.a.w)};
    v[2] = (f32x4){bflo(x.c.x), bfhi(x.c.x), bflo(x.c.y), bfhi(x.c.y)}; v[3] = (f32x4){bflo(x.c.z), bfhi(x.c.z), bflo(x.c.w), bfhi(x.c.w)};
    v[4] = (f32x4){bflo(x.e.x), bfhi(x.e.x), bflo(x.e.y), bfhi(x.e.y)};
}
struct ProjOut { __amdgpu_buffer_rsrc_t x, st; unsigned v0, v1, v2, vs; };
DEVI ProjOut proj_out(char* xout, char* stout, int rt, int cg, int lane) {
    const int r = lane & 15, q = lane >> 4;
    ProjOut p = {RSRC(xout + (size_t)rt * 18432 + cg * 2304), RSRC(stout + (size_t)rt * 1024 + cg * 8),
                 pin((unsigned)lane * 16u), pin((unsigned)lane * 16u + 1024u), pin((unsigned)(2048 + r * 16 + (q & 1) * 8)), pin((unsigned)(r * 64 + (q >> 1) * 4))};
    asm volatile("" :: "s"(p.x), "s"(p.st));
    return p;
}
DEVI ProjOut proj_out_final(char* out, int rt, int cg, int lane) {
    const int q = lane >> 4;
    const unsigned o = (unsigned)(slot2row(rt * 16 + (lane & 15)) * 1152 + cg * 144);
    ProjOut p = {RSRC(out), RSRC(out), pin(o + q * 16), pin(o + 64 + q * 16), pin(o + 128 + (q & 1) * 8), 0u};
    return p;
}
template <bool FINAL = false>
DEVI void proj_store(f32x4 (&v)[5], const ProjOut& p) {
    ST16(p.x, p.v0, 0, ((u32x4){pack2(v[0][0], v[0][1]), pack2(v[0][2], v[0][3]), pack2(v[1][0], v[1][1]), pack2(v[1][2], v[1][3])}), FINAL ? 0 : 16);
    SCHED0();
    asm volatile("" : "+v"(v[2]), "+v"(v[3]), "+v"(v[4]));
    ST16(p.x, p.v1, 0, ((u32x4){pack2(v[2][0], v[2][1]), pack2(v[2][2], v[2][3]), pack2(v[3][0], v[3][1]), pack2(v[3][2], v[3][3])}), FINAL ? 0 : 16);
    SCHED0();
    ST8(p.x, p.v2, 0, ((u32x2){pack2(v[4][0], v[4][1]), pack2(v[4][2], v[4][3])}), FINAL ? 0 : 16);
    SCHED0();
    if (!FINAL) {
        const f32x4 s4 = ((v[0] + v[1]) + (v[2] + v[3])) + v[4] * 0.5f;
        const f32x4 q4 = ((v[0] * v[0] + v[1] * v[1]) + (v[2] * v[2] + v[3] * v[3])) + v[4] * v[4] * 0.5f;
        auto x = __builtin_amdgcn_permlane32_swap(__float_as_uint((s4[0] + s4[2]) + (s4[1] + s4[3])), __float_as_uint((q4[0] + q4[2]) + (q4[1] + q4[3])), false, false);
        const float t = __uint_as_float(x[0]) + __uint_as_float(x[1]);
        auto y = __builtin_amdgcn_permlane16_swap(__float_as_uint(t), __float_as_uint(t), false, false);
        __builtin_amdgcn_raw_buffer_store_b32(__float_as_uint(__uint_as_float(y[0]) + __uint_as_float(y[1])), p.st, p.vs, 0, 16);
        SCHED0();
    }
}

DEVI void side_load(f32x4 (&d)[3], const char* p, const char* p2) {
    asm volatile("global_load_dwordx4 %0, %3, off sc1\n\tglobal_load_dwordx4 %1, %3, off offset:1024 sc1\n\tglobal_load_dwordx4 %2, %4, off sc1"
                 : "=&v"(d[0]), "=&v"(d[1]), "=&v"(d[2]) : "v"(p), "v"(p2) : "memory");
}
DEVI void side_fence(f32x4 (&d)[3]) { asm volatile("" : "+v"(d[0]), "+v"(d[1]), "+v"(d[2])); }
DEVI void lds_word_set(LAS char* p, unsigned v) { asm volatile("ds_write_b32 %0, %1" :: "v"((unsigned)(size_t)p), "v"(v) : "memory"); }
template <int T0, int T1, int P, int S0 = 0, class ISSUE>
DEVI void ring_steps(ISSUE&& issue) {
    int slot = (T0 + S0) % 3;
    LOOP_ALIGN();
#pragma unroll 1
    for (int t = T0; t < T1; ++t) {
        WAIT_V(P); BAR();
        issue(t + 3, slot);
        slot = slot == 2 ? 0 : slot + 1;
    }
}

DEVI unsigned flag_out(unsigned use, int pitch, int group, int writer, int lane) { return pin(lane < 16 ? use + (group * 16 + lane) * pitch + writer * 16 : 0x80000000u); }
DEVI unsigned flag_in(unsigned use, int pitch, int reader, int n, int lane) { return pin(use + reader * pitch + (lane < n ? lane : 0) * 16); }
DEVI void flag_raise(const char* ws, unsigned vo) { ST16(RSRC(ws), vo, 0, ((u32x4){1u, 1u, 1u, 1u}), 16); SCHED0(); }
template <int FIRST>
DEVI void flags_wait(const char* ws, unsigned vo) {
    unsigned v, n = 400000u;
    asm volatile("s_nop 4\n\ts_sleep %4\n"
                 "1:\n\tbuffer_load_dword %0, %2, %3, 0 offen sc1\n\ts_waitcnt vmcnt(0)\n\tv_cmp_eq_u32 vcc, 0, %0\n\ts_cbranch_vccz 2f\n\t"
                 "s_sleep 1\n\ts_sub_u32 %1, %1, 1\n\ts_cbranch_scc0 1b\n2:"
                 : "=&v"(v), "+s"(n) : "v"(vo), "s"(RSRC(ws)), "i"(FIRST) : "vcc", "scc", "memory");
    SCHED0();
}
DEVI void lds_word_wait(LAS char* p) {
#pragma unroll 1
    for (unsigned spin = 0; spin < 40000000u; ++spin) {
        unsigned v;
        asm volatile("ds_read_b32 %0, %1\n\ts_waitcnt lgkmcnt(0)" : "=v"(v) : "v"((unsigned)(size_t)p) : "memory");
        if (__builtin_amdgcn_readfirstlane(v) != 0u) break;
        __builtin_amdgcn_s_sleep(1);
    }
}

constexpr int K1_STAGE = 32768;
constexpr int WO_OFF = 69632;
constexpr int G_PART = WO_OFF + 82944;
constexpr int G_TAB = G_PART + 2048;
constexpr int K1_SCR = G_TAB + 1536;
constexpr int G_WORD = K1_SCR + 576;
constexpr int NET_LDS = G_WORD + 64;

#define WO_PRE 12
#define WO_W 33
#define O_FIRST 10
#define X1_FIRST 6
#define X2_FIRST 6
#define Y_FIRST 10
#define Z2_FIRST 10
DEVI void attn_issueB(LAS char* lds, const char* ws, int hp, int wid, int lane, int s, int slot) {
    const auto rB = RSRC(ws + OFF_TWQKV + (size_t)hp * (18 * 7168));
    LAS char* sl = lds + slot * K1_STAGE + 18432 + wid * 4096;
    const int off = wid * 4096 + s * 14336;
    BLDS(rB, sl, lane * 16, off, 0); BLDS(rB, sl, lane * 16, off, 1024);
    if (wid < 3) { BLDS(rB, sl, lane * 16, off, 2048); BLDS(rB, sl, lane * 16, off, 3072); }
}
template <class NEXT>
DEVI void attn_producer(LAS char* lds, const char* ws, const char* wl, unsigned fl, int layer, int pm, int hp, int wid, NEXT&& next) {
    const int lane = lane_id_fresh(), r = lane & 15, q = lane >> 4;
    const int pmh = pm > 0 ? pm : 1;
    const auto rA = RSRC(wl + L_XA);
    const unsigned vo = lane * 16, voh = (unsigned)((r >> 3) * 18432 + q * 256 + (8 + (r & 7)) * 16);
    const int oA = (pm * 8 + 2 * wid) * 18432, oh = (pmh * 8 - 2) * 18432;
    auto issueA = [&](int s, int slot) {
        LAS char* sl = lds + slot * K1_STAGE;
        if (wid == 3) { BLDS1(rA, sl, voh, oh + s * 2048, 0); BLDS1(rA, sl, voh, oh + s * 2048, 1024); }
        BLDS1(rA, sl + 2048 + wid * 4096, vo, oA + s * 2048, 0); BLDS1(rA, sl + 2048 + wid * 4096, vo, oA + s * 2048, 1024);
        BLDS1(rA, sl + 4096 + wid * 4096, vo, oA + s * 2048 + 18432, 0); BLDS1(rA, sl + 4096 + wid * 4096, vo, oA + s * 2048 + 18432, 1024);
    };
    auto issue = [&](int s, int slot) { issueA(s, slot); attn_issueB(lds, ws, hp, wid, lane, s, slot); };
    __builtin_amdgcn_s_setprio(3);
    if (layer == 0) {
        issue(0, 0); issue(1, 1);
        WAIT_V(8);
    } else {
        attn_issueB(lds, ws, hp, wid, lane, 2, 2);
        const unsigned fi = flag_in(F_X2, 512, pm * 16 + hp, pm > 0 ? 24 : 16, lane);
        if (wid == 0) flags_wait<X2_FIRST>(ws, fi);
        BARX();
        issueA(0, 0); issueA(1, 1); issueA(2, 2);
        WAIT_V(8);
    }
    BAR();
    __builtin_amdgcn_s_setprio(0);
    if (layer == 0) issue(2, 2); else WAIT_V(4);
    ring_steps<0, 2, 8>(issue);
    {
        const int hr = lane >> 2;
        f32x4 sp[3];
        side_load(sp, wl + L_STA + (size_t)(pm * 128 + wid * 32) * 64 + lane * 16, wl + L_STA + (size_t)((pmh * 8 - 2 + (hr >> 3)) * 16 + 8 + (hr & 7)) * 64 + (lane & 3) * 16);
        WAIT_V(11); BAR(); issue(5, 2);
        ring_steps<3, 6, 8>(issue);
        __builtin_amdgcn_s_setprio(3);
        side_fence(sp);
#pragma unroll
        for (int i = 0; i < 3; ++i) lds_word_set(lds + K1_SCR + (i < 2 ? 16 + wid * 32 + i * 16 + hr : hr) * 4, __float_as_uint(ln_rstd(sp[i])));
        WAIT_L0();
        __builtin_amdgcn_s_setprio(0);
    }
    WAIT_V(8); BAR();
    WAIT_V(0); BAR();
    BAR();
    const auto rW = RSRC(ws + OFF_TWO + (size_t)(hp >> 1) * 82944);
#pragma unroll
    for (int i = 0; i < 5; ++i) {
        const int off = (i * 4 + wid) * 4096;
        LAS char* d = lds + WO_OFF + off;
        BLDS(rW, d, vo, off, 0); BLDS(rW, d, vo, off, 1024); BLDS(rW, d, vo, off, 2048); BLDS(rW, d, vo, off, 3072);
    }
    if (wid == 0) BLDS(rW, lds + WO_OFF + 81920, vo, 81920, 0);
    next();
    const unsigned fo = flag_out(fl + F_O, 256, pm, hp, lane), fi = flag_in(fl + F_O, 256, pm * 16 + hp, 16, lane);
    WAIT_V(0);
    if (wid == 0) __builtin_amdgcn_s_setprio(3);
    BARX();
    if (wid == 0) { flag_raise(ws, fo); flags_wait<O_FIRST>(ws, fi); }
    BARX();
}

DEVI void attn_consumer(LAS char* lds, char* wl, int layer, int pm, int hp, int wid STAMP_PARAM) {
    const int lane = lane_id_fresh(), r = lane & 15, q = lane >> 4;
    const char* xa = wl + L_XA;
    char* o = wl + L_O;
    const int T = wid & 1, h = wid >> 1;
    const LAS char* ph = lds + q * 256 + ((r ^ (T ? 0 : 8)) * 16);
    const LAS char* pa = lds + T * 2048 + lane * 16;
    const LAS char* pb = lds + h * 3072 + lane * 16;
    const LAS char* pt = lds + lane * 16;
    f32x4 acc[5][4];
#pragma unroll
    for (int m = 0; m < 5; ++m)
#pragma unroll
        for (int n = 0; n < 4; ++n) if (m + n) acc[m][n] = pin((f32x4){0.f, 0.f, 0.f, 0.f});
    bf16x8 fa[2][5], fb[2][4];
    auto rd1 = [&](int kk, int i) {
        if (i < 3) fb[kk][i] = LDS_FRAG(pb + 18432 + kk * 7168 + i * 1024);
        else if (i == 3) fb[kk][3] = LDS_FRAG(pt + 24576 + kk * 7168);
        else if (i == 4) fa[kk][0] = LDS_FRAG(ph + kk * 1024);
        else if (i < 9) fa[kk][i - 4] = LDS_FRAG(pa + 2048 + kk * 1024 + (i - 5) * 4096);
    };
    auto chunk = [&](int kk, int d) {
#pragma unroll
        for (int i = 1; i < 20; ++i) {
            const int m = i >> 2, n = i & 3;
            const bf16x8 &A = fa[kk][m], &B = fb[kk][n];
            acc[m][n] = n != 2 ? MFMA(B, A, acc[m][n]) : MFMA(A, B, acc[m][n]);
            SCHED0();
            if (i <= 9) rd1(kk ^ 1, i - 1);
            if (kk == 0) { if (i == 11) ph = opaque(ph + d); if (i == 12) pa = opaque(pa + d); if (i == 13) pb = opaque(pb + d); if (i == 14) pt = opaque(pt + d); }
            SCHED0();
        }
    };
    f32x4 mb, mb0;
#pragma unroll
    for (int j = 0; j < 4; ++j) {
        const bool ok = ((r - 4 * q - j) & 15) < 8;
        mb[j] = ok ? 0.f : -__builtin_inff();
        mb0[j] = (ok && (pm > 0 || 4 * q + j <= r)) ? 0.f : -__builtin_inff();
    }
    mb = pin(mb); mb0 = pin(mb0);
    const unsigned mp0 = pin((4 * q > r ? 0xffffu : 0u) | (4 * q + 1 > r ? 0xffff0000u : 0u)), mp1 = pin((4 * q + 2 > r ? 0xffffu : 0u) | (4 * q + 3 > r ? 0xffff0000u : 0u));
    const unsigned qtm = pin(q == h ? 0xffffffffu : 0u);
    const unsigned vom = pin((unsigned)(r * 16 + hp * 1024 + (2 * h + (q >> 1)) * 256 + (q & 1) * 8)), vot = pin((unsigned)(r * 16 + 16384 + (hp >> 1) * 256 + (hp & 1) * 8 + h * 4));
    const LAS char* tk0 = opaque(lds + K1_SCR + ((q * 4) ^ (T ? 0 : 8)) * 4);
    const LAS char* tk = opaque(lds + K1_SCR + ((1 + T) * 16 + q * 4) * 4);
    const LAS char* tq = opaque(lds + K1_SCR + ((1 + T) * 16 + r) * 4);
    ph = opaque(ph); pa = opaque(pa); pb = opaque(pb); pt = opaque(pt);
    STAMP(2);
    if (layer) BAR();
    BARX();
    STAMP(3);
#pragma unroll
    for (int i = 0; i < 9; ++i) rd1(0, i);
    SCHED0();
    const int rt = pm * 8 + (hp & 1) * 4 + wid, cg = hp >> 1;
    const Res rx = proj_residual_load(xa, rt, cg, lane);
    SCHED0();
    int slot = 0;
    LOOP_ALIGN();
#pragma unroll 1
    for (int t = 0; t < 9; ++t) {
        const int d = slot == 2 ? -2 * K1_STAGE : K1_STAGE;
        slot = slot == 2 ? 0 : slot + 1;
        SCHED0();
        chunk(0, d);
        WAIT_L0(); BAR();
        SCHED0();
        chunk(1, 0);
    }
    STAMP(4);
    u32x2 ts[5], tf[5];
    {
        LAS char* xb = lds + wid * 320;
#pragma unroll
        for (int m = 0; m < 5; ++m) ts[m] = (u32x2){pack2(acc[m][3][0], acc[m][3][1]), pack2(acc[m][3][2], acc[m][3][3])};
        if (q == 2) {
#pragma unroll
            for (int m = 0; m < 5; ++m) {
                const unsigned w = h ? ts[m].y : ts[m].x;
                *(LAS u16*)(xb + m * 64 + r * 2) = (u16)w; *(LAS u16*)(xb + m * 64 + 32 + r * 2) = (u16)(w >> 16);
            }
        }
        asm volatile("" ::: "memory");
#pragma unroll
        for (int m = 0; m < 5; ++m) tf[m] = *(const LAS u32x2*)(xb + m * 64 + (r & 1) * 32 + q * 8);
    }
    {
        f32x4 rk[5]; float rq[4];
        rk[0] = *(const LAS f32x4*)(tk0);
#pragma unroll
        for (int g = 0; g < 4; ++g) { rk[g + 1] = *(const LAS f32x4*)(tk + g * 128); rq[g] = *(const LAS float*)(tq + g * 128); }
        const f32x4 zero = {0.f, 0.f, 0.f, 0.f};
        u32x4 kf[5];
#pragma unroll
        for (int m = 0; m < 5; ++m) kf[m] = (u32x4){pack2(acc[m][1][0], acc[m][1][1]), pack2(acc[m][1][2], acc[m][1][3]), ts[m].y, 0u};
        f32x4 sp[4], sc[4];
#pragma unroll
        for (int g = 0; g < 4; ++g) {
            const u32x4 qf = {pack2(acc[g + 1][0][0], acc[g + 1][0][1]), pack2(acc[g + 1][0][2], acc[g + 1][0][3]), ts[g + 1].x & qtm, 0u};
            sp[g] = MFMA(__builtin_bit_cast(bf16x8, kf[g]), __builtin_bit_cast(bf16x8, qf), zero);
            sc[g] = MFMA(__builtin_bit_cast(bf16x8, kf[g + 1]), __builtin_bit_cast(bf16x8, qf), zero);
        }
        u32x2 vf[5];
#pragma unroll
        for (int m = 0; m < 5; ++m) vf[m] = (u32x2){pack2(acc[m][2][0], acc[m][2][1]), pack2(acc[m][2][2], acc[m][2][3])};
        f32x4 rs[4], t[4]; float mx[4], sm[4];
#pragma unroll
        for (int g = 0; g < 4; ++g) {
#pragma unroll
            for (int j = 0; j < 4; ++j) { const bool pv = 4 * q + j > r; rs[g][j] = pv ? rk[g][j] : rk[g + 1][j]; t[g][j] = pv ? sp[g][j] : sc[g][j]; }
            t[g] = (t[g] * rs[g]) * (rq[g] * (0.23570226039551584f * 1.4426950408889634f)) + (g == 0 ? mb0 : mb);
            mx[g] = fmaxf(fmaxf(t[g][0], t[g][1]), fmaxf(t[g][2], t[g][3]));
        }
        xq4_max(mx);
#pragma unroll
        for (int g = 0; g < 4; ++g) {
            t[g] = t[g] - mx[g];
#pragma unroll
            for (int j = 0; j < 4; ++j) t[g][j] = __builtin_amdgcn_exp2f(t[g][j]);
            sm[g] = (t[g][0] + t[g][2]) + (t[g][1] + t[g][3]);
        }
        f32x4 om[4], ot[4];
#pragma unroll
        for (int g = 0; g < 4; ++g) {
            const f32x4 p = t[g] * rs[g];
            const unsigned p01 = pack2(p[0], p[1]), p23 = pack2(p[2], p[3]);
            const u32x4 pf = {p01 & mp0, p23 & mp1, p01 & ~mp0, p23 & ~mp1};
            om[g] = MFMA(__builtin_bit_cast(bf16x8, ((u32x4){vf[g].x, vf[g].y, vf[g + 1].x, vf[g + 1].y})), __builtin_bit_cast(bf16x8, pf), zero);
            ot[g] = MFMA(__builtin_bit_cast(bf16x8, ((u32x4){tf[g].x, tf[g].y, tf[g + 1].x, tf[g + 1].y})), __builtin_bit_cast(bf16x8, pf), zero);
        }
        xq4_sum_rcp(sm);
        const auto ro = RSRC(o + (size_t)(pm * 8 + T) * 18432);
        unsigned tw2[4];
#pragma unroll
        for (int g = 0; g < 4; ++g) {
            const f32x4 v = om[g] * sm[g];
            __builtin_amdgcn_raw_buffer_store_b64((u32x2){pack2(v[0], v[1]), pack2(v[2], v[3])}, ro, vom, g * 36864, 16);
            const f32x2 w = ot[g].xy * sm[g];
            tw2[g] = pack2(w.x, w.y);
        }
        if (q == 2) {
#pragma unroll
            for (int g = 0; g < 4; ++g) __builtin_amdgcn_raw_buffer_store_b32(tw2[g], ro, vot, g * 36864, 16);
        }
    }
    STAMP(5);
    {
        WAIT_V(0); BARX();
        STAMP(6);
        f32x4 v[5];
        proj_residual(v, rx);
        const LAS char* pw = opaque(lds + WO_OFF + lane * 16);
        const LAS char* pw2 = opaque(lds + WO_OFF + 41472 + lane * 16);
        const LAS char* ph2 = opaque(lds + WO_OFF + 4096 + q * 128 + (r & 7) * 16);
        const LAS char* ph3 = opaque(lds + WO_OFF + 41472 + 4096 + q * 128 + (r & 7) * 16);
        bf16x8 wf[90];
        auto rdw = [&](int i) {
            const int kc = i / 5, n = i % 5;
            wf[i] = n < 4 ? LDS_FRAG((kc < 9 ? pw : pw2) + (kc % 9) * 4608 + n * 1024) : LDS_FRAG((kc < 9 ? ph2 : ph3) + (kc % 9) * 4608);
        };
#pragma unroll
        for (int i = 0; i < WO_W; ++i) rdw(i);
#pragma unroll
        for (int n = 0; n < 5; ++n) v[n] = pin(v[n]);
        const auto rO = RSRC(o + (size_t)rt * 18432);
        const ProjOut po = proj_out(wl + L_XB, wl + L_STB, rt, cg, lane);
        asm volatile("" :: "s"(rO));
        WAIT_L(0);
        BARX();
        STAMP(7);
        bf16x8 af[18];
        auto ldo = [&](int kc) { af[kc] = __builtin_bit_cast(bf16x8, __builtin_amdgcn_raw_buffer_load_b128(rO, lane * 16 + (kc & 3) * 1024, (kc >> 2) * 4096, 16)); };
#pragma unroll
        for (int kc = 0; kc < WO_PRE; ++kc) { ldo(kc); SCHED0(); }
        static_for<90>([&](auto ic) {
            constexpr int i = ic.value, kc = i / 5, n = i % 5, j = WO_W + 2 * (i / 3) + i % 3;
            MFMA_T(wf[i], af[kc], v[n]);
            SCHED0();
            if (i % 3 < 2 && j < 90) rdw(j);
            if (i % 3 == 2 && (i / 3) % 2 == 0 && i / 6 + WO_PRE < 18) ldo(i / 6 + WO_PRE);
            SCHED0();
        });
        asm volatile("s_nop 4" : "+v"(v[0]), "+v"(v[1]));
        STAMP(8);
        proj_store(v, po);
    }
}

DEVI f32x2 gelu2(f32x2 v) {
    const f32x2 w = v * (v * v * -0.10294324f + -2.3022082f);
    const f32x2 d = (f32x2){__builtin_amdgcn_exp2f(w.x), __builtin_amdgcn_exp2f(w.y)} + 1.0f;
    return v * (f32x2){__builtin_amdgcn_rcpf(d.x), __builtin_amdgcn_rcpf(d.y)};
}
DEVI f32x4 gelu4(f32x4 v) { const f32x2 a = gelu2(v.xy), b = gelu2(v.zw); return (f32x4){a.x, a.y, b.x, b.y}; }
DEVI void rowsum16x8(const f32x4 a, const f32x4 b, float& o0, float& o1) {
    float t0, t1, t2, t3;
    asm volatile(
        "s_nop 1\n\t"
        "v_add_f32_dpp %2, %6, %6 row_ror:8 row_mask:0xf bank_mask:0x3\n\t"
        "v_add_f32_dpp %3, %7, %7 row_ror:8 row_mask:0xf bank_mask:0x3\n\t"
        "v_add_f32_dpp %4, %10, %10 row_ror:8 row_mask:0xf bank_mask:0x3\n\t"
        "v_add_f32_dpp %5, %11, %11 row_ror:8 row_mask:0xf bank_mask:0x3\n\t"
        "v_add_f32_dpp %2, %8, %8 row_ror:8 row_mask:0xf bank_mask:0xc\n\t"
        "v_add_f32_dpp %3, %9, %9 row_ror:8 row_mask:0xf bank_mask:0xc\n\t"
        "v_add_f32_dpp %4, %12, %12 row_ror:8 row_mask:0xf bank_mask:0xc\n\t"
        "v_add_f32_dpp %5, %13, %13 row_ror:8 row_mask:0xf bank_mask:0xc\n\t"
        "v_add_f32_dpp %0, %2, %2 row_half_mirror row_mask:0xf bank_mask:0x5\n\t"
        "s_nop 0\n\t"
        "v_add_f32_dpp %1, %4, %4 row_half_mirror row_mask:0xf bank_mask:0x5\n\t"
        "v_add_f32_dpp %0, %3, %3 row_half_mirror row_mask:0xf bank_mask:0xa\n\t"
        "v_add_f32_dpp %1, %5, %5 row_half_mirror row_mask:0xf bank_mask:0xa\n\t"
        "s_nop 0\n\t"
        "v_add_f32_dpp %0, %0, %0 quad_perm:[1,0,3,2] row_mask:0xf bank_mask:0xf\n\t"
        "v_add_f32_dpp %1, %1, %1 quad_perm:[1,0,3,2] row_mask:0xf bank_mask:0xf\n\t"
        "s_nop 0\n\t"
        "v_add_f32_dpp %0, %0, %0 quad_perm:[2,3,0,1] row_mask:0xf bank_mask:0xf\n\t"
        "v_add_f32_dpp %1, %1, %1 quad_perm:[2,3,0,1] row_mask:0xf bank_mask:0xf\n\t"
        : "=&v"(o0), "=&v"(o1), "=&v"(t0), "=&v"(t1), "=&v"(t2), "=&v"(t3)
        : "v"(a[0]), "v"(a[1]), "v"(a[2]), "v"(a[3]), "v"(b[0]), "v"(b[1]), "v"(b[2]), "v"(b[3]));
}

#define K5_PRE 8
#define RH_MAP 0
#define NET_LAYERS 2
constexpr int G_STAGE = 34816;
constexpr int K5_STAGE = 18432, K5_NS = 8;

constexpr int y_load_at(int slot) {
    int next = K5_PRE;
    for (int s = 0;; ++s) { const bool go = next < 18 + 2 * (s / 3); if (s == slot) return go ? next : -1; next += go; }
}

struct GTile { f32x4 a[4][4], e[2]; };
struct GFrag { bf16x8 a[2][4], b[2][5]; const LAS char *pa[2][2], *pb, *p8; int slot; };
DEVI void g_rd(GFrag& f, int kk, int i) {
    if (i < 4) f.a[kk][i] = LDS_FRAG(f.pa[kk][i >> 1] + (i & 1) * 2048);
    else if (i < 8) f.b[kk][i - 4] = LDS_FRAG(f.pb + (kk * 9 + i - 4) * 1024);
    else f.b[kk][4] = LDS_FRAG(f.p8 + kk * 9216);
}
template <bool ROWS_IN_REGS>
DEVI void g_chunk(GTile& c, GFrag& f, int kk, int d) {
#pragma unroll
    for (int i = 0; i < 18; ++i) {
        const int n = i >> 2, m = i & 3;
        f32x4& e = n < 4 ? c.a[m][n] : c.e[m & 1];
        e = ROWS_IN_REGS ? MFMA(f.a[kk][m], f.b[kk][n], e) : MFMA(f.b[kk][n], f.a[kk][m], e);
        SCHED0();
        if (i < 9) g_rd(f, kk ^ 1, i);
        if (kk == 0) {
            if (i >= 10 && i < 14) f.pa[(i - 10) >> 1][i & 1] = opaque(f.pa[(i - 10) >> 1][i & 1] + d);
            if (i == 14) f.pb = opaque(f.pb + d);
            if (i == 15) f.p8 = opaque(f.p8 + d);
        }
        SCHED0();
    }
}
template <int NS, bool ROWS_IN_REGS, bool DRAIN_LAST = false>
DEVI void g_steps(GTile& c, GFrag& f, int n) {
    LOOP_ALIGN();
#pragma unroll 1
    for (int t = 0; t < n; ++t) {
        const int d = f.slot == NS - 1 ? -(NS - 1) * G_STAGE : G_STAGE;
        f.slot = f.slot == NS - 1 ? 0 : f.slot + 1;
        SCHED0();
        g_chunk<ROWS_IN_REGS>(c, f, 0, d);
        WAIT_L0();
        if (DRAIN_LAST && t == n - 1) WAIT_V(0);
        BAR();
        SCHED0();
        g_chunk<ROWS_IN_REGS>(c, f, 1, 0);
    }
}
DEVI void g_fill(GTile& c, const float (&e)[4]) {
#pragma unroll
    for (int m = 0; m < 4; ++m) {
#pragma unroll
        for (int n = 0; n < 4; ++n) c.a[m][n] = pin((f32x4){e[m], e[m], e[m], e[m]});
        if (m < 2) c.e[m] = pin((f32x4){e[m], e[m], e[m], e[m]});
    }
}

DEVI void gmlp_body(LAS char* lds, int wm, int wn, int pm, int cg, char* z2t, char* y, const u16* __restrict__ bsp STAMP_PARAM) {
    const int mlo = wm * 4 + wn * 2, mhi = wm * 4 + (1 - wn) * 2;
    const float zero4[4] = {0.f, 0.f, 0.f, 0.f};
    GTile z2, z1, acc;
    GFrag f;
    {
        const int lane = lane_id_fresh();
#pragma unroll
        for (int kk = 0; kk < 2; ++kk) { f.pa[kk][0] = lds + mlo * 2048 + kk * 1024 + lane * 16; f.pa[kk][1] = lds + mhi * 2048 + kk * 1024 + lane * 16; }
        f.pb = lds + 16384 + wn * 4096 + lane * 16; f.p8 = lds + 16384 + 8192 + lane * 16; f.slot = 0;
    }
    g_fill(z2, zero4);
#pragma unroll
    for (int i = 0; i < 4; ++i) f.pa[i >> 1][i & 1] = opaque(f.pa[i >> 1][i & 1]);
    f.pb = opaque(f.pb); f.p8 = opaque(f.p8);
    SCHED0();
    WAIT_V(0); BARX();
    BAR();
    BARX();
    STAMP(2);
#pragma unroll
    for (int i = 0; i < 9; ++i) g_rd(f, 0, i);
    g_steps<4, true>(z2, f, 9);
    STAMP(3);
    {
        const int lane = lane_id_fresh(), r = lane & 15, q = lane >> 4;
        f32x4 ra[4];
#pragma unroll
        for (int m = 0; m < 4; ++m) ra[m] = *(const LAS f32x4*)(lds + G_TAB + ((m < 2 ? mlo : mhi) + (m & 1)) * 64 + q * 16);
        SCHED0();
#pragma unroll
        for (int m = 0; m < 4; ++m) {
            f32x4 s1, s2;
#pragma unroll
            for (int n = 0; n < 4; ++n) {
                const f32x4 g = gelu4(z2.a[m][n] * ra[m]);
                z2.a[m][n] = g;
                if (n == 0) { s1 = g; s2 = g * g; } else { s1 += g; s2 += g * g; }
            }
            if (m < 2) { const f32x4 g = gelu4(z2.e[m] * ra[m]); z2.e[m] = g; s1 += g; s2 += g * g; }
            float u0, u1;
            rowsum16x8(s1, s2, u0, u1);
            if ((r & 3) == 0) *(LAS f32x2*)(lds + G_PART + wn * 1024 + ((m < 2 ? mlo : mhi) + (m & 1)) * 128 + (q * 4 + (r >> 2)) * 8) = (f32x2){u0, u1};
            SCHED0();
        }
    }
    WAIT_L0(); BAR();
    STAMP(4);
    g_fill(z1, zero4);
    g_steps<4, false>(z1, f, 6);
    STAMP(5);
    {
        const int lane = lane_id_fresh(), q = lane >> 4;
#pragma unroll
        for (int g = 0; g < 2; ++g) {
            const auto rz2 = RSRC(z2t + (size_t)pm * 589824 + cg * 36864 + ((g ? mhi : mlo) >> 1) * 9216);
            const LAS char* tab = lds + G_TAB + (g ? mhi : mlo) * 64 + q * 16;
            f32x4 ra[2], rb[2];
#pragma unroll
            for (int T = 0; T < 2; ++T) { ra[T] = *(const LAS f32x4*)(tab + 512 + T * 64); rb[T] = *(const LAS f32x4*)(tab + 1024 + T * 64); }
#pragma unroll
            for (int n = 0; n < (g ? 4 : 5); ++n) {
                const f32x4 v0 = (n < 4 ? z2.a[2 * g][n & 3] : z2.e[0]) * ra[0] + rb[0], v1 = (n < 4 ? z2.a[2 * g + 1][n & 3] : z2.e[1]) * ra[1] + rb[1];
                ST16(rz2, lane * 16, n < 4 ? wn * 4096 + n * 1024 : 8192, ((u32x4){pack2(v0[0], v1[0]), pack2(v0[1], v1[1]), pack2(v0[2], v1[2]), pack2(v0[3], v1[3])}), 16);
            }
        }
    }
    STAMP(6);
    g_steps<4, false, true>(z1, f, 3);
    STAMP(7);
    {
        const int lane = lane_id_fresh(), r = lane & 15, q = lane >> 4;
        const unsigned b0 = *(const unsigned*)(bsp + pm * 128 + mlo * 16 + 2 * r), b1 = *(const unsigned*)(bsp + pm * 128 + mhi * 16 + 2 * r);
        float ra[4];
#pragma unroll
        for (int m = 0; m < 4; ++m) ra[m] = *(const LAS float*)(lds + G_TAB + ((m < 2 ? mlo : mhi) + (m & 1)) * 64 + r * 4);
        SCHED0();
#pragma unroll
        for (int m = 0; m < 4; ++m) {
#pragma unroll
            for (int n = 0; n < 4; ++n) z1.a[m][n] = pin(gelu4(z1.a[m][n] * ra[m]));
            if (m < 2) z1.e[m] = pin(gelu4(z1.e[m] * ra[m]));
            SCHED0();
        }
        const float bias[4] = {bflo(b0), bfhi(b0), bflo(b1), bfhi(b1)};
        g_fill(acc, bias);
#pragma unroll
        for (int kk = 0; kk < 2; ++kk) {
            f.pa[kk][0] = opaque(lds + 2 * G_STAGE + (mlo * 16 + r) * 128 + (((4 * kk + q) ^ (r & 7)) * 16));
            f.pa[kk][1] = opaque(lds + 2 * G_STAGE + (mhi * 16 + r) * 128 + (((4 * kk + q) ^ (r & 7)) * 16));
        }
        f.pb = opaque(lds + 2 * G_STAGE + 16384 + wn * 4096 + lane * 16); f.p8 = opaque(lds + 2 * G_STAGE + 16384 + 8192 + lane * 16); f.slot = 2;
    }
    STAMP(8);
    BAR();
    STAMP(9);
#pragma unroll
    for (int i = 0; i < 9; ++i) g_rd(f, 0, i);
    g_steps<3, false>(acc, f, 32);
    STAMP(10);
    const int lane = lane_id_fresh();
#pragma unroll
    for (int g = 0; g < 2; ++g) {
        const auto ry = RSRC(y + (size_t)(pm * 8 + (g ? mhi : mlo)) * 73728 + cg * 4608);
#pragma unroll
        for (int T = 0; T < 2; ++T) {
            const int m = 2 * g + T;
#pragma unroll
            for (int p = 0; p < 2; ++p) {
                const f32x4 a = acc.a[m][2 * p] * z1.a[m][2 * p], c = acc.a[m][2 * p + 1] * z1.a[m][2 * p + 1];
                ST16(ry, lane * 16 + wn * 2048 + p * 1024, T * 73728, ((u32x4){pack2(a[0], a[1]), pack2(a[2], a[3]), pack2(c[0], c[1]), pack2(c[2], c[3])}), 16);
            }
            if (g == 0) {
                const f32x4 a = acc.e[T] * z1.e[T];
                ST8(ry, (lane >> 5) * 256 + (lane & 15) * 16 + ((lane >> 4) & 1) * 8, T * 73728 + 4096, ((u32x2){pack2(a[0], a[1]), pack2(a[2], a[3])}), 16);
            }
        }
    }
}

#define NET_IDS \
    int b = blockIdx.x, wid = wid8 & 3; \
    asm volatile("" : "+s"(b), "+s"(wid));     \
    const int xcd = b & 7, idx = b >> 3; \
    const int pa = (xcd >> 1) * 4 + (idx & 3), hp = (xcd & 1) * 8 + (idx >> 2); \
    const int pm = (xcd >> 2) * 8 + (idx & 7), cg = (xcd & 3) * 4 + (idx >> 3); \
    const int rh = RH_MAP ? xcd * 4 + (idx & 3) : (xcd >> 1) * 8 + (idx & 7), c5 = RH_MAP ? idx >> 2 : (xcd & 1) * 4 + (idx >> 3); \
    const int lead = (idx + (idx >> 3)) & 3; \
    auto kord = [&](int t, int n) { \
        const int k = t + ((int)(0x1F000000u << (((t - lead) & 3) * 4)) >> 28); \
        return (unsigned)k < (unsigned)n ? k : t; \
    }; \
    auto kord5 = [&](int t) { \
        const int k = t + ((int)(0x1F000000u << (((t - (RH_MAP ? c5 : lead)) & 3) * 4)) >> 28); \
        return (unsigned)k < 18u ? k : t; \
    };

__global__ __launch_bounds__(512, 2) void k_net(char* ws, const u16* wsp, const u16* bsp, char* out DBG_ARG) {
    asm volatile("gm_code_begin:" :: "s"(ws), "s"(wsp), "s"(bsp), "s"(out));
    extern __shared__ __attribute__((aligned(16))) char smem[];
    LAS char* lds = (LAS char*)smem;
    const char *twin = ws + OFF_TWIN, *twout = ws + OFF_TWOUT;
    STAMP_DECL;
    const int wid8 = __builtin_amdgcn_readfirstlane(threadIdx.x >> 6);
    if (wid8 >= 4) {
        lds_word_set(lds + G_WORD + (threadIdx.x & 15) * 4, 0u);
        WAIT_L0();
        u32x4 wr[5][9];
#pragma unroll 1
      for (int layer = 0; layer < NET_LAYERS; ++layer) {
        NET_IDS
        char* wl = ws + OFF_L0 + layer * SZ_L;
        const unsigned fl = layer * F_LAYER;
        const char *xin = wl + L_XB, *st = wl + L_STB, *z2t = wl + (layer ? L_Y : L_Z2T);
        unsigned long long* zst = (unsigned long long*)(ws + OFF_ZST + layer * SZ_ZST);
        const int lane = lane_id_fresh(), l3 = lane >> 3;
        const unsigned vo = lane * 16;
        const auto rX = RSRC(xin + (size_t)(pm * 8 + 2 * wid) * 18432), rI = RSRC(twin + (size_t)cg * (18 * 18432));
        auto issue1B = [&](int g, int slot) {
            const int k = kord(g < 9 ? g : (g < 13 ? g - 4 : g - 13), 9), ob = (g < 9 ? 9 + k : k) * 18432, o2 = ob + wid * 4096;
            LAS char* sl = lds + slot * G_STAGE + 16384;
            if (wid < 2) BLDS(rI, sl + 16384 + wid * 1024, vo, ob + 16384 + wid * 1024, 0);
            sl += wid * 4096;
            BLDS(rI, sl, vo, o2, 0); BLDS(rI, sl, vo, o2, 1024); BLDS(rI, sl, vo, o2, 2048); BLDS(rI, sl, vo, o2, 3072);
        };
        auto issue1A = [&](int g, int slot) {
            const int k = kord(g < 9 ? g : g - 13, 9);
            LAS char* sl = lds + slot * G_STAGE + wid * 4096;
            BLDS1(rX, sl, vo, k * 2048, 0); BLDS1(rX, sl, vo, k * 2048, 1024);
            BLDS1(rX, sl + 2048, vo, k * 2048 + 18432, 0); BLDS1(rX, sl + 2048, vo, k * 2048 + 18432, 1024);
        };
        auto issue1 = [&](int g, int slot) { issue1B(g, slot); issue1A(g, slot); };
        const auto rA = RSRC((const char*)wsp + (size_t)(pm * 128 + wid * 32) * 4096), rB = RSRC(z2t + cg * 36864);
        const unsigned voA = (unsigned)(2 * l3 * 4096 + ((lane & 7) ^ l3) * 16);
        auto issueA = [&](int k, int slot) {
            LAS char* la = lds + slot * G_STAGE + wid * 4096;
            BLDS(rA, la, voA, k * 128, 0);
            BLDS(rA, la + 1024, voA, k * 128 + 16 * 4096, 0);
            BLDS(rA, la + 2048, voA, k * 128 + 4096, 0);
            BLDS(rA, la + 3072, voA, k * 128 + 17 * 4096, 0);
        };
        auto issueB = [&](int k, int slot) {
            LAS char* lb = lds + slot * G_STAGE + 16384;
            const int ok = (k >> 1) * 589824 + (k & 1) * 18432, ob = ok + wid * 4096;
            if (wid < 2) BLDS1(rB, lb + 16384 + wid * 1024, vo, ok + 16384 + wid * 1024, 0);
            lb += wid * 4096;
            BLDS1(rB, lb, vo, ob, 0); BLDS1(rB, lb, vo, ob, 1024); BLDS1(rB, lb, vo, ob, 2048); BLDS1(rB, lb, vo, ob, 3072);
        };
        const auto rW = RSRC(twout + (size_t)c5 * (72 * 4608));
        auto issue5 = [&](int t) {
            const int ow = kord5(t) * K5_STAGE;
            LAS char* sl = lds + ((t + 6) & 7) * K5_STAGE;
            if (wid < 2) BLDS(rW, sl + 16384 + wid * 1024, vo, ow + 16384 + wid * 1024, 0);
            BLDS(rW, sl + wid * 4096, vo, ow + wid * 4096, 0); BLDS(rW, sl + wid * 4096, vo, ow + wid * 4096, 1024);
            BLDS(rW, sl + wid * 4096, vo, ow + wid * 4096, 2048); BLDS(rW, sl + wid * 4096, vo, ow + wid * 4096, 3072);
        };
        auto load5 = [&](int p, int i) {
            const int t0 = p ? 2 * p + 6 : 2, ow = kord5(t0 + i) * K5_STAGE + wid * 4096;
            asm volatile("s_nop 4\n\tbuffer_load_dwordx4 %0, %4, %5, %6 offen\n\tbuffer_load_dwordx4 %1, %4, %5, %6 offen offset:1024\n\t"
                         "buffer_load_dwordx4 %2, %4, %5, %6 offen offset:2048\n\tbuffer_load_dwordx4 %3, %4, %5, %6 offen offset:3072"
                         : "+v"(wr[p][4 * i]), "+v"(wr[p][4 * i + 1]), "+v"(wr[p][4 * i + 2]), "+v"(wr[p][4 * i + 3]) : "v"(vo), "s"(rW), "s"(ow) : "memory");
            if (i) {
                const int oe = kord5(t0 + (wid >> 1)) * K5_STAGE + 16384 + (wid & 1) * 1024;
                asm volatile("s_nop 4\n\tbuffer_load_dwordx4 %0, %1, %2, %3 offen" : "+v"(wr[p][8]) : "v"(vo), "s"(rW), "s"(oe) : "memory");
            }
        };
        auto put5 = [&](int w0, int w1) {
#pragma unroll
            for (int w = w0; w < w1; ++w) {
                const int p = w / 9, i = w % 9 - 1;
                LAS char* sl = lds + (((p ? 2 * p + 6 : 2) + 6) & 7) * K5_STAGE + lane * 16;
                if (i < 0) *(LAS u32x4*)(sl + (wid >> 1) * K5_STAGE + 16384 + (wid & 1) * 1024) = wr[p][8];
                else *(LAS u32x4*)(sl + (i >> 2) * K5_STAGE + wid * 4096 + (i & 3) * 1024) = wr[p][i];
            }
            WAIT_L0();
        };
        const int row = wid * 32 + (lane & 31);
        auto step1 = [&](int t) { BAR(); issue1(t + 4, t & 3); };
        attn_producer(lds, ws, wl, fl, layer, pa, hp, wid, [&] { issue1B(0, 0); issue1B(1, 1); });
        __builtin_amdgcn_s_setprio(3);
        {
            const unsigned fo = flag_out(fl + F_X1, 256, pa, hp, lane), fi = flag_in(fl + F_X1, 256, pm * 16 + cg, 16, lane);
            BARX();
            if (wid == 0) flag_raise(ws, fo);
            issue1B(2, 2); issue1B(3, 3);
            asm volatile("" :: "s"(rX));
            if (wid == 0) flags_wait<X1_FIRST>(ws, fi);
            BARX();
        }
        issue1A(0, 0); issue1A(1, 1); issue1A(2, 2);
        WAIT_V(8); BAR();
        __builtin_amdgcn_s_setprio(0);
        issue1A(3, 3);
        WAIT_V(8); step1(0);
        WAIT_V(12); step1(1);
        {
            f32x4 sp[3];
            const char* ps = st + (size_t)(pm * 128 + wid * 32) * 64 + lane * 16;
            side_load(sp, ps, ps);
            WAIT_V(19); step1(2);
            WAIT_V(19); step1(3);
            WAIT_V(16); step1(4);
            WAIT_V(16); BAR(); if (layer == 0) load5(3, 0); issue1B(9, 1);
            __builtin_amdgcn_s_setprio(3);
            side_fence(sp);
#pragma unroll
            for (int i = 0; i < 2; ++i) lds_word_set(lds + G_TAB + (wid * 32 + i * 16 + (lane >> 2)) * 4, __float_as_uint(ln_rstd(sp[i])));
            WAIT_L0();
            __builtin_amdgcn_s_setprio(0);
        }
        WAIT_V(12); BAR(); if (layer == 0) load5(3, 1); issue1B(10, 2);
        WAIT_V(8); BAR(); if (layer == 0) load5(4, 0); issue1B(11, 3);
        WAIT_V(8); BAR(); if (layer == 0) load5(4, 1); issue1B(12, 0);
        if (layer == 1) {
            const auto rd = RSRC(out + (size_t)b * 9216 + wid * 2304);
            ST16(rd, vo, 0, ((u32x4){0u, 0u, 0u, 0u}), 16); ST16(rd, vo, 1024, ((u32x4){0u, 0u, 0u, 0u}), 16);
            if (lane < 16) ST16(rd, vo, 2048, ((u32x4){0u, 0u, 0u, 0u}), 16);
        }
        if (layer == 0) {
#pragma unroll
            for (int p = 0; p < 6; ++p) load5(p >> 1, p & 1);
        }
        WAIT_V(0);
        __builtin_amdgcn_s_setprio(3);
        BAR();
        if (lane < 32) {
            const f32x2 a = *(const LAS f32x2*)(lds + G_PART + row * 8), bq = *(const LAS f32x2*)(lds + G_PART + 1024 + row * 8);
            unsigned lo = __float_as_uint(a.x + bq.x), hi = __float_as_uint(a.y + bq.y);
            if (hi == 0xffffffffu) hi = 0x7fc00000u;
            __hip_atomic_store(zst + (size_t)(pm * 16 + cg) * 128 + row, ((unsigned long long)hi << 32) | lo, __ATOMIC_RELAXED, __HIP_MEMORY_SCOPE_AGENT);
        }
        step1(9); step1(10); step1(11);
        {
            const unsigned long long* src = zst + (size_t)(pm * 16 + (lane >> 5) * 8) * 128 + row + 512;
            u32x2 g[8];
            auto gather = [&] {
                asm volatile("global_load_dwordx2 %0, %8, off offset:-4096 sc1\n\tglobal_load_dwordx2 %1, %8, off offset:-3072 sc1\n\t"
                             "global_load_dwordx2 %2, %8, off offset:-2048 sc1\n\tglobal_load_dwordx2 %3, %8, off offset:-1024 sc1\n\t"
                             "global_load_dwordx2 %4, %8, off sc1\n\tglobal_load_dwordx2 %5, %8, off offset:1024 sc1\n\t"
                             "global_load_dwordx2 %6, %8, off offset:2048 sc1\n\tglobal_load_dwordx2 %7, %8, off offset:3072 sc1"
                             : "=&v"(g[0]), "=&v"(g[1]), "=&v"(g[2]), "=&v"(g[3]), "=&v"(g[4]), "=&v"(g[5]), "=&v"(g[6]), "=&v"(g[7]) : "v"(src) : "memory");
            };
            gather();
            WAIT_V(24); step1(12);
            WAIT_V(24); step1(13);
            WAIT_V(16);
            float s1, s2;
            for (unsigned spin = 0;; ++spin) {
                asm volatile("" : "+v"(g[0]), "+v"(g[1]), "+v"(g[2]), "+v"(g[3]), "+v"(g[4]), "+v"(g[5]), "+v"(g[6]), "+v"(g[7]));
                bool ok = true; s1 = 0.f; s2 = 0.f;
#pragma unroll
                for (int i = 0; i < 8; ++i) { ok &= g[i].y != 0xffffffffu; s1 += __uint_as_float(g[i].x); s2 += __uint_as_float(g[i].y); }
                if (__all(ok) || spin > 400000u) break;
                gather();
                WAIT_V(0);
            }
            auto x1 = __builtin_amdgcn_permlane32_swap(__float_as_uint(s1), __float_as_uint(s1), false, false);
            auto x2 = __builtin_amdgcn_permlane32_swap(__float_as_uint(s2), __float_as_uint(s2), false, false);
            const float mean = (__uint_as_float(x1[0]) + __uint_as_float(x1[1])) * (1.0f / 2304.0f);
            const float var = (__uint_as_float(x2[0]) + __uint_as_float(x2[1])) * (1.0f / 2304.0f) - mean * mean;
            const float rs = __builtin_amdgcn_rsqf(fmax2(var, 0.f) + 1e-5f);
            asm volatile("ds_write_b32 %0, %1" :: "v"((unsigned)(size_t)(lds + G_TAB + 512 + (lane >> 5) * 512 + row * 4)), "v"(lane < 32 ? rs : -mean * rs) : "memory");
            WAIT_L0();
        }
        __builtin_amdgcn_s_setprio(0);
        BAR(); issueA(kord(0, 32), 2);
        WAIT_V(12); BAR(); issue5(0); issue5(1);
        WAIT_V(12); BAR(); issueA(kord(1, 32), 0);
        __builtin_amdgcn_s_setprio(3);
        const unsigned fz = flag_out(fl + F_Z2, 256, cg, pm, lane);
        BARX();
        if (wid == 0) flag_raise(ws, fz);
        issueA(kord(2, 32), 1);
        if (wid == 0) { flags_wait<Z2_FIRST>(ws, flag_in(fl + F_Z2, 256, cg * 16 + pm, 2, lane)); lds_word_set(lds + G_WORD + 8 + layer * 8, 1u); }
        else lds_word_wait(lds + G_WORD + 8 + layer * 8);
        issueB(kord(0, 32), 2); issueB(kord(1, 32), 0); issueB(kord(2, 32), 1);
        WAIT_V(8); BAR();
        if (wid == 0) flags_wait<0>(ws, flag_in(fl + F_Z2, 256, cg * 16 + pm, 16, lane));
        __builtin_amdgcn_s_setprio(0);
        auto issue = [&](int t, int sl) { const int k = kord(t, 32); issueB(k, sl); issueA(k, sl); };
        WAIT_V(4); BAR(); issue(3, 2);
        ring_steps<1, 29, 8, 2>(issue);
        WAIT_V(8); BAR(); issue5(4);
        WAIT_V(4); BAR(); issue5(5); issue5(6);
        BAR(); issue5(7); put5(0, 9);
        {
            const unsigned fo = flag_out(fl + F_Y, 256, pm, cg, lane), fi = flag_in(fl + F_Y, 256, rh * 8 + c5, 16, lane);
            __builtin_amdgcn_s_setprio(3);
            WAIT_V(0); BARX();
            if (wid == 0) { flag_raise(ws, fo); flags_wait<Y_FIRST>(ws, fi); }
            BARX();
            __builtin_amdgcn_s_setprio(0);
        }
        BAR(); put5(9, 16);
        BAR(); put5(16, 23);
        BAR(); put5(23, 30);
        BAR(); put5(30, 36);
        BAR(); issue5(16); put5(36, 41);
        BAR(); issue5(17); put5(41, 45);
        if (wid < 2) WAIT_V(5); else WAIT_V(4);
        BAR();
        WAIT_V(0); BAR();
        if (layer == 0) { attn_issueB(lds, ws, hp, wid, lane, 0, 0); attn_issueB(lds, ws, hp, wid, lane, 1, 1); }
        BAR();
        if (layer == 0) {
            __builtin_amdgcn_s_setprio(3);
            const unsigned fo = flag_out(F_X2, 512, (rh >> 1) + wid, wid ? 16 + c5 : (rh & 1) * 8 + c5, wid == 0 || (wid == 1 && (rh & 1) && rh < 31) ? lane : 16);
            BARX();
            if (wid < 2) flag_raise(ws, fo);
        }
      }
        return;
    }
#pragma unroll 1
  for (int layer = 0; layer < NET_LAYERS; ++layer) {
    NET_IDS
    char* wl = ws + OFF_L0 + layer * SZ_L;
    const unsigned fl = layer * F_LAYER;
    const char* xin = wl + L_XB;
    char* y = wl + (layer ? L_Z2T : L_Y);
    STAMP_BEGIN;
    attn_consumer(lds, wl, layer, pa, hp, wid STAMP_FWD);
    STAMP_END(1 + 5 * layer);
    STAMP_BEGIN;
    gmlp_body(lds, wid & 1, wid >> 1, pm, cg, wl + (layer ? L_Y : L_Z2T), y, bsp STAMP_FWD);
    {
        WAIT_V(0);
        BARX();
        STAMP(11);
        const int lane = lane_id_fresh(), r = lane & 15, q = lane >> 4;
        const int wk = wid >> 1, rt = rh * 4 + (wid & 1) * 2 + wk;
        const Res rx = proj_residual_load(xin, rt, c5, lane);
        const LAS char *pw[3], *ph[3];
#pragma unroll
        for (int i = 0; i < 3; ++i) {
            pw[i] = opaque(lds + 3 * i * K5_STAGE + wk * 4608 + lane * 16);
            ph[i] = opaque(lds + 3 * i * K5_STAGE + wk * 4608 + 4096 + q * 128 + (r & 7) * 16);
        }
        const auto rY = RSRC(y + (size_t)(rt & ~1) * 73728 + wk * 1024);
        const unsigned vy[2] = {pin((unsigned)(wk * 73728 + lane * 16)), pin((unsigned)((1 - wk) * 73728 + lane * 16))};
        int dk[4];
#pragma unroll
        for (int i = 0; i < 4; ++i) dk[i] = pin_s((kord5(4 + i) - 4 - i) * 4096);
        auto yoff = [&](int t) { return pin_s(t == 0 || t == 17 ? kord5(t) * 4096 : t * 4096 + dk[t & 3]); };
        bf16x8 af[5][4], wf[4][5];
        auto lda = [&](int so, int i) { return __builtin_bit_cast(bf16x8, __builtin_amdgcn_raw_buffer_load_b128(rY, vy[i & 1] + (i >> 1) * 2048, so, 16)); };
        auto rdw = [&](int t, int c, int n) {
            const int sl = (t + 6) & 7, o = (sl % 3) * K5_STAGE + c * 9216;
            wf[2 * (t & 1) + c][n] = n < 4 ? LDS_FRAG(pw[sl / 3] + o + n * 1024) : LDS_FRAG(ph[sl / 3] + o);
        };
#pragma unroll
        for (int n = 0; n < 10; ++n) rdw(0, n / 5, n % 5);
        int so[18];
#pragma unroll
        for (int i = 0; i < 5; ++i) so[i] = yoff(i);
        so[17] = yoff(17);
        f32x4 v[5], u[5];
        proj_residual(v, rx);
#pragma unroll
        for (int n = 0; n < 5; ++n) v[n] = pin(v[n]);
        LAS char* xo = (LAS char*)opaque(lds + wid * 4608 + lane * 16);
        LAS char* xo4 = (LAS char*)opaque(lds + wid * 4608 + 4096 + (lane & 31) * 16);
        const LAS char* xi = opaque(lds + (wid ^ 2) * 4608 + lane * 16);
        const LAS char* xi4 = opaque(lds + (wid ^ 2) * 4608 + 4096 + (lane & 31) * 16);
        const ProjOut po = layer ? proj_out_final(out, rt, c5, lane) : proj_out(wl + SZ_L + L_XA, wl + SZ_L + L_STA, rt, c5, lane);
        asm volatile("" :: "s"(rY));
        BARX();
        STAMP(12);
#pragma unroll
        for (int i = 0; i < K5_PRE; ++i) { af[i >> 2][i & 3] = lda(so[i >> 2], i & 3); SCHED0(); }
        static_for<18>([&](auto tc) {
            constexpr int t = tc.value;
            bf16x8 (&a)[4] = af[t % 5];
#pragma unroll
            for (int h = 0; h < 4; ++h) {
                const int c = t < 17 ? h >> 1 : h & 1, m = t < 17 ? h & 1 : 1 - (h >> 1);
                if (t < 17 ? m == 0 : h < 2) { if (t == 17 && h == 1) WAIT_L(0); else WAIT_L(5); }
#pragma unroll
                for (int n = 0; n < 5; ++n) {
                    f32x4& e = m ? u[n] : v[n];
                    if (t == 0 && h == 1) MFMA_0(wf[0][n], a[1], e); else MFMA_T(wf[2 * (t & 1) + c][n], a[2 * c + m], e);
                    SCHED0();
                    if (t < 17 && m == 0) rdw(t + 1, c, n);
                    if (t < 14 && m == 1 && !(n & 1)) { const int i = y_load_at(6 * t + 3 * c + n / 2); if (i >= 0) af[(i >> 2) % 5][i & 3] = lda(so[i >> 2], i & 3); }
                    if (t < 12 && h == 3 && n == 3) so[t + 5] = yoff(t + 5);
                    if (t == 17 && h * 5 + n >= 13 && h * 5 + n < 18) { const int i = h * 5 + n - 13; *(LAS f32x4*)(i < 4 ? xo + i * 1024 : xo4) = u[i]; }
                    SCHED0();
                }
            }
            if ((t & 1) && t < 17) BAR();
            SCHED0();
        });
        {
            WAIT_L0(); BARX();
#pragma unroll
            for (int n = 0; n < 5; ++n) u[n] = *(const LAS f32x4*)(n < 4 ? xi + n * 1024 : xi4);
            SCHED0();
            asm volatile("s_nop 1" : "+v"(v[0]), "+v"(v[1]), "+v"(v[2]), "+v"(v[3]), "+v"(v[4]));
#pragma unroll
            for (int n = 0; n < 5; ++n) v[n] += u[n];
        }
        STAMP(13);
        if (layer) proj_store<true>(v, po);
        else { proj_store(v, po); WAIT_V(0); BARX(); }
    }
    STAMP_END(3 + 5 * layer);
  }
}

typedef short s16x4 __attribute__((ext_vector_type(4)));
DEVI u32x4 tr_frag(const LAS char* p, int four_rows) {
    const u32x2 a = __builtin_bit_cast(u32x2, __builtin_amdgcn_ds_read_tr16_b64_v4i16((LAS s16x4*)p));
    const u32x2 b = __builtin_bit_cast(u32x2, __builtin_amdgcn_ds_read_tr16_b64_v4i16((LAS s16x4*)(p + four_rows)));
    return (u32x4){a.x, a.y, b.x, b.y};
}
DEVI unsigned centre2(float a, float b, f32x2& e, float mean) {
    const f32x2 t = (f32x2){a, b} + e;
    const unsigned r = pack2(t.x, t.y);
    e = (t - (f32x2){bflo(r), bfhi(r)}) - mean;
    return r;
}
#define PREP_AUX 16
struct Retile { u32x4 f[9]; };
DEVI void retile_in_make(Retile& o, int u, int kh, const LAS char* img, int lane) {
    const int j = u % 9, c = lane & 15, q = lane >> 4, p = lane & 3;
    const LAS char* src = img + (q * 8 + (c >> 2)) * 64 + (u & 1) * 32 + (j < 8 ? (p & 1) * 16 + (p >> 1) * 8 : p * 8);
    f32x2 sum = {0.f, 0.f};
#pragma unroll
    for (int i = 0; i < 18; ++i) {
        const u32x4 f = tr_frag(src + (i < 9 ? 9 - 9 * kh + i : 9 * kh + i - 9) * 2048, 256);
        if (i >= 9) o.f[i - 9] = f;
#pragma unroll
        for (int e = 0; e < 4; ++e) sum += (f32x2){bflo(f[e]), bfhi(f[e])};
    }
    const float mean = xq_sum(sum.x + sum.y) * (1.0f / 576.0f);
    f32x2 er[2] = {{-mean, -mean}, {-mean, -mean}};
#pragma unroll
    for (int i = 0; i < 9; ++i)
#pragma unroll
        for (int e = 0; e < 4; ++e) o.f[i][e] = centre2(bflo(o.f[i][e]), bfhi(o.f[i][e]), er[e & 1], mean);
}
DEVI void retile_in_store(const Retile& o, char* twin, int u, int kh, int lane) {
    const int half = u >= 144, g = u - half * 144, cg = g / 9, j = g - cg * 9, c = lane & 15, q = lane >> 4;
    const auto rd = RSRC(twin + (size_t)cg * (18 * 18432) + (half * 18 + kh * 9) * 9216);
    const unsigned vo = (unsigned)(q * 256 + (j < 8 ? ((j & ~1) + (c >> 3)) * 1024 + ((j & 1) * 8 + (c & 7)) * 16 : 8192 + c * 16));
#pragma unroll
    for (int i = 0; i < 9; ++i) ST16(rd, vo, i * 9216, o.f[i], PREP_AUX);
}
template <int PITCH>
DEVI void retile_out_make(Retile& o, const LAS char* img, int lane) {
    const int c = lane & 15, q = lane >> 4, p = lane & 3;
    const LAS char* src = img + (q * 8 + (c >> 2)) * PITCH;
#pragma unroll
    for (int i = 0; i < 8; ++i) o.f[i] = tr_frag(src + p * 16 + (i >> 2) * 144 + ((i >> 1) & 1) * 64 + (i & 1) * 8, 4 * PITCH);
    o.f[8] = tr_frag(src + 128 + (p >> 1) * 144 + (p & 1) * 8, 4 * PITCH);
}
DEVI void retile_out_store(const Retile& o, char* dst, int nkc, int kc, int w, int lane) {
    const int c = lane & 15, q = lane >> 4;
    const auto rd = RSRC(dst + (size_t)(2 * w * nkc + kc) * 4608);
#pragma unroll
    for (int i = 0; i < 8; ++i) ST16(rd, lane * 16 + (i & 3) * 1024, (i >> 2) * nkc * 4608, o.f[i], PREP_AUX);
    ST16(rd, (c >> 3) * nkc * 4608 + 4096 + q * 128 + (c & 7) * 16, 0, o.f[8], PREP_AUX);
}

constexpr int P_CODE = 64, P_CW = 64  , P_WIN = 144, P_WOUT = 72, P_WO = 18, P_X = 64, P_Z = 32;
constexpr int P_TOTAL = P_CW + P_WIN + P_WOUT + P_WO + P_X + P_Z + P_CODE;

DEVI void prep_body(const u16* __restrict__ x, const u16* __restrict__ wqkv, const u16* __restrict__ wo, const u16* __restrict__ w_in,
                    const u16* __restrict__ w_out, char* __restrict__ ws) {
    __shared__ __attribute__((aligned(16))) char smem[37376 + 512];
    LAS char* lds = (LAS char*)smem;
    const int tid = threadIdx.x;
    int u = blockIdx.x;
    if (u < P_CW) {
        const int st = (u & ~15) + 2 * (u & 7) + ((u >> 3) & 1);
        if (st >= 54) return;
        const u16* src = wqkv + st * 32;
        u32x4 v[9];
#pragma unroll
        for (int i = 0; i < 9; ++i) v[i] = *(const u32x4*)(src + (size_t)((tid >> 2) + 64 * i) * 1728 + (tid & 3) * 8);
        const int c = tid & 31;
        const int col = st * 32 + c, part = col / 576, cc = col % 576, hh = cc / 18, dim = cc % 18, h = hh & 1;
        const int tile = dim < 16 ? 3 * h + part : 6;
        const int sl = dim < 16 ? dim : (part == 2 ? 8 + 2 * h : 4 * h + 2 * part) + (dim - 16);
        char* dst = ws + OFF_TWQKV + pin((unsigned)((hh >> 1) * (18 * 7168) + tile * 1024 + sl * 16 + (tid >> 7) * 7168 + ((tid >> 5) & 3) * 256));
        float a[8];
#pragma unroll
        for (int j = 0; j < 8; ++j) a[j] = 0.f;
#pragma unroll
        for (int i = 0; i < 9; ++i) {
            *(LAS u32x4*)(lds + ((tid >> 2) + 64 * i) * 64 + (tid & 3) * 16) = v[i];
            a[0] += bflo(v[i].x); a[1] += bfhi(v[i].x); a[2] += bflo(v[i].y); a[3] += bfhi(v[i].y);
            a[4] += bflo(v[i].z); a[5] += bfhi(v[i].z); a[6] += bflo(v[i].w); a[7] += bfhi(v[i].w);
        }
#pragma unroll
        for (int j = 0; j < 8; ++j) {
            a[j] += __builtin_amdgcn_update_dpp(0.f, a[j], 0x124, 0xf, 0xf, true);
            a[j] += __builtin_amdgcn_update_dpp(0.f, a[j], 0x128, 0xf, 0xf, true);
            a[j] = xq_sum(a[j]);
        }
        LAS float* red = (LAS float*)(lds + 36864);
        if ((tid & 63) < 4) {
#pragma unroll
            for (int j = 0; j < 8; ++j) red[(tid >> 6) * 32 + (tid & 3) * 8 + j] = a[j];
        }
        __syncthreads();
        const float mean = ((red[c] + red[32 + c]) + (red[64 + c] + red[96 + c])) * (1.0f / 576.0f);
        f32x2 er[2] = {{-mean, -mean}, {-mean, -mean}};
#pragma unroll
        for (int i = 0; i < 9; ++i) {
            const LAS u16* p = (const LAS u16*)lds + ((tid >> 5) + 8 * i) * 256 + c;
            u32x4 o;
#pragma unroll
            for (int j = 0; j < 4; ++j) o[j] = centre2(__uint_as_float((unsigned)p[j * 64] << 16), __uint_as_float((unsigned)p[j * 64 + 32] << 16), er[j & 1], mean);
            *(u32x4*)(dst + i * 14336) = o;
        }
        return;
    }
    u -= P_CW;
    if (u < P_WIN + P_WOUT) {
        const int w = __builtin_amdgcn_readfirstlane(tid >> 6), lane = tid & 63;
        const bool isin = u < P_WIN;
        const int st = (u & ~15) + 2 * (u & 7) + ((u >> 3) & 1);
        const auto rs = RSRC(isin ? (const char*)(w_in + st * 32) : (const char*)w_out + (size_t)(u - P_WIN) * 36864);
        const unsigned vo = isin ? (unsigned)((lane >> 2) * 9216 + (lane & 3) * 16) : (unsigned)lane * 16u;
        const int step = isin ? 16 * 9216 : 1024;
#pragma unroll
        for (int j = 0; j < 9; ++j) BLDS(rs, lds + (j * 4 + w) * 1024, vo, (j * 4 + w) * step, 0);
        WAIT_V(0); BAR();
        Retile rt;
        if (isin) { retile_in_make(rt, st * 2 + (w & 1), w >> 1, lds, lane); retile_in_store(rt, ws + OFF_TWIN, st * 2 + (w & 1), w >> 1, lane); }
        else { retile_out_make<1152>(rt, lds + w * 288, lane); retile_out_store(rt, ws + OFF_TWOUT, 72, u - P_WIN, w, lane); }
        return;
    }
    u -= P_WIN + P_WOUT;
    if (u < P_WO) {
        const int w = __builtin_amdgcn_readfirstlane(tid >> 6);
        const int lane = tid & 63;
        const auto rs = RSRC(wo + w * 144);
#pragma unroll
        for (int j = 0; j < 9; ++j) {
            const int i = lane + 64 * j, rl = (i * 3641) >> 16, kp = u * 32 + rl;
            const int row = kp < 512 ? 18 * (kp >> 4) + (kp & 15) : 18 * ((kp - 512) >> 1) + 16 + (kp & 1);
            BLDS(rs, lds + w * 9216 + j * 1024, (unsigned)(row * 1152 + (i - rl * 18) * 16), 0, 0);
        }
        WAIT_V(0);
        Retile rt;
        retile_out_make<288>(rt, lds + w * 9216, lane);
        retile_out_store(rt, ws + OFF_TWO, 18, u, w, lane);
        return;
    }
    u -= P_WO;
    if (u < P_X) {
        const int rt = u * 2 + (tid >> 7), r = tid & 15, cg = (tid >> 4) & 7;
        const int sg = rt * 16 + r;
        u32x4 v[9];
#pragma unroll
        for (int i = 0; i < 9; ++i) v[i] = *(const u32x4*)(x + (size_t)u * 18432 + (tid + 256 * i) * 8);
#pragma unroll
        for (int i = 0; i < 9; ++i) { const int p = tid + 256 * i, row = (p * 3641) >> 18; *(LAS u32x4*)(lds + p * 16 + row * 16) = v[i]; }
        __syncthreads();
#pragma unroll
        for (int i = 0; i < 9; ++i) v[i] = *(const LAS u32x4*)(lds + (2 * r + (tid >> 7)) * 1168 + cg * 144 + i * 16);
        float sum = 0.f;
#pragma unroll
        for (int i = 0; i < 9; ++i) {
            sum += ((bflo(v[i].x) + bfhi(v[i].x)) + (bflo(v[i].y) + bfhi(v[i].y))) + ((bflo(v[i].z) + bfhi(v[i].z)) + (bflo(v[i].w) + bfhi(v[i].w)));
            ST16(RSRC(ws + OFF_L0 + L_XA), rt * 18432 + cg * 2304 + r * 16, i * 256, v[i], 16);
        }
        float m2 = 0.f;
#pragma unroll
        for (int i = 0; i < 9; ++i) {
            float d;
            d = bflo(v[i].x); m2 += d * d; d = bfhi(v[i].x); m2 += d * d; d = bflo(v[i].y); m2 += d * d; d = bfhi(v[i].y); m2 += d * d;
            d = bflo(v[i].z); m2 += d * d; d = bfhi(v[i].z); m2 += d * d; d = bflo(v[i].w); m2 += d * d; d = bfhi(v[i].w); m2 += d * d;
        }
        *(f32x2*)(ws + OFF_L0 + L_STA + (size_t)sg * 64 + cg * 8) = (f32x2){sum, m2};
        return;
    }
    u -= P_X;
    if (u >= P_Z) {
        u -= P_Z;
        unsigned long long c0;
        asm volatile("s_getpc_b64 s[40:41]\n\ts_add_u32 s40, s40, gm_code_begin@rel32@lo+4\n\ts_addc_u32 s41, s41, gm_code_begin@rel32@hi+12\n\ts_mov_b64 %0, s[40:41]"
                     : "=s"(c0) :: "s40", "s41", "scc");
        const unsigned long long pc = __builtin_amdgcn_s_getpc();
        const unsigned len = pc > c0 && pc - c0 < 262144ull ? (unsigned)(pc - c0) & ~15u : 0u;
        const auto rs = __builtin_amdgcn_make_buffer_rsrc((void*)(c0 & ~15ull), 0, len, 0x00020000);
        unsigned x = 0;
        for (unsigned o = ((u >> 3) * 256 + tid) * 16; o < len; o += 8 * 256 * 16) x ^= __builtin_amdgcn_raw_buffer_load_b128(rs, o, 0, 0)[0];
        asm volatile("" :: "v"(x));
        return;
    }
    {
        const auto rw = RSRC(ws);
        const u32x4 ones = {0xffffffffu, 0xffffffffu, 0xffffffffu, 0xffffffffu}, zero = {0u, 0u, 0u, 0u};
#pragma unroll
        for (int i = 0; i < 4; ++i) ST16(rw, (unsigned)OFF_ZST + (u * 1024 + i * 256 + tid) * 16, 0, ones, 16);
#pragma unroll
        for (int i = 0; i < 5; ++i) ST16(rw, (unsigned)OFF_CNT + (u * 1280 + i * 256 + tid) * 16, 0, zero, 16);
        ST16(rw, (unsigned)(OFF_L0 + L_STB) + (u * 256 + tid) * 16, 0, zero, 16);
#pragma unroll
        for (int i = 0; i < 2; ++i) ST16(rw, (unsigned)(OFF_L0 + L_O) + (u * 4 + i * 2 + (tid >> 7)) * 18432 + 16384 + (tid & 127) * 16, 0, zero, 16);
        if (tid < 144) {
            const int e = u * 144 + tid, f = e >> 4;
            *(u32x4*)(ws + OFF_TWQKV + (size_t)f * 7168 + 6144 + ((e >> 2) & 3) * 256 + (12 + (e & 3)) * 16) = (u32x4){0u, 0u, 0u, 0u};
        }
    }
}

__global__ __launch_bounds__(256) void k_prep(const u16* __restrict__ x, const u16* __restrict__ wqkv, const u16* __restrict__ wo,
                                              const u16* __restrict__ w_in, const u16* __restrict__ w_out, char* __restrict__ ws DBG_ARG) {
    asm volatile("" :: "s"(x), "s"(wqkv), "s"(wo), "s"(w_in), "s"(w_out), "s"(ws));
    prep_body(x, wqkv, wo, w_in, w_out, ws);
}

}

#define STOP_CHECK(n)

extern "C" void kernel_launch(void* const* d_in, const int* in_sizes, int n_in, void* d_out, int out_size, void* d_ws, size_t ws_size,
                              hipStream_t stream) {
    (void)in_sizes; (void)n_in; (void)out_size; (void)ws_size;
    const u16* x = (const u16*)d_in[0];
    const u16* wqkv = (const u16*)d_in[1];
    const u16* wo = (const u16*)d_in[2];
    const u16* w_in = (const u16*)d_in[4];
    const u16* w_sp = (const u16*)d_in[5];
    const u16* b_sp = (const u16*)d_in[6];
    const u16* w_out = (const u16*)d_in[7];
    char* ws = (char*)d_ws;
    (void)hipFuncSetAttribute((const void*)k_net, hipFuncAttributeMaxDynamicSharedMemorySize, NET_LDS);
    k_prep<<<P_TOTAL, 256, 0, stream>>>(x, wqkv, wo, w_in, w_out, ws DBG_LAUNCH(0));
    STOP_CHECK(1);
    k_net<<<256, 512, NET_LDS, stream>>>(ws, w_sp, b_sp, (char*)d_out DBG_LAUNCH(0));
}
```

```cpp
#define PROBE_A (-1)
#define PROBE_B (1)
#define PROBE_MUL 1
#include <hip/hip_runtime.h>
#include <stdint.h>

namespace {

typedef unsigned short u16;
typedef short bf16x8 __attribute__((ext_vector_type(8)));
typedef float f32x4 __attribute__((ext_vector_type(4)));
typedef float f32x2 __attribute__((ext_vector_type(2)));
typedef unsigned u32x4 __attribute__((ext_vector_type(4)));
typedef unsigned u32x2 __attribute__((ext_vector_type(2)));
typedef __bf16 bf16x2_t __attribute__((ext_vector_type(2)));
#define LAS __attribute__((address_space(3)))
#define DEVI __device__ __forceinline__

constexpr int S_ = 2048, D_ = 576, FH = 2304;

constexpr size_t SZ_TWQKV = (size_t)16 * 18 * 7 * 1024;
constexpr size_t SZ_TWO = (size_t)8 * 18 * 4608;
constexpr size_t SZ_TWIN = (size_t)16 * 18 * 18 * 1024;
constexpr size_t SZ_TWOUT = (size_t)8 * 72 * 4608;
constexpr size_t SZ_X = (size_t)S_ * D_ * 2;
constexpr size_t SZ_Z = (size_t)S_ * FH * 2;
constexpr size_t SZ_ST = (size_t)S_ * 64;
constexpr size_t SZ_ZST = (size_t)16 * 16 * 128 * 8;
constexpr size_t OFF_TWQKV = 0;
constexpr size_t OFF_TWO = OFF_TWQKV + SZ_TWQKV;
constexpr size_t OFF_TWIN = OFF_TWO + SZ_TWO;
constexpr size_t OFF_TWOUT = OFF_TWIN + SZ_TWIN;
constexpr size_t OFF_CNT = OFF_TWOUT + SZ_TWOUT;
constexpr size_t SZ_CNT = 10 * 65536;
constexpr size_t OFF_ZST = OFF_CNT + SZ_CNT;
constexpr size_t OFF_L0 = OFF_ZST + 2 * SZ_ZST;
constexpr size_t L_XA = 0, L_STA = L_XA + SZ_X, L_O = L_STA + SZ_ST, L_XB = L_O + SZ_X, L_STB = L_XB + SZ_X, L_Z2T = L_STB + SZ_ST, L_Y = L_Z2T + SZ_Z;
constexpr size_t SZ_L = 0;
static_assert(OFF_L0 + L_Y + SZ_Z <= ((size_t)256 << 20), "workspace");
constexpr unsigned F_O = OFF_CNT, F_X1 = F_O + 65536, F_Z2 = F_O + 131072, F_Y = F_O + 196608, F_LAYER = 262144, F_X2 = F_O + 2 * F_LAYER;

DEVI unsigned pack2(float a, float b) { f32x2 v = {a, b}; bf16x2_t r = __builtin_convertvector(v, bf16x2_t); return __builtin_bit_cast(unsigned, r); }
DEVI float bflo(unsigned u) { return __uint_as_float(u << 16); }
DEVI float bfhi(unsigned u) { return __uint_as_float(u & 0xffff0000u); }
DEVI int slot2row(int sg) { return (sg & ~31) + 2 * (sg & 15) + ((sg >> 4) & 1); }
DEVI float fmax2(float a, float b) { return __builtin_amdgcn_fmed3f(a, b, __builtin_inff()); }
#define XQ4(OP, MID) \
    asm volatile("s_nop 1\n\tv_permlane32_swap_b32 %0, %1\n\tv_permlane32_swap_b32 %2, %3\n\t" OP " %0, %0, %1\n\t" OP " %2, %2, %3\n\t" \
                 "s_nop 1\n\tv_permlane16_swap_b32 %0, %2\n\t" OP " %0, %0, %2\n\t" MID \
                 "v_mov_b32 %2, %0\n\ts_nop 1\n\tv_permlane16_swap_b32 %0, %2\n\tv_mov_b32 %1, %0\n\tv_mov_b32 %3, %2\n\ts_nop 0\n\t" \
                 "v_permlane32_swap_b32 %0, %1\n\tv_permlane32_swap_b32 %2, %3" : "+v"(x[0]), "+v"(x[1]), "+v"(x[2]), "+v"(x[3]))
DEVI void xq4_max(float (&x)[4]) { XQ4("v_max_f32", ""); }
DEVI void xq4_sum_rcp(float (&x)[4]) { XQ4("v_add_f32", "v_rcp_f32 %0, %0\n\ts_nop 0\n\t"); }
template <class T> DEVI T pin(T v) { asm volatile("" : "+v"(v)); return v; }
template <int V> struct IC { static constexpr int value = V; };
template <int N, int I = 0, class F> DEVI void static_for(F&& f) { if constexpr (I < N) { f(IC<I>{}); static_for<N, I + 1>(f); } }
DEVI int pin_s(int v) { asm volatile("" : "+s"(v)); return v; }
DEVI float xq_sum(float v) {
    auto a = __builtin_amdgcn_permlane32_swap(__float_as_uint(v), __float_as_uint(v), false, false);
    v = __uint_as_float(a[0]) + __uint_as_float(a[1]);
    auto b = __builtin_amdgcn_permlane16_swap(__float_as_uint(v), __float_as_uint(v), false, false);
    return __uint_as_float(b[0]) + __uint_as_float(b[1]);
}
#define BLDS(rs, l, vo, so, off) __builtin_amdgcn_raw_ptr_buffer_load_lds((rs), (LAS void*)(l), 16, (vo), (so), (off), 0)
#define BLDS1(rs, l, vo, so, off) __builtin_amdgcn_raw_ptr_buffer_load_lds((rs), (LAS void*)(l), 16, (vo), (so), (off), 16)
#define RSRC(p) __builtin_amdgcn_make_buffer_rsrc((void*)(p), 0, 0x7fffffff, 0x00020000)
#define ST16(rs, vo, so, v, aux) __builtin_amdgcn_raw_buffer_store_b128((v), (rs), (vo) + (so), 0, aux)
#define ST8(rs, vo, so, v, aux) __builtin_amdgcn_raw_buffer_store_b64((v), (rs), (vo), (so), aux)
#define WAIT_V(n) asm volatile("s_waitcnt vmcnt(%0)" ::"i"(n) : "memory")
#define WAIT_L0() asm volatile("s_waitcnt lgkmcnt(0)" ::: "memory")
#define BAR() do { asm volatile("" ::: "memory"); __builtin_amdgcn_s_barrier(); asm volatile("" ::: "memory"); } while (0)
#define BARX() do { __builtin_amdgcn_sched_barrier(0); BAR(); __builtin_amdgcn_sched_barrier(0); } while (0)
#define LOOP_ALIGN() asm volatile(".p2align 6")
#define SCHED0() __builtin_amdgcn_sched_barrier(0)
#define MFMA(a, b, c) __builtin_amdgcn_mfma_f32_16x16x32_bf16((a), (b), (c), 0, 0, 0)
#define MFMA_T(a, b, c) asm volatile("v_mfma_f32_16x16x32_bf16 %0, %1, %2, %0" : "+v"(c) : "v"(a), "v"(b))
#define MFMA_0(a, b, c) asm volatile("v_mfma_f32_16x16x32_bf16 %0, %1, %2, 0" : "=&v"(c) : "v"(a), "v"(b))
#define WAIT_L(n) __builtin_amdgcn_s_waitcnt(0xC07F | ((n) << 8))
#define LDS_FRAG(p) (*(const LAS bf16x8*)(p))
DEVI int lane_id_fresh() { int z = 0; asm volatile("" : "+v"(z)); return __builtin_amdgcn_mbcnt_hi(~0u, __builtin_amdgcn_mbcnt_lo(~0u, z)); }
DEVI const LAS char* opaque(const LAS char* p) { asm volatile("" : "+v"(p)); return p; }
#define DBG_ARG
#define DBG_LAUNCH(n)
#define STAMP(i) do { if (STAMP_SEC * 16 + (i) == (PROBE_A & 31) && st_l == (PROBE_A >> 5)) { SCHED0(); asm volatile("s_memrealtime %0\n\ts_waitcnt lgkmcnt(0)" : "=s"(st_[0]) :: "memory"); SCHED0(); } \
    if (STAMP_SEC * 16 + (i) == (PROBE_B & 31) && st_l == (PROBE_B >> 5)) { SCHED0(); asm volatile("s_memrealtime %0\n\ts_waitcnt lgkmcnt(0)" : "=s"(st_[1]) :: "memory"); SCHED0(); } } while (0)
#define STAMP_DECL unsigned long long st_[2] = {0ull, 0ull}
#define STAMP_BEGIN const int st_l = layer; STAMP(1)
#define STAMP_END(n) STAMP(14)
#define STAMP_PARAM , unsigned long long (&st_)[2], const int st_l
#define STAMP_FWD , st_, st_l

DEVI float ln_rstd(const f32x4 p) {
    float s = p[0] + p[2], q = p[1] + p[3];
    s += __builtin_amdgcn_update_dpp(0.f, s, 0xB1, 0xf, 0xf, true); q += __builtin_amdgcn_update_dpp(0.f, q, 0xB1, 0xf, 0xf, true);
    s += __builtin_amdgcn_update_dpp(0.f, s, 0x4E, 0xf, 0xf, true); q += __builtin_amdgcn_update_dpp(0.f, q, 0x4E, 0xf, 0xf, true);
    const float mean = s * (1.0f / 576.0f);
    return __builtin_amdgcn_rsqf(fmax2(q * (1.0f / 576.0f) - mean * mean, 0.f) + 1e-5f);
}

struct Res { u32x4 a, c; u32x2 e; };
DEVI Res proj_residual_load(const char* xin, int rt, int cg, int lane) {
    const auto rs = RSRC(xin + (size_t)rt * 18432 + cg * 2304);
    Res x;
    x.a = __builtin_amdgcn_raw_buffer_load_b128(rs, lane * 16, 0, 16); x.c = __builtin_amdgcn_raw_buffer_load_b128(rs, lane * 16 + 1024, 0, 16);
    x.e = __builtin_amdgcn_raw_buffer_load_b64(rs, 2048 + (lane & 15) * 16 + ((lane >> 4) & 1) * 8, 0, 16);
    return x;
}
DEVI void proj_residual(f32x4 (&v)[5], const Res& x) {
    v[0] = (f32x4){bflo(x.a.x), bfhi(x.a.x), bflo(x.a.y), bfhi(x.a.y)}; v[1] = (f32x4){bflo(x.a.z), bfhi(x.a.z), bflo(x.a.w), bfhi(x.a.w)};
    v[2] = (f32x4){bflo(x.c.x), bfhi(x.c.x), bflo(x.c.y), bfhi(x.c.y)}; v[3] = (f32x4){bflo(x.c.z), bfhi(x.c.z), bflo(x.c.w), bfhi(x.c.w)};
    v[4] = (f32x4){bflo(x.e.x), bfhi(x.e.x), bflo(x.e.y), bfhi(x.e.y)};
}
struct ProjOut { __amdgpu_buffer_rsrc_t x, st; unsigned v0, v1, v2, vs; };
DEVI ProjOut proj_out(char* xout, char* stout, int rt, int cg, int lane) {
    const int r = lane & 15, q = lane >> 4;
    ProjOut p = {RSRC(xout + (size_t)rt * 18432 + cg * 2304), RSRC(stout + (size_t)rt * 1024 + cg * 8),
                 pin((unsigned)lane * 16u), pin((unsigned)lane * 16u + 1024u), pin((unsigned)(2048 + r * 16 + (q & 1) * 8)), pin((unsigned)(r * 64 + (q >> 1) * 4))};
    asm volatile("" :: "s"(p.x), "s"(p.st));
    return p;
}
DEVI ProjOut proj_out_final(char* out, int rt, int cg, int lane) {
    const int q = lane >> 4;
    const unsigned o = (unsigned)(slot2row(rt * 16 + (lane & 15)) * 1152 + cg * 144);
    ProjOut p = {RSRC(out), RSRC(out), pin(o + q * 16), pin(o + 64 + q * 16), pin(o + 128 + (q & 1) * 8), 0u};
    return p;
}
template <bool FINAL = false>
DEVI void proj_store(f32x4 (&v)[5], const ProjOut& p) {
    ST16(p.x, p.v0, 0, ((u32x4){pack2(v[0][0], v[0][1]), pack2(v[0][2], v[0][3]), pack2(v[1][0], v[1][1]), pack2(v[1][2], v[1][3])}), FINAL ? 0 : 16);
    SCHED0();
    asm volatile("" : "+v"(v[2]), "+v"(v[3]), "+v"(v[4]));
    ST16(p.x, p.v1, 0, ((u32x4){pack2(v[2][0], v[2][1]), pack2(v[2][2], v[2][3]), pack2(v[3][0], v[3][1]), pack2(v[3][2], v[3][3])}), FINAL ? 0 : 16);
    SCHED0();
    ST8(p.x, p.v2, 0, ((u32x2){pack2(v[4][0], v[4][1]), pack2(v[4][2], v[4][3])}), FINAL ? 0 : 16);
    SCHED0();
    if (!FINAL) {
        const f32x4 s4 = ((v[0] + v[1]) + (v[2] + v[3])) + v[4] * 0.5f;
        const f32x4 q4 = ((v[0] * v[0] + v[1] * v[1]) + (v[2] * v[2] + v[3] * v[3])) + v[4] * v[4] * 0.5f;
        auto x = __builtin_amdgcn_permlane32_swap(__float_as_uint((s4[0] + s4[2]) + (s4[1] + s4[3])), __float_as_uint((q4[0] + q4[2]) + (q4[1] + q4[3])), false, false);
        const float t = __uint_as_float(x[0]) + __uint_as_float(x[1]);
        auto y = __builtin_amdgcn_permlane16_swap(__float_as_uint(t), __float_as_uint(t), false, false);
        __builtin_amdgcn_raw_buffer_store_b32(__float_as_uint(__uint_as_float(y[0]) + __uint_as_float(y[1])), p.st, p.vs, 0, 16);
        SCHED0();
    }
}

DEVI void side_load(f32x4 (&d)[3], const char* p, const char* p2) {
    asm volatile("global_load_dwordx4 %0, %3, off sc1\n\tglobal_load_dwordx4 %1, %3, off offset:1024 sc1\n\tglobal_load_dwordx4 %2, %4, off sc1"
                 : "=&v"(d[0]), "=&v"(d[1]), "=&v"(d[2]) : "v"(p), "v"(p2) : "memory");
}
DEVI void side_fence(f32x4 (&d)[3]) { asm volatile("" : "+v"(d[0]), "+v"(d[1]), "+v"(d[2])); }
DEVI void lds_word_set(LAS char* p, unsigned v) { asm volatile("ds_write_b32 %0, %1" :: "v"((unsigned)(size_t)p), "v"(v) : "memory"); }
template <int T0, int T1, int P, int S0 = 0, class ISSUE>
DEVI void ring_steps(ISSUE&& issue) {
    int slot = (T0 + S0) % 3;
    LOOP_ALIGN();
#pragma unroll 1
    for (int t = T0; t < T1; ++t) {
        WAIT_V(P); BAR();
        issue(t + 3, slot);
        slot = slot == 2 ? 0 : slot + 1;
    }
}

DEVI unsigned flag_out(unsigned use, int pitch, int group, int writer, int lane) { return pin(lane < 16 ? use + (group * 16 + lane) * pitch + writer * 16 : 0x80000000u); }
DEVI unsigned flag_in(unsigned use, int pitch, int reader, int n, int lane) { return pin(use + reader * pitch + (lane < n ? lane : 0) * 16); }
DEVI void flag_raise(const char* ws, unsigned vo) { ST16(RSRC(ws), vo, 0, ((u32x4){1u, 1u, 1u, 1u}), 16); SCHED0(); }
template <int FIRST>
DEVI void flags_wait(const char* ws, unsigned vo) {
    unsigned v, n = 400000u;
    asm volatile("s_nop 4\n\ts_sleep %4\n"
                 "1:\n\tbuffer_load_dword %0, %2, %3, 0 offen sc1\n\ts_waitcnt vmcnt(0)\n\tv_cmp_eq_u32 vcc, 0, %0\n\ts_cbranch_vccz 2f\n\t"
                 "s_sleep 1\n\ts_sub_u32 %1, %1, 1\n\ts_cbranch_scc0 1b\n2:"
                 : "=&v"(v), "+s"(n) : "v"(vo), "s"(RSRC(ws)), "i"(FIRST) : "vcc", "scc", "memory");
    SCHED0();
}
DEVI void lds_word_wait(LAS char* p) {
#pragma unroll 1
    for (unsigned spin = 0; spin < 40000000u; ++spin) {
        unsigned v;
        asm volatile("ds_read_b32 %0, %1\n\ts_waitcnt lgkmcnt(0)" : "=v"(v) : "v"((unsigned)(size_t)p) : "memory");
        if (__builtin_amdgcn_readfirstlane(v) != 0u) break;
        __builtin_amdgcn_s_sleep(1);
    }
}

constexpr int K1_STAGE = 32768;
constexpr int WO_OFF = 69632;
constexpr int G_PART = WO_OFF + 82944;
constexpr int G_TAB = G_PART + 2048;
constexpr int K1_SCR = G_TAB + 1536;
constexpr int G_WORD = K1_SCR + 576;
constexpr int NET_LDS = G_WORD + 64;

#define WO_PRE 12
#define WO_W 33
#define O_FIRST 10
#define X1_FIRST 6
#define X2_FIRST 6
#define Y_FIRST 10
#define Z2_FIRST 10
DEVI void attn_issueB(LAS char* lds, const char* ws, int hp, int wid, int lane, int s, int slot) {
    const auto rB = RSRC(ws + OFF_TWQKV + (size_t)hp * (18 * 7168));
    LAS char* sl = lds + slot * K1_STAGE + 18432 + wid * 4096;
    const int off = wid * 4096 + s * 14336;
    BLDS(rB, sl, lane * 16, off, 0); BLDS(rB, sl, lane * 16, off, 1024);
    if (wid < 3) { BLDS(rB, sl, lane * 16, off, 2048); BLDS(rB, sl, lane * 16, off, 3072); }
}
template <class NEXT>
DEVI void attn_producer(LAS char* lds, const char* ws, const char* wl, unsigned fl, int layer, int pm, int hp, int wid, NEXT&& next) {
    const int lane = lane_id_fresh(), r = lane & 15, q = lane >> 4;
    const int pmh = pm > 0 ? pm : 1;
    const auto rA = RSRC(wl + L_XA);
    const unsigned vo = lane * 16, voh = (unsigned)((r >> 3) * 18432 + q * 256 + (8 + (r & 7)) * 16);
    const int oA = (pm * 8 + 2 * wid) * 18432, oh = (pmh * 8 - 2) * 18432;
    auto issueA = [&](int s, int slot) {
        LAS char* sl = lds + slot * K1_STAGE;
        if (wid == 3) { BLDS1(rA, sl, voh, oh + s * 2048, 0); BLDS1(rA, sl, voh, oh + s * 2048, 1024); }
        BLDS1(rA, sl + 2048 + wid * 4096, vo, oA + s * 2048, 0); BLDS1(rA, sl + 2048 + wid * 4096, vo, oA + s * 2048, 1024);
        BLDS1(rA, sl + 4096 + wid * 4096, vo, oA + s * 2048 + 18432, 0); BLDS1(rA, sl + 4096 + wid * 4096, vo, oA + s * 2048 + 18432, 1024);
    };
    auto issue = [&](int s, int slot) { issueA(s, slot); attn_issueB(lds, ws, hp, wid, lane, s, slot); };
    __builtin_amdgcn_s_setprio(3);
    if (layer == 0) {
        issue(0, 0); issue(1, 1);
        WAIT_V(8);
    } else {
        attn_issueB(lds, ws, hp, wid, lane, 2, 2);
        const unsigned fi = flag_in(F_X2, 512, pm * 16 + hp, pm > 0 ? 24 : 16, lane);
        if (wid == 0) flags_wait<X2_FIRST>(ws, fi);
        BARX();
        issueA(0, 0); issueA(1, 1); issueA(2, 2);
        WAIT_V(8);
    }
    BAR();
    __builtin_amdgcn_s_setprio(0);
    if (layer == 0) issue(2, 2); else WAIT_V(4);
    ring_steps<0, 2, 8>(issue);
    {
        const int hr = lane >> 2;
        f32x4 sp[3];
        side_load(sp, wl + L_STA + (size_t)(pm * 128 + wid * 32) * 64 + lane * 16, wl + L_STA + (size_t)((pmh * 8 - 2 + (hr >> 3)) * 16 + 8 + (hr & 7)) * 64 + (lane & 3) * 16);
        WAIT_V(11); BAR(); issue(5, 2);
        ring_steps<3, 6, 8>(issue);
        __builtin_amdgcn_s_setprio(3);
        side_fence(sp);
#pragma unroll
        for (int i = 0; i < 3; ++i) lds_word_set(lds + K1_SCR + (i < 2 ? 16 + wid * 32 + i * 16 + hr : hr) * 4, __float_as_uint(ln_rstd(sp[i])));
        WAIT_L0();
        __builtin_amdgcn_s_setprio(0);
    }
    WAIT_V(8); BAR();
    WAIT_V(0); BAR();
    BAR();
    const auto rW = RSRC(ws + OFF_TWO + (size_t)(hp >> 1) * 82944);
#pragma unroll
    for (int i = 0; i < 5; ++i) {
        const int off = (i * 4 + wid) * 4096;
        LAS char* d = lds + WO_OFF + off;
        BLDS(rW, d, vo, off, 0); BLDS(rW, d, vo, off, 1024); BLDS(rW, d, vo, off, 2048); BLDS(rW, d, vo, off, 3072);
    }
    if (wid == 0) BLDS(rW, lds + WO_OFF + 81920, vo, 81920, 0);
    next();
    const unsigned fo = flag_out(fl + F_O, 256, pm, hp, lane), fi = flag_in(fl + F_O, 256, pm * 16 + hp, 16, lane);
    WAIT_V(0);
    if (wid == 0) __builtin_amdgcn_s_setprio(3);
    BARX();
    if (wid == 0) { flag_raise(ws, fo); flags_wait<O_FIRST>(ws, fi); }
    BARX();
}

#define STAMP_SEC 0
DEVI void attn_consumer(LAS char* lds, char* wl, int layer, int pm, int hp, int wid STAMP_PARAM) {
    const int lane = lane_id_fresh(), r = lane & 15, q = lane >> 4;
    const char* xa = wl + L_XA;
    char* o = wl + L_O;
    const int T = wid & 1, h = wid >> 1;
    const LAS char* ph = lds + q * 256 + ((r ^ (T ? 0 : 8)) * 16);
    const LAS char* pa = lds + T * 2048 + lane * 16;
    const LAS char* pb = lds + h * 3072 + lane * 16;
    const LAS char* pt = lds + lane * 16;
    f32x4 acc[5][4];
#pragma unroll
    for (int m = 0; m < 5; ++m)
#pragma unroll
        for (int n = 0; n < 4; ++n) if (m + n) acc[m][n] = pin((f32x4){0.f, 0.f, 0.f, 0.f});
    bf16x8 fa[2][5], fb[2][4];
    auto rd1 = [&](int kk, int i) {
        if (i < 3) fb[kk][i] = LDS_FRAG(pb + 18432 + kk * 7168 + i * 1024);
        else if (i == 3) fb[kk][3] = LDS_FRAG(pt + 24576 + kk * 7168);
        else if (i == 4) fa[kk][0] = LDS_FRAG(ph + kk * 1024);
        else if (i < 9) fa[kk][i - 4] = LDS_FRAG(pa + 2048 + kk * 1024 + (i - 5) * 4096);
    };
    auto chunk = [&](int kk, int d) {
#pragma unroll
        for (int i = 1; i < 20; ++i) {
            const int m = i >> 2, n = i & 3;
            const bf16x8 &A = fa[kk][m], &B = fb[kk][n];
            acc[m][n] = n != 2 ? MFMA(B, A, acc[m][n]) : MFMA(A, B, acc[m][n]);
            SCHED0();
            if (i <= 9) rd1(kk ^ 1, i - 1);
            if (kk == 0) { if (i == 11) ph = opaque(ph + d); if (i == 12) pa = opaque(pa + d); if (i == 13) pb = opaque(pb + d); if (i == 14) pt = opaque(pt + d); }
            SCHED0();
        }
    };
    f32x4 mb, mb0;
#pragma unroll
    for (int j = 0; j < 4; ++j) {
        const bool ok = ((r - 4 * q - j) & 15) < 8;
        mb[j] = ok ? 0.f : -__builtin_inff();
        mb0[j] = (ok && (pm > 0 || 4 * q + j <= r)) ? 0.f : -__builtin_inff();
    }
    mb = pin(mb); mb0 = pin(mb0);
    const unsigned mp0 = pin((4 * q > r ? 0xffffu : 0u) | (4 * q + 1 > r ? 0xffff0000u : 0u)), mp1 = pin((4 * q + 2 > r ? 0xffffu : 0u) | (4 * q + 3 > r ? 0xffff0000u : 0u));
    const unsigned qtm = pin(q == h ? 0xffffffffu : 0u);
    const unsigned vom = pin((unsigned)(r * 16 + hp * 1024 + (2 * h + (q >> 1)) * 256 + (q & 1) * 8)), vot = pin((unsigned)(r * 16 + 16384 + (hp >> 1) * 256 + (hp & 1) * 8 + h * 4));
    const LAS char* tk0 = opaque(lds + K1_SCR + ((q * 4) ^ (T ? 0 : 8)) * 4);
    const LAS char* tk = opaque(lds + K1_SCR + ((1 + T) * 16 + q * 4) * 4);
    const LAS char* tq = opaque(lds + K1_SCR + ((1 + T) * 16 + r) * 4);
    ph = opaque(ph); pa = opaque(pa); pb = opaque(pb); pt = opaque(pt);
    STAMP(2);
    if (layer) BAR();
    BARX();
    STAMP(3);
#pragma unroll
    for (int i = 0; i < 9; ++i) rd1(0, i);
    SCHED0();
    const int rt = pm * 8 + (hp & 1) * 4 + wid, cg = hp >> 1;
    const Res rx = proj_residual_load(xa, rt, cg, lane);
    SCHED0();
    int slot = 0;
    LOOP_ALIGN();
#pragma unroll 1
    for (int t = 0; t < 9; ++t) {
        const int d = slot == 2 ? -2 * K1_STAGE : K1_STAGE;
        slot = slot == 2 ? 0 : slot + 1;
        SCHED0();
        chunk(0, d);
        WAIT_L0(); BAR();
        SCHED0();
        chunk(1, 0);
    }
    STAMP(4);
    u32x2 ts[5], tf[5];
    {
        LAS char* xb = lds + wid * 320;
#pragma unroll
        for (int m = 0; m < 5; ++m) ts[m] = (u32x2){pack2(acc[m][3][0], acc[m][3][1]), pack2(acc[m][3][2], acc[m][3][3])};
        if (q == 2) {
#pragma unroll
            for (int m = 0; m < 5; ++m) {
                const unsigned w = h ? ts[m].y : ts[m].x;
                *(LAS u16*)(xb + m * 64 + r * 2) = (u16)w; *(LAS u16*)(xb + m * 64 + 32 + r * 2) = (u16)(w >> 16);
            }
        }
        asm volatile("" ::: "memory");
#pragma unroll
        for (int m = 0; m < 5; ++m) tf[m] = *(const LAS u32x2*)(xb + m * 64 + (r & 1) * 32 + q * 8);
    }
    {
        f32x4 rk[5]; float rq[4];
        rk[0] = *(const LAS f32x4*)(tk0);
#pragma unroll
        for (int g = 0; g < 4; ++g) { rk[g + 1] = *(const LAS f32x4*)(tk + g * 128); rq[g] = *(const LAS float*)(tq + g * 128); }
        const f32x4 zero = {0.f, 0.f, 0.f, 0.f};
        u32x4 kf[5];
#pragma unroll
        for (int m = 0; m < 5; ++m) kf[m] = (u32x4){pack2(acc[m][1][0], acc[m][1][1]), pack2(acc[m][1][2], acc[m][1][3]), ts[m].y, 0u};
        f32x4 sp[4], sc[4];
#pragma unroll
        for (int g = 0; g < 4; ++g) {
            const u32x4 qf = {pack2(acc[g + 1][0][0], acc[g + 1][0][1]), pack2(acc[g + 1][0][2], acc[g + 1][0][3]), ts[g + 1].x & qtm, 0u};
            sp[g] = MFMA(__builtin_bit_cast(bf16x8, kf[g]), __builtin_bit_cast(bf16x8, qf), zero);
            sc[g] = MFMA(__builtin_bit_cast(bf16x8, kf[g + 1]), __builtin_bit_cast(bf16x8, qf), zero);
        }
        u32x2 vf[5];
#pragma unroll
        for (int m = 0; m < 5; ++m) vf[m] = (u32x2){pack2(acc[m][2][0], acc[m][2][1]), pack2(acc[m][2][2], acc[m][2][3])};
        f32x4 rs[4], t[4]; float mx[4], sm[4];
#pragma unroll
        for (int g = 0; g < 4; ++g) {
#pragma unroll
            for (int j = 0; j < 4; ++j) { const bool pv = 4 * q + j > r; rs[g][j] = pv ? rk[g][j] : rk[g + 1][j]; t[g][j] = pv ? sp[g][j] : sc[g][j]; }
            t[g] = (t[g] * rs[g]) * (rq[g] * (0.23570226039551584f * 1.4426950408889634f)) + (g == 0 ? mb0 : mb);
            mx[g] = fmaxf(fmaxf(t[g][0], t[g][1]), fmaxf(t[g][2], t[g][3]));
        }
        xq4_max(mx);
#pragma unroll
        for (int g = 0; g < 4; ++g) {
            t[g] = t[g] - mx[g];
#pragma unroll
            for (int j = 0; j < 4; ++j) t[g][j] = __builtin_amdgcn_exp2f(t[g][j]);
            sm[g] = (t[g][0] + t[g][2]) + (t[g][1] + t[g][3]);
        }
        f32x4 om[4], ot[4];
#pragma unroll
        for (int g = 0; g < 4; ++g) {
            const f32x4 p = t[g] * rs[g];
            const unsigned p01 = pack2(p[0], p[1]), p23 = pack2(p[2], p[3]);
            const u32x4 pf = {p01 & mp0, p23 & mp1, p01 & ~mp0, p23 & ~mp1};
            om[g] = MFMA(__builtin_bit_cast(bf16x8, ((u32x4){vf[g].x, vf[g].y, vf[g + 1].x, vf[g + 1].y})), __builtin_bit_cast(bf16x8, pf), zero);
            ot[g] = MFMA(__builtin_bit_cast(bf16x8, ((u32x4){tf[g].x, tf[g].y, tf[g + 1].x, tf[g + 1].y})), __builtin_bit_cast(bf16x8, pf), zero);
        }
        xq4_sum_rcp(sm);
        const auto ro = RSRC(o + (size_t)(pm * 8 + T) * 18432);
        unsigned tw2[4];
#pragma unroll
        for (int g = 0; g < 4; ++g) {
            const f32x4 v = om[g] * sm[g];
            __builtin_amdgcn_raw_buffer_store_b64((u32x2){pack2(v[0], v[1]), pack2(v[2], v[3])}, ro, vom, g * 36864, 16);
            const f32x2 w = ot[g].xy * sm[g];
            tw2[g] = pack2(w.x, w.y);
        }
        if (q == 2) {
#pragma unroll
            for (int g = 0; g < 4; ++g) __builtin_amdgcn_raw_buffer_store_b32(tw2[g], ro, vot, g * 36864, 16);
        }
    }
    STAMP(5);
    {
        WAIT_V(0); BARX();
        STAMP(6);
        f32x4 v[5];
        proj_residual(v, rx);
        const LAS char* pw = opaque(lds + WO_OFF + lane * 16);
        const LAS char* pw2 = opaque(lds + WO_OFF + 41472 + lane * 16);
        const LAS char* ph2 = opaque(lds + WO_OFF + 4096 + q * 128 + (r & 7) * 16);
        const LAS char* ph3 = opaque(lds + WO_OFF + 41472 + 4096 + q * 128 + (r & 7) * 16);
        bf16x8 wf[90];
        auto rdw = [&](int i) {
            const int kc = i / 5, n = i % 5;
            wf[i] = n < 4 ? LDS_FRAG((kc < 9 ? pw : pw2) + (kc % 9) * 4608 + n * 1024) : LDS_FRAG((kc < 9 ? ph2 : ph3) + (kc % 9) * 4608);
        };
#pragma unroll
        for (int i = 0; i < WO_W; ++i) rdw(i);
#pragma unroll
        for (int n = 0; n < 5; ++n) v[n] = pin(v[n]);
        const auto rO = RSRC(o + (size_t)rt * 18432);
        const ProjOut po = proj_out(wl + L_XB, wl + L_STB, rt, cg, lane);
        asm volatile("" :: "s"(rO));
        WAIT_L(0);
        BARX();
        STAMP(7);
        bf16x8 af[18];
        auto ldo = [&](int kc) { af[kc] = __builtin_bit_cast(bf16x8, __builtin_amdgcn_raw_buffer_load_b128(rO, lane * 16 + (kc & 3) * 1024, (kc >> 2) * 4096, 16)); };
#pragma unroll
        for (int kc = 0; kc < WO_PRE; ++kc) { ldo(kc); SCHED0(); }
        static_for<90>([&](auto ic) {
            constexpr int i = ic.value, kc = i / 5, n = i % 5, j = WO_W + 2 * (i / 3) + i % 3;
            MFMA_T(wf[i], af[kc], v[n]);
            SCHED0();
            if (i % 3 < 2 && j < 90) rdw(j);
            if (i % 3 == 2 && (i / 3) % 2 == 0 && i / 6 + WO_PRE < 18) ldo(i / 6 + WO_PRE);
            SCHED0();
        });
        asm volatile("s_nop 4" : "+v"(v[0]), "+v"(v[1]));
        STAMP(8);
        proj_store(v, po);
    }
}

DEVI f32x2 gelu2(f32x2 v) {
    const f32x2 w = v * (v * v * -0.10294324f + -2.3022082f);
    const f32x2 d = (f32x2){__builtin_amdgcn_exp2f(w.x), __builtin_amdgcn_exp2f(w.y)} + 1.0f;
    return v * (f32x2){__builtin_amdgcn_rcpf(d.x), __builtin_amdgcn_rcpf(d.y)};
}
DEVI f32x4 gelu4(f32x4 v) { const f32x2 a = gelu2(v.xy), b = gelu2(v.zw); return (f32x4){a.x, a.y, b.x, b.y}; }
DEVI void rowsum16x8(const f32x4 a, const f32x4 b, float& o0, float& o1) {
    float t0, t1, t2, t3;
    asm volatile(
        "s_nop 1\n\t"
        "v_add_f32_dpp %2, %6, %6 row_ror:8 row_mask:0xf bank_mask:0x3\n\t"
        "v_add_f32_dpp %3, %7, %7 row_ror:8 row_mask:0xf bank_mask:0x3\n\t"
        "v_add_f32_dpp %4, %10, %10 row_ror:8 row_mask:0xf bank_mask:0x3\n\t"
        "v_add_f32_dpp %5, %11, %11 row_ror:8 row_mask:0xf bank_mask:0x3\n\t"
        "v_add_f32_dpp %2, %8, %8 row_ror:8 row_mask:0xf bank_mask:0xc\n\t"
        "v_add_f32_dpp %3, %9, %9 row_ror:8 row_mask:0xf bank_mask:0xc\n\t"
        "v_add_f32_dpp %4, %12, %12 row_ror:8 row_mask:0xf bank_mask:0xc\n\t"
        "v_add_f32_dpp %5, %13, %13 row_ror:8 row_mask:0xf bank_mask:0xc\n\t"
        "v_add_f32_dpp %0, %2, %2 row_half_mirror row_mask:0xf bank_mask:0x5\n\t"
        "s_nop 0\n\t"
        "v_add_f32_dpp %1, %4, %4 row_half_mirror row_mask:0xf bank_mask:0x5\n\t"
        "v_add_f32_dpp %0, %3, %3 row_half_mirror row_mask:0xf bank_mask:0xa\n\t"
        "v_add_f32_dpp %1, %5, %5 row_half_mirror row_mask:0xf bank_mask:0xa\n\t"
        "s_nop 0\n\t"
        "v_add_f32_dpp %0, %0, %0 quad_perm:[1,0,3,2] row_mask:0xf bank_mask:0xf\n\t"
        "v_add_f32_dpp %1, %1, %1 quad_perm:[1,0,3,2] row_mask:0xf bank_mask:0xf\n\t"
        "s_nop 0\n\t"
        "v_add_f32_dpp %0, %0, %0 quad_perm:[2,3,0,1] row_mask:0xf bank_mask:0xf\n\t"
        "v_add_f32_dpp %1, %1, %1 quad_perm:[2,3,0,1] row_mask:0xf bank_mask:0xf\n\t"
        : "=&v"(o0), "=&v"(o1), "=&v"(t0), "=&v"(t1), "=&v"(t2), "=&v"(t3)
        : "v"(a[0]), "v"(a[1]), "v"(a[2]), "v"(a[3]), "v"(b[0]), "v"(b[1]), "v"(b[2]), "v"(b[3]));
}

#define K5_PRE 8
#define RH_MAP 0
#define NET_LAYERS 2
constexpr int G_STAGE = 34816;
constexpr int K5_STAGE = 18432, K5_NS = 8;

constexpr int y_load_at(int slot) {
    int next = K5_PRE;
    for (int s = 0;; ++s) { const bool go = next < 18 + 2 * (s / 3); if (s == slot) return go ? next : -1; next += go; }
}

struct GTile { f32x4 a[4][4], e[2]; };
struct GFrag { bf16x8 a[2][4], b[2][5]; const LAS char *pa[2][2], *pb, *p8; int slot; };
DEVI void g_rd(GFrag& f, int kk, int i) {
    if (i < 4) f.a[kk][i] = LDS_FRAG(f.pa[kk][i >> 1] + (i & 1) * 2048);
    else if (i < 8) f.b[kk][i - 4] = LDS_FRAG(f.pb + (kk * 9 + i - 4) * 1024);
    else f.b[kk][4] = LDS_FRAG(f.p8 + kk * 9216);
}
template <bool ROWS_IN_REGS>
DEVI void g_chunk(GTile& c, GFrag& f, int kk, int d) {
#pragma unroll
    for (int i = 0; i < 18; ++i) {
        const int n = i >> 2, m = i & 3;
        f32x4& e = n < 4 ? c.a[m][n] : c.e[m & 1];
        e = ROWS_IN_REGS ? MFMA(f.a[kk][m], f.b[kk][n], e) : MFMA(f.b[kk][n], f.a[kk][m], e);
        SCHED0();
        if (i < 9) g_rd(f, kk ^ 1, i);
        if (kk == 0) {
            if (i >= 10 && i < 14) f.pa[(i - 10) >> 1][i & 1] = opaque(f.pa[(i - 10) >> 1][i & 1] + d);
            if (i == 14) f.pb = opaque(f.pb + d);
            if (i == 15) f.p8 = opaque(f.p8 + d);
        }
        SCHED0();
    }
}
template <int NS, bool ROWS_IN_REGS, bool DRAIN_LAST = false>
DEVI void g_steps(GTile& c, GFrag& f, int n) {
    LOOP_ALIGN();
#pragma unroll 1
    for (int t = 0; t < n; ++t) {
        const int d = f.slot == NS - 1 ? -(NS - 1) * G_STAGE : G_STAGE;
        f.slot = f.slot == NS - 1 ? 0 : f.slot + 1;
        SCHED0();
        g_chunk<ROWS_IN_REGS>(c, f, 0, d);
        WAIT_L0();
        if (DRAIN_LAST && t == n - 1) WAIT_V(0);
        BAR();
        SCHED0();
        g_chunk<ROWS_IN_REGS>(c, f, 1, 0);
    }
}
DEVI void g_fill(GTile& c, const float (&e)[4]) {
#pragma unroll
    for (int m = 0; m < 4; ++m) {
#pragma unroll
        for (int n = 0; n < 4; ++n) c.a[m][n] = pin((f32x4){e[m], e[m], e[m], e[m]});
        if (m < 2) c.e[m] = pin((f32x4){e[m], e[m], e[m], e[m]});
    }
}

#undef STAMP_SEC
#define STAMP_SEC 1
DEVI void gmlp_body(LAS char* lds, int wm, int wn, int pm, int cg, char* z2t, char* y, const u16* __restrict__ bsp STAMP_PARAM) {
    const int mlo = wm * 4 + wn * 2, mhi = wm * 4 + (1 - wn) * 2;
    const float zero4[4] = {0.f, 0.f, 0.f, 0.f};
    GTile z2, z1, acc;
    GFrag f;
    {
        const int lane = lane_id_fresh();
#pragma unroll
        for (int kk = 0; kk < 2; ++kk) { f.pa[kk][0] = lds + mlo * 2048 + kk * 1024 + lane * 16; f.pa[kk][1] = lds + mhi * 2048 + kk * 1024 + lane * 16; }
        f.pb = lds + 16384 + wn * 4096 + lane * 16; f.p8 = lds + 16384 + 8192 + lane * 16; f.slot = 0;
    }
    g_fill(z2, zero4);
#pragma unroll
    for (int i = 0; i < 4; ++i) f.pa[i >> 1][i & 1] = opaque(f.pa[i >> 1][i & 1]);
    f.pb = opaque(f.pb); f.p8 = opaque(f.p8);
    SCHED0();
    WAIT_V(0); BARX();
    BAR();
    BARX();
    STAMP(2);
#pragma unroll
    for (int i = 0; i < 9; ++i) g_rd(f, 0, i);
    g_steps<4, true>(z2, f, 9);
    STAMP(3);
    {
        const int lane = lane_id_fresh(), r = lane & 15, q = lane >> 4;
        f32x4 ra[4];
#pragma unroll
        for (int m = 0; m < 4; ++m) ra[m] = *(const LAS f32x4*)(lds + G_TAB + ((m < 2 ? mlo : mhi) + (m & 1)) * 64 + q * 16);
        SCHED0();
#pragma unroll
        for (int m = 0; m < 4; ++m) {
            f32x4 s1, s2;
#pragma unroll
            for (int n = 0; n < 4; ++n) {
                const f32x4 g = gelu4(z2.a[m][n] * ra[m]);
                z2.a[m][n] = g;
                if (n == 0) { s1 = g; s2 = g * g; } else { s1 += g; s2 += g * g; }
            }
            if (m < 2) { const f32x4 g = gelu4(z2.e[m] * ra[m]); z2.e[m] = g; s1 += g; s2 += g * g; }
            float u0, u1;
            rowsum16x8(s1, s2, u0, u1);
            if ((r & 3) == 0) *(LAS f32x2*)(lds + G_PART + wn * 1024 + ((m < 2 ? mlo : mhi) + (m & 1)) * 128 + (q * 4 + (r >> 2)) * 8) = (f32x2){u0, u1};
            SCHED0();
        }
    }
    WAIT_L0(); BAR();
    STAMP(4);
    g_fill(z1, zero4);
    g_steps<4, false>(z1, f, 6);
    STAMP(5);
    {
        const int lane = lane_id_fresh(), q = lane >> 4;
#pragma unroll
        for (int g = 0; g < 2; ++g) {
            const auto rz2 = RSRC(z2t + (size_t)pm * 589824 + cg * 36864 + ((g ? mhi : mlo) >> 1) * 9216);
            const LAS char* tab = lds + G_TAB + (g ? mhi : mlo) * 64 + q * 16;
            f32x4 ra[2], rb[2];
#pragma unroll
            for (int T = 0; T < 2; ++T) { ra[T] = *(const LAS f32x4*)(tab + 512 + T * 64); rb[T] = *(const LAS f32x4*)(tab + 1024 + T * 64); }
#pragma unroll
            for (int n = 0; n < (g ? 4 : 5); ++n) {
                const f32x4 v0 = (n < 4 ? z2.a[2 * g][n & 3] : z2.e[0]) * ra[0] + rb[0], v1 = (n < 4 ? z2.a[2 * g + 1][n & 3] : z2.e[1]) * ra[1] + rb[1];
                ST16(rz2, lane * 16, n < 4 ? wn * 4096 + n * 1024 : 8192, ((u32x4){pack2(v0[0], v1[0]), pack2(v0[1], v1[1]), pack2(v0[2], v1[2]), pack2(v0[3], v1[3])}), 16);
            }
        }
    }
    STAMP(6);
    g_steps<4, false, true>(z1, f, 3);
    STAMP(7);
    {
        const int lane = lane_id_fresh(), r = lane & 15, q = lane >> 4;
        const unsigned b0 = *(const unsigned*)(bsp + pm * 128 + mlo * 16 + 2 * r), b1 = *(const unsigned*)(bsp + pm * 128 + mhi * 16 + 2 * r);
        float ra[4];
#pragma unroll
        for (int m = 0; m < 4; ++m) ra[m] = *(const LAS float*)(lds + G_TAB + ((m < 2 ? mlo : mhi) + (m & 1)) * 64 + r * 4);
        SCHED0();
#pragma unroll
        for (int m = 0; m < 4; ++m) {
#pragma unroll
            for (int n = 0; n < 4; ++n) z1.a[m][n] = pin(gelu4(z1.a[m][n] * ra[m]));
            if (m < 2) z1.e[m] = pin(gelu4(z1.e[m] * ra[m]));
            SCHED0();
        }
        const float bias[4] = {bflo(b0), bfhi(b0), bflo(b1), bfhi(b1)};
        g_fill(acc, bias);
#pragma unroll
        for (int kk = 0; kk < 2; ++kk) {
            f.pa[kk][0] = opaque(lds + 2 * G_STAGE + (mlo * 16 + r) * 128 + (((4 * kk + q) ^ (r & 7)) * 16));
            f.pa[kk][1] = opaque(lds + 2 * G_STAGE + (mhi * 16 + r) * 128 + (((4 * kk + q) ^ (r & 7)) * 16));
        }
        f.pb = opaque(lds + 2 * G_STAGE + 16384 + wn * 4096 + lane * 16); f.p8 = opaque(lds + 2 * G_STAGE + 16384 + 8192 + lane * 16); f.slot = 2;
    }
    STAMP(8);
    BAR();
    STAMP(9);
#pragma unroll
    for (int i = 0; i < 9; ++i) g_rd(f, 0, i);
    g_steps<3, false>(acc, f, 32);
    STAMP(10);
    const int lane = lane_id_fresh();
#pragma unroll
    for (int g = 0; g < 2; ++g) {
        const auto ry = RSRC(y + (size_t)(pm * 8 + (g ? mhi : mlo)) * 73728 + cg * 4608);
#pragma unroll
        for (int T = 0; T < 2; ++T) {
            const int m = 2 * g + T;
#pragma unroll
            for (int p = 0; p < 2; ++p) {
                const f32x4 a = acc.a[m][2 * p] * z1.a[m][2 * p], c = acc.a[m][2 * p + 1] * z1.a[m][2 * p + 1];
                ST16(ry, lane * 16 + wn * 2048 + p * 1024, T * 73728, ((u32x4){pack2(a[0], a[1]), pack2(a[2], a[3]), pack2(c[0], c[1]), pack2(c[2], c[3])}), 16);
            }
            if (g == 0) {
                const f32x4 a = acc.e[T] * z1.e[T];
                ST8(ry, (lane >> 5) * 256 + (lane & 15) * 16 + ((lane >> 4) & 1) * 8, T * 73728 + 4096, ((u32x2){pack2(a[0], a[1]), pack2(a[2], a[3])}), 16);
            }
        }
    }
}

#define NET_IDS \
    int b = blockIdx.x, wid = wid8 & 3; \
    asm volatile("" : "+s"(b), "+s"(wid));     \
    const int xcd = b & 7, idx = b >> 3; \
    const int pa = (xcd >> 1) * 4 + (idx & 3), hp = (xcd & 1) * 8 + (idx >> 2); \
    const int pm = (xcd >> 2) * 8 + (idx & 7), cg = (xcd & 3) * 4 + (idx >> 3); \
    const int rh = RH_MAP ? xcd * 4 + (idx & 3) : (xcd >> 1) * 8 + (idx & 7), c5 = RH_MAP ? idx >> 2 : (xcd & 1) * 4 + (idx >> 3); \
    const int lead = (idx + (idx >> 3)) & 3; \
    auto kord = [&](int t, int n) { \
        const int k = t + ((int)(0x1F000000u << (((t - lead) & 3) * 4)) >> 28); \
        return (unsigned)k < (unsigned)n ? k : t; \
    }; \
    auto kord5 = [&](int t) { \
        const int k = t + ((int)(0x1F000000u << (((t - (RH_MAP ? c5 : lead)) & 3) * 4)) >> 28); \
        return (unsigned)k < 18u ? k : t; \
    };

__global__ __launch_bounds__(512, 2) void k_net(char* ws, const u16* wsp, const u16* bsp, char* out DBG_ARG) {
    asm volatile("gm_code_begin:" :: "s"(ws), "s"(wsp), "s"(bsp), "s"(out));
    extern __shared__ __attribute__((aligned(16))) char smem[];
    LAS char* lds = (LAS char*)smem;
    const char *twin = ws + OFF_TWIN, *twout = ws + OFF_TWOUT;
    STAMP_DECL;
    const int wid8 = __builtin_amdgcn_readfirstlane(threadIdx.x >> 6);
    if (wid8 >= 4) {
        lds_word_set(lds + G_WORD + (threadIdx.x & 15) * 4, 0u);
        WAIT_L0();
        u32x4 wr[5][9];
#pragma unroll 1
      for (int layer = 0; layer < NET_LAYERS; ++layer) {
        NET_IDS
        char* wl = ws + OFF_L0 + layer * SZ_L;
        const unsigned fl = layer * F_LAYER;
        const char *xin = wl + L_XB, *st = wl + L_STB, *z2t = wl + (layer ? L_Y : L_Z2T);
        unsigned long long* zst = (unsigned long long*)(ws + OFF_ZST + layer * SZ_ZST);
        const int lane = lane_id_fresh(), l3 = lane >> 3;
        const unsigned vo = lane * 16;
        const auto rX = RSRC(xin + (size_t)(pm * 8 + 2 * wid) * 18432), rI = RSRC(twin + (size_t)cg * (18 * 18432));
        auto issue1B = [&](int g, int slot) {
            const int k = kord(g < 9 ? g : (g < 13 ? g - 4 : g - 13), 9), ob = (g < 9 ? 9 + k : k) * 18432, o2 = ob + wid * 4096;
            LAS char* sl = lds + slot * G_STAGE + 16384;
            if (wid < 2) BLDS(rI, sl + 16384 + wid * 1024, vo, ob + 16384 + wid * 1024, 0);
            sl += wid * 4096;
            BLDS(rI, sl, vo, o2, 0); BLDS(rI, sl, vo, o2, 1024); BLDS(rI, sl, vo, o2, 2048); BLDS(rI, sl, vo, o2, 3072);
        };
        auto issue1A = [&](int g, int slot) {
            const int k = kord(g < 9 ? g : g - 13, 9);
            LAS char* sl = lds + slot * G_STAGE + wid * 4096;
            BLDS1(rX, sl, vo, k * 2048, 0); BLDS1(rX, sl, vo, k * 2048, 1024);
            BLDS1(rX, sl + 2048, vo, k * 2048 + 18432, 0); BLDS1(rX, sl + 2048, vo, k * 2048 + 18432, 1024);
        };
        auto issue1 = [&](int g, int slot) { issue1B(g, slot); issue1A(g, slot); };
        const auto rA = RSRC((const char*)wsp + (size_t)(pm * 128 + wid * 32) * 4096), rB = RSRC(z2t + cg * 36864);
        const unsigned voA = (unsigned)(2 * l3 * 4096 + ((lane & 7) ^ l3) * 16);
        auto issueA = [&](int k, int slot) {
            LAS char* la = lds + slot * G_STAGE + wid * 4096;
            BLDS(rA, la, voA, k * 128, 0);
            BLDS(rA, la + 1024, voA, k * 128 + 16 * 4096, 0);
            BLDS(rA, la + 2048, voA, k * 128 + 4096, 0);
            BLDS(rA, la + 3072, voA, k * 128 + 17 * 4096, 0);
        };
        auto issueB = [&](int k, int slot) {
            LAS char* lb = lds + slot * G_STAGE + 16384;
            const int ok = (k >> 1) * 589824 + (k & 1) * 18432, ob = ok + wid * 4096;
            if (wid < 2) BLDS1(rB, lb + 16384 + wid * 1024, vo, ok + 16384 + wid * 1024, 0);
            lb += wid * 4096;
            BLDS1(rB, lb, vo, ob, 0); BLDS1(rB, lb, vo, ob, 1024); BLDS1(rB, lb, vo, ob, 2048); BLDS1(rB, lb, vo, ob, 3072);
        };
        const auto rW = RSRC(twout + (size_t)c5 * (72 * 4608));
        auto issue5 = [&](int t) {
            const int ow = kord5(t) * K5_STAGE;
            LAS char* sl = lds + ((t + 6) & 7) * K5_STAGE;
            if (wid < 2) BLDS(rW, sl + 16384 + wid * 1024, vo, ow + 16384 + wid * 1024, 0);
            BLDS(rW, sl + wid * 4096, vo, ow + wid * 4096, 0); BLDS(rW, sl + wid * 4096, vo, ow + wid * 4096, 1024);
            BLDS(rW, sl + wid * 4096, vo, ow + wid * 4096, 2048); BLDS(rW, sl + wid * 4096, vo, ow + wid * 4096, 3072);
        };
        auto load5 = [&](int p, int i) {
            const int t0 = p ? 2 * p + 6 : 2, ow = kord5(t0 + i) * K5_STAGE + wid * 4096;
            asm volatile("s_nop 4\n\tbuffer_load_dwordx4 %0, %4, %5, %6 offen\n\tbuffer_load_dwordx4 %1, %4, %5, %6 offen offset:1024\n\t"
                         "buffer_load_dwordx4 %2, %4, %5, %6 offen offset:2048\n\tbuffer_load_dwordx4 %3, %4, %5, %6 offen offset:3072"
                         : "+v"(wr[p][4 * i]), "+v"(wr[p][4 * i + 1]), "+v"(wr[p][4 * i + 2]), "+v"(wr[p][4 * i + 3]) : "v"(vo), "s"(rW), "s"(ow) : "memory");
            if (i) {
                const int oe = kord5(t0 + (wid >> 1)) * K5_STAGE + 16384 + (wid & 1) * 1024;
                asm volatile("s_nop 4\n\tbuffer_load_dwordx4 %0, %1, %2, %3 offen" : "+v"(wr[p][8]) : "v"(vo), "s"(rW), "s"(oe) : "memory");
            }
        };
        auto put5 = [&](int w0, int w1) {
#pragma unroll
            for (int w = w0; w < w1; ++w) {
                const int p = w / 9, i = w % 9 - 1;
                LAS char* sl = lds + (((p ? 2 * p + 6 : 2) + 6) & 7) * K5_STAGE + lane * 16;
                if (i < 0) *(LAS u32x4*)(sl + (wid >> 1) * K5_STAGE + 16384 + (wid & 1) * 1024) = wr[p][8];
                else *(LAS u32x4*)(sl + (i >> 2) * K5_STAGE + wid * 4096 + (i & 3) * 1024) = wr[p][i];
            }
            WAIT_L0();
        };
        const int row = wid * 32 + (lane & 31);
        auto step1 = [&](int t) { BAR(); issue1(t + 4, t & 3); };
        attn_producer(lds, ws, wl, fl, layer, pa, hp, wid, [&] { issue1B(0, 0); issue1B(1, 1); });
        __builtin_amdgcn_s_setprio(3);
        {
            const unsigned fo = flag_out(fl + F_X1, 256, pa, hp, lane), fi = flag_in(fl + F_X1, 256, pm * 16 + cg, 16, lane);
            BARX();
            if (wid == 0) flag_raise(ws, fo);
            issue1B(2, 2); issue1B(3, 3);
            asm volatile("" :: "s"(rX));
            if (wid == 0) flags_wait<X1_FIRST>(ws, fi);
            BARX();
        }
        issue1A(0, 0); issue1A(1, 1); issue1A(2, 2);
        WAIT_V(8); BAR();
        __builtin_amdgcn_s_setprio(0);
        issue1A(3, 3);
        WAIT_V(8); step1(0);
        WAIT_V(12); step1(1);
        {
            f32x4 sp[3];
            const char* ps = st + (size_t)(pm * 128 + wid * 32) * 64 + lane * 16;
            side_load(sp, ps, ps);
            WAIT_V(19); step1(2);
            WAIT_V(19); step1(3);
            WAIT_V(16); step1(4);
            WAIT_V(16); BAR(); if (layer == 0) load5(3, 0); issue1B(9, 1);
            __builtin_amdgcn_s_setprio(3);
            side_fence(sp);
#pragma unroll
            for (int i = 0; i < 2; ++i) lds_word_set(lds + G_TAB + (wid * 32 + i * 16 + (lane >> 2)) * 4, __float_as_uint(ln_rstd(sp[i])));
            WAIT_L0();
            __builtin_amdgcn_s_setprio(0);
        }
        WAIT_V(12); BAR(); if (layer == 0) load5(3, 1); issue1B(10, 2);
        WAIT_V(8); BAR(); if (layer == 0) load5(4, 0); issue1B(11, 3);
        WAIT_V(8); BAR(); if (layer == 0) load5(4, 1); issue1B(12, 0);
        if (layer == 1) {
            const auto rd = RSRC(out + (size_t)b * 9216 + wid * 2304);
            ST16(rd, vo, 0, ((u32x4){0u, 0u, 0u, 0u}), 16); ST16(rd, vo, 1024, ((u32x4){0u, 0u, 0u, 0u}), 16);
            if (lane < 16) ST16(rd, vo, 2048, ((u32x4){0u, 0u, 0u, 0u}), 16);
        }
        if (layer == 0) {
#pragma unroll
            for (int p = 0; p < 6; ++p) load5(p >> 1, p & 1);
        }
        WAIT_V(0);
        __builtin_amdgcn_s_setprio(3);
        BAR();
        if (lane < 32) {
            const f32x2 a = *(const LAS f32x2*)(lds + G_PART + row * 8), bq = *(const LAS f32x2*)(lds + G_PART + 1024 + row * 8);
            unsigned lo = __float_as_uint(a.x + bq.x), hi = __float_as_uint(a.y + bq.y);
            if (hi == 0xffffffffu) hi = 0x7fc00000u;
            __hip_atomic_store(zst + (size_t)(pm * 16 + cg) * 128 + row, ((unsigned long long)hi << 32) | lo, __ATOMIC_RELAXED, __HIP_MEMORY_SCOPE_AGENT);
        }
        step1(9); step1(10); step1(11);
        {
            const unsigned long long* src = zst + (size_t)(pm * 16 + (lane >> 5) * 8) * 128 + row + 512;
            u32x2 g[8];
            auto gather = [&] {
                asm volatile("global_load_dwordx2 %0, %8, off offset:-4096 sc1\n\tglobal_load_dwordx2 %1, %8, off offset:-3072 sc1\n\t"
                             "global_load_dwordx2 %2, %8, off offset:-2048 sc1\n\tglobal_load_dwordx2 %3, %8, off offset:-1024 sc1\n\t"
                             "global_load_dwordx2 %4, %8, off sc1\n\tglobal_load_dwordx2 %5, %8, off offset:1024 sc1\n\t"
                             "global_load_dwordx2 %6, %8, off offset:2048 sc1\n\tglobal_load_dwordx2 %7, %8, off offset:3072 sc1"
                             : "=&v"(g[0]), "=&v"(g[1]), "=&v"(g[2]), "=&v"(g[3]), "=&v"(g[4]), "=&v"(g[5]), "=&v"(g[6]), "=&v"(g[7]) : "v"(src) : "memory");
            };
            gather();
            WAIT_V(24); step1(12);
            WAIT_V(24); step1(13);
            WAIT_V(16);
            float s1, s2;
            for (unsigned spin = 0;; ++spin) {
                asm volatile("" : "+v"(g[0]), "+v"(g[1]), "+v"(g[2]), "+v"(g[3]), "+v"(g[4]), "+v"(g[5]), "+v"(g[6]), "+v"(g[7]));
                bool ok = true; s1 = 0.f; s2 = 0.f;
#pragma unroll
                for (int i = 0; i < 8; ++i) { ok &= g[i].y != 0xffffffffu; s1 += __uint_as_float(g[i].x); s2 += __uint_as_float(g[i].y); }
                if (__all(ok) || spin > 400000u) break;
                gather();
                WAIT_V(0);
            }
            auto x1 = __builtin_amdgcn_permlane32_swap(__float_as_uint(s1), __float_as_uint(s1), false, false);
            auto x2 = __builtin_amdgcn_permlane32_swap(__float_as_uint(s2), __float_as_uint(s2), false, false);
            const float mean = (__uint_as_float(x1[0]) + __uint_as_float(x1[1])) * (1.0f / 2304.0f);
            const float var = (__uint_as_float(x2[0]) + __uint_as_float(x2[1])) * (1.0f / 2304.0f) - mean * mean;
            const float rs = __builtin_amdgcn_rsqf(fmax2(var, 0.f) + 1e-5f);
            asm volatile("ds_write_b32 %0, %1" :: "v"((unsigned)(size_t)(lds + G_TAB + 512 + (lane >> 5) * 512 + row * 4)), "v"(lane < 32 ? rs : -mean * rs) : "memory");
            WAIT_L0();
        }
        __builtin_amdgcn_s_setprio(0);
        BAR(); issueA(kord(0, 32), 2);
        WAIT_V(12); BAR(); issue5(0); issue5(1);
        WAIT_V(12); BAR(); issueA(kord(1, 32), 0);
        __builtin_amdgcn_s_setprio(3);
        const unsigned fz = flag_out(fl + F_Z2, 256, cg, pm, lane);
        BARX();
        if (wid == 0) flag_raise(ws, fz);
        issueA(kord(2, 32), 1);
        if (wid == 0) { flags_wait<Z2_FIRST>(ws, flag_in(fl + F_Z2, 256, cg * 16 + pm, 2, lane)); lds_word_set(lds + G_WORD + 8 + layer * 8, 1u); }
        else lds_word_wait(lds + G_WORD + 8 + layer * 8);
        issueB(kord(0, 32), 2); issueB(kord(1, 32), 0); issueB(kord(2, 32), 1);
        WAIT_V(8); BAR();
        if (wid == 0) flags_wait<0>(ws, flag_in(fl + F_Z2, 256, cg * 16 + pm, 16, lane));
        __builtin_amdgcn_s_setprio(0);
        auto issue = [&](int t, int sl) { const int k = kord(t, 32); issueB(k, sl); issueA(k, sl); };
        WAIT_V(4); BAR(); issue(3, 2);
        ring_steps<1, 29, 8, 2>(issue);
        WAIT_V(8); BAR(); issue5(4);
        WAIT_V(4); BAR(); issue5(5); issue5(6);
        BAR(); issue5(7); put5(0, 9);
        {
            const unsigned fo = flag_out(fl + F_Y, 256, pm, cg, lane), fi = flag_in(fl + F_Y, 256, rh * 8 + c5, 16, lane);
            __builtin_amdgcn_s_setprio(3);
            WAIT_V(0); BARX();
            if (wid == 0) { flag_raise(ws, fo); flags_wait<Y_FIRST>(ws, fi); }
            BARX();
            __builtin_amdgcn_s_setprio(0);
        }
        BAR(); put5(9, 16);
        BAR(); put5(16, 23);
        BAR(); put5(23, 30);
        BAR(); put5(30, 36);
        BAR(); issue5(16); put5(36, 41);
        BAR(); issue5(17); put5(41, 45);
        if (wid < 2) WAIT_V(5); else WAIT_V(4);
        BAR();
        WAIT_V(0); BAR();
        if (layer == 0) { attn_issueB(lds, ws, hp, wid, lane, 0, 0); attn_issueB(lds, ws, hp, wid, lane, 1, 1); }
        BAR();
        if (layer == 0) {
            __builtin_amdgcn_s_setprio(3);
            const unsigned fo = flag_out(F_X2, 512, (rh >> 1) + wid, wid ? 16 + c5 : (rh & 1) * 8 + c5, wid == 0 || (wid == 1 && (rh & 1) && rh < 31) ? lane : 16);
            BARX();
            if (wid < 2) flag_raise(ws, fo);
        }
      }
        return;
    }
#pragma unroll 1
  for (int layer = 0; layer < NET_LAYERS; ++layer) {
    NET_IDS
    char* wl = ws + OFF_L0 + layer * SZ_L;
    const unsigned fl = layer * F_LAYER;
    const char* xin = wl + L_XB;
    char* y = wl + (layer ? L_Z2T : L_Y);
#undef STAMP_SEC
#define STAMP_SEC 0
    {
    STAMP_BEGIN;
    attn_consumer(lds, wl, layer, pa, hp, wid STAMP_FWD);
    STAMP_END(1 + 5 * layer);
    }
#undef STAMP_SEC
#define STAMP_SEC 1
    STAMP_BEGIN;
    gmlp_body(lds, wid & 1, wid >> 1, pm, cg, wl + (layer ? L_Y : L_Z2T), y, bsp STAMP_FWD);
    {
        WAIT_V(0);
        BARX();
        STAMP(11);
        const int lane = lane_id_fresh(), r = lane & 15, q = lane >> 4;
        const int wk = wid >> 1, rt = rh * 4 + (wid & 1) * 2 + wk;
        const Res rx = proj_residual_load(xin, rt, c5, lane);
        const LAS char *pw[3], *ph[3];
#pragma unroll
        for (int i = 0; i < 3; ++i) {
            pw[i] = opaque(lds + 3 * i * K5_STAGE + wk * 4608 + lane * 16);
            ph[i] = opaque(lds + 3 * i * K5_STAGE + wk * 4608 + 4096 + q * 128 + (r & 7) * 16);
        }
        const auto rY = RSRC(y + (size_t)(rt & ~1) * 73728 + wk * 1024);
        const unsigned vy[2] = {pin((unsigned)(wk * 73728 + lane * 16)), pin((unsigned)((1 - wk) * 73728 + lane * 16))};
        int dk[4];
#pragma unroll
        for (int i = 0; i < 4; ++i) dk[i] = pin_s((kord5(4 + i) - 4 - i) * 4096);
        auto yoff = [&](int t) { return pin_s(t == 0 || t == 17 ? kord5(t) * 4096 : t * 4096 + dk[t & 3]); };
        bf16x8 af[5][4], wf[4][5];
        auto lda = [&](int so, int i) { return __builtin_bit_cast(bf16x8, __builtin_amdgcn_raw_buffer_load_b128(rY, vy[i & 1] + (i >> 1) * 2048, so, 16)); };
        auto rdw = [&](int t, int c, int n) {
            const int sl = (t + 6) & 7, o = (sl % 3) * K5_STAGE + c * 9216;
            wf[2 * (t & 1) + c][n] = n < 4 ? LDS_FRAG(pw[sl / 3] + o + n * 1024) : LDS_FRAG(ph[sl / 3] + o);
        };
#pragma unroll
        for (int n = 0; n < 10; ++n) rdw(0, n / 5, n % 5);
        int so[18];
#pragma unroll
        for (int i = 0; i < 5; ++i) so[i] = yoff(i);
        so[17] = yoff(17);
        f32x4 v[5], u[5];
        proj_residual(v, rx);
#pragma unroll
        for (int n = 0; n < 5; ++n) v[n] = pin(v[n]);
        LAS char* xo = (LAS char*)opaque(lds + wid * 4608 + lane * 16);
        LAS char* xo4 = (LAS char*)opaque(lds + wid * 4608 + 4096 + (lane & 31) * 16);
        const LAS char* xi = opaque(lds + (wid ^ 2) * 4608 + lane * 16);
        const LAS char* xi4 = opaque(lds + (wid ^ 2) * 4608 + 4096 + (lane & 31) * 16);
        const ProjOut po = layer ? proj_out_final(out, rt, c5, lane) : proj_out(wl + SZ_L + L_XA, wl + SZ_L + L_STA, rt, c5, lane);
        asm volatile("" :: "s"(rY));
        BARX();
        STAMP(12);
#pragma unroll
        for (int i = 0; i < K5_PRE; ++i) { af[i >> 2][i & 3] = lda(so[i >> 2], i & 3); SCHED0(); }
        static_for<18>([&](auto tc) {
            constexpr int t = tc.value;
            bf16x8 (&a)[4] = af[t % 5];
#pragma unroll
            for (int h = 0; h < 4; ++h) {
                const int c = t < 17 ? h >> 1 : h & 1, m = t < 17 ? h & 1 : 1 - (h >> 1);
                if (t < 17 ? m == 0 : h < 2) { if (t == 17 && h == 1) WAIT_L(0); else WAIT_L(5); }
#pragma unroll
                for (int n = 0; n < 5; ++n) {
                    f32x4& e = m ? u[n] : v[n];
                    if (t == 0 && h == 1) MFMA_0(wf[0][n], a[1], e); else MFMA_T(wf[2 * (t & 1) + c][n], a[2 * c + m], e);
                    SCHED0();
                    if (t < 17 && m == 0) rdw(t + 1, c, n);
                    if (t < 14 && m == 1 && !(n & 1)) { const int i = y_load_at(6 * t + 3 * c + n / 2); if (i >= 0) af[(i >> 2) % 5][i & 3] = lda(so[i >> 2], i & 3); }
                    if (t < 12 && h == 3 && n == 3) so[t + 5] = yoff(t + 5);
                    if (t == 17 && h * 5 + n >= 13 && h * 5 + n < 18) { const int i = h * 5 + n - 13; *(LAS f32x4*)(i < 4 ? xo + i * 1024 : xo4) = u[i]; }
                    SCHED0();
                }
            }
            if ((t & 1) && t < 17) BAR();
            SCHED0();
        });
        {
            WAIT_L0(); BARX();
#pragma unroll
            for (int n = 0; n < 5; ++n) u[n] = *(const LAS f32x4*)(n < 4 ? xi + n * 1024 : xi4);
            SCHED0();
            asm volatile("s_nop 1" : "+v"(v[0]), "+v"(v[1]), "+v"(v[2]), "+v"(v[3]), "+v"(v[4]));
#pragma unroll
            for (int n = 0; n < 5; ++n) v[n] += u[n];
        }
        STAMP(13);
        if (layer) proj_store<true>(v, po);
        else { proj_store(v, po); WAIT_V(0); BARX(); }
    }
    STAMP_END(3 + 5 * layer);
  }
  { if (PROBE_A < 0) st_[0] = *(const unsigned long long*)(ws + (200u << 20) + blockIdx.x * 8);
    while (__builtin_amdgcn_s_memrealtime() - st_[0] < 15000ull) __builtin_amdgcn_s_sleep(1); }
}

typedef short s16x4 __attribute__((ext_vector_type(4)));
DEVI u32x4 tr_frag(const LAS char* p, int four_rows) {
    const u32x2 a = __builtin_bit_cast(u32x2, __builtin_amdgcn_ds_read_tr16_b64_v4i16((LAS s16x4*)p));
    const u32x2 b = __builtin_bit_cast(u32x2, __builtin_amdgcn_ds_read_tr16_b64_v4i16((LAS s16x4*)(p + four_rows)));
    return (u32x4){a.x, a.y, b.x, b.y};
}
DEVI unsigned centre2(float a, float b, f32x2& e, float mean) {
    const f32x2 t = (f32x2){a, b} + e;
    const unsigned r = pack2(t.x, t.y);
    e = (t - (f32x2){bflo(r), bfhi(r)}) - mean;
    return r;
}
#define PREP_AUX 16
struct Retile { u32x4 f[9]; };
DEVI void retile_in_make(Retile& o, int u, int kh, const LAS char* img, int lane) {
    const int j = u % 9, c = lane & 15, q = lane >> 4, p = lane & 3;
    const LAS char* src = img + (q * 8 + (c >> 2)) * 64 + (u & 1) * 32 + (j < 8 ? (p & 1) * 16 + (p >> 1) * 8 : p * 8);
    f32x2 sum = {0.f, 0.f};
#pragma unroll
    for (int i = 0; i < 18; ++i) {
        const u32x4 f = tr_frag(src + (i < 9 ? 9 - 9 * kh + i : 9 * kh + i - 9) * 2048, 256);
        if (i >= 9) o.f[i - 9] = f;
#pragma unroll
        for (int e = 0; e < 4; ++e) sum += (f32x2){bflo(f[e]), bfhi(f[e])};
    }
    const float mean = xq_sum(sum.x + sum.y) * (1.0f / 576.0f);
    f32x2 er[2] = {{-mean, -mean}, {-mean, -mean}};
#pragma unroll
    for (int i = 0; i < 9; ++i)
#pragma unroll
        for (int e = 0; e < 4; ++e) o.f[i][e] = centre2(bflo(o.f[i][e]), bfhi(o.f[i][e]), er[e & 1], mean);
}
DEVI void retile_in_store(const Retile& o, char* twin, int u, int kh, int lane) {
    const int half = u >= 144, g = u - half * 144, cg = g / 9, j = g - cg * 9, c = lane & 15, q = lane >> 4;
    const auto rd = RSRC(twin + (size_t)cg * (18 * 18432) + (half * 18 + kh * 9) * 9216);
    const unsigned vo = (unsigned)(q * 256 + (j < 8 ? ((j & ~1) + (c >> 3)) * 1024 + ((j & 1) * 8 + (c & 7)) * 16 : 8192 + c * 16));
#pragma unroll
    for (int i = 0; i < 9; ++i) ST16(rd, vo, i * 9216, o.f[i], PREP_AUX);
}
template <int PITCH>
DEVI void retile_out_make(Retile& o, const LAS char* img, int lane) {
    const int c = lane & 15, q = lane >> 4, p = lane & 3;
    const LAS char* src = img + (q * 8 + (c >> 2)) * PITCH;
#pragma unroll
    for (int i = 0; i < 8; ++i) o.f[i] = tr_frag(src + p * 16 + (i >> 2) * 144 + ((i >> 1) & 1) * 64 + (i & 1) * 8, 4 * PITCH);
    o.f[8] = tr_frag(src + 128 + (p >> 1) * 144 + (p & 1) * 8, 4 * PITCH);
}
DEVI void retile_out_store(const Retile& o, char* dst, int nkc, int kc, int w, int lane) {
    const int c = lane & 15, q = lane >> 4;
    const auto rd = RSRC(dst + (size_t)(2 * w * nkc + kc) * 4608);
#pragma unroll
    for (int i = 0; i < 8; ++i) ST16(rd, lane * 16 + (i & 3) * 1024, (i >> 2) * nkc * 4608, o.f[i], PREP_AUX);
    ST16(rd, (c >> 3) * nkc * 4608 + 4096 + q * 128 + (c & 7) * 16, 0, o.f[8], PREP_AUX);
}

constexpr int P_CODE = 64, P_CW = 64  , P_WIN = 144, P_WOUT = 72, P_WO = 18, P_X = 64, P_Z = 32;
constexpr int P_TOTAL = P_CW + P_WIN + P_WOUT + P_WO + P_X + P_Z + P_CODE;

DEVI void prep_body(const u16* __restrict__ x, const u16* __restrict__ wqkv, const u16* __restrict__ wo, const u16* __restrict__ w_in,
                    const u16* __restrict__ w_out, char* __restrict__ ws) {
    __shared__ __attribute__((aligned(16))) char smem[37376 + 512];
    LAS char* lds = (LAS char*)smem;
    const int tid = threadIdx.x;
    int u = blockIdx.x;
    if (u < P_CW) {
        const int st = (u & ~15) + 2 * (u & 7) + ((u >> 3) & 1);
        if (st >= 54) return;
        const u16* src = wqkv + st * 32;
        u32x4 v[9];
#pragma unroll
        for (int i = 0; i < 9; ++i) v[i] = *(const u32x4*)(src + (size_t)((tid >> 2) + 64 * i) * 1728 + (tid & 3) * 8);
        const int c = tid & 31;
        const int col = st * 32 + c, part = col / 576, cc = col % 576, hh = cc / 18, dim = cc % 18, h = hh & 1;
        const int tile = dim < 16 ? 3 * h + part : 6;
        const int sl = dim < 16 ? dim : (part == 2 ? 8 + 2 * h : 4 * h + 2 * part) + (dim - 16);
        char* dst = ws + OFF_TWQKV + pin((unsigned)((hh >> 1) * (18 * 7168) + tile * 1024 + sl * 16 + (tid >> 7) * 7168 + ((tid >> 5) & 3) * 256));
        float a[8];
#pragma unroll
        for (int j = 0; j < 8; ++j) a[j] = 0.f;
#pragma unroll
        for (int i = 0; i < 9; ++i) {
            *(LAS u32x4*)(lds + ((tid >> 2) + 64 * i) * 64 + (tid & 3) * 16) = v[i];
            a[0] += bflo(v[i].x); a[1] += bfhi(v[i].x); a[2] += bflo(v[i].y); a[3] += bfhi(v[i].y);
            a[4] += bflo(v[i].z); a[5] += bfhi(v[i].z); a[6] += bflo(v[i].w); a[7] += bfhi(v[i].w);
        }
#pragma unroll
        for (int j = 0; j < 8; ++j) {
            a[j] += __builtin_amdgcn_update_dpp(0.f, a[j], 0x124, 0xf, 0xf, true);
            a[j] += __builtin_amdgcn_update_dpp(0.f, a[j], 0x128, 0xf, 0xf, true);
            a[j] = xq_sum(a[j]);
        }
        LAS float* red = (LAS float*)(lds + 36864);
        if ((tid & 63) < 4) {
#pragma unroll
            for (int j = 0; j < 8; ++j) red[(tid >> 6) * 32 + (tid & 3) * 8 + j] = a[j];
        }
        __syncthreads();
        const float mean = ((red[c] + red[32 + c]) + (red[64 + c] + red[96 + c])) * (1.0f / 576.0f);
        f32x2 er[2] = {{-mean, -mean}, {-mean, -mean}};
#pragma unroll
        for (int i = 0; i < 9; ++i) {
            const LAS u16* p = (const LAS u16*)lds + ((tid >> 5) + 8 * i) * 256 + c;
            u32x4 o;
#pragma unroll
            for (int j = 0; j < 4; ++j) o[j] = centre2(__uint_as_float((unsigned)p[j * 64] << 16), __uint_as_float((unsigned)p[j * 64 + 32] << 16), er[j & 1], mean);
            *(u32x4*)(dst + i * 14336) = o;
        }
        return;
    }
    u -= P_CW;
    if (u < P_WIN + P_WOUT) {
        const int w = __builtin_amdgcn_readfirstlane(tid >> 6), lane = tid & 63;
        const bool isin = u < P_WIN;
        const int st = (u & ~15) + 2 * (u & 7) + ((u >> 3) & 1);
        const auto rs = RSRC(isin ? (const char*)(w_in + st * 32) : (const char*)w_out + (size_t)(u - P_WIN) * 36864);
        const unsigned vo = isin ? (unsigned)((lane >> 2) * 9216 + (lane & 3) * 16) : (unsigned)lane * 16u;
        const int step = isin ? 16 * 9216 : 1024;
#pragma unroll
        for (int j = 0; j < 9; ++j) BLDS(rs, lds + (j * 4 + w) * 1024, vo, (j * 4 + w) * step, 0);
        WAIT_V(0); BAR();
        Retile rt;
        if (isin) { retile_in_make(rt, st * 2 + (w & 1), w >> 1, lds, lane); retile_in_store(rt, ws + OFF_TWIN, st * 2 + (w & 1), w >> 1, lane); }
        else { retile_out_make<1152>(rt, lds + w * 288, lane); retile_out_store(rt, ws + OFF_TWOUT, 72, u - P_WIN, w, lane); }
        return;
    }
    u -= P_WIN + P_WOUT;
    if (u < P_WO) {
        const int w = __builtin_amdgcn_readfirstlane(tid >> 6);
        const int lane = tid & 63;
        const auto rs = RSRC(wo + w * 144);
#pragma unroll
        for (int j = 0; j < 9; ++j) {
            const int i = lane + 64 * j, rl = (i * 3641) >> 16, kp = u * 32 + rl;
            const int row = kp < 512 ? 18 * (kp >> 4) + (kp & 15) : 18 * ((kp - 512) >> 1) + 16 + (kp & 1);
            BLDS(rs, lds + w * 9216 + j * 1024, (unsigned)(row * 1152 + (i - rl * 18) * 16), 0, 0);
        }
        WAIT_V(0);
        Retile rt;
        retile_out_make<288>(rt, lds + w * 9216, lane);
        retile_out_store(rt, ws + OFF_TWO, 18, u, w, lane);
        return;
    }
    u -= P_WO;
    if (u < P_X) {
        const int rt = u * 2 + (tid >> 7), r = tid & 15, cg = (tid >> 4) & 7;
        const int sg = rt * 16 + r;
        u32x4 v[9];
#pragma unroll
        for (int i = 0; i < 9; ++i) v[i] = *(const u32x4*)(x + (size_t)u * 18432 + (tid + 256 * i) * 8);
#pragma unroll
        for (int i = 0; i < 9; ++i) { const int p = tid + 256 * i, row = (p * 3641) >> 18; *(LAS u32x4*)(lds + p * 16 + row * 16) = v[i]; }
        __syncthreads();
#pragma unroll
        for (int i = 0; i < 9; ++i) v[i] = *(const LAS u32x4*)(lds + (2 * r + (tid >> 7)) * 1168 + cg * 144 + i * 16);
        float sum = 0.f;
#pragma unroll
        for (int i = 0; i < 9; ++i) {
            sum += ((bflo(v[i].x) + bfhi(v[i].x)) + (bflo(v[i].y) + bfhi(v[i].y))) + ((bflo(v[i].z) + bfhi(v[i].z)) + (bflo(v[i].w) + bfhi(v[i].w)));
            ST16(RSRC(ws + OFF_L0 + L_XA), rt * 18432 + cg * 2304 + r * 16, i * 256, v[i], 16);
        }
        float m2 = 0.f;
#pragma unroll
        for (int i = 0; i < 9; ++i) {
            float d;
            d = bflo(v[i].x); m2 += d * d; d = bfhi(v[i].x); m2 += d * d; d = bflo(v[i].y); m2 += d * d; d = bfhi(v[i].y); m2 += d * d;
            d = bflo(v[i].z); m2 += d * d; d = bfhi(v[i].z); m2 += d * d; d = bflo(v[i].w); m2 += d * d; d = bfhi(v[i].w); m2 += d * d;
        }
        *(f32x2*)(ws + OFF_L0 + L_STA + (size_t)sg * 64 + cg * 8) = (f32x2){sum, m2};
        return;
    }
    u -= P_X;
    if (u >= P_Z) {
        u -= P_Z;
        unsigned long long c0;
        asm volatile("s_getpc_b64 s[40:41]\n\ts_add_u32 s40, s40, gm_code_begin@rel32@lo+4\n\ts_addc_u32 s41, s41, gm_code_begin@rel32@hi+12\n\ts_mov_b64 %0, s[40:41]"
                     : "=s"(c0) :: "s40", "s41", "scc");
        const unsigned long long pc = __builtin_amdgcn_s_getpc();
        const unsigned len = pc > c0 && pc - c0 < 262144ull ? (unsigned)(pc - c0) & ~15u : 0u;
        const auto rs = __builtin_amdgcn_make_buffer_rsrc((void*)(c0 & ~15ull), 0, len, 0x00020000);
        unsigned x = 0;
        for (unsigned o = ((u >> 3) * 256 + tid) * 16; o < len; o += 8 * 256 * 16) x ^= __builtin_amdgcn_raw_buffer_load_b128(rs, o, 0, 0)[0];
        asm volatile("" :: "v"(x));
        return;
    }
    {
        const auto rw = RSRC(ws);
        const u32x4 ones = {0xffffffffu, 0xffffffffu, 0xffffffffu, 0xffffffffu}, zero = {0u, 0u, 0u, 0u};
#pragma unroll
        for (int i = 0; i < 4; ++i) ST16(rw, (unsigned)OFF_ZST + (u * 1024 + i * 256 + tid) * 16, 0, ones, 16);
#pragma unroll
        for (int i = 0; i < 5; ++i) ST16(rw, (unsigned)OFF_CNT + (u * 1280 + i * 256 + tid) * 16, 0, zero, 16);
        ST16(rw, (unsigned)(OFF_L0 + L_STB) + (u * 256 + tid) * 16, 0, zero, 16);
#pragma unroll
        for (int i = 0; i < 2; ++i) ST16(rw, (unsigned)(OFF_L0 + L_O) + (u * 4 + i * 2 + (tid >> 7)) * 18432 + 16384 + (tid & 127) * 16, 0, zero, 16);
        if (tid < 144) {
            const int e = u * 144 + tid, f = e >> 4;
            *(u32x4*)(ws + OFF_TWQKV + (size_t)f * 7168 + 6144 + ((e >> 2) & 3) * 256 + (12 + (e & 3)) * 16) = (u32x4){0u, 0u, 0u, 0u};
        }
    }
}

__global__ __launch_bounds__(256) void k_prep(const u16* __restrict__ x, const u16* __restrict__ wqkv, const u16* __restrict__ wo,
                                              const u16* __restrict__ w_in, const u16* __restrict__ w_out, char* __restrict__ ws DBG_ARG) {
    unsigned long long q0; asm volatile("s_memrealtime %0" : "=s"(q0) :: "memory");
    asm volatile("" :: "s"(x), "s"(wqkv), "s"(wo), "s"(w_in), "s"(w_out), "s"(ws));
    if (threadIdx.x == 0) *(unsigned long long*)(ws + (200u << 20) + blockIdx.x * 8) = q0;
    prep_body(x, wqkv, wo, w_in, w_out, ws);
}

}

#define STOP_CHECK(n)

extern "C" void kernel_launch(void* const* d_in, const int* in_sizes, int n_in, void* d_out, int out_size, void* d_ws, size_t ws_size,
                              hipStream_t stream) {
    (void)in_sizes; (void)n_in; (void)out_size; (void)ws_size;
    const u16* x = (const u16*)d_in[0];
    const u16* wqkv = (const u16*)d_in[1];
    const u16* wo = (const u16*)d_in[2];
    const u16* w_in = (const u16*)d_in[4];
    const u16* w_sp = (const u16*)d_in[5];
    const u16* b_sp = (const u16*)d_in[6];
    const u16* w_out = (const u16*)d_in[7];
    char* ws = (char*)d_ws;
    (void)hipFuncSetAttribute((const void*)k_net, hipFuncAttributeMaxDynamicSharedMemorySize, NET_LDS);
    k_prep<<<P_TOTAL, 256, 0, stream>>>(x, wqkv, wo, w_in, w_out, ws DBG_LAUNCH(0));
    STOP_CHECK(1);
    k_net<<<256, 512, NET_LDS, stream>>>(ws, w_sp, b_sp, (char*)d_out DBG_LAUNCH(0));
}
```

```cpp
#define PROBE_A (-1)
#define PROBE_B (1)
#define PROBE_MUL 1
#include <hip/hip_runtime.h>
#include <stdint.h>

namespace {

typedef unsigned short u16;
typedef short bf16x8 __attribute__((ext_vector_type(8)));
typedef float f32x4 __attribute__((ext_vector_type(4)));
typedef float f32x2 __attribute__((ext_vector_type(2)));
typedef unsigned u32x4 __attribute__((ext_vector_type(4)));
typedef unsigned u32x2 __attribute__((ext_vector_type(2)));
typedef __bf16 bf16x2_t __attribute__((ext_vector_type(2)));
#define LAS __attribute__((address_space(3)))
#define DEVI __device__ __forceinline__

constexpr int S_ = 2048, D_ = 576, FH = 2304;

constexpr size_t SZ_TWQKV = (size_t)16 * 18 * 7 * 1024;
constexpr size_t SZ_TWO = (size_t)8 * 18 * 4608;
constexpr size_t SZ_TWIN = (size_t)16 * 18 * 18 * 1024;
constexpr size_t SZ_TWOUT = (size_t)8 * 72 * 4608;
constexpr size_t SZ_X = (size_t)S_ * D_ * 2;
constexpr size_t SZ_Z = (size_t)S_ * FH * 2;
constexpr size_t SZ_ST = (size_t)S_ * 64;
constexpr size_t SZ_ZST = (size_t)16 * 16 * 128 * 8;
constexpr size_t OFF_TWQKV = 0;
constexpr size_t OFF_TWO = OFF_TWQKV + SZ_TWQKV;
constexpr size_t OFF_TWIN = OFF_TWO + SZ_TWO;
constexpr size_t OFF_TWOUT = OFF_TWIN + SZ_TWIN;
constexpr size_t OFF_CNT = OFF_TWOUT + SZ_TWOUT;
constexpr size_t SZ_CNT = 10 * 65536;
constexpr size_t OFF_ZST = OFF_CNT + SZ_CNT;
constexpr size_t OFF_L0 = OFF_ZST + 2 * SZ_ZST;
constexpr size_t L_XA = 0, L_STA = L_XA + SZ_X, L_O = L_STA + SZ_ST, L_XB = L_O + SZ_X, L_STB = L_XB + SZ_X, L_Z2T = L_STB + SZ_ST, L_Y = L_Z2T + SZ_Z;
constexpr size_t SZ_L = 0;
static_assert(OFF_L0 + L_Y + SZ_Z <= ((size_t)256 << 20), "workspace");
constexpr unsigned F_O = OFF_CNT, F_X1 = F_O + 65536, F_Z2 = F_O + 131072, F_Y = F_O + 196608, F_LAYER = 262144, F_X2 = F_O + 2 * F_LAYER;

DEVI unsigned pack2(float a, float b) { f32x2 v = {a, b}; bf16x2_t r = __builtin_convertvector(v, bf16x2_t); return __builtin_bit_cast(unsigned, r); }
DEVI float bflo(unsigned u) { return __uint_as_float(u << 16); }
DEVI float bfhi(unsigned u) { return __uint_as_float(u & 0xffff0000u); }
DEVI int slot2row(int sg) { return (sg & ~31) + 2 * (sg & 15) + ((sg >> 4) & 1); }
DEVI float fmax2(float a, float b) { return __builtin_amdgcn_fmed3f(a, b, __builtin_inff()); }
#define XQ4(OP, MID) \
    asm volatile("s_nop 1\n\tv_permlane32_swap_b32 %0, %1\n\tv_permlane32_swap_b32 %2, %3\n\t" OP " %0, %0, %1\n\t" OP " %2, %2, %3\n\t" \
                 "s_nop 1\n\tv_permlane16_swap_b32 %0, %2\n\t" OP " %0, %0, %2\n\t" MID \
                 "v_mov_b32 %2, %0\n\ts_nop 1\n\tv_permlane16_swap_b32 %0, %2\n\tv_mov_b32 %1, %0\n\tv_mov_b32 %3, %2\n\ts_nop 0\n\t" \
                 "v_permlane32_swap_b32 %0, %1\n\tv_permlane32_swap_b32 %2, %3" : "+v"(x[0]), "+v"(x[1]), "+v"(x[2]), "+v"(x[3]))
DEVI void xq4_max(float (&x)[4]) { XQ4("v_max_f32", ""); }
DEVI void xq4_sum_rcp(float (&x)[4]) { XQ4("v_add_f32", "v_rcp_f32 %0, %0\n\ts_nop 0\n\t"); }
template <class T> DEVI T pin(T v) { asm volatile("" : "+v"(v)); return v; }
template <int V> struct IC { static constexpr int value = V; };
template <int N, int I = 0, class F> DEVI void static_for(F&& f) { if constexpr (I < N) { f(IC<I>{}); static_for<N, I + 1>(f); } }
DEVI int pin_s(int v) { asm volatile("" : "+s"(v)); return v; }
DEVI float xq_sum(float v) {
    auto a = __builtin_amdgcn_permlane32_swap(__float_as_uint(v), __float_as_uint(v), false, false);
    v = __uint_as_float(a[0]) + __uint_as_float(a[1]);
    auto b = __builtin_amdgcn_permlane16_swap(__float_as_uint(v), __float_as_uint(v), false, false);
    return __uint_as_float(b[0]) + __uint_as_float(b[1]);
}
#define BLDS(rs, l, vo, so, off) __builtin_amdgcn_raw_ptr_buffer_load_lds((rs), (LAS void*)(l), 16, (vo), (so), (off), 0)
#define BLDS1(rs, l, vo, so, off) __builtin_amdgcn_raw_ptr_buffer_load_lds((rs), (LAS void*)(l), 16, (vo), (so), (off), 16)
#define RSRC(p) __builtin_amdgcn_make_buffer_rsrc((void*)(p), 0, 0x7fffffff, 0x00020000)
#define ST16(rs, vo, so, v, aux) __builtin_amdgcn_raw_buffer_store_b128((v), (rs), (vo) + (so), 0, aux)
#define ST8(rs, vo, so, v, aux) __builtin_amdgcn_raw_buffer_store_b64((v), (rs), (vo), (so), aux)
#define WAIT_V(n) asm volatile("s_waitcnt vmcnt(%0)" ::"i"(n) : "memory")
#define WAIT_L0() asm volatile("s_waitcnt lgkmcnt(0)" ::: "memory")
#define BAR() do { asm volatile("" ::: "memory"); __builtin_amdgcn_s_barrier(); asm volatile("" ::: "memory"); } while (0)
#define BARX() do { __builtin_amdgcn_sched_barrier(0); BAR(); __builtin_amdgcn_sched_barrier(0); } while (0)
#define LOOP_ALIGN() asm volatile(".p2align 6")
#define SCHED0() __builtin_amdgcn_sched_barrier(0)
#define MFMA(a, b, c) __builtin_amdgcn_mfma_f32_16x16x32_bf16((a), (b), (c), 0, 0, 0)
#define MFMA_T(a, b, c) asm volatile("v_mfma_f32_16x16x32_bf16 %0, %1, %2, %0" : "+v"(c) : "v"(a), "v"(b))
#define MFMA_0(a, b, c) asm volatile("v_mfma_f32_16x16x32_bf16 %0, %1, %2, 0" : "=&v"(c) : "v"(a), "v"(b))
#define WAIT_L(n) __builtin_amdgcn_s_waitcnt(0xC07F | ((n) << 8))
#define LDS_FRAG(p) (*(const LAS bf16x8*)(p))
DEVI int lane_id_fresh() { int z = 0; asm volatile("" : "+v"(z)); return __builtin_amdgcn_mbcnt_hi(~0u, __builtin_amdgcn_mbcnt_lo(~0u, z)); }
DEVI const LAS char* opaque(const LAS char* p) { asm volatile("" : "+v"(p)); return p; }
#define DBG_ARG
#define DBG_LAUNCH(n)
#define STAMP(i) do { if (STAMP_SEC * 16 + (i) == (PROBE_A & 31) && st_l == (PROBE_A >> 5)) { SCHED0(); asm volatile("s_memrealtime %0\n\ts_waitcnt lgkmcnt(0)" : "=s"(st_[0]) :: "memory"); SCHED0(); } \
    if (STAMP_SEC * 16 + (i) == (PROBE_B & 31) && st_l == (PROBE_B >> 5)) { SCHED0(); asm volatile("s_memrealtime %0\n\ts_waitcnt lgkmcnt(0)" : "=s"(st_[1]) :: "memory"); SCHED0(); } } while (0)
#define STAMP_DECL unsigned long long st_[2] = {0ull, 0ull}
#define STAMP_BEGIN const int st_l = layer; STAMP(1)
#define STAMP_END(n) STAMP(14)
#define STAMP_PARAM , unsigned long long (&st_)[2], const int st_l
#define STAMP_FWD , st_, st_l

DEVI float ln_rstd(const f32x4 p) {
    float s = p[0] + p[2], q = p[1] + p[3];
    s += __builtin_amdgcn_update_dpp(0.f, s, 0xB1, 0xf, 0xf, true); q += __builtin_amdgcn_update_dpp(0.f, q, 0xB1, 0xf, 0xf, true);
    s += __builtin_amdgcn_update_dpp(0.f, s, 0x4E, 0xf, 0xf, true); q += __builtin_amdgcn_update_dpp(0.f, q, 0x4E, 0xf, 0xf, true);
    const float mean = s * (1.0f / 576.0f);
    return __builtin_amdgcn_rsqf(fmax2(q * (1.0f / 576.0f) - mean * mean, 0.f) + 1e-5f);
}

struct Res { u32x4 a, c; u32x2 e; };
DEVI Res proj_residual_load(const char* xin, int rt, int cg, int lane) {
    const auto rs = RSRC(xin + (size_t)rt * 18432 + cg * 2304);
    Res x;
    x.a = __builtin_amdgcn_raw_buffer_load_b128(rs, lane * 16, 0, 16); x.c = __builtin_amdgcn_raw_buffer_load_b128(rs, lane * 16 + 1024, 0, 16);
    x.e = __builtin_amdgcn_raw_buffer_load_b64(rs, 2048 + (lane & 15) * 16 + ((lane >> 4) & 1) * 8, 0, 16);
    return x;
}
DEVI void proj_residual(f32x4 (&v)[5], const Res& x) {
    v[0] = (f32x4){bflo(x.a.x), bfhi(x.a.x), bflo(x.a.y), bfhi(x.a.y)}; v[1] = (f32x4){bflo(x.a.z), bfhi(x.a.z), bflo(x.a.w), bfhi(x.a.w)};
    v[2] = (f32x4){bflo(x.c.x), bfhi(x.c.x), bflo(x.c.y), bfhi(x.c.y)}; v[3] = (f32x4){bflo(x.c.z), bfhi(x.c.z), bflo(x.c.w), bfhi(x.c.w)};
    v[4] = (f32x4){bflo(x.e.x), bfhi(x.e.x), bflo(x.e.y), bfhi(x.e.y)};
}
struct ProjOut { __amdgpu_buffer_rsrc_t x, st; unsigned v0, v1, v2, vs; };
DEVI ProjOut proj_out(char* xout, char* stout, int rt, int cg, int lane) {
    const int r = lane & 15, q = lane >> 4;
    ProjOut p = {RSRC(xout + (size_t)rt * 18432 + cg * 2304), RSRC(stout + (size_t)rt * 1024 + cg * 8),
                 pin((unsigned)lane * 16u), pin((unsigned)lane * 16u + 1024u), pin((unsigned)(2048 + r * 16 + (q & 1) * 8)), pin((unsigned)(r * 64 + (q >> 1) * 4))};
    asm volatile("" :: "s"(p.x), "s"(p.st));
    return p;
}
DEVI ProjOut proj_out_final(char* out, int rt, int cg, int lane) {
    const int q = lane >> 4;
    const unsigned o = (unsigned)(slot2row(rt * 16 + (lane & 15)) * 1152 + cg * 144);
    ProjOut p = {RSRC(out), RSRC(out), pin(o + q * 16), pin(o + 64 + q * 16), pin(o + 128 + (q & 1) * 8), 0u};
    return p;
}
template <bool FINAL = false>
DEVI void proj_store(f32x4 (&v)[5], const ProjOut& p) {
    ST16(p.x, p.v0, 0, ((u32x4){pack2(v[0][0], v[0][1]), pack2(v[0][2], v[0][3]), pack2(v[1][0], v[1][1]), pack2(v[1][2], v[1][3])}), FINAL ? 0 : 16);
    SCHED0();
    asm volatile("" : "+v"(v[2]), "+v"(v[3]), "+v"(v[4]));
    ST16(p.x, p.v1, 0, ((u32x4){pack2(v[2][0], v[2][1]), pack2(v[2][2], v[2][3]), pack2(v[3][0], v[3][1]), pack2(v[3][2], v[3][3])}), FINAL ? 0 : 16);
    SCHED0();
    ST8(p.x, p.v2, 0, ((u32x2){pack2(v[4][0], v[4][1]), pack2(v[4][2], v[4][3])}), FINAL ? 0 : 16);
    SCHED0();
    if (!FINAL) {
        const f32x4 s4 = ((v[0] + v[1]) + (v[2] + v[3])) + v[4] * 0.5f;
        const f32x4 q4 = ((v[0] * v[0] + v[1] * v[1]) + (v[2] * v[2] + v[3] * v[3])) + v[4] * v[4] * 0.5f;
        auto x = __builtin_amdgcn_permlane32_swap(__float_as_uint((s4[0] + s4[2]) + (s4[1] + s4[3])), __float_as_uint((q4[0] + q4[2]) + (q4[1] + q4[3])), false, false);
        const float t = __uint_as_float(x[0]) + __uint_as_float(x[1]);
        auto y = __builtin_amdgcn_permlane16_swap(__float_as_uint(t), __float_as_uint(t), false, false);
        __builtin_amdgcn_raw_buffer_store_b32(__float_as_uint(__uint_as_float(y[0]) + __uint_as_float(y[1])), p.st, p.vs, 0, 16);
        SCHED0();
    }
}

DEVI void side_load(f32x4 (&d)[3], const char* p, const char* p2) {
    asm volatile("global_load_dwordx4 %0, %3, off sc1\n\tglobal_load_dwordx4 %1, %3, off offset:1024 sc1\n\tglobal_load_dwordx4 %2, %4, off sc1"
                 : "=&v"(d[0]), "=&v"(d[1]), "=&v"(d[2]) : "v"(p), "v"(p2) : "memory");
}
DEVI void side_fence(f32x4 (&d)[3]) { asm volatile("" : "+v"(d[0]), "+v"(d[1]), "+v"(d[2])); }
DEVI void lds_word_set(LAS char* p, unsigned v) { asm volatile("ds_write_b32 %0, %1" :: "v"((unsigned)(size_t)p), "v"(v) : "memory"); }
template <int T0, int T1, int P, int S0 = 0, class ISSUE>
DEVI void ring_steps(ISSUE&& issue) {
    int slot = (T0 + S0) % 3;
    LOOP_ALIGN();
#pragma unroll 1
    for (int t = T0; t < T1; ++t) {
        WAIT_V(P); BAR();
        issue(t + 3, slot);
        slot = slot == 2 ? 0 : slot + 1;
    }
}

DEVI unsigned flag_out(unsigned use, int pitch, int group, int writer, int lane) { return pin(lane < 16 ? use + (group * 16 + lane) * pitch + writer * 16 : 0x80000000u); }
DEVI unsigned flag_in(unsigned use, int pitch, int reader, int n, int lane) { return pin(use + reader * pitch + (lane < n ? lane : 0) * 16); }
DEVI void flag_raise(const char* ws, unsigned vo) { ST16(RSRC(ws), vo, 0, ((u32x4){1u, 1u, 1u, 1u}), 16); SCHED0(); }
template <int FIRST>
DEVI void flags_wait(const char* ws, unsigned vo) {
    unsigned v, n = 400000u;
    asm volatile("s_nop 4\n\ts_sleep %4\n"
                 "1:\n\tbuffer_load_dword %0, %2, %3, 0 offen sc1\n\ts_waitcnt vmcnt(0)\n\tv_cmp_eq_u32 vcc, 0, %0\n\ts_cbranch_vccz 2f\n\t"
                 "s_sleep 1\n\ts_sub_u32 %1, %1, 1\n\ts_cbranch_scc0 1b\n2:"
                 : "=&v"(v), "+s"(n) : "v"(vo), "s"(RSRC(ws)), "i"(FIRST) : "vcc", "scc", "memory");
    SCHED0();
}
DEVI void lds_word_wait(LAS char* p) {
#pragma unroll 1
    for (unsigned spin = 0; spin < 40000000u; ++spin) {
        unsigned v;
        asm volatile("ds_read_b32 %0, %1\n\ts_waitcnt lgkmcnt(0)" : "=v"(v) : "v"((unsigned)(size_t)p) : "memory");
        if (__builtin_amdgcn_readfirstlane(v) != 0u) break;
        __builtin_amdgcn_s_sleep(1);
    }
}

constexpr int K1_STAGE = 32768;
constexpr int WO_OFF = 69632;
constexpr int G_PART = WO_OFF + 82944;
constexpr int G_TAB = G_PART + 2048;
constexpr int K1_SCR = G_TAB + 1536;
constexpr int G_WORD = K1_SCR + 576;
constexpr int NET_LDS = G_WORD + 64;

#define WO_PRE 12
#define WO_W 33
#define O_FIRST 10
#define X1_FIRST 6
#define X2_FIRST 6
#define Y_FIRST 10
#define Z2_FIRST 10
DEVI void attn_issueB(LAS char* lds, const char* ws, int hp, int wid, int lane, int s, int slot) {
    const auto rB = RSRC(ws + OFF_TWQKV + (size_t)hp * (18 * 7168));
    LAS char* sl = lds + slot * K1_STAGE + 18432 + wid * 4096;
    const int off = wid * 4096 + s * 14336;
    BLDS(rB, sl, lane * 16, off, 0); BLDS(rB, sl, lane * 16, off, 1024);
    if (wid < 3) { BLDS(rB, sl, lane * 16, off, 2048); BLDS(rB, sl, lane * 16, off, 3072); }
}
template <class NEXT>
DEVI void attn_producer(LAS char* lds, const char* ws, const char* wl, unsigned fl, int layer, int pm, int hp, int wid, NEXT&& next) {
    const int lane = lane_id_fresh(), r = lane & 15, q = lane >> 4;
    const int pmh = pm > 0 ? pm : 1;
    const auto rA = RSRC(wl + L_XA);
    const unsigned vo = lane * 16, voh = (unsigned)((r >> 3) * 18432 + q * 256 + (8 + (r & 7)) * 16);
    const int oA = (pm * 8 + 2 * wid) * 18432, oh = (pmh * 8 - 2) * 18432;
    auto issueA = [&](int s, int slot) {
        LAS char* sl = lds + slot * K1_STAGE;
        if (wid == 3) { BLDS1(rA, sl, voh, oh + s * 2048, 0); BLDS1(rA, sl, voh, oh + s * 2048, 1024); }
        BLDS1(rA, sl + 2048 + wid * 4096, vo, oA + s * 2048, 0); BLDS1(rA, sl + 2048 + wid * 4096, vo, oA + s * 2048, 1024);
        BLDS1(rA, sl + 4096 + wid * 4096, vo, oA + s * 2048 + 18432, 0); BLDS1(rA, sl + 4096 + wid * 4096, vo, oA + s * 2048 + 18432, 1024);
    };
    auto issue = [&](int s, int slot) { issueA(s, slot); attn_issueB(lds, ws, hp, wid, lane, s, slot); };
    __builtin_amdgcn_s_setprio(3);
    if (layer == 0) {
        issue(0, 0); issue(1, 1);
        WAIT_V(8);
    } else {
        attn_issueB(lds, ws, hp, wid, lane, 2, 2);
        const unsigned fi = flag_in(F_X2, 512, pm * 16 + hp, pm > 0 ? 24 : 16, lane);
        if (wid == 0) flags_wait<X2_FIRST>(ws, fi);
        BARX();
        issueA(0, 0); issueA(1, 1); issueA(2, 2);
        WAIT_V(8);
    }
    BAR();
    __builtin_amdgcn_s_setprio(0);
    if (layer == 0) issue(2, 2); else WAIT_V(4);
    ring_steps<0, 2, 8>(issue);
    {
        const int hr = lane >> 2;
        f32x4 sp[3];
        side_load(sp, wl + L_STA + (size_t)(pm * 128 + wid * 32) * 64 + lane * 16, wl + L_STA + (size_t)((pmh * 8 - 2 + (hr >> 3)) * 16 + 8 + (hr & 7)) * 64 + (lane & 3) * 16);
        WAIT_V(11); BAR(); issue(5, 2);
        ring_steps<3, 6, 8>(issue);
        __builtin_amdgcn_s_setprio(3);
        side_fence(sp);
#pragma unroll
        for (int i = 0; i < 3; ++i) lds_word_set(lds + K1_SCR + (i < 2 ? 16 + wid * 32 + i * 16 + hr : hr) * 4, __float_as_uint(ln_rstd(sp[i])));
        WAIT_L0();
        __builtin_amdgcn_s_setprio(0);
    }
    WAIT_V(8); BAR();
    WAIT_V(0); BAR();
    BAR();
    const auto rW = RSRC(ws + OFF_TWO + (size_t)(hp >> 1) * 82944);
#pragma unroll
    for (int i = 0; i < 5; ++i) {
        const int off = (i * 4 + wid) * 4096;
        LAS char* d = lds + WO_OFF + off;
        BLDS(rW, d, vo, off, 0); BLDS(rW, d, vo, off, 1024); BLDS(rW, d, vo, off, 2048); BLDS(rW, d, vo, off, 3072);
    }
    if (wid == 0) BLDS(rW, lds + WO_OFF + 81920, vo, 81920, 0);
    next();
    const unsigned fo = flag_out(fl + F_O, 256, pm, hp, lane), fi = flag_in(fl + F_O, 256, pm * 16 + hp, 16, lane);
    WAIT_V(0);
    if (wid == 0) __builtin_amdgcn_s_setprio(3);
    BARX();
    if (wid == 0) { flag_raise(ws, fo); flags_wait<O_FIRST>(ws, fi); }
    BARX();
}

#define STAMP_SEC 0
DEVI void attn_consumer(LAS char* lds, char* wl, int layer, int pm, int hp, int wid STAMP_PARAM) {
    const int lane = lane_id_fresh(), r = lane & 15, q = lane >> 4;
    const char* xa = wl + L_XA;
    char* o = wl + L_O;
    const int T = wid & 1, h = wid >> 1;
    const LAS char* ph = lds + q * 256 + ((r ^ (T ? 0 : 8)) * 16);
    const LAS char* pa = lds + T * 2048 + lane * 16;
    const LAS char* pb = lds + h * 3072 + lane * 16;
    const LAS char* pt = lds + lane * 16;
    f32x4 acc[5][4];
#pragma unroll
    for (int m = 0; m < 5; ++m)
#pragma unroll
        for (int n = 0; n < 4; ++n) if (m + n) acc[m][n] = pin((f32x4){0.f, 0.f, 0.f, 0.f});
    bf16x8 fa[2][5], fb[2][4];
    auto rd1 = [&](int kk, int i) {
        if (i < 3) fb[kk][i] = LDS_FRAG(pb + 18432 + kk * 7168 + i * 1024);
        else if (i == 3) fb[kk][3] = LDS_FRAG(pt + 24576 + kk * 7168);
        else if (i == 4) fa[kk][0] = LDS_FRAG(ph + kk * 1024);
        else if (i < 9) fa[kk][i - 4] = LDS_FRAG(pa + 2048 + kk * 1024 + (i - 5) * 4096);
    };
    auto chunk = [&](int kk, int d) {
#pragma unroll
        for (int i = 1; i < 20; ++i) {
            const int m = i >> 2, n = i & 3;
            const bf16x8 &A = fa[kk][m], &B = fb[kk][n];
            acc[m][n] = n != 2 ? MFMA(B, A, acc[m][n]) : MFMA(A, B, acc[m][n]);
            SCHED0();
            if (i <= 9) rd1(kk ^ 1, i - 1);
            if (kk == 0) { if (i == 11) ph = opaque(ph + d); if (i == 12) pa = opaque(pa + d); if (i == 13) pb = opaque(pb + d); if (i == 14) pt = opaque(pt + d); }
            SCHED0();
        }
    };
    f32x4 mb, mb0;
#pragma unroll
    for (int j = 0; j < 4; ++j) {
        const bool ok = ((r - 4 * q - j) & 15) < 8;
        mb[j] = ok ? 0.f : -__builtin_inff();
        mb0[j] = (ok && (pm > 0 || 4 * q + j <= r)) ? 0.f : -__builtin_inff();
    }
    mb = pin(mb); mb0 = pin(mb0);
    const unsigned mp0 = pin((4 * q > r ? 0xffffu : 0u) | (4 * q + 1 > r ? 0xffff0000u : 0u)), mp1 = pin((4 * q + 2 > r ? 0xffffu : 0u) | (4 * q + 3 > r ? 0xffff0000u : 0u));
    const unsigned qtm = pin(q == h ? 0xffffffffu : 0u);
    const unsigned vom = pin((unsigned)(r * 16 + hp * 1024 + (2 * h + (q >> 1)) * 256 + (q & 1) * 8)), vot = pin((unsigned)(r * 16 + 16384 + (hp >> 1) * 256 + (hp & 1) * 8 + h * 4));
    const LAS char* tk0 = opaque(lds + K1_SCR + ((q * 4) ^ (T ? 0 : 8)) * 4);
    const LAS char* tk = opaque(lds + K1_SCR + ((1 + T) * 16 + q * 4) * 4);
    const LAS char* tq = opaque(lds + K1_SCR + ((1 + T) * 16 + r) * 4);
    ph = opaque(ph); pa = opaque(pa); pb = opaque(pb); pt = opaque(pt);
    STAMP(2);
    if (layer) BAR();
    BARX();
    STAMP(3);
#pragma unroll
    for (int i = 0; i < 9; ++i) rd1(0, i);
    SCHED0();
    const int rt = pm * 8 + (hp & 1) * 4 + wid, cg = hp >> 1;
    const Res rx = proj_residual_load(xa, rt, cg, lane);
    SCHED0();
    int slot = 0;
    LOOP_ALIGN();
#pragma unroll 1
    for (int t = 0; t < 9; ++t) {
        const int d = slot == 2 ? -2 * K1_STAGE : K1_STAGE;
        slot = slot == 2 ? 0 : slot + 1;
        SCHED0();
        chunk(0, d);
        WAIT_L0(); BAR();
        SCHED0();
        chunk(1, 0);
    }
    STAMP(4);
    u32x2 ts[5], tf[5];
    {
        LAS char* xb = lds + wid * 320;
#pragma unroll
        for (int m = 0; m < 5; ++m) ts[m] = (u32x2){pack2(acc[m][3][0], acc[m][3][1]), pack2(acc[m][3][2], acc[m][3][3])};
        if (q == 2) {
#pragma unroll
            for (int m = 0; m < 5; ++m) {
                const unsigned w = h ? ts[m].y : ts[m].x;
                *(LAS u16*)(xb + m * 64 + r * 2) = (u16)w; *(LAS u16*)(xb + m * 64 + 32 + r * 2) = (u16)(w >> 16);
            }
        }
        asm volatile("" ::: "memory");
#pragma unroll
        for (int m = 0; m < 5; ++m) tf[m] = *(const LAS u32x2*)(xb + m * 64 + (r & 1) * 32 + q * 8);
    }
    {
        f32x4 rk[5]; float rq[4];
        rk[0] = *(const LAS f32x4*)(tk0);
#pragma unroll
        for (int g = 0; g < 4; ++g) { rk[g + 1] = *(const LAS f32x4*)(tk + g * 128); rq[g] = *(const LAS float*)(tq + g * 128); }
        const f32x4 zero = {0.f, 0.f, 0.f, 0.f};
        u32x4 kf[5];
#pragma unroll
        for (int m = 0; m < 5; ++m) kf[m] = (u32x4){pack2(acc[m][1][0], acc[m][1][1]), pack2(acc[m][1][2], acc[m][1][3]), ts[m].y, 0u};
        f32x4 sp[4], sc[4];
#pragma unroll
        for (int g = 0; g < 4; ++g) {
            const u32x4 qf = {pack2(acc[g + 1][0][0], acc[g + 1][0][1]), pack2(acc[g + 1][0][2], acc[g + 1][0][3]), ts[g + 1].x & qtm, 0u};
            sp[g] = MFMA(__builtin_bit_cast(bf16x8, kf[g]), __builtin_bit_cast(bf16x8, qf), zero);
            sc[g] = MFMA(__builtin_bit_cast(bf16x8, kf[g + 1]), __builtin_bit_cast(bf16x8, qf), zero);
        }
        u32x2 vf[5];
#pragma unroll
        for (int m = 0; m < 5; ++m) vf[m] = (u32x2){pack2(acc[m][2][0], acc[m][2][1]), pack2(acc[m][2][2], acc[m][2][3])};
        f32x4 rs[4], t[4]; float mx[4], sm[4];
#pragma unroll
        for (int g = 0; g < 4; ++g) {
#pragma unroll
            for (int j = 0; j < 4; ++j) { const bool pv = 4 * q + j > r; rs[g][j] = pv ? rk[g][j] : rk[g + 1][j]; t[g][j] = pv ? sp[g][j] : sc[g][j]; }
            t[g] = (t[g] * rs[g]) * (rq[g] * (0.23570226039551584f * 1.4426950408889634f)) + (g == 0 ? mb0 : mb);
            mx[g] = fmaxf(fmaxf(t[g][0], t[g][1]), fmaxf(t[g][2], t[g][3]));
        }
        xq4_max(mx);
#pragma unroll
        for (int g = 0; g < 4; ++g) {
            t[g] = t[g] - mx[g];
#pragma unroll
            for (int j = 0; j < 4; ++j) t[g][j] = __builtin_amdgcn_exp2f(t[g][j]);
            sm[g] = (t[g][0] + t[g][2]) + (t[g][1] + t[g][3]);
        }
        f32x4 om[4], ot[4];
#pragma unroll
        for (int g = 0; g < 4; ++g) {
            const f32x4 p = t[g] * rs[g];
            const unsigned p01 = pack2(p[0], p[1]), p23 = pack2(p[2], p[3]);
            const u32x4 pf = {p01 & mp0, p23 & mp1, p01 & ~mp0, p23 & ~mp1};
            om[g] = MFMA(__builtin_bit_cast(bf16x8, ((u32x4){vf[g].x, vf[g].y, vf[g + 1].x, vf[g + 1].y})), __builtin_bit_cast(bf16x8, pf), zero);
            ot[g] = MFMA(__builtin_bit_cast(bf16x8, ((u32x4){tf[g].x, tf[g].y, tf[g + 1].x, tf[g + 1].y})), __builtin_bit_cast(bf16x8, pf), zero);
        }
        xq4_sum_rcp(sm);
        const auto ro = RSRC(o + (size_t)(pm * 8 + T) * 18432);
        unsigned tw2[4];
#pragma unroll
        for (int g = 0; g < 4; ++g) {
            const f32x4 v = om[g] * sm[g];
            __builtin_amdgcn_raw_buffer_store_b64((u32x2){pack2(v[0], v[1]), pack2(v[2], v[3])}, ro, vom, g * 36864, 16);
            const f32x2 w = ot[g].xy * sm[g];
            tw2[g] = pack2(w.x, w.y);
        }
        if (q == 2) {
#pragma unroll
            for (int g = 0; g < 4; ++g) __builtin_amdgcn_raw_buffer_store_b32(tw2[g], ro, vot, g * 36864, 16);
        }
    }
    STAMP(5);
    {
        WAIT_V(0); BARX();
        STAMP(6);
        f32x4 v[5];
        proj_residual(v, rx);
        const LAS char* pw = opaque(lds + WO_OFF + lane * 16);
        const LAS char* pw2 = opaque(lds + WO_OFF + 41472 + lane * 16);
        const LAS char* ph2 = opaque(lds + WO_OFF + 4096 + q * 128 + (r & 7) * 16);
        const LAS char* ph3 = opaque(lds + WO_OFF + 41472 + 4096 + q * 128 + (r & 7) * 16);
        bf16x8 wf[90];
        auto rdw = [&](int i) {
            const int kc = i / 5, n = i % 5;
            wf[i] = n < 4 ? LDS_FRAG((kc < 9 ? pw : pw2) + (kc % 9) * 4608 + n * 1024) : LDS_FRAG((kc < 9 ? ph2 : ph3) + (kc % 9) * 4608);
        };
#pragma unroll
        for (int i = 0; i < WO_W; ++i) rdw(i);
#pragma unroll
        for (int n = 0; n < 5; ++n) v[n] = pin(v[n]);
        const auto rO = RSRC(o + (size_t)rt * 18432);
        const ProjOut po = proj_out(wl + L_XB, wl + L_STB, rt, cg, lane);
        asm volatile("" :: "s"(rO));
        WAIT_L(0);
        BARX();
        STAMP(7);
        bf16x8 af[18];
        auto ldo = [&](int kc) { af[kc] = __builtin_bit_cast(bf16x8, __builtin_amdgcn_raw_buffer_load_b128(rO, lane * 16 + (kc & 3) * 1024, (kc >> 2) * 4096, 16)); };
#pragma unroll
        for (int kc = 0; kc < WO_PRE; ++kc) { ldo(kc); SCHED0(); }
        static_for<90>([&](auto ic) {
            constexpr int i = ic.value, kc = i / 5, n = i % 5, j = WO_W + 2 * (i / 3) + i % 3;
            MFMA_T(wf[i], af[kc], v[n]);
            SCHED0();
            if (i % 3 < 2 && j < 90) rdw(j);
            if (i % 3 == 2 && (i / 3) % 2 == 0 && i / 6 + WO_PRE < 18) ldo(i / 6 + WO_PRE);
            SCHED0();
        });
        asm volatile("s_nop 4" : "+v"(v[0]), "+v"(v[1]));
        STAMP(8);
        proj_store(v, po);
    }
}

DEVI f32x2 gelu2(f32x2 v) {
    const f32x2 w = v * (v * v * -0.10294324f + -2.3022082f);
    const f32x2 d = (f32x2){__builtin_amdgcn_exp2f(w.x), __builtin_amdgcn_exp2f(w.y)} + 1.0f;
    return v * (f32x2){__builtin_amdgcn_rcpf(d.x), __builtin_amdgcn_rcpf(d.y)};
}
DEVI f32x4 gelu4(f32x4 v) { const f32x2 a = gelu2(v.xy), b = gelu2(v.zw); return (f32x4){a.x, a.y, b.x, b.y}; }
DEVI void rowsum16x8(const f32x4 a, const f32x4 b, float& o0, float& o1) {
    float t0, t1, t2, t3;
    asm volatile(
        "s_nop 1\n\t"
        "v_add_f32_dpp %2, %6, %6 row_ror:8 row_mask:0xf bank_mask:0x3\n\t"
        "v_add_f32_dpp %3, %7, %7 row_ror:8 row_mask:0xf bank_mask:0x3\n\t"
        "v_add_f32_dpp %4, %10, %10 row_ror:8 row_mask:0xf bank_mask:0x3\n\t"
        "v_add_f32_dpp %5, %11, %11 row_ror:8 row_mask:0xf bank_mask:0x3\n\t"
        "v_add_f32_dpp %2, %8, %8 row_ror:8 row_mask:0xf bank_mask:0xc\n\t"
        "v_add_f32_dpp %3, %9, %9 row_ror:8 row_mask:0xf bank_mask:0xc\n\t"
        "v_add_f32_dpp %4, %12, %12 row_ror:8 row_mask:0xf bank_mask:0xc\n\t"
        "v_add_f32_dpp %5, %13, %13 row_ror:8 row_mask:0xf bank_mask:0xc\n\t"
        "v_add_f32_dpp %0, %2, %2 row_half_mirror row_mask:0xf bank_mask:0x5\n\t"
        "s_nop 0\n\t"
        "v_add_f32_dpp %1, %4, %4 row_half_mirror row_mask:0xf bank_mask:0x5\n\t"
        "v_add_f32_dpp %0, %3, %3 row_half_mirror row_mask:0xf bank_mask:0xa\n\t"
        "v_add_f32_dpp %1, %5, %5 row_half_mirror row_mask:0xf bank_mask:0xa\n\t"
        "s_nop 0\n\t"
        "v_add_f32_dpp %0, %0, %0 quad_perm:[1,0,3,2] row_mask:0xf bank_mask:0xf\n\t"
        "v_add_f32_dpp %1, %1, %1 quad_perm:[1,0,3,2] row_mask:0xf bank_mask:0xf\n\t"
        "s_nop 0\n\t"
        "v_add_f32_dpp %0, %0, %0 quad_perm:[2,3,0,1] row_mask:0xf bank_mask:0xf\n\t"
        "v_add_f32_dpp %1, %1, %1 quad_perm:[2,3,0,1] row_mask:0xf bank_mask:0xf\n\t"
        : "=&v"(o0), "=&v"(o1), "=&v"(t0), "=&v"(t1), "=&v"(t2), "=&v"(t3)
        : "v"(a[0]), "v"(a[1]), "v"(a[2]), "v"(a[3]), "v"(b[0]), "v"(b[1]), "v"(b[2]), "v"(b[3]));
}

#define K5_PRE 8
#define RH_MAP 0
#define NET_LAYERS 2
constexpr int G_STAGE = 34816;
constexpr int K5_STAGE = 18432, K5_NS = 8;

constexpr int y_load_at(int slot) {
    int next = K5_PRE;
    for (int s = 0;; ++s) { const bool go = next < 18 + 2 * (s / 3); if (s == slot) return go ? next : -1; next += go; }
}

struct GTile { f32x4 a[4][4], e[2]; };
struct GFrag { bf16x8 a[2][4], b[2][5]; const LAS char *pa[2][2], *pb, *p8; int slot; };
DEVI void g_rd(GFrag& f, int kk, int i) {
    if (i < 4) f.a[kk][i] = LDS_FRAG(f.pa[kk][i >> 1] + (i & 1) * 2048);
    else if (i < 8) f.b[kk][i - 4] = LDS_FRAG(f.pb + (kk * 9 + i - 4) * 1024);
    else f.b[kk][4] = LDS_FRAG(f.p8 + kk * 9216);
}
template <bool ROWS_IN_REGS>
DEVI void g_chunk(GTile& c, GFrag& f, int kk, int d) {
#pragma unroll
    for (int i = 0; i < 18; ++i) {
        const int n = i >> 2, m = i & 3;
        f32x4& e = n < 4 ? c.a[m][n] : c.e[m & 1];
        e = ROWS_IN_REGS ? MFMA(f.a[kk][m], f.b[kk][n], e) : MFMA(f.b[kk][n], f.a[kk][m], e);
        SCHED0();
        if (i < 9) g_rd(f, kk ^ 1, i);
        if (kk == 0) {
            if (i >= 10 && i < 14) f.pa[(i - 10) >> 1][i & 1] = opaque(f.pa[(i - 10) >> 1][i & 1] + d);
            if (i == 14) f.pb = opaque(f.pb + d);
            if (i == 15) f.p8 = opaque(f.p8 + d);
        }
        SCHED0();
    }
}
template <int NS, bool ROWS_IN_REGS, bool DRAIN_LAST = false>
DEVI void g_steps(GTile& c, GFrag& f, int n) {
    LOOP_ALIGN();
#pragma unroll 1
    for (int t = 0; t < n; ++t) {
        const int d = f.slot == NS - 1 ? -(NS - 1) * G_STAGE : G_STAGE;
        f.slot = f.slot == NS - 1 ? 0 : f.slot + 1;
        SCHED0();
        g_chunk<ROWS_IN_REGS>(c, f, 0, d);
        WAIT_L0();
        if (DRAIN_LAST && t == n - 1) WAIT_V(0);
        BAR();
        SCHED0();
        g_chunk<ROWS_IN_REGS>(c, f, 1, 0);
    }
}
DEVI void g_fill(GTile& c, const float (&e)[4]) {
#pragma unroll
    for (int m = 0; m < 4; ++m) {
#pragma unroll
        for (int n = 0; n < 4; ++n) c.a[m][n] = pin((f32x4){e[m], e[m], e[m], e[m]});
        if (m < 2) c.e[m] = pin((f32x4){e[m], e[m], e[m], e[m]});
    }
}

#undef STAMP_SEC
#define STAMP_SEC 1
DEVI void gmlp_body(LAS char* lds, int wm, int wn, int pm, int cg, char* z2t, char* y, const u16* __restrict__ bsp STAMP_PARAM) {
    const int mlo = wm * 4 + wn * 2, mhi = wm * 4 + (1 - wn) * 2;
    const float zero4[4] = {0.f, 0.f, 0.f, 0.f};
    GTile z2, z1, acc;
    GFrag f;
    {
        const int lane = lane_id_fresh();
#pragma unroll
        for (int kk = 0; kk < 2; ++kk) { f.pa[kk][0] = lds + mlo * 2048 + kk * 1024 + lane * 16; f.pa[kk][1] = lds + mhi * 2048 + kk * 1024 + lane * 16; }
        f.pb = lds + 16384 + wn * 4096 + lane * 16; f.p8 = lds + 16384 + 8192 + lane * 16; f.slot = 0;
    }
    g_fill(z2, zero4);
#pragma unroll
    for (int i = 0; i < 4; ++i) f.pa[i >> 1][i & 1] = opaque(f.pa[i >> 1][i & 1]);
    f.pb = opaque(f.pb); f.p8 = opaque(f.p8);
    SCHED0();
    WAIT_V(0); BARX();
    BAR();
    BARX();
    STAMP(2);
#pragma unroll
    for (int i = 0; i < 9; ++i) g_rd(f, 0, i);
    g_steps<4, true>(z2, f, 9);
    STAMP(3);
    {
        const int lane = lane_id_fresh(), r = lane & 15, q = lane >> 4;
        f32x4 ra[4];
#pragma unroll
        for (int m = 0; m < 4; ++m) ra[m] = *(const LAS f32x4*)(lds + G_TAB + ((m < 2 ? mlo : mhi) + (m & 1)) * 64 + q * 16);
        SCHED0();
#pragma unroll
        for (int m = 0; m < 4; ++m) {
            f32x4 s1, s2;
#pragma unroll
            for (int n = 0; n < 4; ++n) {
                const f32x4 g = gelu4(z2.a[m][n] * ra[m]);
                z2.a[m][n] = g;
                if (n == 0) { s1 = g; s2 = g * g; } else { s1 += g; s2 += g * g; }
            }
            if (m < 2) { const f32x4 g = gelu4(z2.e[m] * ra[m]); z2.e[m] = g; s1 += g; s2 += g * g; }
            float u0, u1;
            rowsum16x8(s1, s2, u0, u1);
            if ((r & 3) == 0) *(LAS f32x2*)(lds + G_PART + wn * 1024 + ((m < 2 ? mlo : mhi) + (m & 1)) * 128 + (q * 4 + (r >> 2)) * 8) = (f32x2){u0, u1};
            SCHED0();
        }
    }
    WAIT_L0(); BAR();
    STAMP(4);
    g_fill(z1, zero4);
    g_steps<4, false>(z1, f, 6);
    STAMP(5);
    {
        const int lane = lane_id_fresh(), q = lane >> 4;
#pragma unroll
        for (int g = 0; g < 2; ++g) {
            const auto rz2 = RSRC(z2t + (size_t)pm * 589824 + cg * 36864 + ((g ? mhi : mlo) >> 1) * 9216);
            const LAS char* tab = lds + G_TAB + (g ? mhi : mlo) * 64 + q * 16;
            f32x4 ra[2], rb[2];
#pragma unroll
            for (int T = 0; T < 2; ++T) { ra[T] = *(const LAS f32x4*)(tab + 512 + T * 64); rb[T] = *(const LAS f32x4*)(tab + 1024 + T * 64); }
#pragma unroll
            for (int n = 0; n < (g ? 4 : 5); ++n) {
                const f32x4 v0 = (n < 4 ? z2.a[2 * g][n & 3] : z2.e[0]) * ra[0] + rb[0], v1 = (n < 4 ? z2.a[2 * g + 1][n & 3] : z2.e[1]) * ra[1] + rb[1];
                ST16(rz2, lane * 16, n < 4 ? wn * 4096 + n * 1024 : 8192, ((u32x4){pack2(v0[0], v1[0]), pack2(v0[1], v1[1]), pack2(v0[2], v1[2]), pack2(v0[3], v1[3])}), 16);
            }
        }
    }
    STAMP(6);
    g_steps<4, false, true>(z1, f, 3);
    STAMP(7);
    {
        const int lane = lane_id_fresh(), r = lane & 15, q = lane >> 4;
        const unsigned b0 = *(const unsigned*)(bsp + pm * 128 + mlo * 16 + 2 * r), b1 = *(const unsigned*)(bsp + pm * 128 + mhi * 16 + 2 * r);
        float ra[4];
#pragma unroll
        for (int m = 0; m < 4; ++m) ra[m] = *(const LAS float*)(lds + G_TAB + ((m < 2 ? mlo : mhi) + (m & 1)) * 64 + r * 4);
        SCHED0();
#pragma unroll
        for (int m = 0; m < 4; ++m) {
#pragma unroll
            for (int n = 0; n < 4; ++n) z1.a[m][n] = pin(gelu4(z1.a[m][n] * ra[m]));
            if (m < 2) z1.e[m] = pin(gelu4(z1.e[m] * ra[m]));
            SCHED0();
        }
        const float bias[4] = {bflo(b0), bfhi(b0), bflo(b1), bfhi(b1)};
        g_fill(acc, bias);
#pragma unroll
        for (int kk = 0; kk < 2; ++kk) {
            f.pa[kk][0] = opaque(lds + 2 * G_STAGE + (mlo * 16 + r) * 128 + (((4 * kk + q) ^ (r & 7)) * 16));
            f.pa[kk][1] = opaque(lds + 2 * G_STAGE + (mhi * 16 + r) * 128 + (((4 * kk + q) ^ (r & 7)) * 16));
        }
        f.pb = opaque(lds + 2 * G_STAGE + 16384 + wn * 4096 + lane * 16); f.p8 = opaque(lds + 2 * G_STAGE + 16384 + 8192 + lane * 16); f.slot = 2;
    }
    STAMP(8);
    BAR();
    STAMP(9);
#pragma unroll
    for (int i = 0; i < 9; ++i) g_rd(f, 0, i);
    g_steps<3, false>(acc, f, 32);
    STAMP(10);
    const int lane = lane_id_fresh();
#pragma unroll
    for (int g = 0; g < 2; ++g) {
        const auto ry = RSRC(y + (size_t)(pm * 8 + (g ? mhi : mlo)) * 73728 + cg * 4608);
#pragma unroll
        for (int T = 0; T < 2; ++T) {
            const int m = 2 * g + T;
#pragma unroll
            for (int p = 0; p < 2; ++p) {
                const f32x4 a = acc.a[m][2 * p] * z1.a[m][2 * p], c = acc.a[m][2 * p + 1] * z1.a[m][2 * p + 1];
                ST16(ry, lane * 16 + wn * 2048 + p * 1024, T * 73728, ((u32x4){pack2(a[0], a[1]), pack2(a[2], a[3]), pack2(c[0], c[1]), pack2(c[2], c[3])}), 16);
            }
            if (g == 0) {
                const f32x4 a = acc.e[T] * z1.e[T];
                ST8(ry, (lane >> 5) * 256 + (lane & 15) * 16 + ((lane >> 4) & 1) * 8, T * 73728 + 4096, ((u32x2){pack2(a[0], a[1]), pack2(a[2], a[3])}), 16);
            }
        }
    }
}

#define NET_IDS \
    int b = __builtin_amdgcn_readfirstlane(blockIdx.x), wid = wid8 & 3; \
    asm volatile("" : "+s"(b), "+s"(wid));     \
    const int xcd = b & 7, idx = b >> 3; \
    const int pa = (xcd >> 1) * 4 + (idx & 3), hp = (xcd & 1) * 8 + (idx >> 2); \
    const int pm = (xcd >> 2) * 8 + (idx & 7), cg = (xcd & 3) * 4 + (idx >> 3); \
    const int rh = RH_MAP ? xcd * 4 + (idx & 3) : (xcd >> 1) * 8 + (idx & 7), c5 = RH_MAP ? idx >> 2 : (xcd & 1) * 4 + (idx >> 3); \
    const int lead = (idx + (idx >> 3)) & 3; \
    auto kord = [&](int t, int n) { \
        const int k = t + ((int)(0x1F000000u << (((t - lead) & 3) * 4)) >> 28); \
        return (unsigned)k < (unsigned)n ? k : t; \
    }; \
    auto kord5 = [&](int t) { \
        const int k = t + ((int)(0x1F000000u << (((t - (RH_MAP ? c5 : lead)) & 3) * 4)) >> 28); \
        return (unsigned)k < 18u ? k : t; \
    };

typedef short s16x4 __attribute__((ext_vector_type(4)));
DEVI u32x4 tr_frag(const LAS char* p, int four_rows) {
    const u32x2 a = __builtin_bit_cast(u32x2, __builtin_amdgcn_ds_read_tr16_b64_v4i16((LAS s16x4*)p));
    const u32x2 b = __builtin_bit_cast(u32x2, __builtin_amdgcn_ds_read_tr16_b64_v4i16((LAS s16x4*)(p + four_rows)));
    return (u32x4){a.x, a.y, b.x, b.y};
}
DEVI unsigned centre2(float a, float b, f32x2& e, float mean) {
    const f32x2 t = (f32x2){a, b} + e;
    const unsigned r = pack2(t.x, t.y);
    e = (t - (f32x2){bflo(r), bfhi(r)}) - mean;
    return r;
}
#define PREP_AUX 16
struct Retile { u32x4 f[9]; };
DEVI void retile_in_make(Retile& o, int u, int kh, const LAS char* img, int lane) {
    const int j = u % 9, c = lane & 15, q = lane >> 4, p = lane & 3;
    const LAS char* src = img + (q * 8 + (c >> 2)) * 64 + (u & 1) * 32 + (j < 8 ? (p & 1) * 16 + (p >> 1) * 8 : p * 8);
    f32x2 sum = {0.f, 0.f};
#pragma unroll
    for (int i = 0; i < 18; ++i) {
        const u32x4 f = tr_frag(src + (i < 9 ? 9 - 9 * kh + i : 9 * kh + i - 9) * 2048, 256);
        if (i >= 9) o.f[i - 9] = f;
#pragma unroll
        for (int e = 0; e < 4; ++e) sum += (f32x2){bflo(f[e]), bfhi(f[e])};
    }
    const float mean = xq_sum(sum.x + sum.y) * (1.0f / 576.0f);
    f32x2 er[2] = {{-mean, -mean}, {-mean, -mean}};
#pragma unroll
    for (int i = 0; i < 9; ++i)
#pragma unroll
        for (int e = 0; e < 4; ++e) o.f[i][e] = centre2(bflo(o.f[i][e]), bfhi(o.f[i][e]), er[e & 1], mean);
}
DEVI void retile_in_store(const Retile& o, char* twin, int u, int kh, int lane) {
    const int half = u >= 144, g = u - half * 144, cg = g / 9, j = g - cg * 9, c = lane & 15, q = lane >> 4;
    const auto rd = RSRC(twin + (size_t)cg * (18 * 18432) + (half * 18 + kh * 9) * 9216);
    const unsigned vo = (unsigned)(q * 256 + (j < 8 ? ((j & ~1) + (c >> 3)) * 1024 + ((j & 1) * 8 + (c & 7)) * 16 : 8192 + c * 16));
#pragma unroll
    for (int i = 0; i < 9; ++i) ST16(rd, vo, i * 9216, o.f[i], PREP_AUX);
}
template <int PITCH>
DEVI void retile_out_make(Retile& o, const LAS char* img, int lane) {
    const int c = lane & 15, q = lane >> 4, p = lane & 3;
    const LAS char* src = img + (q * 8 + (c >> 2)) * PITCH;
#pragma unroll
    for (int i = 0; i < 8; ++i) o.f[i] = tr_frag(src + p * 16 + (i >> 2) * 144 + ((i >> 1) & 1) * 64 + (i & 1) * 8, 4 * PITCH);
    o.f[8] = tr_frag(src + 128 + (p >> 1) * 144 + (p & 1) * 8, 4 * PITCH);
}
DEVI void retile_out_store(const Retile& o, char* dst, int nkc, int kc, int w, int lane) {
    const int c = lane & 15, q = lane >> 4;
    const auto rd = RSRC(dst + (size_t)(2 * w * nkc + kc) * 4608);
#pragma unroll
    for (int i = 0; i < 8; ++i) ST16(rd, lane * 16 + (i & 3) * 1024, (i >> 2) * nkc * 4608, o.f[i], PREP_AUX);
    ST16(rd, (c >> 3) * nkc * 4608 + 4096 + q * 128 + (c & 7) * 16, 0, o.f[8], PREP_AUX);
}

constexpr int P_CODE = 64, P_CW = 64  , P_WIN = 144, P_WOUT = 72, P_WO = 18, P_X = 64, P_Z = 32;
constexpr int P_TOTAL = P_CW + P_WIN + P_WOUT + P_WO + P_X + P_Z + P_CODE;

DEVI void prep_body(const u16* __restrict__ x, const u16* __restrict__ wqkv, const u16* __restrict__ wo, const u16* __restrict__ w_in,
                    const u16* __restrict__ w_out, char* __restrict__ ws, LAS char* lds, int u) {
    const int tid = threadIdx.x & 255;
    if (u < P_CW) {
        const int st = (u & ~15) + 2 * (u & 7) + ((u >> 3) & 1);
        if (st >= 54) return;
        const u16* src = wqkv + st * 32;
        u32x4 v[9];
#pragma unroll
        for (int i = 0; i < 9; ++i) v[i] = *(const u32x4*)(src + (size_t)((tid >> 2) + 64 * i) * 1728 + (tid & 3) * 8);
        const int c = tid & 31;
        const int col = st * 32 + c, part = col / 576, cc = col % 576, hh = cc / 18, dim = cc % 18, h = hh & 1;
        const int tile = dim < 16 ? 3 * h + part : 6;
        const int sl = dim < 16 ? dim : (part == 2 ? 8 + 2 * h : 4 * h + 2 * part) + (dim - 16);
        char* dst = ws + OFF_TWQKV + pin((unsigned)((hh >> 1) * (18 * 7168) + tile * 1024 + sl * 16 + (tid >> 7) * 7168 + ((tid >> 5) & 3) * 256));
        float a[8];
#pragma unroll
        for (int j = 0; j < 8; ++j) a[j] = 0.f;
#pragma unroll
        for (int i = 0; i < 9; ++i) {
            *(LAS u32x4*)(lds + ((tid >> 2) + 64 * i) * 64 + (tid & 3) * 16) = v[i];
            a[0] += bflo(v[i].x); a[1] += bfhi(v[i].x); a[2] += bflo(v[i].y); a[3] += bfhi(v[i].y);
            a[4] += bflo(v[i].z); a[5] += bfhi(v[i].z); a[6] += bflo(v[i].w); a[7] += bfhi(v[i].w);
        }
#pragma unroll
        for (int j = 0; j < 8; ++j) {
            a[j] += __builtin_amdgcn_update_dpp(0.f, a[j], 0x124, 0xf, 0xf, true);
            a[j] += __builtin_amdgcn_update_dpp(0.f, a[j], 0x128, 0xf, 0xf, true);
            a[j] = xq_sum(a[j]);
        }
        LAS float* red = (LAS float*)(lds + 36864);
        if ((tid & 63) < 4) {
#pragma unroll
            for (int j = 0; j < 8; ++j) red[(tid >> 6) * 32 + (tid & 3) * 8 + j] = a[j];
        }
        __syncthreads();
        const float mean = ((red[c] + red[32 + c]) + (red[64 + c] + red[96 + c])) * (1.0f / 576.0f);
        f32x2 er[2] = {{-mean, -mean}, {-mean, -mean}};
#pragma unroll
        for (int i = 0; i < 9; ++i) {
            const LAS u16* p = (const LAS u16*)lds + ((tid >> 5) + 8 * i) * 256 + c;
            u32x4 o;
#pragma unroll
            for (int j = 0; j < 4; ++j) o[j] = centre2(__uint_as_float((unsigned)p[j * 64] << 16), __uint_as_float((unsigned)p[j * 64 + 32] << 16), er[j & 1], mean);
            *(u32x4*)(dst + i * 14336) = o;
        }
        return;
    }
    u -= P_CW;
    if (u < P_WIN + P_WOUT) {
        const int w = __builtin_amdgcn_readfirstlane(tid >> 6), lane = tid & 63;
        const bool isin = u < P_WIN;
        const int st = (u & ~15) + 2 * (u & 7) + ((u >> 3) & 1);
        const auto rs = RSRC(isin ? (const char*)(w_in + st * 32) : (const char*)w_out + (size_t)(u - P_WIN) * 36864);
        const unsigned vo = isin ? (unsigned)((lane >> 2) * 9216 + (lane & 3) * 16) : (unsigned)lane * 16u;
        const int step = isin ? 16 * 9216 : 1024;
#pragma unroll
        for (int j = 0; j < 9; ++j) BLDS(rs, lds + (j * 4 + w) * 1024, vo, (j * 4 + w) * step, 0);
        WAIT_V(0); BAR();
        Retile rt;
        if (isin) { retile_in_make(rt, st * 2 + (w & 1), w >> 1, lds, lane); retile_in_store(rt, ws + OFF_TWIN, st * 2 + (w & 1), w >> 1, lane); }
        else { retile_out_make<1152>(rt, lds + w * 288, lane); retile_out_store(rt, ws + OFF_TWOUT, 72, u - P_WIN, w, lane); }
        return;
    }
    u -= P_WIN + P_WOUT;
    if (u < P_WO) {
        const int w = __builtin_amdgcn_readfirstlane(tid >> 6);
        const int lane = tid & 63;
        const auto rs = RSRC(wo + w * 144);
#pragma unroll
        for (int j = 0; j < 9; ++j) {
            const int i = lane + 64 * j, rl = (i * 3641) >> 16, kp = u * 32 + rl;
            const int row = kp < 512 ? 18 * (kp >> 4) + (kp & 15) : 18 * ((kp - 512) >> 1) + 16 + (kp & 1);
            BLDS(rs, lds + w * 9216 + j * 1024, (unsigned)(row * 1152 + (i - rl * 18) * 16), 0, 0);
        }
        WAIT_V(0);
        Retile rt;
        retile_out_make<288>(rt, lds + w * 9216, lane);
        retile_out_store(rt, ws + OFF_TWO, 18, u, w, lane);
        return;
    }
    u -= P_WO;
    if (u < P_X) {
        const int rt = u * 2 + (tid >> 7), r = tid & 15, cg = (tid >> 4) & 7;
        const int sg = rt * 16 + r;
        u32x4 v[9];
#pragma unroll
        for (int i = 0; i < 9; ++i) v[i] = *(const u32x4*)(x + (size_t)u * 18432 + (tid + 256 * i) * 8);
#pragma unroll
        for (int i = 0; i < 9; ++i) { const int p = tid + 256 * i, row = (p * 3641) >> 18; *(LAS u32x4*)(lds + p * 16 + row * 16) = v[i]; }
        __syncthreads();
#pragma unroll
        for (int i = 0; i < 9; ++i) v[i] = *(const LAS u32x4*)(lds + (2 * r + (tid >> 7)) * 1168 + cg * 144 + i * 16);
        float sum = 0.f;
#pragma unroll
        for (int i = 0; i < 9; ++i) {
            sum += ((bflo(v[i].x) + bfhi(v[i].x)) + (bflo(v[i].y) + bfhi(v[i].y))) + ((bflo(v[i].z) + bfhi(v[i].z)) + (bflo(v[i].w) + bfhi(v[i].w)));
            ST16(RSRC(ws + OFF_L0 + L_XA), rt * 18432 + cg * 2304 + r * 16, i * 256, v[i], 16);
        }
        float m2 = 0.f;
#pragma unroll
        for (int i = 0; i < 9; ++i) {
            float d;
            d = bflo(v[i].x); m2 += d * d; d = bfhi(v[i].x); m2 += d * d; d = bflo(v[i].y); m2 += d * d; d = bfhi(v[i].y); m2 += d * d;
            d = bflo(v[i].z); m2 += d * d; d = bfhi(v[i].z); m2 += d * d; d = bflo(v[i].w); m2 += d * d; d = bfhi(v[i].w); m2 += d * d;
        }
        *(f32x2*)(ws + OFF_L0 + L_STA + (size_t)sg * 64 + cg * 8) = (f32x2){sum, m2};
        return;
    }
    u -= P_X;
    if (u >= P_Z) return;
    {
        const auto rw = RSRC(ws);
        const u32x4 ones = {0xffffffffu, 0xffffffffu, 0xffffffffu, 0xffffffffu}, zero = {0u, 0u, 0u, 0u};
#pragma unroll
        for (int i = 0; i < 4; ++i) ST16(rw, (unsigned)OFF_ZST + (u * 1024 + i * 256 + tid) * 16, 0, ones, 16);
#pragma unroll
        for (int i = 0; i < 5; ++i) ST16(rw, (unsigned)OFF_CNT + (u * 1280 + i * 256 + tid) * 16, 0, zero, 16);
        ST16(rw, (unsigned)(OFF_L0 + L_STB) + (u * 256 + tid) * 16, 0, zero, 16);
#pragma unroll
        for (int i = 0; i < 2; ++i) ST16(rw, (unsigned)(OFF_L0 + L_O) + (u * 4 + i * 2 + (tid >> 7)) * 18432 + 16384 + (tid & 127) * 16, 0, zero, 16);
        if (tid < 144) {
            const int e = u * 144 + tid, f = e >> 4;
            *(u32x4*)(ws + OFF_TWQKV + (size_t)f * 7168 + 6144 + ((e >> 2) & 3) * 256 + (12 + (e & 3)) * 16) = (u32x4){0u, 0u, 0u, 0u};
        }
    }
}

__global__ __launch_bounds__(512, 2) void k_net(char* ws, const u16* wsp, const u16* bsp, char* out, const u16* x, const u16* wqkv, const u16* wo, const u16* w_in, const u16* w_out) {
    asm volatile("gm_code_begin:" :: "s"(ws), "s"(wsp), "s"(bsp), "s"(out));
    extern __shared__ __attribute__((aligned(16))) char smem[];
    LAS char* lds = (LAS char*)smem;
    const char *twin = ws + OFF_TWIN, *twout = ws + OFF_TWOUT;
    STAMP_DECL;
    {
        const unsigned long long q0 = __builtin_amdgcn_s_memrealtime();
        *(unsigned long long*)(ws + (200u << 20) + blockIdx.x * 8) = q0;
#pragma unroll 1
        for (int u = blockIdx.x; u < P_TOTAL; u += 256) { prep_body(x, wqkv, wo, w_in, w_out, ws, lds, u); __syncthreads(); }
        __builtin_amdgcn_fence(__ATOMIC_RELEASE, "agent"); WAIT_V(0);
        while (__builtin_amdgcn_s_memrealtime() - q0 < 15000ull) __builtin_amdgcn_s_sleep(1);
        __builtin_amdgcn_fence(__ATOMIC_ACQUIRE, "agent"); WAIT_V(0);
        __syncthreads();
    }
    const int wid8 = __builtin_amdgcn_readfirstlane(threadIdx.x >> 6);
    if (wid8 >= 4) {
        lds_word_set(lds + G_WORD + (threadIdx.x & 15) * 4, 0u);
        WAIT_L0();
        u32x4 wr[5][9];
#pragma unroll 1
      for (int layer = 0; layer < NET_LAYERS; ++layer) {
        NET_IDS
        char* wl = ws + OFF_L0 + layer * SZ_L;
        const unsigned fl = layer * F_LAYER;
        const char *xin = wl + L_XB, *st = wl + L_STB, *z2t = wl + (layer ? L_Y : L_Z2T);
        unsigned long long* zst = (unsigned long long*)(ws + OFF_ZST + layer * SZ_ZST);
        const int lane = lane_id_fresh(), l3 = lane >> 3;
        const unsigned vo = lane * 16;
        const auto rX = RSRC(xin + (size_t)(pm * 8 + 2 * wid) * 18432), rI = RSRC(twin + (size_t)cg * (18 * 18432));
        auto issue1B = [&](int g, int slot) {
            const int k = kord(g < 9 ? g : (g < 13 ? g - 4 : g - 13), 9), ob = (g < 9 ? 9 + k : k) * 18432, o2 = ob + wid * 4096;
            LAS char* sl = lds + slot * G_STAGE + 16384;
            if (wid < 2) BLDS(rI, sl + 16384 + wid * 1024, vo, ob + 16384 + wid * 1024, 0);
            sl += wid * 4096;
            BLDS(rI, sl, vo, o2, 0); BLDS(rI, sl, vo, o2, 1024); BLDS(rI, sl, vo, o2, 2048); BLDS(rI, sl, vo, o2, 3072);
        };
        auto issue1A = [&](int g, int slot) {
            const int k = kord(g < 9 ? g : g - 13, 9);
            LAS char* sl = lds + slot * G_STAGE + wid * 4096;
            BLDS1(rX, sl, vo, k * 2048, 0); BLDS1(rX, sl, vo, k * 2048, 1024);
            BLDS1(rX, sl + 2048, vo, k * 2048 + 18432, 0); BLDS1(rX, sl + 2048, vo, k * 2048 + 18432, 1024);
        };
        auto issue1 = [&](int g, int slot) { issue1B(g, slot); issue1A(g, slot); };
        const auto rA = RSRC((const char*)wsp + (size_t)(pm * 128 + wid * 32) * 4096), rB = RSRC(z2t + cg * 36864);
        const unsigned voA = (unsigned)(2 * l3 * 4096 + ((lane & 7) ^ l3) * 16);
        auto issueA = [&](int k, int slot) {
            LAS char* la = lds + slot * G_STAGE + wid * 4096;
            BLDS(rA, la, voA, k * 128, 0);
            BLDS(rA, la + 1024, voA, k * 128 + 16 * 4096, 0);
            BLDS(rA, la + 2048, voA, k * 128 + 4096, 0);
            BLDS(rA, la + 3072, voA, k * 128 + 17 * 4096, 0);
        };
        auto issueB = [&](int k, int slot) {
            LAS char* lb = lds + slot * G_STAGE + 16384;
            const int ok = (k >> 1) * 589824 + (k & 1) * 18432, ob = ok + wid * 4096;
            if (wid < 2) BLDS1(rB, lb + 16384 + wid * 1024, vo, ok + 16384 + wid * 1024, 0);
            lb += wid * 4096;
            BLDS1(rB, lb, vo, ob, 0); BLDS1(rB, lb, vo, ob, 1024); BLDS1(rB, lb, vo, ob, 2048); BLDS1(rB, lb, vo, ob, 3072);
        };
        const auto rW = RSRC(twout + (size_t)c5 * (72 * 4608));
        auto issue5 = [&](int t) {
            const int ow = kord5(t) * K5_STAGE;
            LAS char* sl = lds + ((t + 6) & 7) * K5_STAGE;
            if (wid < 2) BLDS(rW, sl + 16384 + wid * 1024, vo, ow + 16384 + wid * 1024, 0);
            BLDS(rW, sl + wid * 4096, vo, ow + wid * 4096, 0); BLDS(rW, sl + wid * 4096, vo, ow + wid * 4096, 1024);
            BLDS(rW, sl + wid * 4096, vo, ow + wid * 4096, 2048); BLDS(rW, sl + wid * 4096, vo, ow + wid * 4096, 3072);
        };
        auto load5 = [&](int p, int i) {
            const int t0 = p ? 2 * p + 6 : 2, ow = kord5(t0 + i) * K5_STAGE + wid * 4096;
            asm volatile("s_nop 4\n\tbuffer_load_dwordx4 %0, %4, %5, %6 offen\n\tbuffer_load_dwordx4 %1, %4, %5, %6 offen offset:1024\n\t"
                         "buffer_load_dwordx4 %2, %4, %5, %6 offen offset:2048\n\tbuffer_load_dwordx4 %3, %4, %5, %6 offen offset:3072"
                         : "+v"(wr[p][4 * i]), "+v"(wr[p][4 * i + 1]), "+v"(wr[p][4 * i + 2]), "+v"(wr[p][4 * i + 3]) : "v"(vo), "s"(rW), "s"(ow) : "memory");
            if (i) {
                const int oe = kord5(t0 + (wid >> 1)) * K5_STAGE + 16384 + (wid & 1) * 1024;
                asm volatile("s_nop 4\n\tbuffer_load_dwordx4 %0, %1, %2, %3 offen" : "+v"(wr[p][8]) : "v"(vo), "s"(rW), "s"(oe) : "memory");
            }
        };
        auto put5 = [&](int w0, int w1) {
#pragma unroll
            for (int w = w0; w < w1; ++w) {
                const int p = w / 9, i = w % 9 - 1;
                LAS char* sl = lds + (((p ? 2 * p + 6 : 2) + 6) & 7) * K5_STAGE + lane * 16;
                if (i < 0) *(LAS u32x4*)(sl + (wid >> 1) * K5_STAGE + 16384 + (wid & 1) * 1024) = wr[p][8];
                else *(LAS u32x4*)(sl + (i >> 2) * K5_STAGE + wid * 4096 + (i & 3) * 1024) = wr[p][i];
            }
            WAIT_L0();
        };
        const int row = wid * 32 + (lane & 31);
        auto step1 = [&](int t) { BAR(); issue1(t + 4, t & 3); };
        attn_producer(lds, ws, wl, fl, layer, pa, hp, wid, [&] { issue1B(0, 0); issue1B(1, 1); });
        __builtin_amdgcn_s_setprio(3);
        {
            const unsigned fo = flag_out(fl + F_X1, 256, pa, hp, lane), fi = flag_in(fl + F_X1, 256, pm * 16 + cg, 16, lane);
            BARX();
            if (wid == 0) flag_raise(ws, fo);
            issue1B(2, 2); issue1B(3, 3);
            asm volatile("" :: "s"(rX));
            if (wid == 0) flags_wait<X1_FIRST>(ws, fi);
            BARX();
        }
        issue1A(0, 0); issue1A(1, 1); issue1A(2, 2);
        WAIT_V(8); BAR();
        __builtin_amdgcn_s_setprio(0);
        issue1A(3, 3);
        WAIT_V(8); step1(0);
        WAIT_V(12); step1(1);
        {
            f32x4 sp[3];
            const char* ps = st + (size_t)(pm * 128 + wid * 32) * 64 + lane * 16;
            side_load(sp, ps, ps);
            WAIT_V(19); step1(2);
            WAIT_V(19); step1(3);
            WAIT_V(16); step1(4);
            WAIT_V(16); BAR(); if (layer == 0) load5(3, 0); issue1B(9, 1);
            __builtin_amdgcn_s_setprio(3);
            side_fence(sp);
#pragma unroll
            for (int i = 0; i < 2; ++i) lds_word_set(lds + G_TAB + (wid * 32 + i * 16 + (lane >> 2)) * 4, __float_as_uint(ln_rstd(sp[i])));
            WAIT_L0();
            __builtin_amdgcn_s_setprio(0);
        }
        WAIT_V(12); BAR(); if (layer == 0) load5(3, 1); issue1B(10, 2);
        WAIT_V(8); BAR(); if (layer == 0) load5(4, 0); issue1B(11, 3);
        WAIT_V(8); BAR(); if (layer == 0) load5(4, 1); issue1B(12, 0);
        if (layer == 1) {
            const auto rd = RSRC(out + (size_t)b * 9216 + wid * 2304);
            ST16(rd, vo, 0, ((u32x4){0u, 0u, 0u, 0u}), 16); ST16(rd, vo, 1024, ((u32x4){0u, 0u, 0u, 0u}), 16);
            if (lane < 16) ST16(rd, vo, 2048, ((u32x4){0u, 0u, 0u, 0u}), 16);
        }
        if (layer == 0) {
#pragma unroll
            for (int p = 0; p < 6; ++p) load5(p >> 1, p & 1);
        }
        WAIT_V(0);
        __builtin_amdgcn_s_setprio(3);
        BAR();
        if (lane < 32) {
            const f32x2 a = *(const LAS f32x2*)(lds + G_PART + row * 8), bq = *(const LAS f32x2*)(lds + G_PART + 1024 + row * 8);
            unsigned lo = __float_as_uint(a.x + bq.x), hi = __float_as_uint(a.y + bq.y);
            if (hi == 0xffffffffu) hi = 0x7fc00000u;
            __hip_atomic_store(zst + (size_t)(pm * 16 + cg) * 128 + row, ((unsigned long long)hi << 32) | lo, __ATOMIC_RELAXED, __HIP_MEMORY_SCOPE_AGENT);
        }
        step1(9); step1(10); step1(11);
        {
            const unsigned long long* src = zst + (size_t)(pm * 16 + (lane >> 5) * 8) * 128 + row + 512;
            u32x2 g[8];
            auto gather = [&] {
                asm volatile("global_load_dwordx2 %0, %8, off offset:-4096 sc1\n\tglobal_load_dwordx2 %1, %8, off offset:-3072 sc1\n\t"
                             "global_load_dwordx2 %2, %8, off offset:-2048 sc1\n\tglobal_load_dwordx2 %3, %8, off offset:-1024 sc1\n\t"
                             "global_load_dwordx2 %4, %8, off sc1\n\tglobal_load_dwordx2 %5, %8, off offset:1024 sc1\n\t"
                             "global_load_dwordx2 %6, %8, off offset:2048 sc1\n\tglobal_load_dwordx2 %7, %8, off offset:3072 sc1"
                             : "=&v"(g[0]), "=&v"(g[1]), "=&v"(g[2]), "=&v"(g[3]), "=&v"(g[4]), "=&v"(g[5]), "=&v"(g[6]), "=&v"(g[7]) : "v"(src) : "memory");
            };
            gather();
            WAIT_V(24); step1(12);
            WAIT_V(24); step1(13);
            WAIT_V(16);
            float s1, s2;
            for (unsigned spin = 0;; ++spin) {
                asm volatile("" : "+v"(g[0]), "+v"(g[1]), "+v"(g[2]), "+v"(g[3]), "+v"(g[4]), "+v"(g[5]), "+v"(g[6]), "+v"(g[7]));
                bool ok = true; s1 = 0.f; s2 = 0.f;
#pragma unroll
                for (int i = 0; i < 8; ++i) { ok &= g[i].y != 0xffffffffu; s1 += __uint_as_float(g[i].x); s2 += __uint_as_float(g[i].y); }
                if (__all(ok) || spin > 400000u) break;
                gather();
                WAIT_V(0);
            }
            auto x1 = __builtin_amdgcn_permlane32_swap(__float_as_uint(s1), __float_as_uint(s1), false, false);
            auto x2 = __builtin_amdgcn_permlane32_swap(__float_as_uint(s2), __float_as_uint(s2), false, false);
            const float mean = (__uint_as_float(x1[0]) + __uint_as_float(x1[1])) * (1.0f / 2304.0f);
            const float var = (__uint_as_float(x2[0]) + __uint_as_float(x2[1])) * (1.0f / 2304.0f) - mean * mean;
            const float rs = __builtin_amdgcn_rsqf(fmax2(var, 0.f) + 1e-5f);
            asm volatile("ds_write_b32 %0, %1" :: "v"((unsigned)(size_t)(lds + G_TAB + 512 + (lane >> 5) * 512 + row * 4)), "v"(lane < 32 ? rs : -mean * rs) : "memory");
            WAIT_L0();
        }
        __builtin_amdgcn_s_setprio(0);
        BAR(); issueA(kord(0, 32), 2);
        WAIT_V(12); BAR(); issue5(0); issue5(1);
        WAIT_V(12); BAR(); issueA(kord(1, 32), 0);
        __builtin_amdgcn_s_setprio(3);
        const unsigned fz = flag_out(fl + F_Z2, 256, cg, pm, lane);
        BARX();
        if (wid == 0) flag_raise(ws, fz);
        issueA(kord(2, 32), 1);
        if (wid == 0) { flags_wait<Z2_FIRST>(ws, flag_in(fl + F_Z2, 256, cg * 16 + pm, 2, lane)); lds_word_set(lds + G_WORD + 8 + layer * 8, 1u); }
        else lds_word_wait(lds + G_WORD + 8 + layer * 8);
        issueB(kord(0, 32), 2); issueB(kord(1, 32), 0); issueB(kord(2, 32), 1);
        WAIT_V(8); BAR();
        if (wid == 0) flags_wait<0>(ws, flag_in(fl + F_Z2, 256, cg * 16 + pm, 16, lane));
        __builtin_amdgcn_s_setprio(0);
        auto issue = [&](int t, int sl) { const int k = kord(t, 32); issueB(k, sl); issueA(k, sl); };
        WAIT_V(4); BAR(); issue(3, 2);
        ring_steps<1, 29, 8, 2>(issue);
        WAIT_V(8); BAR(); issue5(4);
        WAIT_V(4); BAR(); issue5(5); issue5(6);
        BAR(); issue5(7); put5(0, 9);
        {
            const unsigned fo = flag_out(fl + F_Y, 256, pm, cg, lane), fi = flag_in(fl + F_Y, 256, rh * 8 + c5, 16, lane);
            __builtin_amdgcn_s_setprio(3);
            WAIT_V(0); BARX();
            if (wid == 0) { flag_raise(ws, fo); flags_wait<Y_FIRST>(ws, fi); }
            BARX();
            __builtin_amdgcn_s_setprio(0);
        }
        BAR(); put5(9, 16);
        BAR(); put5(16, 23);
        BAR(); put5(23, 30);
        BAR(); put5(30, 36);
        BAR(); issue5(16); put5(36, 41);
        BAR(); issue5(17); put5(41, 45);
        if (wid < 2) WAIT_V(5); else WAIT_V(4);
        BAR();
        WAIT_V(0); BAR();
        if (layer == 0) { attn_issueB(lds, ws, hp, wid, lane, 0, 0); attn_issueB(lds, ws, hp, wid, lane, 1, 1); }
        BAR();
        if (layer == 0) {
            __builtin_amdgcn_s_setprio(3);
            const unsigned fo = flag_out(F_X2, 512, (rh >> 1) + wid, wid ? 16 + c5 : (rh & 1) * 8 + c5, wid == 0 || (wid == 1 && (rh & 1) && rh < 31) ? lane : 16);
            BARX();
            if (wid < 2) flag_raise(ws, fo);
        }
      }
        return;
    }
#pragma unroll 1
  for (int layer = 0; layer < NET_LAYERS; ++layer) {
    NET_IDS
    char* wl = ws + OFF_L0 + layer * SZ_L;
    const unsigned fl = layer * F_LAYER;
    const char* xin = wl + L_XB;
    char* y = wl + (layer ? L_Z2T : L_Y);
#undef STAMP_SEC
#define STAMP_SEC 0
    {
    STAMP_BEGIN;
    attn_consumer(lds, wl, layer, pa, hp, wid STAMP_FWD);
    STAMP_END(1 + 5 * layer);
    }
#undef STAMP_SEC
#define STAMP_SEC 1
    STAMP_BEGIN;
    gmlp_body(lds, wid & 1, wid >> 1, pm, cg, wl + (layer ? L_Y : L_Z2T), y, bsp STAMP_FWD);
    {
        WAIT_V(0);
        BARX();
        STAMP(11);
        const int lane = lane_id_fresh(), r = lane & 15, q = lane >> 4;
        const int wk = wid >> 1, rt = rh * 4 + (wid & 1) * 2 + wk;
        const Res rx = proj_residual_load(xin, rt, c5, lane);
        const LAS char *pw[3], *ph[3];
#pragma unroll
        for (int i = 0; i < 3; ++i) {
            pw[i] = opaque(lds + 3 * i * K5_STAGE + wk * 4608 + lane * 16);
            ph[i] = opaque(lds + 3 * i * K5_STAGE + wk * 4608 + 4096 + q * 128 + (r & 7) * 16);
        }
        const auto rY = RSRC(y + (size_t)(rt & ~1) * 73728 + wk * 1024);
        const unsigned vy[2] = {pin((unsigned)(wk * 73728 + lane * 16)), pin((unsigned)((1 - wk) * 73728 + lane * 16))};
        int dk[4];
#pragma unroll
        for (int i = 0; i < 4; ++i) dk[i] = pin_s((kord5(4 + i) - 4 - i) * 4096);
        auto yoff = [&](int t) { return pin_s(t == 0 || t == 17 ? kord5(t) * 4096 : t * 4096 + dk[t & 3]); };
        bf16x8 af[5][4], wf[4][5];
        auto lda = [&](int so, int i) { return __builtin_bit_cast(bf16x8, __builtin_amdgcn_raw_buffer_load_b128(rY, vy[i & 1] + (i >> 1) * 2048, so, 16)); };
        auto rdw = [&](int t, int c, int n) {
            const int sl = (t + 6) & 7, o = (sl % 3) * K5_STAGE + c * 9216;
            wf[2 * (t & 1) + c][n] = n < 4 ? LDS_FRAG(pw[sl / 3] + o + n * 1024) : LDS_FRAG(ph[sl / 3] + o);
        };
#pragma unroll
        for (int n = 0; n < 10; ++n) rdw(0, n / 5, n % 5);
        int so[18];
#pragma unroll
        for (int i = 0; i < 5; ++i) so[i] = yoff(i);
        so[17] = yoff(17);
        f32x4 v[5], u[5];
        proj_residual(v, rx);
#pragma unroll
        for (int n = 0; n < 5; ++n) v[n] = pin(v[n]);
        LAS char* xo = (LAS char*)opaque(lds + wid * 4608 + lane * 16);
        LAS char* xo4 = (LAS char*)opaque(lds + wid * 4608 + 4096 + (lane & 31) * 16);
        const LAS char* xi = opaque(lds + (wid ^ 2) * 4608 + lane * 16);
        const LAS char* xi4 = opaque(lds + (wid ^ 2) * 4608 + 4096 + (lane & 31) * 16);
        const ProjOut po = layer ? proj_out_final(out, rt, c5, lane) : proj_out(wl + SZ_L + L_XA, wl + SZ_L + L_STA, rt, c5, lane);
        asm volatile("" :: "s"(rY));
        BARX();
        STAMP(12);
#pragma unroll
        for (int i = 0; i < K5_PRE; ++i) { af[i >> 2][i & 3] = lda(so[i >> 2], i & 3); SCHED0(); }
        static_for<18>([&](auto tc) {
            constexpr int t = tc.value;
            bf16x8 (&a)[4] = af[t % 5];
#pragma unroll
            for (int h = 0; h < 4; ++h) {
                const int c = t < 17 ? h >> 1 : h & 1, m = t < 17 ? h & 1 : 1 - (h >> 1);
                if (t < 17 ? m == 0 : h < 2) { if (t == 17 && h == 1) WAIT_L(0); else WAIT_L(5); }
#pragma unroll
                for (int n = 0; n < 5; ++n) {
                    f32x4& e = m ? u[n] : v[n];
                    if (t == 0 && h == 1) MFMA_0(wf[0][n], a[1], e); else MFMA_T(wf[2 * (t & 1) + c][n], a[2 * c + m], e);
                    SCHED0();
                    if (t < 17 && m == 0) rdw(t + 1, c, n);
                    if (t < 14 && m == 1 && !(n & 1)) { const int i = y_load_at(6 * t + 3 * c + n / 2); if (i >= 0) af[(i >> 2) % 5][i & 3] = lda(so[i >> 2], i & 3); }
                    if (t < 12 && h == 3 && n == 3) so[t + 5] = yoff(t + 5);
                    if (t == 17 && h * 5 + n >= 13 && h * 5 + n < 18) { const int i = h * 5 + n - 13; *(LAS f32x4*)(i < 4 ? xo + i * 1024 : xo4) = u[i]; }
                    SCHED0();
                }
            }
            if ((t & 1) && t < 17) BAR();
            SCHED0();
        });
        {
            WAIT_L0(); BARX();
#pragma unroll
            for (int n = 0; n < 5; ++n) u[n] = *(const LAS f32x4*)(n < 4 ? xi + n * 1024 : xi4);
            SCHED0();
            asm volatile("s_nop 1" : "+v"(v[0]), "+v"(v[1]), "+v"(v[2]), "+v"(v[3]), "+v"(v[4]));
#pragma unroll
            for (int n = 0; n < 5; ++n) v[n] += u[n];
        }
        STAMP(13);
        if (layer) proj_store<true>(v, po);
        else { proj_store(v, po); WAIT_V(0); BARX(); }
    }
    STAMP_END(3 + 5 * layer);
  }
  { if (PROBE_A < 0) st_[0] = *(const unsigned long long*)(ws + (200u << 20) + blockIdx.x * 8);
    while (__builtin_amdgcn_s_memrealtime() - st_[0] < 30000ull) __builtin_amdgcn_s_sleep(1); }
}

}

#define STOP_CHECK(n)

extern "C" void kernel_launch(void* const* d_in, const int* in_sizes, int n_in, void* d_out, int out_size, void* d_ws, size_t ws_size,
                              hipStream_t stream) {
    (void)in_sizes; (void)n_in; (void)out_size; (void)ws_size;
    const u16* x = (const u16*)d_in[0];
    const u16* wqkv = (const u16*)d_in[1];
    const u16* wo = (const u16*)d_in[2];
    const u16* w_in = (const u16*)d_in[4];
    const u16* w_sp = (const u16*)d_in[5];
    const u16* b_sp = (const u16*)d_in[6];
    const u16* w_out = (const u16*)d_in[7];
    char* ws = (char*)d_ws;
    (void)hipFuncSetAttribute((const void*)k_net, hipFuncAttributeMaxDynamicSharedMemorySize, NET_LDS);
    STOP_CHECK(1);
    k_net<<<256, 512, NET_LDS, stream>>>(ws, w_sp, b_sp, (char*)d_out, x, wqkv, wo, w_in, w_out);
}
```

```cpp
#include <hip/hip_runtime.h>
#include <stdint.h>

namespace {

typedef unsigned short u16;
typedef short bf16x8 __attribute__((ext_vector_type(8)));
typedef float f32x4 __attribute__((ext_vector_type(4)));
typedef float f32x2 __attribute__((ext_vector_type(2)));
typedef unsigned u32x4 __attribute__((ext_vector_type(4)));
typedef unsigned u32x2 __attribute__((ext_vector_type(2)));
typedef __bf16 bf16x2_t __attribute__((ext_vector_type(2)));
#define LAS __attribute__((address_space(3)))
#define DEVI __device__ __forceinline__

constexpr int S_ = 2048, D_ = 576, FH = 2304;

constexpr size_t SZ_TWQKV = (size_t)16 * 18 * 7 * 1024;
constexpr size_t SZ_TWO = (size_t)8 * 18 * 4608;
constexpr size_t SZ_TWIN = (size_t)16 * 18 * 18 * 1024;
constexpr size_t SZ_TWOUT = (size_t)8 * 72 * 4608;
constexpr size_t SZ_X = (size_t)S_ * D_ * 2;
constexpr size_t SZ_Z = (size_t)S_ * FH * 2;
constexpr size_t SZ_ST = (size_t)S_ * 64;
constexpr size_t SZ_ZST = (size_t)16 * 16 * 128 * 8;
constexpr size_t OFF_TWQKV = 0;
constexpr size_t OFF_TWO = OFF_TWQKV + SZ_TWQKV;
constexpr size_t OFF_TWIN = OFF_TWO + SZ_TWO;
constexpr size_t OFF_TWOUT = OFF_TWIN + SZ_TWIN;
constexpr size_t OFF_CNT = OFF_TWOUT + SZ_TWOUT;
constexpr size_t SZ_CNT = 10 * 65536;
constexpr size_t OFF_ZST = OFF_CNT + SZ_CNT;
constexpr size_t OFF_L0 = OFF_ZST + 2 * SZ_ZST;
constexpr size_t L_XA = 0, L_STA = L_XA + SZ_X, L_O = L_STA + SZ_ST, L_XB = L_O + SZ_X, L_STB = L_XB + SZ_X, L_Z2T = L_STB + SZ_ST, L_Y = L_Z2T + SZ_Z;
constexpr size_t SZ_L = 0;
static_assert(OFF_L0 + L_Y + SZ_Z <= ((size_t)256 << 20), "workspace");
constexpr unsigned F_O = OFF_CNT, F_X1 = F_O + 65536, F_Z2 = F_O + 131072, F_Y = F_O + 196608, F_LAYER = 262144, F_X2 = F_O + 2 * F_LAYER;

DEVI unsigned pack2(float a, float b) { f32x2 v = {a, b}; bf16x2_t r = __builtin_convertvector(v, bf16x2_t); return __builtin_bit_cast(unsigned, r); }
DEVI float bflo(unsigned u) { return __uint_as_float(u << 16); }
DEVI float bfhi(unsigned u) { return __uint_as_float(u & 0xffff0000u); }
DEVI int slot2row(int sg) { return (sg & ~31) + 2 * (sg & 15) + ((sg >> 4) & 1); }
DEVI float fmax2(float a, float b) { return __builtin_amdgcn_fmed3f(a, b, __builtin_inff()); }
#define XQ4(OP, MID) \
    asm volatile("s_nop 1\n\tv_permlane32_swap_b32 %0, %1\n\tv_permlane32_swap_b32 %2, %3\n\t" OP " %0, %0, %1\n\t" OP " %2, %2, %3\n\t" \
                 "s_nop 1\n\tv_permlane16_swap_b32 %0, %2\n\t" OP " %0, %0, %2\n\t" MID \
                 "v_mov_b32 %2, %0\n\ts_nop 1\n\tv_permlane16_swap_b32 %0, %2\n\tv_mov_b32 %1, %0\n\tv_mov_b32 %3, %2\n\ts_nop 0\n\t" \
                 "v_permlane32_swap_b32 %0, %1\n\tv_permlane32_swap_b32 %2, %3" : "+v"(x[0]), "+v"(x[1]), "+v"(x[2]), "+v"(x[3]))
DEVI void xq4_max(float (&x)[4]) { XQ4("v_max_f32", ""); }
DEVI void xq4_sum(float (&x)[4]) { XQ4("v_add_f32", ""); }
DEVI void xq4_sum_rcp(float (&x)[4]) { XQ4("v_add_f32", "v_rcp_f32 %0, %0\n\ts_nop 0\n\t"); }
template <class T> DEVI T pin(T v) { asm volatile("" : "+v"(v)); return v; }
template <int V> struct IC { static constexpr int value = V; };
template <int N, int I = 0, class F> DEVI void static_for(F&& f) { if constexpr (I < N) { f(IC<I>{}); static_for<N, I + 1>(f); } }
DEVI int pin_s(int v) { asm volatile("" : "+s"(v)); return v; }
DEVI float xq_sum(float v) {
    auto a = __builtin_amdgcn_permlane32_swap(__float_as_uint(v), __float_as_uint(v), false, false);
    v = __uint_as_float(a[0]) + __uint_as_float(a[1]);
    auto b = __builtin_amdgcn_permlane16_swap(__float_as_uint(v), __float_as_uint(v), false, false);
    return __uint_as_float(b[0]) + __uint_as_float(b[1]);
}
#define BLDS(rs, l, vo, so, off) __builtin_amdgcn_raw_ptr_buffer_load_lds((rs), (LAS void*)(l), 16, (vo), (so), (off), 0)
#define BLDS1(rs, l, vo, so, off) __builtin_amdgcn_raw_ptr_buffer_load_lds((rs), (LAS void*)(l), 16, (vo), (so), (off), 16)
#define RSRC(p) __builtin_amdgcn_make_buffer_rsrc((void*)(p), 0, 0x7fffffff, 0x00020000)
#define ST16(rs, vo, so, v, aux) __builtin_amdgcn_raw_buffer_store_b128((v), (rs), (vo) + (so), 0, aux)
#define ST8(rs, vo, so, v, aux) __builtin_amdgcn_raw_buffer_store_b64((v), (rs), (vo), (so), aux)
#define WAIT_V(n) asm volatile("s_waitcnt vmcnt(%0)" ::"i"(n) : "memory")
#define WAIT_L0() asm volatile("s_waitcnt lgkmcnt(0)" ::: "memory")
#define BAR() do { asm volatile("" ::: "memory"); __builtin_amdgcn_s_barrier(); asm volatile("" ::: "memory"); } while (0)
#define BARX() do { __builtin_amdgcn_sched_barrier(0); BAR(); __builtin_amdgcn_sched_barrier(0); } while (0)
#define LOOP_ALIGN() asm volatile(".p2align 6")
#define SCHED0() __builtin_amdgcn_sched_barrier(0)
#define MFMA(a, b, c) __builtin_amdgcn_mfma_f32_16x16x32_bf16((a), (b), (c), 0, 0, 0)
#define MFMA_T(a, b, c) asm volatile("v_mfma_f32_16x16x32_bf16 %0, %1, %2, %0" : "+v"(c) : "v"(a), "v"(b))
#define MFMA_0(a, b, c) asm volatile("v_mfma_f32_16x16x32_bf16 %0, %1, %2, 0" : "=&v"(c) : "v"(a), "v"(b))
#define WAIT_L(n) __builtin_amdgcn_s_waitcnt(0xC07F | ((n) << 8))
#define LDS_FRAG(p) (*(const LAS bf16x8*)(p))
DEVI int lane_id_fresh() { int z = 0; asm volatile("" : "+v"(z)); return __builtin_amdgcn_mbcnt_hi(~0u, __builtin_amdgcn_mbcnt_lo(~0u, z)); }
DEVI const LAS char* opaque(const LAS char* p) { asm volatile("" : "+v"(p)); return p; }
#define DBG_ARG
#define DBG_LAUNCH(n)
#define STAMP(i)
#define STAMP_DECL
#define STAMP_BEGIN
#define STAMP_END(n)
#define STAMP_PARAM
#define STAMP_FWD

#define TRACE_ATTR
#define TRACE_BEGIN
#define TRACE_DUMP

DEVI float add1(float a, float b) { float r; asm("v_add_f32 %0, %1, %2" : "=v"(r) : "v"(a), "v"(b)); return r; }
DEVI float rstd_of(float s, float q, float inv_n, float& mean) {
    float var;
    asm("v_mul_f32 %0, %2, %3\n\tv_mul_f32 %1, %2, %4\n\tv_fma_f32 %1, -%0, %0, %1" : "=&v"(mean), "=&v"(var) : "s"(inv_n), "v"(s), "v"(q));
    return __builtin_amdgcn_rsqf(fmax2(var, 0.f) + 1e-5f);
}
DEVI float ln_rstd(const f32x4 p) {
    float s, q, mean;
    asm("v_add_f32 %0, %2, %4\n\tv_add_f32 %1, %3, %5\n\ts_nop 0\n\t"
        "v_add_f32_dpp %0, %0, %0 quad_perm:[1,0,3,2] row_mask:0xf bank_mask:0xf\n\tv_add_f32_dpp %1, %1, %1 quad_perm:[1,0,3,2] row_mask:0xf bank_mask:0xf\n\ts_nop 0\n\t"
        "v_add_f32_dpp %0, %0, %0 quad_perm:[2,3,0,1] row_mask:0xf bank_mask:0xf\n\tv_add_f32_dpp %1, %1, %1 quad_perm:[2,3,0,1] row_mask:0xf bank_mask:0xf"
        : "=&v"(s), "=&v"(q) : "v"(p[0]), "v"(p[1]), "v"(p[2]), "v"(p[3]));
    return rstd_of(s, q, 1.0f / 576.0f, mean);
}

struct Res { u32x4 a, c; u32x2 e; };
DEVI Res proj_residual_load(const char* xin, int rt, int cg, int lane) {
    const auto rs = RSRC(xin + (size_t)rt * 18432 + cg * 2304);
    Res x;
    x.a = __builtin_amdgcn_raw_buffer_load_b128(rs, lane * 16, 0, 16); x.c = __builtin_amdgcn_raw_buffer_load_b128(rs, lane * 16 + 1024, 0, 16);
    x.e = __builtin_amdgcn_raw_buffer_load_b64(rs, 2048 + (lane & 15) * 16 + ((lane >> 4) & 1) * 8, 0, 16);
    return x;
}
DEVI void proj_residual(f32x4 (&v)[5], const Res& x) {
    v[0] = (f32x4){bflo(x.a.x), bfhi(x.a.x), bflo(x.a.y), bfhi(x.a.y)}; v[1] = (f32x4){bflo(x.a.z), bfhi(x.a.z), bflo(x.a.w), bfhi(x.a.w)};
    v[2] = (f32x4){bflo(x.c.x), bfhi(x.c.x), bflo(x.c.y), bfhi(x.c.y)}; v[3] = (f32x4){bflo(x.c.z), bfhi(x.c.z), bflo(x.c.w), bfhi(x.c.w)};
    v[4] = (f32x4){bflo(x.e.x), bfhi(x.e.x), bflo(x.e.y), bfhi(x.e.y)};
}
struct ProjOut { __amdgpu_buffer_rsrc_t x, st; unsigned v0, v1, v2, vs; };
DEVI ProjOut proj_out(char* xout, char* stout, int rt, int cg, int lane) {
    const int r = lane & 15, q = lane >> 4;
    ProjOut p = {RSRC(xout + (size_t)rt * 18432 + cg * 2304), RSRC(stout + (size_t)rt * 1024 + cg * 8),
                 pin((unsigned)lane * 16u), pin((unsigned)lane * 16u + 1024u), pin((unsigned)(2048 + r * 16 + (q & 1) * 8)), pin((unsigned)(r * 64 + (q >> 1) * 4))};
    asm volatile("" :: "s"(p.x), "s"(p.st));
    return p;
}
DEVI ProjOut proj_out_final(char* out, int rt, int cg, int lane) {
    const int q = lane >> 4;
    const unsigned o = (unsigned)(slot2row(rt * 16 + (lane & 15)) * 1152 + cg * 144);
    ProjOut p = {RSRC(out), RSRC(out), pin(o + q * 16), pin(o + 64 + q * 16), pin(o + 128 + (q & 1) * 8), 0u};
    return p;
}
template <bool FINAL = false>
DEVI void proj_store(f32x4 (&v)[5], const ProjOut& p) {
    ST16(p.x, p.v0, 0, ((u32x4){pack2(v[0][0], v[0][1]), pack2(v[0][2], v[0][3]), pack2(v[1][0], v[1][1]), pack2(v[1][2], v[1][3])}), FINAL ? 0 : 16);
    SCHED0();
    asm volatile("" : "+v"(v[2]), "+v"(v[3]), "+v"(v[4]));
    ST16(p.x, p.v1, 0, ((u32x4){pack2(v[2][0], v[2][1]), pack2(v[2][2], v[2][3]), pack2(v[3][0], v[3][1]), pack2(v[3][2], v[3][3])}), FINAL ? 0 : 16);
    SCHED0();
    ST8(p.x, p.v2, 0, ((u32x2){pack2(v[4][0], v[4][1]), pack2(v[4][2], v[4][3])}), FINAL ? 0 : 16);
    SCHED0();
    if (!FINAL) {
        const f32x4 s4 = ((v[0] + v[1]) + (v[2] + v[3])) + v[4] * 0.5f;
        const f32x4 q4 = ((v[0] * v[0] + v[1] * v[1]) + (v[2] * v[2] + v[3] * v[3])) + v[4] * v[4] * 0.5f;
        auto x = __builtin_amdgcn_permlane32_swap(__float_as_uint((s4[0] + s4[2]) + (s4[1] + s4[3])), __float_as_uint((q4[0] + q4[2]) + (q4[1] + q4[3])), false, false);
        const float t = __uint_as_float(x[0]) + __uint_as_float(x[1]);
        auto y = __builtin_amdgcn_permlane16_swap(__float_as_uint(t), __float_as_uint(t), false, false);
        __builtin_amdgcn_raw_buffer_store_b32(__float_as_uint(__uint_as_float(y[0]) + __uint_as_float(y[1])), p.st, p.vs, 0, 16);
        SCHED0();
    }
}

DEVI void side_load(f32x4 (&d)[3], const char* p, const char* p2) {
    asm volatile("global_load_dwordx4 %0, %3, off sc1\n\tglobal_load_dwordx4 %1, %3, off offset:1024 sc1\n\tglobal_load_dwordx4 %2, %4, off sc1"
                 : "=&v"(d[0]), "=&v"(d[1]), "=&v"(d[2]) : "v"(p), "v"(p2) : "memory");
}
DEVI void side_fence(f32x4 (&d)[3]) { asm volatile("" : "+v"(d[0]), "+v"(d[1]), "+v"(d[2])); }
DEVI void lds_word_set(LAS char* p, unsigned v) { asm volatile("ds_write_b32 %0, %1" :: "v"((unsigned)(size_t)p), "v"(v) : "memory"); }
#define DMA_I(v, off, sc) "buffer_load_dwordx4 " v ", %1, %2 offen offset:" off sc " lds\n\t"
#define DMA5(sc) asm volatile("s_mov_b32 m0, %0\n\ts_mov_b32 exec_hi, 0\n\ts_nop 2\n\t" DMA_I("%3", "0", sc) "s_mov_b32 exec_hi, -1\n\t" \
    DMA_I("%3", "512", sc) DMA_I("%3", "1536", sc) DMA_I("%3", "2560", sc) DMA_I("%3", "3584", sc) :: "s"(la), "s"(rs), "s"(so), "v"(vo) : "m0", "memory")
#define DMA4(sc) asm volatile("s_mov_b32 m0, %0\n\ts_nop 3\n\t" DMA_I("%3", "0", sc) DMA_I("%4", "1024", sc) DMA_I("%5", "2048", sc) DMA_I("%6", "3072", sc) \
    :: "s"(la), "s"(rs), "s"(so), "v"(v0), "v"(v1), "v"(v2), "v"(v3) : "m0", "memory")
template <bool SC1> DEVI void dma5(unsigned la, __amdgpu_buffer_rsrc_t rs, int so, unsigned vo) { if (SC1) DMA5(" sc1"); else DMA5(""); }
template <bool SC1> DEVI void dma4(unsigned la, __amdgpu_buffer_rsrc_t rs, int so, unsigned v0, unsigned v1, unsigned v2, unsigned v3) { if (SC1) DMA4(" sc1"); else DMA4(""); }
#define WAIT_IF(c, a, b) do { if (c) WAIT_V(a); else WAIT_V(b); } while (0)
template <int T0, int T1, int P, int S0 = 0, class ISSUE>
DEVI void ring_steps(ISSUE&& issue) {
    int slot = (T0 + S0) % 3;
    LOOP_ALIGN();
#pragma unroll 1
    for (int t = T0; t < T1; ++t) {
        WAIT_V(P); BAR();
        issue(t + 3, slot);
        slot = slot == 2 ? 0 : slot + 1;
    }
}

DEVI unsigned flag_out(unsigned use, int pitch, int group, int writer, int lane) { return pin(lane < 16 ? use + (group * 16 + lane) * pitch + writer * 16 : 0x80000000u); }
DEVI unsigned flag_in(unsigned use, int pitch, int reader, int n, int lane) { return pin(use + reader * pitch + (lane < n ? lane : 0) * 16); }
DEVI void flag_raise(const char* ws, unsigned vo) { ST16(RSRC(ws), vo, 0, ((u32x4){1u, 1u, 1u, 1u}), 16); SCHED0(); }
template <int FIRST>
DEVI void flags_wait(const char* ws, unsigned vo) {
    unsigned v, n = 400000u;
    asm volatile("s_nop 4\n\ts_sleep %4\n"
                 "1:\n\tbuffer_load_dword %0, %2, %3, 0 offen sc1\n\ts_waitcnt vmcnt(0)\n\tv_cmp_eq_u32 vcc, 0, %0\n\ts_cbranch_vccz 2f\n\t"
                 "s_sleep 1\n\ts_sub_u32 %1, %1, 1\n\ts_cbranch_scc0 1b\n2:"
                 : "=&v"(v), "+s"(n) : "v"(vo), "s"(RSRC(ws)), "i"(FIRST) : "vcc", "scc", "memory");
    SCHED0();
}
DEVI void lds_word_wait(LAS char* p) {
#pragma unroll 1
    for (unsigned spin = 0; spin < 40000000u; ++spin) {
        unsigned v;
        asm volatile("ds_read_b32 %0, %1\n\ts_waitcnt lgkmcnt(0)" : "=v"(v) : "v"((unsigned)(size_t)p) : "memory");
        if (__builtin_amdgcn_readfirstlane(v) != 0u) break;
        __builtin_amdgcn_s_sleep(1);
    }
}

constexpr int K1_STAGE = 32768;
constexpr int WO_OFF = 69632;
constexpr int G_OWN = 139264;
constexpr int G_PART = G_OWN + 18432;
constexpr int G_TAB = G_PART + 2048;
constexpr int K1_SCR = G_TAB + 1536;
constexpr int G_WORD = K1_SCR + 576;
constexpr int NET_LDS = G_WORD + 64;

#define WO_PRE 12
#define WO_W 33
#define REL_PRIO 0
#define O_FIRST 10
#define X1_FIRST 6
#define X2_FIRST 6
#define Y_FIRST 10
#define Z2_FIRST 10
DEVI void attn_issueB(LAS char* lds, const char* ws, int hp, int wid, int lane, int s, int slot) {
    const auto rB = RSRC(ws + OFF_TWQKV + (size_t)hp * (18 * 7168));
    LAS char* sl = lds + slot * K1_STAGE + 18432 + wid * 4096;
    const int off = wid * 4096 + s * 14336;
    BLDS(rB, sl, lane * 16, off, 0); BLDS(rB, sl, lane * 16, off, 1024);
    if (wid < 3) { BLDS(rB, sl, lane * 16, off, 2048); BLDS(rB, sl, lane * 16, off, 3072); }
}
template <class NEXT>
DEVI void attn_producer(LAS char* lds, const char* ws, const char* wl, unsigned fl, int layer, int pm, int hp, int wid, NEXT&& next) {
    const int lane = lane_id_fresh(), r = lane & 15, q = lane >> 4;
    const int pmh = pm > 0 ? pm : 1;
    const auto rA = RSRC(wl + L_XA);
    const unsigned vo = lane * 16, voh = (unsigned)((r >> 3) * 18432 + q * 256 + (8 + (r & 7)) * 16);
    const int oA = (pm * 8 + 2 * wid) * 18432, oh = (pmh * 8 - 2) * 18432;
    auto issueA = [&](int s, int slot) {
        LAS char* sl = lds + slot * K1_STAGE;
        if (wid == 3) { BLDS1(rA, sl, voh, oh + s * 2048, 0); BLDS1(rA, sl, voh, oh + s * 2048, 1024); }
        BLDS1(rA, sl + 2048 + wid * 4096, vo, oA + s * 2048, 0); BLDS1(rA, sl + 2048 + wid * 4096, vo, oA + s * 2048, 1024);
        BLDS1(rA, sl + 4096 + wid * 4096, vo, oA + s * 2048 + 18432, 0); BLDS1(rA, sl + 4096 + wid * 4096, vo, oA + s * 2048 + 18432, 1024);
    };
    auto issue = [&](int s, int slot) { issueA(s, slot); attn_issueB(lds, ws, hp, wid, lane, s, slot); };
    __builtin_amdgcn_s_setprio(3);
    if (layer == 0) { issue(0, 0); issue(1, 1); }
    const int hr = lane >> 2;
    const char* ps = pin(wl + L_STA + (size_t)(pm * 128 + wid * 32) * 64 + lane * 16);
    const char* psh = pin(wl + L_STA + (size_t)((pmh * 8 - 2 + (hr >> 3)) * 16 + 8 + (hr & 7)) * 64 + (lane & 3) * 16);
    if (layer == 0) {
        WAIT_V(8);
    } else {
        attn_issueB(lds, ws, hp, wid, lane, 2, 2);
        const unsigned fi = flag_in(F_X2, 512, pm * 16 + hp, pm > 0 ? 24 : 16, lane);
        if (wid == 0) flags_wait<X2_FIRST>(ws, fi);
        BARX();
        issueA(0, 0); issueA(1, 1); issueA(2, 2);
        WAIT_IF(wid == 3, 12, 8);
    }
    BAR();
    __builtin_amdgcn_s_setprio(0);
    if (layer == 0) issue(2, 2); else WAIT_IF(wid == 3, 6, 4);
    ring_steps<0, 6, 8>(issue);
    {
        f32x4 sp[3];
        side_load(sp, ps, psh);
        WAIT_V(11); BAR();
        WAIT_V(0); BARX();
        side_fence(sp);
#pragma unroll
        for (int i = 0; i < 3; ++i) lds_word_set(lds + K1_SCR + (i < 2 ? 16 + wid * 32 + i * 16 + hr : hr) * 4, __float_as_uint(ln_rstd(sp[i])));
        WAIT_L0();
    }
    BAR();
    const auto rW = RSRC(ws + OFF_TWO + (size_t)(hp >> 1) * 82944);
#pragma unroll
    for (int i = 0; i < 5; ++i) {
        const int off = (i * 4 + wid) * 4096;
        LAS char* d = lds + WO_OFF + off;
        BLDS(rW, d, vo, off, 0); BLDS(rW, d, vo, off, 1024); BLDS(rW, d, vo, off, 2048); BLDS(rW, d, vo, off, 3072);
    }
    if (wid == 0) BLDS(rW, lds + WO_OFF + 81920, vo, 81920, 0);
    next();
    const unsigned fo = flag_out(fl + F_O, 256, pm, hp, lane), fi = flag_in(fl + F_O, 256, pm * 16 + hp, 16, lane);
    WAIT_V(0);
    if (wid == 0) __builtin_amdgcn_s_setprio(3);
    BARX();
    if (wid == 0) { flag_raise(ws, fo); flags_wait<O_FIRST>(ws, fi); }
    __builtin_amdgcn_s_setprio(0);
    BARX();
}

#define STAMP_SEC 0
DEVI void attn_consumer(LAS char* lds, char* wl, int layer, int pm, int hp, int wid STAMP_PARAM) {
    const int lane = lane_id_fresh(), r = lane & 15, q = lane >> 4;
    const char* xa = wl + L_XA;
    char* o = wl + L_O;
    const int T = wid & 1, h = wid >> 1;
    const LAS char* ph = lds + q * 256 + ((r ^ (T ? 0 : 8)) * 16);
    const LAS char* pa = lds + T * 2048 + lane * 16;
    const LAS char* pb = lds + h * 3072 + lane * 16;
    const LAS char* pt = lds + lane * 16;
    f32x4 acc[5][4];
#pragma unroll
    for (int m = 0; m < 5; ++m)
#pragma unroll
        for (int n = 0; n < 4; ++n) if (m + n) acc[m][n] = pin((f32x4){0.f, 0.f, 0.f, 0.f});
    bf16x8 fa[2][5], fb[2][4];
    auto rd1 = [&](int kk, int i) {
        if (i < 3) fb[kk][i] = LDS_FRAG(pb + 18432 + kk * 7168 + i * 1024);
        else if (i == 3) fb[kk][3] = LDS_FRAG(pt + 24576 + kk * 7168);
        else if (i == 4) fa[kk][0] = LDS_FRAG(ph + kk * 1024);
        else if (i < 9) fa[kk][i - 4] = LDS_FRAG(pa + 2048 + kk * 1024 + (i - 5) * 4096);
    };
    auto chunk = [&](int kk, int d) {
#pragma unroll
        for (int i = 1; i < 20; ++i) {
            const int m = i >> 2, n = i & 3;
            const bf16x8 &A = fa[kk][m], &B = fb[kk][n];
            acc[m][n] = n != 2 ? MFMA(B, A, acc[m][n]) : MFMA(A, B, acc[m][n]);
            SCHED0();
            if (i <= 9) rd1(kk ^ 1, i - 1);
            if (kk == 0) { if (i == 11) ph = opaque(ph + d); if (i == 12) pa = opaque(pa + d); if (i == 13) pb = opaque(pb + d); if (i == 14) pt = opaque(pt + d); }
            SCHED0();
        }
    };
    f32x4 mb, mb0;
#pragma unroll
    for (int j = 0; j < 4; ++j) {
        const bool ok = ((r - 4 * q - j) & 15) < 8;
        mb[j] = ok ? 0.f : -__builtin_inff();
        mb0[j] = (ok && (pm > 0 || 4 * q + j <= r)) ? 0.f : -__builtin_inff();
    }
    mb = pin(mb); mb0 = pin(mb0);
    const unsigned mp0 = pin((4 * q > r ? 0xffffu : 0u) | (4 * q + 1 > r ? 0xffff0000u : 0u)), mp1 = pin((4 * q + 2 > r ? 0xffffu : 0u) | (4 * q + 3 > r ? 0xffff0000u : 0u));
    const unsigned qtm = pin(q == h ? 0xffffffffu : 0u);
    const unsigned vom = pin((unsigned)(r * 16 + hp * 1024 + (2 * h + (q >> 1)) * 256 + (q & 1) * 8)), vot = pin((unsigned)(r * 16 + 16384 + (hp >> 1) * 256 + (hp & 1) * 8 + h * 4));
    const LAS char* tk0 = opaque(lds + K1_SCR + ((q * 4) ^ (T ? 0 : 8)) * 4);
    const LAS char* tk = opaque(lds + K1_SCR + ((1 + T) * 16 + q * 4) * 4);
    const LAS char* tq = opaque(lds + K1_SCR + ((1 + T) * 16 + r) * 4);
    ph = opaque(ph); pa = opaque(pa); pb = opaque(pb); pt = opaque(pt);
    STAMP(2);
    if (layer) BAR();
    BARX();
    STAMP(3);
#pragma unroll
    for (int i = 0; i < 9; ++i) rd1(0, i);
    SCHED0();
    const int rt = pm * 8 + (hp & 1) * 4 + wid, cg = hp >> 1;
    const Res rx = proj_residual_load(xa, rt, cg, lane);
    SCHED0();
    int slot = 0;
    LOOP_ALIGN();
#pragma unroll 1
    for (int t = 0; t < 9; ++t) {
        const int d = slot == 2 ? -2 * K1_STAGE : K1_STAGE;
        slot = slot == 2 ? 0 : slot + 1;
        SCHED0();
        chunk(0, d);
        WAIT_L0(); BAR();
        SCHED0();
        chunk(1, 0);
    }
    STAMP(4);
    u32x2 ts[5], tf[5];
    {
        LAS char* xb = lds + wid * 320;
#pragma unroll
        for (int m = 0; m < 5; ++m) ts[m] = (u32x2){pack2(acc[m][3][0], acc[m][3][1]), pack2(acc[m][3][2], acc[m][3][3])};
        if (q == 2) {
#pragma unroll
            for (int m = 0; m < 5; ++m) {
                const unsigned w = h ? ts[m].y : ts[m].x;
                *(LAS u16*)(xb + m * 64 + r * 2) = (u16)w; *(LAS u16*)(xb + m * 64 + 32 + r * 2) = (u16)(w >> 16);
            }
        }
        asm volatile("" ::: "memory");
#pragma unroll
        for (int m = 0; m < 5; ++m) tf[m] = *(const LAS u32x2*)(xb + m * 64 + (r & 1) * 32 + q * 8);
    }
    {
        f32x4 rk[5]; float rq[4];
        rk[0] = *(const LAS f32x4*)(tk0);
#pragma unroll
        for (int g = 0; g < 4; ++g) { rk[g + 1] = *(const LAS f32x4*)(tk + g * 128); rq[g] = *(const LAS float*)(tq + g * 128); }
        const f32x4 zero = {0.f, 0.f, 0.f, 0.f};
        u32x4 kf[5];
#pragma unroll
        for (int m = 0; m < 5; ++m) kf[m] = (u32x4){pack2(acc[m][1][0], acc[m][1][1]), pack2(acc[m][1][2], acc[m][1][3]), ts[m].y, 0u};
        f32x4 sp[4], sc[4];
#pragma unroll
        for (int g = 0; g < 4; ++g) {
            const u32x4 qf = {pack2(acc[g + 1][0][0], acc[g + 1][0][1]), pack2(acc[g + 1][0][2], acc[g + 1][0][3]), ts[g + 1].x & qtm, 0u};
            sp[g] = MFMA(__builtin_bit_cast(bf16x8, kf[g]), __builtin_bit_cast(bf16x8, qf), zero);
            sc[g] = MFMA(__builtin_bit_cast(bf16x8, kf[g + 1]), __builtin_bit_cast(bf16x8, qf), zero);
        }
        u32x2 vf[5];
#pragma unroll
        for (int m = 0; m < 5; ++m) vf[m] = (u32x2){pack2(acc[m][2][0], acc[m][2][1]), pack2(acc[m][2][2], acc[m][2][3])};
        f32x4 rs[4], t[4]; float mx[4], sm[4];
#pragma unroll
        for (int g = 0; g < 4; ++g) {
#pragma unroll
            for (int j = 0; j < 4; ++j) { const bool pv = 4 * q + j > r; rs[g][j] = pv ? rk[g][j] : rk[g + 1][j]; t[g][j] = pv ? sp[g][j] : sc[g][j]; }
            t[g] = (t[g] * rs[g]) * (rq[g] * (0.23570226039551584f * 1.4426950408889634f)) + (g == 0 ? mb0 : mb);
            mx[g] = fmaxf(fmaxf(t[g][0], t[g][1]), fmaxf(t[g][2], t[g][3]));
        }
        xq4_max(mx);
#pragma unroll
        for (int g = 0; g < 4; ++g) {
            t[g] = t[g] - mx[g];
#pragma unroll
            for (int j = 0; j < 4; ++j) t[g][j] = __builtin_amdgcn_exp2f(t[g][j]);
            sm[g] = (t[g][0] + t[g][2]) + (t[g][1] + t[g][3]);
        }
        f32x4 om[4], ot[4];
#pragma unroll
        for (int g = 0; g < 4; ++g) {
            const f32x4 p = t[g] * rs[g];
            const unsigned p01 = pack2(p[0], p[1]), p23 = pack2(p[2], p[3]);
            const u32x4 pf = {p01 & mp0, p23 & mp1, p01 & ~mp0, p23 & ~mp1};
            om[g] = MFMA(__builtin_bit_cast(bf16x8, ((u32x4){vf[g].x, vf[g].y, vf[g + 1].x, vf[g + 1].y})), __builtin_bit_cast(bf16x8, pf), zero);
            ot[g] = MFMA(__builtin_bit_cast(bf16x8, ((u32x4){tf[g].x, tf[g].y, tf[g + 1].x, tf[g + 1].y})), __builtin_bit_cast(bf16x8, pf), zero);
        }
        xq4_sum_rcp(sm);
        const auto ro = RSRC(o + (size_t)(pm * 8 + T) * 18432);
        unsigned tw2[4];
#pragma unroll
        for (int g = 0; g < 4; ++g) {
            const f32x4 v = om[g] * sm[g];
            __builtin_amdgcn_raw_buffer_store_b64((u32x2){pack2(v[0], v[1]), pack2(v[2], v[3])}, ro, vom, g * 36864, 16);
            const f32x2 w = ot[g].xy * sm[g];
            tw2[g] = pack2(w.x, w.y);
        }
        if (q == 2) {
#pragma unroll
            for (int g = 0; g < 4; ++g) __builtin_amdgcn_raw_buffer_store_b32(tw2[g], ro, vot, g * 36864, 16);
        }
    }
    STAMP(5);
    {
        WAIT_V(0); BARX();
        STAMP(6);
        f32x4 v[5];
        proj_residual(v, rx);
        const LAS char* pw = opaque(lds + WO_OFF + lane * 16);
        const LAS char* pw2 = opaque(lds + WO_OFF + 41472 + lane * 16);
        const LAS char* ph2 = opaque(lds + WO_OFF + 4096 + q * 128 + (r & 7) * 16);
        const LAS char* ph3 = opaque(lds + WO_OFF + 41472 + 4096 + q * 128 + (r & 7) * 16);
        bf16x8 wf[90];
        auto rdw = [&](int i) {
            const int kc = i / 5, n = i % 5;
            wf[i] = n < 4 ? LDS_FRAG((kc < 9 ? pw : pw2) + (kc % 9) * 4608 + n * 1024) : LDS_FRAG((kc < 9 ? ph2 : ph3) + (kc % 9) * 4608);
        };
#pragma unroll
        for (int i = 0; i < WO_W; ++i) rdw(i);
#pragma unroll
        for (int n = 0; n < 5; ++n) v[n] = pin(v[n]);
        const auto rO = RSRC(o + (size_t)rt * 18432);
        const ProjOut po = proj_out(wl + L_XB, wl + L_STB, rt, cg, lane);
        asm volatile("" :: "s"(rO));
        WAIT_L(0);
        __builtin_amdgcn_s_setprio(REL_PRIO);
        BARX();
        STAMP(7);
        bf16x8 af[18];
        auto ldo = [&](int kc) { af[kc] = __builtin_bit_cast(bf16x8, __builtin_amdgcn_raw_buffer_load_b128(rO, lane * 16 + (kc & 3) * 1024, (kc >> 2) * 4096, 16)); };
#pragma unroll
        for (int kc = 0; kc < WO_PRE; ++kc) { ldo(kc); SCHED0(); }
        static_for<90>([&](auto ic) {
            constexpr int i = ic.value, kc = i / 5, n = i % 5, j = WO_W + 2 * (i / 3) + i % 3;
            MFMA_T(wf[i], af[kc], v[n]);
            SCHED0();
            if (i % 3 < 2 && j < 90) rdw(j);
            if (i % 3 == 2 && (i / 3) % 2 == 0 && i / 6 + WO_PRE < 18) ldo(i / 6 + WO_PRE);
            SCHED0();
        });
        asm volatile("s_nop 4" : "+v"(v[0]), "+v"(v[1]));
        STAMP(8);
        proj_store(v, po);
        __builtin_amdgcn_s_setprio(0);
    }
}

DEVI f32x2 gelu2(f32x2 v) {
    const f32x2 w = v * (v * v * -0.10294324f + -2.3022082f);
    const f32x2 d = (f32x2){__builtin_amdgcn_exp2f(w.x), __builtin_amdgcn_exp2f(w.y)} + 1.0f;
    return v * (f32x2){__builtin_amdgcn_rcpf(d.x), __builtin_amdgcn_rcpf(d.y)};
}
DEVI f32x4 gelu4(f32x4 v) { const f32x2 a = gelu2(v.xy), b = gelu2(v.zw); return (f32x4){a.x, a.y, b.x, b.y}; }
DEVI void rowsum16x8(const f32x4 a, const f32x4 b, float& o0, float& o1) {
    float t0, t1, t2, t3;
    asm volatile(
        "s_nop 1\n\t"
        "v_add_f32_dpp %2, %6, %6 row_ror:8 row_mask:0xf bank_mask:0x3\n\t"
        "v_add_f32_dpp %3, %7, %7 row_ror:8 row_mask:0xf bank_mask:0x3\n\t"
        "v_add_f32_dpp %4, %10, %10 row_ror:8 row_mask:0xf bank_mask:0x3\n\t"
        "v_add_f32_dpp %5, %11, %11 row_ror:8 row_mask:0xf bank_mask:0x3\n\t"
        "v_add_f32_dpp %2, %8, %8 row_ror:8 row_mask:0xf bank_mask:0xc\n\t"
        "v_add_f32_dpp %3, %9, %9 row_ror:8 row_mask:0xf bank_mask:0xc\n\t"
        "v_add_f32_dpp %4, %12, %12 row_ror:8 row_mask:0xf bank_mask:0xc\n\t"
        "v_add_f32_dpp %5, %13, %13 row_ror:8 row_mask:0xf bank_mask:0xc\n\t"
        "v_add_f32_dpp %0, %2, %2 row_half_mirror row_mask:0xf bank_mask:0x5\n\t"
        "s_nop 0\n\t"
        "v_add_f32_dpp %1, %4, %4 row_half_mirror row_mask:0xf bank_mask:0x5\n\t"
        "v_add_f32_dpp %0, %3, %3 row_half_mirror row_mask:0xf bank_mask:0xa\n\t"
        "v_add_f32_dpp %1, %5, %5 row_half_mirror row_mask:0xf bank_mask:0xa\n\t"
        "s_nop 0\n\t"
        "v_add_f32_dpp %0, %0, %0 quad_perm:[1,0,3,2] row_mask:0xf bank_mask:0xf\n\t"
        "v_add_f32_dpp %1, %1, %1 quad_perm:[1,0,3,2] row_mask:0xf bank_mask:0xf\n\t"
        "s_nop 0\n\t"
        "v_add_f32_dpp %0, %0, %0 quad_perm:[2,3,0,1] row_mask:0xf bank_mask:0xf\n\t"
        "v_add_f32_dpp %1, %1, %1 quad_perm:[2,3,0,1] row_mask:0xf bank_mask:0xf\n\t"
        : "=&v"(o0), "=&v"(o1), "=&v"(t0), "=&v"(t1), "=&v"(t2), "=&v"(t3)
        : "v"(a[0]), "v"(a[1]), "v"(a[2]), "v"(a[3]), "v"(b[0]), "v"(b[1]), "v"(b[2]), "v"(b[3]));
}

#define K5_PRE 8
#define RH_MAP 0
#define NET_LAYERS 2
constexpr int G_STAGE = 34816;
constexpr int K5_STAGE = 18432, K5_NS = 8;

constexpr int y_load_at(int slot) {
    int next = K5_PRE;
    for (int s = 0;; ++s) { const bool go = next < 18 + 2 * (s / 3); if (s == slot) return go ? next : -1; next += go; }
}

struct GTile { f32x4 a[4][4], e[2]; };
struct GFrag { bf16x8 a[2][4], b[2][5]; const LAS char *pa[2][2], *pb, *p8; int slot; };
DEVI void g_rd(GFrag& f, int kk, int i) {
    if (i < 4) f.a[kk][i] = LDS_FRAG(f.pa[kk][i >> 1] + (i & 1) * 2048);
    else if (i < 8) f.b[kk][i - 4] = LDS_FRAG(f.pb + (kk * 9 + i - 4) * 1024);
    else f.b[kk][4] = LDS_FRAG(f.p8 + kk * 9216);
}
template <bool ROWS_IN_REGS>
DEVI void g_chunk(GTile& c, GFrag& f, int kk, int d) {
#pragma unroll
    for (int i = 0; i < 18; ++i) {
        const int n = i >> 2, m = i & 3;
        f32x4& e = n < 4 ? c.a[m][n] : c.e[m & 1];
        e = ROWS_IN_REGS ? MFMA(f.a[kk][m], f.b[kk][n], e) : MFMA(f.b[kk][n], f.a[kk][m], e);
        SCHED0();
        if (i < 9) g_rd(f, kk ^ 1, i);
        if (kk == 0) {
            if (i >= 10 && i < 14) f.pa[(i - 10) >> 1][i & 1] = opaque(f.pa[(i - 10) >> 1][i & 1] + d);
            if (i == 14) f.pb = opaque(f.pb + d);
            if (i == 15) f.p8 = opaque(f.p8 + d);
        }
        SCHED0();
    }
}
template <int NS, bool ROWS_IN_REGS>
DEVI void g_steps(GTile& c, GFrag& f, int n) {
    LOOP_ALIGN();
#pragma unroll 1
    for (int t = 0; t < n; ++t) {
        const int d = NS > 0 ? (f.slot == NS - 1 ? -(NS - 1) * G_STAGE : G_STAGE) : (f.slot == 0 ? -(NS + 1) * G_STAGE : -G_STAGE);
        f.slot = NS > 0 ? (f.slot == NS - 1 ? 0 : f.slot + 1) : (f.slot == 0 ? -(NS + 1) : f.slot - 1);
        SCHED0();
        g_chunk<ROWS_IN_REGS>(c, f, 0, d);
        WAIT_L0();
        BAR();
        SCHED0();
        g_chunk<ROWS_IN_REGS>(c, f, 1, 0);
    }
}
DEVI void keep4(const f32x4 (&c)[4]) { asm volatile("" :: "v"(c[0]), "v"(c[1]), "v"(c[2]), "v"(c[3])); }
template <int D, int DB, bool FIRST, class HOOK>
DEVI void g_step_u(GTile& c, GFrag& f, const f32x4 (&c0)[4], HOOK&& hook) {
    static_for<36>([&](auto ic) {
        constexpr int kk = ic.value / 18, i = ic.value % 18, n = i >> 2, m = i & 3;
        if (i == 0) { WAIT_L(0); if (kk) BAR(); SCHED0(); }
        f32x4& e = n < 4 ? c.a[m][n] : c.e[m & 1];
        if (FIRST && kk == 0) asm volatile("v_mfma_f32_16x16x32_bf16 %0, %1, %2, %3" : "=&v"(e) : "v"(f.b[kk][n]), "v"(f.a[kk][m]), "v"(c0[m]));
        else MFMA_T(f.b[kk][n], f.a[kk][m], e);
        SCHED0();
        if (i < 9) g_rd(f, kk ^ 1, i);
        if (kk == 0) {
            if (i >= 10 && i < 14) f.pa[(i - 10) >> 1][i & 1] = opaque(f.pa[(i - 10) >> 1][i & 1] + D);
            if (i == 14) f.pb = opaque(f.pb + DB);
            if (i == 15) f.p8 = opaque(f.p8 + DB);
        }
        if (FIRST && ic.value == 20) keep4(c0);
        hook(ic);
        SCHED0();
    });
}
#define MF(d, a, b) "v_mfma_f32_16x16x32_bf16 " d ", " a ", " b ", " d "\n\t"
template <int D, int DB>
DEVI void g_step_gelu(GTile& c, GFrag& f, f32x4& z, float r, float c1) {
#pragma unroll
    for (int kk = 0; kk < 2; ++kk) {
        float va = z[2 * kk], vb = z[2 * kk + 1], ta, tb;
        WAIT_L(0); if (kk) BAR(); SCHED0();
#pragma unroll
        for (int i = 0; i < 9; ++i) {
            MFMA_T(f.b[kk][i >> 2], f.a[kk][i & 3], c.a[i & 3][i >> 2]);
            if (i == 5) asm volatile("v_mul_f32 %0, %0, %1" : "+v"(va) : "v"(r));
            if (i == 6) asm volatile("v_mul_f32 %0, %0, %1" : "+v"(vb) : "v"(r));
            if (i == 7) asm volatile("v_mul_f32 %0, %1, %1" : "=v"(ta) : "v"(va));
            if (i == 8) asm volatile("v_mul_f32 %0, %1, %1" : "=v"(tb) : "v"(vb));
            SCHED0();
            g_rd(f, kk ^ 1, i);
            SCHED0();
        }
        asm volatile(MF("%0", "%5", "%6") "v_fmaak_f32 %3, %3, %11, 0xc0135761\n\tv_fmaak_f32 %4, %4, %11, 0xc0135761\n\t"
                     MF("%1", "%5", "%7") "v_mul_f32 %3, %3, %9\n\tv_mul_f32 %4, %4, %10\n\t" MF("%2", "%5", "%8") "v_exp_f32 %3, %3"
                     : "+v"(c.a[1][2]), "+v"(c.a[2][2]), "+v"(c.a[3][2]), "+v"(ta), "+v"(tb)
                     : "v"(f.b[kk][2]), "v"(f.a[kk][1]), "v"(f.a[kk][2]), "v"(f.a[kk][3]), "v"(va), "v"(vb), "v"(c1));
        SCHED0(); f.pa[kk][0] = opaque(f.pa[kk][0] + D); SCHED0();
        asm volatile(MF("%0", "%6", "%7") "v_exp_f32 %5, %5\n\t" MF("%1", "%6", "%8") "v_add_f32 %4, 1.0, %4\n\tv_add_f32 %5, 1.0, %5\n\t"
                     MF("%2", "%6", "%9") "v_rcp_f32 %4, %4\n\t" MF("%3", "%6", "%10") "v_rcp_f32 %5, %5"
                     : "+v"(c.a[0][3]), "+v"(c.a[1][3]), "+v"(c.a[2][3]), "+v"(c.a[3][3]), "+v"(ta), "+v"(tb)
                     : "v"(f.b[kk][3]), "v"(f.a[kk][0]), "v"(f.a[kk][1]), "v"(f.a[kk][2]), "v"(f.a[kk][3]));
        SCHED0(); f.pa[kk][1] = opaque(f.pa[kk][1] + D); SCHED0();
        asm volatile(MF("%0", "%4", "%5") "v_mul_f32 %2, %2, %7\n\tv_mul_f32 %3, %3, %8\n\t" MF("%1", "%4", "%6")
                     : "+v"(c.e[0]), "+v"(c.e[1]), "+v"(va), "+v"(vb) : "v"(f.b[kk][4]), "v"(f.a[kk][0]), "v"(f.a[kk][1]), "v"(ta), "v"(tb));
        SCHED0();
        if (kk == 0) { f.pb = opaque(f.pb + DB); f.p8 = opaque(f.p8 + DB); }
        SCHED0();
        z[2 * kk] = va; z[2 * kk + 1] = vb;
    }
}
DEVI void g_fill(GTile& c, const float (&e)[4]) {
#pragma unroll
    for (int m = 0; m < 4; ++m) {
#pragma unroll
        for (int n = 0; n < 4; ++n) c.a[m][n] = pin((f32x4){e[m], e[m], e[m], e[m]});
        if (m < 2) c.e[m] = pin((f32x4){e[m], e[m], e[m], e[m]});
    }
}

#undef STAMP_SEC
#define STAMP_SEC 1
DEVI void gmlp_body(LAS char* lds, int wm, int wn, int pm, int cg, char* z2t, char* y, const u16* __restrict__ bsp STAMP_PARAM) {
    const int mlo = wm * 4 + wn * 2, mhi = wm * 4 + (1 - wn) * 2;
    const float zero4[4] = {0.f, 0.f, 0.f, 0.f};
    GTile z2, z1, acc;
    GFrag f;
    {
        const int lane = lane_id_fresh();
#pragma unroll
        for (int kk = 0; kk < 2; ++kk) { f.pa[kk][0] = lds + mlo * 2048 + kk * 1024 + lane * 16; f.pa[kk][1] = lds + mhi * 2048 + kk * 1024 + lane * 16; }
        f.pb = lds + 16384 + wn * 4096 + lane * 16; f.p8 = lds + 16384 + 8192 + lane * 16; f.slot = 0;
    }
    g_fill(z2, zero4); g_fill(z1, zero4);
#pragma unroll
    for (int i = 0; i < 4; ++i) f.pa[i >> 1][i & 1] = opaque(f.pa[i >> 1][i & 1]);
    f.pb = opaque(f.pb); f.p8 = opaque(f.p8);
    SCHED0();
    WAIT_V(0); BARX();
    BAR();
    BARX();
    STAMP(2);
#pragma unroll
    for (int i = 0; i < 9; ++i) g_rd(f, 0, i);
    g_steps<4, true>(z2, f, 9);
    STAMP(3);
    {
        const int lane = lane_id_fresh(), r = lane & 15, q = lane >> 4;
        f32x4 ra[4];
#pragma unroll
        for (int m = 0; m < 4; ++m) ra[m] = *(const LAS f32x4*)(lds + G_TAB + ((m < 2 ? mlo : mhi) + (m & 1)) * 64 + q * 16);
        SCHED0();
#pragma unroll
        for (int m = 0; m < 4; ++m) {
            f32x4 s1, s2;
#pragma unroll
            for (int n = 0; n < 4; ++n) {
                const f32x4 g = gelu4(z2.a[m][n] * ra[m]);
                z2.a[m][n] = g;
                if (n == 0) { s1 = g; s2 = g * g; } else { s1 += g; s2 += g * g; }
            }
            if (m < 2) { const f32x4 g = gelu4(z2.e[m] * ra[m]); z2.e[m] = g; s1 += g; s2 += g * g; }
            float u0, u1;
            rowsum16x8(s1, s2, u0, u1);
            if ((r & 3) == 0) *(LAS f32x2*)(lds + G_PART + wn * 1024 + ((m < 2 ? mlo : mhi) + (m & 1)) * 128 + (q * 4 + (r >> 2)) * 8) = (f32x2){u0, u1};
            SCHED0();
        }
    }
    WAIT_L0(); BAR();
    STAMP(4);
    g_steps<4, false>(z1, f, 4);
    unsigned b0, b1;
    f32x4 c0[4];
    g_step_u<G_STAGE, G_STAGE, false>(z1, f, c0, [&](auto ic) {
        if (ic.value != 18) return;
        STAMP(5);
        const int lane = lane_id_fresh(), q = lane >> 4;
        b0 = *(const unsigned*)(bsp + pm * 128 + mlo * 16 + 2 * (lane & 15)); b1 = *(const unsigned*)(bsp + pm * 128 + mhi * 16 + 2 * (lane & 15));
#pragma unroll
        for (int g = 0; g < 2; ++g) {
            const auto rz2 = RSRC(z2t + (size_t)pm * 589824 + cg * 36864 + ((g ? mhi : mlo) >> 1) * 9216);
            const LAS char* tab = lds + G_TAB + (g ? mhi : mlo) * 64 + q * 16;
            LAS char* own = lds + (wm ? G_OWN : G_STAGE + 16384) + (g ^ wn) * 9216 + lane * 16;
            f32x4 ra[2], rb[2];
#pragma unroll
            for (int T = 0; T < 2; ++T) { ra[T] = *(const LAS f32x4*)(tab + 512 + T * 64); rb[T] = *(const LAS f32x4*)(tab + 1024 + T * 64); }
#pragma unroll
            for (int n = 0; n < (g ? 4 : 5); ++n) {
                const f32x4 v0 = (n < 4 ? z2.a[2 * g][n & 3] : z2.e[0]) * ra[0] + rb[0], v1 = (n < 4 ? z2.a[2 * g + 1][n & 3] : z2.e[1]) * ra[1] + rb[1];
                const u32x4 o = {pack2(v0[0], v1[0]), pack2(v0[1], v1[1]), pack2(v0[2], v1[2]), pack2(v0[3], v1[3])};
                ST16(rz2, lane * 16, n < 4 ? wn * 4096 + n * 1024 : 8192, o, 16);
                *(LAS u32x4*)(own + (n < 4 ? wn * 4096 + n * 1024 : 8192)) = o;
            }
        }
        STAMP(6);
    });
    static_for<3>([&](auto sc) {
        constexpr int D = sc.value == 0 ? G_STAGE : sc.value == 1 ? -3 * G_STAGE : 2 * G_STAGE;
        g_step_u<D, D, false>(z1, f, c0, [&](auto ic) { if (ic.value == 17 && sc.value == 1) WAIT_V(0); });
    });
    float ra[4], c1;
    {
        int lane, da[2];
        g_step_u<-G_STAGE, -G_STAGE, false>(z1, f, c0, [&](auto ic) {
            constexpr int i = ic.value;
            if (i == 9) lane = pin(lane_id_fresh());
            if (i == 14 || i == 15) da[i & 1] = pin((lane & 15) * 128 + (((4 * (i & 1) + (lane >> 4)) ^ (lane & 7)) * 16) - lane * 16 - (i & 1) * 1024);
            if (i == 16 || i == 17) f.pa[0][i & 1] = opaque(f.pa[0][i & 1] + da[0]);
            if (i == 27 || i == 28) f.pa[1][i & 1] = opaque(f.pa[1][i & 1] + da[1]);
            if (i >= 29 && i < 33) {
                constexpr int m = (i - 29) & 3;
                const float e = m & 1 ? bfhi(m < 2 ? b0 : b1) : bflo(m < 2 ? b0 : b1);
                c0[m] = pin((f32x4){e, e, e, e});
            }
            if (i == 33) {
#pragma unroll
                for (int m = 0; m < 4; ++m) ra[m] = *(const LAS float*)(lds + G_TAB + ((m < 2 ? mlo : mhi) + (m & 1)) * 64 + (lane & 15) * 4);
                c1 = pin(-0.10294324f);
            }
        });
    }
    STAMP(7);
    g_step_u<2 * G_STAGE, G_OWN - G_STAGE - 16384, true>(acc, f, c0, [](auto) {});
    static_for<18>([&](auto sc) {
        constexpr int st = sc.value, m = st < 16 ? st >> 2 : st & 1;
        constexpr int D = st == 0 ? -3 * G_STAGE : st % 3 == 1 ? 2 * G_STAGE : -G_STAGE;
        g_step_gelu<D, st == 0 ? 16384 - G_OWN : D>(acc, f, st < 16 ? z1.a[m][st & 3] : z1.e[m], ra[m], c1);
    });
    f.slot = 1;
    STAMP(9);
    g_steps<-3, false>(acc, f, 13);
    STAMP(10);
    const int lane = lane_id_fresh();
#pragma unroll
    for (int g = 0; g < 2; ++g) {
        const auto ry = RSRC(y + (size_t)(pm * 8 + (g ? mhi : mlo)) * 73728 + cg * 4608);
#pragma unroll
        for (int T = 0; T < 2; ++T) {
            const int m = 2 * g + T;
#pragma unroll
            for (int p = 0; p < 2; ++p) {
                const f32x4 a = acc.a[m][2 * p] * z1.a[m][2 * p], c = acc.a[m][2 * p + 1] * z1.a[m][2 * p + 1];
                ST16(ry, lane * 16 + wn * 2048 + p * 1024, T * 73728, ((u32x4){pack2(a[0], a[1]), pack2(a[2], a[3]), pack2(c[0], c[1]), pack2(c[2], c[3])}), 16);
            }
            if (g == 0) {
                const f32x4 a = acc.e[T] * z1.e[T];
                ST8(ry, (lane >> 5) * 256 + (lane & 15) * 16 + ((lane >> 4) & 1) * 8, T * 73728 + 4096, ((u32x2){pack2(a[0], a[1]), pack2(a[2], a[3])}), 16);
            }
        }
    }
}

#define NET_IDS \
    int b = blockIdx.x, wid = wid8 & 3; \
    asm volatile("" : "+s"(b), "+s"(wid));     \
    const int xcd = b & 7, idx = b >> 3; \
    const int pa = (xcd >> 1) * 4 + (idx & 3), hp = (xcd & 1) * 8 + (idx >> 2); \
    const int pm = (xcd >> 2) * 8 + (idx & 7), cg = (xcd & 3) * 4 + (idx >> 3); \
    const int rh = RH_MAP ? xcd * 4 + (idx & 3) : (xcd >> 1) * 8 + (idx & 7), c5 = RH_MAP ? idx >> 2 : (xcd & 1) * 4 + (idx >> 3); \
    const int lead = (idx + (idx >> 3)) & 3; \
    auto kord = [&](int t, int n) { \
        const int k = t + ((int)(0x1F000000u << (((t - lead) & 3) * 4)) >> 28); \
        return (unsigned)k < (unsigned)n ? k : t; \
    }; \
    auto kord5 = [&](int t) { \
        const int k = t + ((int)(0x1F000000u << (((t - (RH_MAP ? c5 : lead)) & 3) * 4)) >> 28); \
        return (unsigned)k < 18u ? k : t; \
    };

__global__ __launch_bounds__(512, 2) TRACE_ATTR void k_net(char* ws, const u16* wsp, const u16* bsp, char* out DBG_ARG) {
    asm volatile("gm_code_begin:" :: "s"(ws), "s"(wsp), "s"(bsp), "s"(out));
    extern __shared__ __attribute__((aligned(16))) char smem[];
    LAS char* lds = (LAS char*)smem;
    const char *twin = ws + OFF_TWIN, *twout = ws + OFF_TWOUT;
    STAMP_DECL;
    const int wid8 = __builtin_amdgcn_readfirstlane(threadIdx.x >> 6);
    if (wid8 >= 4) {
        lds_word_set(lds + G_WORD + (threadIdx.x & 15) * 4, 0u);
        WAIT_L0();
        u32x4 wr[5][9];
#pragma unroll 1
      for (int layer = 0; layer < NET_LAYERS; ++layer) {
        NET_IDS
        TRACE_BEGIN;
        char* wl = ws + OFF_L0 + layer * SZ_L;
        const unsigned fl = layer * F_LAYER;
        const char *xin = wl + L_XB, *st = wl + L_STB, *z2t = wl + (layer ? L_Y : L_Z2T);
        unsigned long long* zst = (unsigned long long*)(ws + OFF_ZST + layer * SZ_ZST);
        const int lane = lane_id_fresh(), l3 = lane >> 3;
        const unsigned vo = lane * 16;
        const unsigned l0 = (unsigned)(size_t)lds, lA = l0 + wid * 4096, lB = l0 + 16384 + wid * 4608;
        const auto rX = RSRC(xin + (size_t)(pm * 8 + 2 * wid) * 18432), rI = RSRC(twin + (size_t)cg * (18 * 18432) + wid * 4608);
        auto issue1B = [&](int g, int slot) { const int k = kord(g < 9 ? g : (g < 13 ? g - 4 : g - 13), 9); dma5<false>(lB + slot * G_STAGE, rI, (g < 9 ? 9 + k : k) * 18432, vo); };
        auto issue1A = [&](int g, int slot) { dma4<true>(lA + slot * G_STAGE, rX, kord(g < 9 ? g : g - 13, 9) * 2048, vo, vo, vo + 16384, vo + 16384); };
        auto issue1 = [&](int g, int slot) { issue1B(g, slot); issue1A(g, slot); };
        const auto rA = RSRC((const char*)wsp + (size_t)(pm * 128 + wid * 32) * 4096), rB = RSRC(z2t + cg * 36864 + wid * 4608);
        const unsigned voA = (unsigned)(2 * l3 * 4096 + ((lane & 7) ^ l3) * 16);
        auto issueB = [&](int k, int slot) { dma5<true>(lB + slot * G_STAGE, rB, (k >> 1) * 589824 + (k & 1) * 18432, vo); };
        const auto rW = RSRC(twout + (size_t)c5 * (72 * 4608));
        auto issue5 = [&](int t) { dma5<false>(l0 + ((t + 6) & 7) * K5_STAGE + wid * 4608, rW, kord5(t) * K5_STAGE + wid * 4608, vo); };
        auto load5 = [&](int p, int i) {
            const int t0 = p ? 2 * p + 6 : 4, ow = kord5(t0 + i) * K5_STAGE + wid * 4096;
            asm volatile("s_nop 4\n\tbuffer_load_dwordx4 %0, %4, %5, %6 offen\n\tbuffer_load_dwordx4 %1, %4, %5, %6 offen offset:1024\n\t"
                         "buffer_load_dwordx4 %2, %4, %5, %6 offen offset:2048\n\tbuffer_load_dwordx4 %3, %4, %5, %6 offen offset:3072"
                         : "+v"(wr[p][4 * i]), "+v"(wr[p][4 * i + 1]), "+v"(wr[p][4 * i + 2]), "+v"(wr[p][4 * i + 3]) : "v"(vo), "s"(rW), "s"(ow) : "memory");
            if (i) {
                const int oe = kord5(t0 + (wid >> 1)) * K5_STAGE + 16384 + (wid & 1) * 1024;
                asm volatile("s_nop 4\n\tbuffer_load_dwordx4 %0, %1, %2, %3 offen" : "+v"(wr[p][8]) : "v"(vo), "s"(rW), "s"(oe) : "memory");
            }
        };
        auto put5 = [&](int w0, int w1) {
#pragma unroll
            for (int w = w0; w < w1; ++w) {
                const int p = w / 9, i = w % 9 - 1;
                LAS char* sl = lds + (((p ? 2 * p + 6 : 4) + 6) & 7) * K5_STAGE + lane * 16;
                if (i < 0) *(LAS u32x4*)(sl + (wid >> 1) * K5_STAGE + 16384 + (wid & 1) * 1024) = wr[p][8];
                else *(LAS u32x4*)(sl + (i >> 2) * K5_STAGE + wid * 4096 + (i & 3) * 1024) = wr[p][i];
            }
            WAIT_L0();
        };
        const int row = wid * 32 + (lane & 31);
        auto step1 = [&](int t) { BAR(); issue1(t + 4, t & 3); };
        const char* ps;
        {
            unsigned fo, fi;
            attn_producer(lds, ws, wl, fl, layer, pa, hp, wid, [&] {
                issue1B(0, 0); issue1B(1, 1);
                fo = flag_out(fl + F_X1, 256, pa, hp, lane); fi = flag_in(fl + F_X1, 256, pm * 16 + cg, 16, lane);
                ps = pin(st + (size_t)(pm * 128 + wid * 32) * 64 + lane * 16);
            });
            BARX();
            if (wid == 0) flag_raise(ws, fo);
            issue1B(2, 2); issue1B(3, 3);
            asm volatile("" :: "s"(rX));
            if (wid == 0) flags_wait<X1_FIRST>(ws, fi);
            BARX();
        }
        issue1A(0, 0); issue1A(1, 1); issue1A(2, 2);
        WAIT_V(8); BAR();
        issue1A(3, 3);
        {
            f32x4 sp[3];
            WAIT_V(8); step1(0);
            WAIT_V(13); step1(1);
            WAIT_V(18); step1(2);
            WAIT_V(18); step1(3);
            WAIT_V(18); step1(4);
            WAIT_V(18); BAR(); issue1B(9, 1); side_load(sp, ps, ps); if (layer == 0) load5(3, 0);
            WAIT_IF(layer, 17, 21); BAR(); issue1B(10, 2); if (layer == 0) load5(3, 1);
            WAIT_IF(layer, 5, 14); BARX();
            side_fence(sp);
#pragma unroll
            for (int i = 0; i < 2; ++i) lds_word_set(lds + G_TAB + (wid * 32 + i * 16 + (lane >> 2)) * 4, __float_as_uint(ln_rstd(sp[i])));
            issue1B(11, 3);
            WAIT_L0();
        }
        BAR(); issue1B(12, 0);
        if (layer == 1) {
            const auto rd = RSRC(out + (size_t)b * 9216 + wid * 2304);
            ST16(rd, vo, 0, ((u32x4){0u, 0u, 0u, 0u}), 16); ST16(rd, vo, 1024, ((u32x4){0u, 0u, 0u, 0u}), 16);
            if (lane < 16) ST16(rd, vo, 2048, ((u32x4){0u, 0u, 0u, 0u}), 16);
        }
        if (layer == 0) {
#pragma unroll
            for (int p = 0; p < 6; ++p) load5(p >> 1, p & 1);
        }
        WAIT_V(0);
        __builtin_amdgcn_s_setprio(3);
        const char* src = pin((const char*)zst + (size_t)(pm * 16 + (lane >> 4) * 4) * 1024 + wid * 256 + (lane & 15) * 16);
        const unsigned tw = pin(l0 + G_TAB + 512 + (lane >> 5) * 512 + (wid * 32 + (lane & 15) * 2 + ((lane >> 4) & 1)) * 4);
        const unsigned fz = flag_out(fl + F_Z2, 256, cg, pm, lane), fzi = pin(fl + F_Z2 + (cg * 16 + pm) * 256 + ((lane ^ pm) & 8) * 16 + (lane & 7) * 16);
        const unsigned vA[4] = {pin(voA), pin(voA + 16 * 4096 - 1024), pin(voA + 4096 - 2048), pin(voA + 17 * 4096 - 3072)};
        auto spA = [&](int so, unsigned la) { dma4<false>(la, rA, so, vA[0], vA[1], vA[2], vA[3]); };
        auto issueA = [&](int k, int slot) { spA(k * 128, lA + slot * G_STAGE); };
        {
            const LAS char* pp = opaque(lds + G_PART + row * 8);
            const auto rs = RSRC(zst + (size_t)(pm * 16 + cg) * 128);
            const unsigned vp = pin(lane < 32 ? (unsigned)row * 8u : 0x80000000u);
            asm volatile("" :: "s"(rs));
            BARX();
            const f32x2 a = *(const LAS f32x2*)pp, c = *(const LAS f32x2*)(pp + 1024);
            ST8(rs, vp, 0, ((u32x2){__float_as_uint(add1(a.x, c.x)), min(__float_as_uint(add1(a.y, c.y)), 0xfffffffeu)}), 16);
            SCHED0();
        }
        __builtin_amdgcn_s_setprio(0);
        {
            f32x4 g[4];
            float sm[4];
            auto gather = [&] {
                asm volatile("global_load_dwordx4 %0, %4, off sc1\n\tglobal_load_dwordx4 %1, %4, off offset:1024 sc1\n\t"
                             "global_load_dwordx4 %2, %4, off offset:2048 sc1\n\tglobal_load_dwordx4 %3, %4, off offset:3072 sc1"
                             : "=&v"(g[0]), "=&v"(g[1]), "=&v"(g[2]), "=&v"(g[3]) : "v"(src) : "memory");
            };
            auto total = [&] {
                asm volatile("" : "+v"(g[0]), "+v"(g[1]), "+v"(g[2]), "+v"(g[3]));
#pragma unroll
                for (int i = 0; i < 4; ++i) sm[i] = add1(add1(g[0][i], g[1][i]), add1(g[2][i], g[3][i]));
            };
            step1(9); step1(10);
            BARX(); gather(); issue1(15, 3);
            WAIT_V(22); BARX();
            __builtin_amdgcn_s_setprio(3);
            WAIT_V(9);
            total();
            if (__any(__builtin_isunordered(sm[1], sm[3]))) {
#pragma unroll 1
                for (unsigned spin = 0; spin < 400000u; ++spin) {
                    bool ok = true;
#pragma unroll
                    for (int i = 0; i < 4; ++i) ok &= __float_as_uint(g[i][1]) != 0xffffffffu && __float_as_uint(g[i][3]) != 0xffffffffu;
                    if (__all(ok)) break;
                    gather();
                    WAIT_V(0);
                    total();
                }
            }
            xq4_sum(sm);
            const bool odd = lane & 16;
            float mean;
            const float rs = rstd_of(odd ? sm[2] : sm[0], odd ? sm[3] : sm[1], 1.0f / 2304.0f, mean);
            asm volatile("ds_write_b32 %0, %1" :: "v"(tw), "v"(lane < 32 ? rs : -mean * rs) : "memory");
            __builtin_amdgcn_s_setprio(0);
            issue1B(16, 0);
            WAIT_L0();
        }
        BARX(); issue1A(16, 0); issueA(2 * pm, 1);
        auto kord2 = [&](int t) { const int j = kord(t - 2, 30); return ((j < 2 * (pm & 7) ? j : j + 2) + 2 * (pm & 8)) & 31; };
        unsigned seen;
        auto look = [&] { if (wid == 0) asm volatile("s_nop 4\n\tbuffer_load_dword %0, %1, %2, 0 offen sc1" : "+v"(seen) : "v"(fzi), "s"(RSRC(ws)) : "memory"); };
        auto need = [&](int n) {
            if (wid == 0) {
                asm volatile("" : "+v"(seen));
                if (__ballot(seen == 0u) & ((1ull << n) - 1ull)) flags_wait<0>(ws, lane < n ? fzi : fl + F_Z2 + (cg * 16 + pm) * 256 + pm * 16);
            }
        };
        auto issue = [&](int t, int sl) { const int k = kord2(t); issueB(k, sl); issueA(k, sl); };
        auto steps2 = [&](int t0, int t1) {
            int sl = (2 * t0 + 2) % 3 * G_STAGE, k = kord2(t0 + 3), ob = (k >> 1) * 589824 + (k & 1) * 18432;
            LOOP_ALIGN();
#pragma unroll 1
            for (int t = t0; t < t1; ++t) {
                WAIT_V(9); BARX();
                dma5<true>(lB + sl, rB, ob, vo);
                const int oa = pin_s(k * 128);
                const unsigned la = pin_s(lA + sl);
                k = pin_s(kord2(t + 4)); ob = pin_s((k >> 1) * 589824 + (k & 1) * 18432);
                sl = pin_s(sl == 0 ? 2 * G_STAGE : sl - G_STAGE);
                spA(oa, la);
            }
        };
        WAIT_V(13); BAR(); issue1(17, 2);
        WAIT_V(13); BARX();
        if (wid == 0) flag_raise(ws, fz);
        issueA(2 * pm + 1, 3); if (layer == 0) load5(4, 0);
        if (wid == 0) WAIT_IF(layer, 5, 9); else WAIT_IF(layer, 4, 8);
        BAR(); issueA(kord2(2), 0); if (layer == 0) load5(4, 1);
        BAR(); look(); issueA(kord2(3), 2);
        WAIT_V(4); need(3); BARX(); issueB(kord2(2), 0); issueB(kord2(3), 2); look();
        WAIT_IF(wid == 0, 6, 5); BAR(); issue(4, 1);
        WAIT_V(9); need(8);
        steps2(2, 11); look();
        WAIT_IF(wid == 0, 10, 9); BAR(); issue(14, 0);
        WAIT_V(9); need(16);
        steps2(12, 29);
        WAIT_V(9); BAR(); issue5(0); issue5(1); issue5(2);
        WAIT_V(15); BAR(); issue5(6); issue5(7);
        BAR(); issue5(3); put5(0, 9);
        {
            const unsigned fo = flag_out(fl + F_Y, 256, pm, cg, lane), fi = flag_in(fl + F_Y, 256, rh * 8 + c5, 16, lane);
            __builtin_amdgcn_s_setprio(3);
            WAIT_V(0); BARX();
            if (wid == 0) { flag_raise(ws, fo); flags_wait<Y_FIRST>(ws, fi); }
            __builtin_amdgcn_s_setprio(0);
            BARX();
        }
        BAR(); put5(9, 16);
        BAR(); put5(16, 23);
        BAR(); put5(23, 30);
        BAR(); put5(30, 36);
        BAR(); issue5(16); put5(36, 41);
        BAR(); issue5(17); put5(41, 45);
        WAIT_V(5); BAR();
        WAIT_V(0); BAR();
        if (layer == 0) { attn_issueB(lds, ws, hp, wid, lane, 0, 0); attn_issueB(lds, ws, hp, wid, lane, 1, 1); }
        BAR();
        if (layer == 0) {
            __builtin_amdgcn_s_setprio(3);
            const unsigned fo = flag_out(F_X2, 512, (rh >> 1) + wid, wid ? 16 + c5 : (rh & 1) * 8 + c5, wid == 0 || (wid == 1 && (rh & 1) && rh < 31) ? lane : 16);
            BARX();
            if (wid < 2) flag_raise(ws, fo);
        }
        TRACE_DUMP;
      }
        return;
    }
#pragma unroll 1
  for (int layer = 0; layer < NET_LAYERS; ++layer) {
    NET_IDS
    TRACE_BEGIN;
    char* wl = ws + OFF_L0 + layer * SZ_L;
    const unsigned fl = layer * F_LAYER;
    const char* xin = wl + L_XB;
    char* y = wl + (layer ? L_Z2T : L_Y);
#undef STAMP_SEC
#define STAMP_SEC 0
    {
    STAMP_BEGIN;
    attn_consumer(lds, wl, layer, pa, hp, wid STAMP_FWD);
    STAMP_END(1 + 5 * layer);
    }
#undef STAMP_SEC
#define STAMP_SEC 1
    STAMP_BEGIN;
    gmlp_body(lds, wid & 1, wid >> 1, pm, cg, wl + (layer ? L_Y : L_Z2T), y, bsp STAMP_FWD);
    {
        WAIT_V(0);
        BARX();
        STAMP(11);
        const int lane = lane_id_fresh(), r = lane & 15, q = lane >> 4;
        const int wk = wid >> 1, rt = rh * 4 + (wid & 1) * 2 + wk;
        const Res rx = proj_residual_load(xin, rt, c5, lane);
        const LAS char *pw[3], *ph[3];
#pragma unroll
        for (int i = 0; i < 3; ++i) {
            pw[i] = opaque(lds + 3 * i * K5_STAGE + wk * 4608 + lane * 16);
            ph[i] = opaque(lds + 3 * i * K5_STAGE + wk * 4608 + 4096 + q * 128 + (r & 7) * 16);
        }
        const auto rY = RSRC(y + (size_t)(rt & ~1) * 73728 + wk * 1024);
        const unsigned vy[2] = {pin((unsigned)(wk * 73728 + lane * 16)), pin((unsigned)((1 - wk) * 73728 + lane * 16))};
        int dk[4];
#pragma unroll
        for (int i = 0; i < 4; ++i) dk[i] = pin_s((kord5(4 + i) - 4 - i) * 4096);
        auto yoff = [&](int t) { return pin_s(t == 0 || t == 17 ? kord5(t) * 4096 : t * 4096 + dk[t & 3]); };
        bf16x8 af[5][4], wf[4][5];
        auto lda = [&](int so, int i) { return __builtin_bit_cast(bf16x8, __builtin_amdgcn_raw_buffer_load_b128(rY, vy[i & 1] + (i >> 1) * 2048, so, 16)); };
        auto rdw = [&](int t, int c, int n) {
            const int sl = (t + 6) & 7, o = (sl % 3) * K5_STAGE + c * 9216;
            wf[2 * (t & 1) + c][n] = n < 4 ? LDS_FRAG(pw[sl / 3] + o + n * 1024) : LDS_FRAG(ph[sl / 3] + o);
        };
#pragma unroll
        for (int n = 0; n < 10; ++n) rdw(0, n / 5, n % 5);
        int so[18];
#pragma unroll
        for (int i = 0; i < 5; ++i) so[i] = yoff(i);
        so[17] = yoff(17);
        f32x4 v[5], u[5];
        proj_residual(v, rx);
#pragma unroll
        for (int n = 0; n < 5; ++n) v[n] = pin(v[n]);
        LAS char* xo = (LAS char*)opaque(lds + wid * 4608 + lane * 16);
        LAS char* xo4 = (LAS char*)opaque(lds + wid * 4608 + 4096 + (lane & 31) * 16);
        const LAS char* xi = opaque(lds + (wid ^ 2) * 4608 + lane * 16);
        const LAS char* xi4 = opaque(lds + (wid ^ 2) * 4608 + 4096 + (lane & 31) * 16);
        const ProjOut po = layer ? proj_out_final(out, rt, c5, lane) : proj_out(wl + SZ_L + L_XA, wl + SZ_L + L_STA, rt, c5, lane);
        asm volatile("" :: "s"(rY));
        BARX();
        STAMP(12);
#pragma unroll
        for (int i = 0; i < K5_PRE; ++i) { af[i >> 2][i & 3] = lda(so[i >> 2], i & 3); SCHED0(); }
        static_for<18>([&](auto tc) {
            constexpr int t = tc.value;
            bf16x8 (&a)[4] = af[t % 5];
#pragma unroll
            for (int h = 0; h < 4; ++h) {
                const int c = t < 17 ? h >> 1 : h & 1, m = t < 17 ? h & 1 : 1 - (h >> 1);
                if (t < 17 ? m == 0 : h < 2) { if (t == 17 && h == 1) WAIT_L(0); else WAIT_L(5); }
#pragma unroll
                for (int n = 0; n < 5; ++n) {
                    f32x4& e = m ? u[n] : v[n];
                    if (t == 0 && h == 1) MFMA_0(wf[0][n], a[1], e); else MFMA_T(wf[2 * (t & 1) + c][n], a[2 * c + m], e);
                    SCHED0();
                    if (t < 17 && m == 0) rdw(t + 1, c, n);
                    if (t < 14 && m == 1 && !(n & 1)) { const int i = y_load_at(6 * t + 3 * c + n / 2); if (i >= 0) af[(i >> 2) % 5][i & 3] = lda(so[i >> 2], i & 3); }
                    if (t < 12 && h == 3 && n == 3) so[t + 5] = yoff(t + 5);
                    if (t == 17 && h * 5 + n >= 13 && h * 5 + n < 18) { const int i = h * 5 + n - 13; *(LAS f32x4*)(i < 4 ? xo + i * 1024 : xo4) = u[i]; }
                    SCHED0();
                }
            }
            if ((t & 1) && t < 17) BAR();
            SCHED0();
        });
        {
            WAIT_L0(); BARX();
#pragma unroll
            for (int n = 0; n < 5; ++n) u[n] = *(const LAS f32x4*)(n < 4 ? xi + n * 1024 : xi4);
            SCHED0();
            asm volatile("s_nop 1" : "+v"(v[0]), "+v"(v[1]), "+v"(v[2]), "+v"(v[3]), "+v"(v[4]));
#pragma unroll
            for (int n = 0; n < 5; ++n) v[n] += u[n];
        }
        STAMP(13);
        if (layer) proj_store<true>(v, po);
        else { proj_store(v, po); WAIT_V(0); BARX(); }
    }
    STAMP_END(3 + 5 * layer);
    TRACE_DUMP;
  }
}

typedef short s16x4 __attribute__((ext_vector_type(4)));
DEVI u32x4 tr_frag(const LAS char* p, int four_rows) {
    const u32x2 a = __builtin_bit_cast(u32x2, __builtin_amdgcn_ds_read_tr16_b64_v4i16((LAS s16x4*)p));
    const u32x2 b = __builtin_bit_cast(u32x2, __builtin_amdgcn_ds_read_tr16_b64_v4i16((LAS s16x4*)(p + four_rows)));
    return (u32x4){a.x, a.y, b.x, b.y};
}
DEVI unsigned centre2(float a, float b, f32x2& e, float mean) {
    const f32x2 t = (f32x2){a, b} + e;
    const unsigned r = pack2(t.x, t.y);
    e = (t - (f32x2){bflo(r), bfhi(r)}) - mean;
    return r;
}
#define PREP_AUX 16
struct Retile { u32x4 f[9]; };
DEVI void retile_in_make(Retile& o, int u, int kh, const LAS char* img, int lane) {
    const int j = u % 9, c = lane & 15, q = lane >> 4, p = lane & 3;
    const LAS char* src = img + (q * 8 + (c >> 2)) * 64 + (u & 1) * 32 + (j < 8 ? (p & 1) * 16 + (p >> 1) * 8 : p * 8);
    f32x2 sum = {0.f, 0.f};
#pragma unroll
    for (int i = 0; i < 18; ++i) {
        const u32x4 f = tr_frag(src + (i < 9 ? 9 - 9 * kh + i : 9 * kh + i - 9) * 2048, 256);
        if (i >= 9) o.f[i - 9] = f;
#pragma unroll
        for (int e = 0; e < 4; ++e) sum += (f32x2){bflo(f[e]), bfhi(f[e])};
    }
    const float mean = xq_sum(sum.x + sum.y) * (1.0f / 576.0f);
    f32x2 er[2] = {{-mean, -mean}, {-mean, -mean}};
#pragma unroll
    for (int i = 0; i < 9; ++i)
#pragma unroll
        for (int e = 0; e < 4; ++e) o.f[i][e] = centre2(bflo(o.f[i][e]), bfhi(o.f[i][e]), er[e & 1], mean);
}
DEVI void retile_in_store(const Retile& o, char* twin, int u, int kh, int lane) {
    const int half = u >= 144, g = u - half * 144, cg = g / 9, j = g - cg * 9, c = lane & 15, q = lane >> 4;
    const auto rd = RSRC(twin + (size_t)cg * (18 * 18432) + (half * 18 + kh * 9) * 9216);
    const unsigned vo = (unsigned)(q * 256 + (j < 8 ? ((j & ~1) + (c >> 3)) * 1024 + ((j & 1) * 8 + (c & 7)) * 16 : 8192 + c * 16));
#pragma unroll
    for (int i = 0; i < 9; ++i) ST16(rd, vo, i * 9216, o.f[i], PREP_AUX);
}
template <int PITCH>
DEVI void retile_out_make(Retile& o, const LAS char* img, int lane) {
    const int c = lane & 15, q = lane >> 4, p = lane & 3;
    const LAS char* src = img + (q * 8 + (c >> 2)) * PITCH;
#pragma unroll
    for (int i = 0; i < 8; ++i) o.f[i] = tr_frag(src + p * 16 + (i >> 2) * 144 + ((i >> 1) & 1) * 64 + (i & 1) * 8, 4 * PITCH);
    o.f[8] = tr_frag(src + 128 + (p >> 1) * 144 + (p & 1) * 8, 4 * PITCH);
}
DEVI void retile_out_store(const Retile& o, char* dst, int nkc, int kc, int w, int lane) {
    const int c = lane & 15, q = lane >> 4;
    const auto rd = RSRC(dst + (size_t)(2 * w * nkc + kc) * 4608);
#pragma unroll
    for (int i = 0; i < 8; ++i) ST16(rd, lane * 16 + (i & 3) * 1024, (i >> 2) * nkc * 4608, o.f[i], PREP_AUX);
    ST16(rd, (c >> 3) * nkc * 4608 + 4096 + q * 128 + (c & 7) * 16, 0, o.f[8], PREP_AUX);
}

constexpr int P_CODE = 64, P_CW = 64  , P_WIN = 144, P_WOUT = 72, P_WO = 18, P_X = 64, P_Z = 32;
constexpr int P_TOTAL = P_CW + P_WIN + P_WOUT + P_WO + P_X + P_Z + P_CODE;

DEVI void prep_body(const u16* __restrict__ x, const u16* __restrict__ wqkv, const u16* __restrict__ wo, const u16* __restrict__ w_in,
                    const u16* __restrict__ w_out, char* __restrict__ ws) {
    __shared__ __attribute__((aligned(16))) char smem[37376 + 512];
    LAS char* lds = (LAS char*)smem;
    const int tid = threadIdx.x;
    int u = blockIdx.x;
    if (u < P_CW) {
        const int st = (u & ~15) + 2 * (u & 7) + ((u >> 3) & 1);
        if (st >= 54) return;
        const u16* src = wqkv + st * 32;
        u32x4 v[9];
#pragma unroll
        for (int i = 0; i < 9; ++i) v[i] = *(const u32x4*)(src + (size_t)((tid >> 2) + 64 * i) * 1728 + (tid & 3) * 8);
        const int c = tid & 31;
        const int col = st * 32 + c, part = col / 576, cc = col % 576, hh = cc / 18, dim = cc % 18, h = hh & 1;
        const int tile = dim < 16 ? 3 * h + part : 6;
        const int sl = dim < 16 ? dim : (part == 2 ? 8 + 2 * h : 4 * h + 2 * part) + (dim - 16);
        char* dst = ws + OFF_TWQKV + pin((unsigned)((hh >> 1) * (18 * 7168) + tile * 1024 + sl * 16 + (tid >> 7) * 7168 + ((tid >> 5) & 3) * 256));
        float a[8];
#pragma unroll
        for (int j = 0; j < 8; ++j) a[j] = 0.f;
#pragma unroll
        for (int i = 0; i < 9; ++i) {
            *(LAS u32x4*)(lds + ((tid >> 2) + 64 * i) * 64 + (tid & 3) * 16) = v[i];
            a[0] += bflo(v[i].x); a[1] += bfhi(v[i].x); a[2] += bflo(v[i].y); a[3] += bfhi(v[i].y);
            a[4] += bflo(v[i].z); a[5] += bfhi(v[i].z); a[6] += bflo(v[i].w); a[7] += bfhi(v[i].w);
        }
#pragma unroll
        for (int j = 0; j < 8; ++j) {
            a[j] += __builtin_amdgcn_update_dpp(0.f, a[j], 0x124, 0xf, 0xf, true);
            a[j] += __builtin_amdgcn_update_dpp(0.f, a[j], 0x128, 0xf, 0xf, true);
            a[j] = xq_sum(a[j]);
        }
        LAS float* red = (LAS float*)(lds + 36864);
        if ((tid & 63) < 4) {
#pragma unroll
            for (int j = 0; j < 8; ++j) red[(tid >> 6) * 32 + (tid & 3) * 8 + j] = a[j];
        }
        __syncthreads();
        const float mean = ((red[c] + red[32 + c]) + (red[64 + c] + red[96 + c])) * (1.0f / 576.0f);
        f32x2 er[2] = {{-mean, -mean}, {-mean, -mean}};
#pragma unroll
        for (int i = 0; i < 9; ++i) {
            const LAS u16* p = (const LAS u16*)lds + ((tid >> 5) + 8 * i) * 256 + c;
            u32x4 o;
#pragma unroll
            for (int j = 0; j < 4; ++j) o[j] = centre2(__uint_as_float((unsigned)p[j * 64] << 16), __uint_as_float((unsigned)p[j * 64 + 32] << 16), er[j & 1], mean);
            *(u32x4*)(dst + i * 14336) = o;
        }
        return;
    }
    u -= P_CW;
    if (u < P_WIN + P_WOUT) {
        const int w = __builtin_amdgcn_readfirstlane(tid >> 6), lane = tid & 63;
        const bool isin = u < P_WIN;
        const int st = (u & ~15) + 2 * (u & 7) + ((u >> 3) & 1);
        const auto rs = RSRC(isin ? (const char*)(w_in + st * 32) : (const char*)w_out + (size_t)(u - P_WIN) * 36864);
        const unsigned vo = isin ? (unsigned)((lane >> 2) * 9216 + (lane & 3) * 16) : (unsigned)lane * 16u;
        const int step = isin ? 16 * 9216 : 1024;
#pragma unroll
        for (int j = 0; j < 9; ++j) BLDS(rs, lds + (j * 4 + w) * 1024, vo, (j * 4 + w) * step, 0);
        WAIT_V(0); BAR();
        Retile rt;
        if (isin) { retile_in_make(rt, st * 2 + (w & 1), w >> 1, lds, lane); retile_in_store(rt, ws + OFF_TWIN, st * 2 + (w & 1), w >> 1, lane); }
        else { retile_out_make<1152>(rt, lds + w * 288, lane); retile_out_store(rt, ws + OFF_TWOUT, 72, u - P_WIN, w, lane); }
        return;
    }
    u -= P_WIN + P_WOUT;
    if (u < P_WO) {
        const int w = __builtin_amdgcn_readfirstlane(tid >> 6);
        const int lane = tid & 63;
        const auto rs = RSRC(wo + w * 144);
#pragma unroll
        for (int j = 0; j < 9; ++j) {
            const int i = lane + 64 * j, rl = (i * 3641) >> 16, kp = u * 32 + rl;
            const int row = kp < 512 ? 18 * (kp >> 4) + (kp & 15) : 18 * ((kp - 512) >> 1) + 16 + (kp & 1);
            BLDS(rs, lds + w * 9216 + j * 1024, (unsigned)(row * 1152 + (i - rl * 18) * 16), 0, 0);
        }
        WAIT_V(0);
        Retile rt;
        retile_out_make<288>(rt, lds + w * 9216, lane);
        retile_out_store(rt, ws + OFF_TWO, 18, u, w, lane);
        return;
    }
    u -= P_WO;
    if (u < P_X) {
        const int rt = u * 2 + (tid >> 7), r = tid & 15, cg = (tid >> 4) & 7;
        const int sg = rt * 16 + r;
        u32x4 v[9];
#pragma unroll
        for (int i = 0; i < 9; ++i) v[i] = *(const u32x4*)(x + (size_t)u * 18432 + (tid + 256 * i) * 8);
#pragma unroll
        for (int i = 0; i < 9; ++i) { const int p = tid + 256 * i, row = (p * 3641) >> 18; *(LAS u32x4*)(lds + p * 16 + row * 16) = v[i]; }
        __syncthreads();
#pragma unroll
        for (int i = 0; i < 9; ++i) v[i] = *(const LAS u32x4*)(lds + (2 * r + (tid >> 7)) * 1168 + cg * 144 + i * 16);
        float sum = 0.f;
#pragma unroll
        for (int i = 0; i < 9; ++i) {
            sum += ((bflo(v[i].x) + bfhi(v[i].x)) + (bflo(v[i].y) + bfhi(v[i].y))) + ((bflo(v[i].z) + bfhi(v[i].z)) + (bflo(v[i].w) + bfhi(v[i].w)));
            ST16(RSRC(ws + OFF_L0 + L_XA), rt * 18432 + cg * 2304 + r * 16, i * 256, v[i], 16);
        }
        float m2 = 0.f;
#pragma unroll
        for (int i = 0; i < 9; ++i) {
            float d;
            d = bflo(v[i].x); m2 += d * d; d = bfhi(v[i].x); m2 += d * d; d = bflo(v[i].y); m2 += d * d; d = bfhi(v[i].y); m2 += d * d;
            d = bflo(v[i].z); m2 += d * d; d = bfhi(v[i].z); m2 += d * d; d = bflo(v[i].w); m2 += d * d; d = bfhi(v[i].w); m2 += d * d;
        }
        *(f32x2*)(ws + OFF_L0 + L_STA + (size_t)sg * 64 + cg * 8) = (f32x2){sum, m2};
        return;
    }
    u -= P_X;
    if (u >= P_Z) {
        u -= P_Z;
        unsigned long long c0;
        asm volatile("s_getpc_b64 s[40:41]\n\ts_add_u32 s40, s40, gm_code_begin@rel32@lo+4\n\ts_addc_u32 s41, s41, gm_code_begin@rel32@hi+12\n\ts_mov_b64 %0, s[40:41]"
                     : "=s"(c0) :: "s40", "s41", "scc");
        const unsigned long long pc = __builtin_amdgcn_s_getpc();
        const unsigned len = pc > c0 && pc - c0 < 262144ull ? (unsigned)(pc - c0) & ~15u : 0u;
        const auto rs = __builtin_amdgcn_make_buffer_rsrc((void*)(c0 & ~15ull), 0, len, 0x00020000);
        unsigned x = 0;
        for (unsigned o = ((u >> 3) * 256 + tid) * 16; o < len; o += 8 * 256 * 16) x ^= __builtin_amdgcn_raw_buffer_load_b128(rs, o, 0, 0)[0];
        asm volatile("" :: "v"(x));
        return;
    }
    {
        const auto rw = RSRC(ws);
        const u32x4 ones = {0xffffffffu, 0xffffffffu, 0xffffffffu, 0xffffffffu}, zero = {0u, 0u, 0u, 0u};
#pragma unroll
        for (int i = 0; i < 4; ++i) ST16(rw, (unsigned)OFF_ZST + (u * 1024 + i * 256 + tid) * 16, 0, ones, 16);
#pragma unroll
        for (int i = 0; i < 5; ++i) ST16(rw, (unsigned)OFF_CNT + (u * 1280 + i * 256 + tid) * 16, 0, zero, 16);
        ST16(rw, (unsigned)(OFF_L0 + L_STB) + (u * 256 + tid) * 16, 0, zero, 16);
#pragma unroll
        for (int i = 0; i < 2; ++i) ST16(rw, (unsigned)(OFF_L0 + L_O) + (u * 4 + i * 2 + (tid >> 7)) * 18432 + 16384 + (tid & 127) * 16, 0, zero, 16);
        if (tid < 144) {
            const int e = u * 144 + tid, f = e >> 4;
            *(u32x4*)(ws + OFF_TWQKV + (size_t)f * 7168 + 6144 + ((e >> 2) & 3) * 256 + (12 + (e & 3)) * 16) = (u32x4){0u, 0u, 0u, 0u};
        }
    }
}

__global__ __launch_bounds__(256) void k_prep(const u16* __restrict__ x, const u16* __restrict__ wqkv, const u16* __restrict__ wo,
                                              const u16* __restrict__ w_in, const u16* __restrict__ w_out, char* __restrict__ ws DBG_ARG) {
    asm volatile("" :: "s"(x), "s"(wqkv), "s"(wo), "s"(w_in), "s"(w_out), "s"(ws));
    prep_body(x, wqkv, wo, w_in, w_out, ws);
}

}

#define STOP_CHECK(n)

extern "C" void kernel_launch(void* const* d_in, const int* in_sizes, int n_in, void* d_out, int out_size, void* d_ws, size_t ws_size,
                              hipStream_t stream) {
    (void)in_sizes; (void)n_in; (void)out_size; (void)ws_size;
    const u16* x = (const u16*)d_in[0];
    const u16* wqkv = (const u16*)d_in[1];
    const u16* wo = (const u16*)d_in[2];
    const u16* w_in = (const u16*)d_in[4];
    const u16* w_sp = (const u16*)d_in[5];
    const u16* b_sp = (const u16*)d_in[6];
    const u16* w_out = (const u16*)d_in[7];
    char* ws = (char*)d_ws;
    (void)hipFuncSetAttribute((const void*)k_net, hipFuncAttributeMaxDynamicSharedMemorySize, NET_LDS);
    k_prep<<<P_TOTAL, 256, 0, stream>>>(x, wqkv, wo, w_in, w_out, ws DBG_LAUNCH(0));
    STOP_CHECK(1);
    k_net<<<256, 512, NET_LDS, stream>>>(ws, w_sp, b_sp, (char*)d_out DBG_LAUNCH(0));
}
```

```cpp
#include <hip/hip_runtime.h>
#include <stdint.h>

namespace {

typedef unsigned short u16;
typedef short bf16x8 __attribute__((ext_vector_type(8)));
typedef float f32x4 __attribute__((ext_vector_type(4)));
typedef float f32x2 __attribute__((ext_vector_type(2)));
typedef unsigned u32x4 __attribute__((ext_vector_type(4)));
typedef unsigned u32x2 __attribute__((ext_vector_type(2)));
typedef __bf16 bf16x2_t __attribute__((ext_vector_type(2)));
#define LAS __attribute__((address_space(3)))
#define DEVI __device__ __forceinline__

constexpr int S_ = 2048, D_ = 576, FH = 2304;

constexpr size_t SZ_TWQKV = (size_t)16 * 18 * 7 * 1024;
constexpr size_t SZ_TWO = (size_t)8 * 18 * 4608;
constexpr size_t SZ_TWIN = (size_t)16 * 18 * 18 * 1024;
constexpr size_t SZ_TWOUT = (size_t)8 * 72 * 4608;
constexpr size_t SZ_X = (size_t)S_ * D_ * 2;
constexpr size_t SZ_Z = (size_t)S_ * FH * 2;
constexpr size_t SZ_ST = (size_t)S_ * 64;
constexpr size_t SZ_ZST = (size_t)16 * 16 * 128 * 8;
constexpr size_t OFF_TWQKV = 0;
constexpr size_t OFF_TWO = OFF_TWQKV + SZ_TWQKV;
constexpr size_t OFF_TWIN = OFF_TWO + SZ_TWO;
constexpr size_t OFF_TWOUT = OFF_TWIN + SZ_TWIN;
constexpr size_t OFF_CNT = OFF_TWOUT + SZ_TWOUT;
constexpr size_t SZ_CNT = 10 * 65536;
constexpr size_t OFF_ZST = OFF_CNT + SZ_CNT;
constexpr size_t OFF_L0 = OFF_ZST + 2 * SZ_ZST;
constexpr size_t L_XA = 0, L_STA = L_XA + SZ_X, L_O = L_STA + SZ_ST, L_XB = L_O + SZ_X, L_STB = L_XB + SZ_X, L_Z2T = L_STB + SZ_ST, L_Y = L_Z2T + SZ_Z;
constexpr size_t SZ_L = 0;
static_assert(OFF_L0 + L_Y + SZ_Z <= ((size_t)256 << 20), "workspace");
constexpr unsigned F_O = OFF_CNT, F_X1 = F_O + 65536, F_Z2 = F_O + 131072, F_Y = F_O + 196608, F_LAYER = 262144, F_X2 = F_O + 2 * F_LAYER;

DEVI unsigned pack2(float a, float b) { f32x2 v = {a, b}; bf16x2_t r = __builtin_convertvector(v, bf16x2_t); return __builtin_bit_cast(unsigned, r); }
DEVI float bflo(unsigned u) { return __uint_as_float(u << 16); }
DEVI float bfhi(unsigned u) { return __uint_as_float(u & 0xffff0000u); }
DEVI int slot2row(int sg) { return (sg & ~31) + 2 * (sg & 15) + ((sg >> 4) & 1); }
DEVI float fmax2(float a, float b) { return __builtin_amdgcn_fmed3f(a, b, __builtin_inff()); }
#define XQ4(OP, MID) \
    asm volatile("s_nop 1\n\tv_permlane32_swap_b32 %0, %1\n\tv_permlane32_swap_b32 %2, %3\n\t" OP " %0, %0, %1\n\t" OP " %2, %2, %3\n\t" \
                 "s_nop 1\n\tv_permlane16_swap_b32 %0, %2\n\t" OP " %0, %0, %2\n\t" MID \
                 "v_mov_b32 %2, %0\n\ts_nop 1\n\tv_permlane16_swap_b32 %0, %2\n\tv_mov_b32 %1, %0\n\tv_mov_b32 %3, %2\n\ts_nop 0\n\t" \
                 "v_permlane32_swap_b32 %0, %1\n\tv_permlane32_swap_b32 %2, %3" : "+v"(x[0]), "+v"(x[1]), "+v"(x[2]), "+v"(x[3]))
DEVI void xq4_max(float (&x)[4]) { XQ4("v_max_f32", ""); }
DEVI void xq4_sum(float (&x)[4]) { XQ4("v_add_f32", ""); }
DEVI void xq4_sum_rcp(float (&x)[4]) { XQ4("v_add_f32", "v_rcp_f32 %0, %0\n\ts_nop 0\n\t"); }
template <class T> DEVI T pin(T v) { asm volatile("" : "+v"(v)); return v; }
template <int V> struct IC { static constexpr int value = V; };
template <int N, int I = 0, class F> DEVI void static_for(F&& f) { if constexpr (I < N) { f(IC<I>{}); static_for<N, I + 1>(f); } }
DEVI int pin_s(int v) { asm volatile("" : "+s"(v)); return v; }
DEVI float xq_sum(float v) {
    auto a = __builtin_amdgcn_permlane32_swap(__float_as_uint(v), __float_as_uint(v), false, false);
    v = __uint_as_float(a[0]) + __uint_as_float(a[1]);
    auto b = __builtin_amdgcn_permlane16_swap(__float_as_uint(v), __float_as_uint(v), false, false);
    return __uint_as_float(b[0]) + __uint_as_float(b[1]);
}
#define BLDS(rs, l, vo, so, off) __builtin_amdgcn_raw_ptr_buffer_load_lds((rs), (LAS void*)(l), 16, (vo), (so), (off), 0)
#define BLDS1(rs, l, vo, so, off) __builtin_amdgcn_raw_ptr_buffer_load_lds((rs), (LAS void*)(l), 16, (vo), (so), (off), 16)
#define RSRC(p) __builtin_amdgcn_make_buffer_rsrc((void*)(p), 0, 0x7fffffff, 0x00020000)
#define ST16(rs, vo, so, v, aux) __builtin_amdgcn_raw_buffer_store_b128((v), (rs), (vo) + (so), 0, aux)
#define ST8(rs, vo, so, v, aux) __builtin_amdgcn_raw_buffer_store_b64((v), (rs), (vo), (so), aux)
#define WAIT_V(n) asm volatile("s_waitcnt vmcnt(%0)" ::"i"(n) : "memory")
#define WAIT_L0() asm volatile("s_waitcnt lgkmcnt(0)" ::: "memory")
#define BAR() do { asm volatile("" ::: "memory"); __builtin_amdgcn_s_barrier(); asm volatile("" ::: "memory"); } while (0)
#define BARX() do { __builtin_amdgcn_sched_barrier(0); BAR(); __builtin_amdgcn_sched_barrier(0); } while (0)
#define LOOP_ALIGN() asm volatile(".p2align 6")
#define SCHED0() __builtin_amdgcn_sched_barrier(0)
#define MFMA(a, b, c) __builtin_amdgcn_mfma_f32_16x16x32_bf16((a), (b), (c), 0, 0, 0)
#define MFMA_T(a, b, c) asm volatile("v_mfma_f32_16x16x32_bf16 %0, %1, %2, %0" : "+v"(c) : "v"(a), "v"(b))
#define MFMA_0(a, b, c) asm volatile("v_mfma_f32_16x16x32_bf16 %0, %1, %2, 0" : "=&v"(c) : "v"(a), "v"(b))
#define WAIT_L(n) __builtin_amdgcn_s_waitcnt(0xC07F | ((n) << 8))
#define LDS_FRAG(p) (*(const LAS bf16x8*)(p))
DEVI int lane_id_fresh() { int z = 0; asm volatile("" : "+v"(z)); return __builtin_amdgcn_mbcnt_hi(~0u, __builtin_amdgcn_mbcnt_lo(~0u, z)); }
DEVI const LAS char* opaque(const LAS char* p) { asm volatile("" : "+v"(p)); return p; }
#define DBG_ARG
#define DBG_LAUNCH(n)
#define STAMP(i)
#define STAMP_DECL
#define STAMP_BEGIN
#define STAMP_END(n)
#define STAMP_PARAM
#define STAMP_FWD

#define TRACE_ATTR
#define TRACE_BEGIN
#define TRACE_DUMP

DEVI float add1(float a, float b) { float r; asm("v_add_f32 %0, %1, %2" : "=v"(r) : "v"(a), "v"(b)); return r; }
DEVI float rstd_of(float s, float q, float inv_n, float& mean) {
    float var;
    asm("v_mul_f32 %0, %2, %3\n\tv_mul_f32 %1, %2, %4\n\tv_fma_f32 %1, -%0, %0, %1" : "=&v"(mean), "=&v"(var) : "s"(inv_n), "v"(s), "v"(q));
    return __builtin_amdgcn_rsqf(fmax2(var, 0.f) + 1e-5f);
}
DEVI float ln_rstd(const f32x4 p) {
    float s, q, mean;
    asm("v_add_f32 %0, %2, %4\n\tv_add_f32 %1, %3, %5\n\ts_nop 0\n\t"
        "v_add_f32_dpp %0, %0, %0 quad_perm:[1,0,3,2] row_mask:0xf bank_mask:0xf\n\tv_add_f32_dpp %1, %1, %1 quad_perm:[1,0,3,2] row_mask:0xf bank_mask:0xf\n\ts_nop 0\n\t"
        "v_add_f32_dpp %0, %0, %0 quad_perm:[2,3,0,1] row_mask:0xf bank_mask:0xf\n\tv_add_f32_dpp %1, %1, %1 quad_perm:[2,3,0,1] row_mask:0xf bank_mask:0xf"
        : "=&v"(s), "=&v"(q) : "v"(p[0]), "v"(p[1]), "v"(p[2]), "v"(p[3]));
    return rstd_of(s, q, 1.0f / 576.0f, mean);
}

struct Res { u32x4 a, c; u32x2 e; };
DEVI Res proj_residual_load(const char* xin, int rt, int cg, int lane) {
    const auto rs = RSRC(xin + (size_t)rt * 18432 + cg * 2304);
    Res x;
    x.a = __builtin_amdgcn_raw_buffer_load_b128(rs, lane * 16, 0, 16); x.c = __builtin_amdgcn_raw_buffer_load_b128(rs, lane * 16 + 1024, 0, 16);
    x.e = __builtin_amdgcn_raw_buffer_load_b64(rs, 2048 + (lane & 15) * 16 + ((lane >> 4) & 1) * 8, 0, 16);
    return x;
}
DEVI void proj_residual(f32x4 (&v)[5], const Res& x) {
    v[0] = (f32x4){bflo(x.a.x), bfhi(x.a.x), bflo(x.a.y), bfhi(x.a.y)}; v[1] = (f32x4){bflo(x.a.z), bfhi(x.a.z), bflo(x.a.w), bfhi(x.a.w)};
    v[2] = (f32x4){bflo(x.c.x), bfhi(x.c.x), bflo(x.c.y), bfhi(x.c.y)}; v[3] = (f32x4){bflo(x.c.z), bfhi(x.c.z), bflo(x.c.w), bfhi(x.c.w)};
    v[4] = (f32x4){bflo(x.e.x), bfhi(x.e.x), bflo(x.e.y), bfhi(x.e.y)};
}
struct ProjOut { __amdgpu_buffer_rsrc_t x, st; unsigned v0, v1, v2, vs; };
DEVI ProjOut proj_out(char* xout, char* stout, int rt, int cg, int lane) {
    const int r = lane & 15, q = lane >> 4;
    ProjOut p = {RSRC(xout + (size_t)rt * 18432 + cg * 2304), RSRC(stout + (size_t)rt * 1024 + cg * 8),
                 pin((unsigned)lane * 16u), pin((unsigned)lane * 16u + 1024u), pin((unsigned)(2048 + r * 16 + (q & 1) * 8)), pin((unsigned)(r * 64 + (q >> 1) * 4))};
    asm volatile("" :: "s"(p.x), "s"(p.st));
    return p;
}
DEVI ProjOut proj_out_final(char* out, int rt, int cg, int lane) {
    const int q = lane >> 4;
    const unsigned o = (unsigned)(slot2row(rt * 16 + (lane & 15)) * 1152 + cg * 144);
    ProjOut p = {RSRC(out), RSRC(out), pin(o + q * 16), pin(o + 64 + q * 16), pin(o + 128 + (q & 1) * 8), 0u};
    return p;
}
template <bool FINAL = false>
DEVI void proj_store(f32x4 (&v)[5], const ProjOut& p) {
    ST16(p.x, p.v0, 0, ((u32x4){pack2(v[0][0], v[0][1]), pack2(v[0][2], v[0][3]), pack2(v[1][0], v[1][1]), pack2(v[1][2], v[1][3])}), FINAL ? 0 : 16);
    SCHED0();
    asm volatile("" : "+v"(v[2]), "+v"(v[3]), "+v"(v[4]));
    ST16(p.x, p.v1, 0, ((u32x4){pack2(v[2][0], v[2][1]), pack2(v[2][2], v[2][3]), pack2(v[3][0], v[3][1]), pack2(v[3][2], v[3][3])}), FINAL ? 0 : 16);
    SCHED0();
    ST8(p.x, p.v2, 0, ((u32x2){pack2(v[4][0], v[4][1]), pack2(v[4][2], v[4][3])}), FINAL ? 0 : 16);
    SCHED0();
    if (!FINAL) {
        const f32x4 s4 = ((v[0] + v[1]) + (v[2] + v[3])) + v[4] * 0.5f;
        const f32x4 q4 = ((v[0] * v[0] + v[1] * v[1]) + (v[2] * v[2] + v[3] * v[3])) + v[4] * v[4] * 0.5f;
        auto x = __builtin_amdgcn_permlane32_swap(__float_as_uint((s4[0] + s4[2]) + (s4[1] + s4[3])), __float_as_uint((q4[0] + q4[2]) + (q4[1] + q4[3])), false, false);
        const float t = __uint_as_float(x[0]) + __uint_as_float(x[1]);
        auto y = __builtin_amdgcn_permlane16_swap(__float_as_uint(t), __float_as_uint(t), false, false);
        __builtin_amdgcn_raw_buffer_store_b32(__float_as_uint(__uint_as_float(y[0]) + __uint_as_float(y[1])), p.st, p.vs, 0, 16);
        SCHED0();
    }
}

DEVI void side_load(f32x4 (&d)[3], const char* p, const char* p2) {
    asm volatile("global_load_dwordx4 %0, %3, off sc1\n\tglobal_load_dwordx4 %1, %3, off offset:1024 sc1\n\tglobal_load_dwordx4 %2, %4, off sc1"
                 : "=&v"(d[0]), "=&v"(d[1]), "=&v"(d[2]) : "v"(p), "v"(p2) : "memory");
}
DEVI void side_fence(f32x4 (&d)[3]) { asm volatile("" : "+v"(d[0]), "+v"(d[1]), "+v"(d[2])); }
DEVI void lds_word_set(LAS char* p, unsigned v) { asm volatile("ds_write_b32 %0, %1" :: "v"((unsigned)(size_t)p), "v"(v) : "memory"); }
#define DMA_I(v, off, sc) "buffer_load_dwordx4 " v ", %1, %2 offen offset:" off sc " lds\n\t"
#define DMA5(sc) asm volatile("s_mov_b32 m0, %0\n\ts_mov_b32 exec_hi, 0\n\ts_nop 2\n\t" DMA_I("%3", "0", sc) "s_mov_b32 exec_hi, -1\n\t" \
    DMA_I("%3", "512", sc) DMA_I("%3", "1536", sc) DMA_I("%3", "2560", sc) DMA_I("%3", "3584", sc) :: "s"(la), "s"(rs), "s"(so), "v"(vo) : "m0", "memory")
#define DMA4(sc) asm volatile("s_mov_b32 m0, %0\n\ts_nop 3\n\t" DMA_I("%3", "0", sc) DMA_I("%4", "1024", sc) DMA_I("%5", "2048", sc) DMA_I("%6", "3072", sc) \
    :: "s"(la), "s"(rs), "s"(so), "v"(v0), "v"(v1), "v"(v2), "v"(v3) : "m0", "memory")
template <bool SC1> DEVI void dma5(unsigned la, __amdgpu_buffer_rsrc_t rs, int so, unsigned vo) { if (SC1) DMA5(" sc1"); else DMA5(""); }
template <bool SC1> DEVI void dma4(unsigned la, __amdgpu_buffer_rsrc_t rs, int so, unsigned v0, unsigned v1, unsigned v2, unsigned v3) { if (SC1) DMA4(" sc1"); else DMA4(""); }
#define WAIT_IF(c, a, b) do { if (c) WAIT_V(a); else WAIT_V(b); } while (0)
template <int T0, int T1, int P, int S0 = 0, class ISSUE>
DEVI void ring_steps(ISSUE&& issue) {
    int slot = (T0 + S0) % 3;
    LOOP_ALIGN();
#pragma unroll 1
    for (int t = T0; t < T1; ++t) {
        WAIT_V(P); BAR();
        issue(t + 3, slot);
        slot = slot == 2 ? 0 : slot + 1;
    }
}

DEVI unsigned flag_out(unsigned use, int pitch, int group, int writer, int lane) { return pin(lane < 16 ? use + (group * 16 + lane) * pitch + writer * 16 : 0x80000000u); }
DEVI unsigned flag_in(unsigned use, int pitch, int reader, int n, int lane) { return pin(use + reader * pitch + (lane < n ? lane : 0) * 16); }
DEVI void flag_raise(const char* ws, unsigned vo) { ST16(RSRC(ws), vo, 0, ((u32x4){1u, 1u, 1u, 1u}), 16); SCHED0(); }
template <int FIRST>
DEVI void flags_wait(const char* ws, unsigned vo) {
    unsigned v, n = 400000u;
    asm volatile("s_nop 4\n\ts_sleep %4\n"
                 "1:\n\tbuffer_load_dword %0, %2, %3, 0 offen sc1\n\ts_waitcnt vmcnt(0)\n\tv_cmp_eq_u32 vcc, 0, %0\n\ts_cbranch_vccz 2f\n\t"
                 "s_sleep 1\n\ts_sub_u32 %1, %1, 1\n\ts_cbranch_scc0 1b\n2:"
                 : "=&v"(v), "+s"(n) : "v"(vo), "s"(RSRC(ws)), "i"(FIRST) : "vcc", "scc", "memory");
    SCHED0();
}
DEVI void lds_word_wait(LAS char* p) {
#pragma unroll 1
    for (unsigned spin = 0; spin < 40000000u; ++spin) {
        unsigned v;
        asm volatile("ds_read_b32 %0, %1\n\ts_waitcnt lgkmcnt(0)" : "=v"(v) : "v"((unsigned)(size_t)p) : "memory");
        if (__builtin_amdgcn_readfirstlane(v) != 0u) break;
        __builtin_amdgcn_s_sleep(1);
    }
}

constexpr int K1_STAGE = 32768;
constexpr int WO_OFF = 69632;
constexpr int G_OWN = 139264;
constexpr int G_PART = G_OWN + 18432;
constexpr int G_TAB = G_PART + 2048;
constexpr int K1_SCR = G_TAB + 1536;
constexpr int G_WORD = K1_SCR + 576;
constexpr int NET_LDS = G_WORD + 64;

#define WO_PRE 12
#define WO_W 33
#define REL_PRIO 0
#define WSP_PF 1
#define O_FIRST 10
#define X1_FIRST 6
#define X2_FIRST 6
#define Y_FIRST 10
#define Z2_FIRST 10
DEVI void attn_issueB(LAS char* lds, const char* ws, int hp, int wid, int lane, int s, int slot) {
    const auto rB = RSRC(ws + OFF_TWQKV + (size_t)hp * (18 * 7168));
    LAS char* sl = lds + slot * K1_STAGE + 18432 + wid * 4096;
    const int off = wid * 4096 + s * 14336;
    BLDS(rB, sl, lane * 16, off, 0); BLDS(rB, sl, lane * 16, off, 1024);
    if (wid < 3) { BLDS(rB, sl, lane * 16, off, 2048); BLDS(rB, sl, lane * 16, off, 3072); }
}
template <class IDLE, class NEXT>
DEVI void attn_producer(LAS char* lds, const char* ws, const char* wl, unsigned fl, int layer, int pm, int hp, int wid, IDLE&& idle8, NEXT&& next) {
    const int lane = lane_id_fresh(), r = lane & 15, q = lane >> 4;
    const int pmh = pm > 0 ? pm : 1;
    const auto rA = RSRC(wl + L_XA);
    const unsigned vo = lane * 16, voh = (unsigned)((r >> 3) * 18432 + q * 256 + (8 + (r & 7)) * 16);
    const int oA = (pm * 8 + 2 * wid) * 18432, oh = (pmh * 8 - 2) * 18432;
    auto issueA = [&](int s, int slot) {
        LAS char* sl = lds + slot * K1_STAGE;
        if (wid == 3) { BLDS1(rA, sl, voh, oh + s * 2048, 0); BLDS1(rA, sl, voh, oh + s * 2048, 1024); }
        BLDS1(rA, sl + 2048 + wid * 4096, vo, oA + s * 2048, 0); BLDS1(rA, sl + 2048 + wid * 4096, vo, oA + s * 2048, 1024);
        BLDS1(rA, sl + 4096 + wid * 4096, vo, oA + s * 2048 + 18432, 0); BLDS1(rA, sl + 4096 + wid * 4096, vo, oA + s * 2048 + 18432, 1024);
    };
    auto issue = [&](int s, int slot) { issueA(s, slot); attn_issueB(lds, ws, hp, wid, lane, s, slot); };
    __builtin_amdgcn_s_setprio(3);
    if (layer == 0) { issue(0, 0); issue(1, 1); }
    const int hr = lane >> 2;
    const char* ps = pin(wl + L_STA + (size_t)(pm * 128 + wid * 32) * 64 + lane * 16);
    const char* psh = pin(wl + L_STA + (size_t)((pmh * 8 - 2 + (hr >> 3)) * 16 + 8 + (hr & 7)) * 64 + (lane & 3) * 16);
    if (layer == 0) {
        WAIT_V(8);
    } else {
        attn_issueB(lds, ws, hp, wid, lane, 2, 2);
        const unsigned fi = flag_in(F_X2, 512, pm * 16 + hp, pm > 0 ? 24 : 16, lane);
        if (wid == 0) flags_wait<X2_FIRST>(ws, fi);
        BARX();
        issueA(0, 0); issueA(1, 1); issueA(2, 2);
        WAIT_IF(wid == 3, 12, 8);
    }
    BAR();
    __builtin_amdgcn_s_setprio(0);
    if (layer == 0) issue(2, 2); else WAIT_IF(wid == 3, 6, 4);
    ring_steps<0, 6, 8>(issue);
    {
        f32x4 sp[3];
        side_load(sp, ps, psh);
        WAIT_V(11); BAR();
        if (WSP_PF && layer == 0) { idle8(); WAIT_V(8); } else WAIT_V(0);
        BARX();
        side_fence(sp);
#pragma unroll
        for (int i = 0; i < 3; ++i) lds_word_set(lds + K1_SCR + (i < 2 ? 16 + wid * 32 + i * 16 + hr : hr) * 4, __float_as_uint(ln_rstd(sp[i])));
        WAIT_L0();
    }
    BAR();
    const auto rW = RSRC(ws + OFF_TWO + (size_t)(hp >> 1) * 82944);
#pragma unroll
    for (int i = 0; i < 5; ++i) {
        const int off = (i * 4 + wid) * 4096;
        LAS char* d = lds + WO_OFF + off;
        BLDS(rW, d, vo, off, 0); BLDS(rW, d, vo, off, 1024); BLDS(rW, d, vo, off, 2048); BLDS(rW, d, vo, off, 3072);
    }
    if (wid == 0) BLDS(rW, lds + WO_OFF + 81920, vo, 81920, 0);
    next();
    const unsigned fo = flag_out(fl + F_O, 256, pm, hp, lane), fi = flag_in(fl + F_O, 256, pm * 16 + hp, 16, lane);
    WAIT_V(0);
    if (wid == 0) __builtin_amdgcn_s_setprio(3);
    BARX();
    if (wid == 0) { flag_raise(ws, fo); flags_wait<O_FIRST>(ws, fi); }
    __builtin_amdgcn_s_setprio(0);
    BARX();
}

#define STAMP_SEC 0
DEVI void attn_consumer(LAS char* lds, char* wl, int layer, int pm, int hp, int wid STAMP_PARAM) {
    const int lane = lane_id_fresh(), r = lane & 15, q = lane >> 4;
    const char* xa = wl + L_XA;
    char* o = wl + L_O;
    const int T = wid & 1, h = wid >> 1;
    const LAS char* ph = lds + q * 256 + ((r ^ (T ? 0 : 8)) * 16);
    const LAS char* pa = lds + T * 2048 + lane * 16;
    const LAS char* pb = lds + h * 3072 + lane * 16;
    const LAS char* pt = lds + lane * 16;
    f32x4 acc[5][4];
#pragma unroll
    for (int m = 0; m < 5; ++m)
#pragma unroll
        for (int n = 0; n < 4; ++n) if (m + n) acc[m][n] = pin((f32x4){0.f, 0.f, 0.f, 0.f});
    bf16x8 fa[2][5], fb[2][4];
    auto rd1 = [&](int kk, int i) {
        if (i < 3) fb[kk][i] = LDS_FRAG(pb + 18432 + kk * 7168 + i * 1024);
        else if (i == 3) fb[kk][3] = LDS_FRAG(pt + 24576 + kk * 7168);
        else if (i == 4) fa[kk][0] = LDS_FRAG(ph + kk * 1024);
        else if (i < 9) fa[kk][i - 4] = LDS_FRAG(pa + 2048 + kk * 1024 + (i - 5) * 4096);
    };
    auto chunk = [&](int kk, int d) {
#pragma unroll
        for (int i = 1; i < 20; ++i) {
            const int m = i >> 2, n = i & 3;
            const bf16x8 &A = fa[kk][m], &B = fb[kk][n];
            acc[m][n] = n != 2 ? MFMA(B, A, acc[m][n]) : MFMA(A, B, acc[m][n]);
            SCHED0();
            if (i <= 9) rd1(kk ^ 1, i - 1);
            if (kk == 0) { if (i == 11) ph = opaque(ph + d); if (i == 12) pa = opaque(pa + d); if (i == 13) pb = opaque(pb + d); if (i == 14) pt = opaque(pt + d); }
            SCHED0();
        }
    };
    f32x4 mb, mb0;
#pragma unroll
    for (int j = 0; j < 4; ++j) {
        const bool ok = ((r - 4 * q - j) & 15) < 8;
        mb[j] = ok ? 0.f : -__builtin_inff();
        mb0[j] = (ok && (pm > 0 || 4 * q + j <= r)) ? 0.f : -__builtin_inff();
    }
    mb = pin(mb); mb0 = pin(mb0);
    const unsigned mp0 = pin((4 * q > r ? 0xffffu : 0u) | (4 * q + 1 > r ? 0xffff0000u : 0u)), mp1 = pin((4 * q + 2 > r ? 0xffffu : 0u) | (4 * q + 3 > r ? 0xffff0000u : 0u));
    const unsigned qtm = pin(q == h ? 0xffffffffu : 0u);
    const unsigned vom = pin((unsigned)(r * 16 + hp * 1024 + (2 * h + (q >> 1)) * 256 + (q & 1) * 8)), vot = pin((unsigned)(r * 16 + 16384 + (hp >> 1) * 256 + (hp & 1) * 8 + h * 4));
    const LAS char* tk0 = opaque(lds + K1_SCR + ((q * 4) ^ (T ? 0 : 8)) * 4);
    const LAS char* tk = opaque(lds + K1_SCR + ((1 + T) * 16 + q * 4) * 4);
    const LAS char* tq = opaque(lds + K1_SCR + ((1 + T) * 16 + r) * 4);
    ph = opaque(ph); pa = opaque(pa); pb = opaque(pb); pt = opaque(pt);
    STAMP(2);
    if (layer) BAR();
    BARX();
    STAMP(3);
#pragma unroll
    for (int i = 0; i < 9; ++i) rd1(0, i);
    SCHED0();
    const int rt = pm * 8 + (hp & 1) * 4 + wid, cg = hp >> 1;
    const Res rx = proj_residual_load(xa, rt, cg, lane);
    SCHED0();
    int slot = 0;
    LOOP_ALIGN();
#pragma unroll 1
    for (int t = 0; t < 9; ++t) {
        const int d = slot == 2 ? -2 * K1_STAGE : K1_STAGE;
        slot = slot == 2 ? 0 : slot + 1;
        SCHED0();
        chunk(0, d);
        WAIT_L0(); BAR();
        SCHED0();
        chunk(1, 0);
    }
    STAMP(4);
    u32x2 ts[5], tf[5];
    {
        LAS char* xb = lds + wid * 320;
#pragma unroll
        for (int m = 0; m < 5; ++m) ts[m] = (u32x2){pack2(acc[m][3][0], acc[m][3][1]), pack2(acc[m][3][2], acc[m][3][3])};
        if (q == 2) {
#pragma unroll
            for (int m = 0; m < 5; ++m) {
                const unsigned w = h ? ts[m].y : ts[m].x;
                *(LAS u16*)(xb + m * 64 + r * 2) = (u16)w; *(LAS u16*)(xb + m * 64 + 32 + r * 2) = (u16)(w >> 16);
            }
        }
        asm volatile("" ::: "memory");
#pragma unroll
        for (int m = 0; m < 5; ++m) tf[m] = *(const LAS u32x2*)(xb + m * 64 + (r & 1) * 32 + q * 8);
    }
    {
        f32x4 rk[5]; float rq[4];
        rk[0] = *(const LAS f32x4*)(tk0);
#pragma unroll
        for (int g = 0; g < 4; ++g) { rk[g + 1] = *(const LAS f32x4*)(tk + g * 128); rq[g] = *(const LAS float*)(tq + g * 128); }
        const f32x4 zero = {0.f, 0.f, 0.f, 0.f};
        u32x4 kf[5];
#pragma unroll
        for (int m = 0; m < 5; ++m) kf[m] = (u32x4){pack2(acc[m][1][0], acc[m][1][1]), pack2(acc[m][1][2], acc[m][1][3]), ts[m].y, 0u};
        f32x4 sp[4], sc[4];
#pragma unroll
        for (int g = 0; g < 4; ++g) {
            const u32x4 qf = {pack2(acc[g + 1][0][0], acc[g + 1][0][1]), pack2(acc[g + 1][0][2], acc[g + 1][0][3]), ts[g + 1].x & qtm, 0u};
            sp[g] = MFMA(__builtin_bit_cast(bf16x8, kf[g]), __builtin_bit_cast(bf16x8, qf), zero);
            sc[g] = MFMA(__builtin_bit_cast(bf16x8, kf[g + 1]), __builtin_bit_cast(bf16x8, qf), zero);
        }
        u32x2 vf[5];
#pragma unroll
        for (int m = 0; m < 5; ++m) vf[m] = (u32x2){pack2(acc[m][2][0], acc[m][2][1]), pack2(acc[m][2][2], acc[m][2][3])};
        f32x4 rs[4], t[4]; float mx[4], sm[4];
#pragma unroll
        for (int g = 0; g < 4; ++g) {
#pragma unroll
            for (int j = 0; j < 4; ++j) { const bool pv = 4 * q + j > r; rs[g][j] = pv ? rk[g][j] : rk[g + 1][j]; t[g][j] = pv ? sp[g][j] : sc[g][j]; }
            t[g] = (t[g] * rs[g]) * (rq[g] * (0.23570226039551584f * 1.4426950408889634f)) + (g == 0 ? mb0 : mb);
            mx[g] = fmaxf(fmaxf(t[g][0], t[g][1]), fmaxf(t[g][2], t[g][3]));
        }
        xq4_max(mx);
#pragma unroll
        for (int g = 0; g < 4; ++g) {
            t[g] = t[g] - mx[g];
#pragma unroll
            for (int j = 0; j < 4; ++j) t[g][j] = __builtin_amdgcn_exp2f(t[g][j]);
            sm[g] = (t[g][0] + t[g][2]) + (t[g][1] + t[g][3]);
        }
        f32x4 om[4], ot[4];
#pragma unroll
        for (int g = 0; g < 4; ++g) {
            const f32x4 p = t[g] * rs[g];
            const unsigned p01 = pack2(p[0], p[1]), p23 = pack2(p[2], p[3]);
            const u32x4 pf = {p01 & mp0, p23 & mp1, p01 & ~mp0, p23 & ~mp1};
            om[g] = MFMA(__builtin_bit_cast(bf16x8, ((u32x4){vf[g].x, vf[g].y, vf[g + 1].x, vf[g + 1].y})), __builtin_bit_cast(bf16x8, pf), zero);
            ot[g] = MFMA(__builtin_bit_cast(bf16x8, ((u32x4){tf[g].x, tf[g].y, tf[g + 1].x, tf[g + 1].y})), __builtin_bit_cast(bf16x8, pf), zero);
        }
        xq4_sum_rcp(sm);
        const auto ro = RSRC(o + (size_t)(pm * 8 + T) * 18432);
        unsigned tw2[4];
#pragma unroll
        for (int g = 0; g < 4; ++g) {
            const f32x4 v = om[g] * sm[g];
            __builtin_amdgcn_raw_buffer_store_b64((u32x2){pack2(v[0], v[1]), pack2(v[2], v[3])}, ro, vom, g * 36864, 16);
            const f32x2 w = ot[g].xy * sm[g];
            tw2[g] = pack2(w.x, w.y);
        }
        if (q == 2) {
#pragma unroll
            for (int g = 0; g < 4; ++g) __builtin_amdgcn_raw_buffer_store_b32(tw2[g], ro, vot, g * 36864, 16);
        }
    }
    STAMP(5);
    {
        WAIT_V(0); BARX();
        STAMP(6);
        f32x4 v[5];
        proj_residual(v, rx);
        const LAS char* pw = opaque(lds + WO_OFF + lane * 16);
        const LAS char* pw2 = opaque(lds + WO_OFF + 41472 + lane * 16);
        const LAS char* ph2 = opaque(lds + WO_OFF + 4096 + q * 128 + (r & 7) * 16);
        const LAS char* ph3 = opaque(lds + WO_OFF + 41472 + 4096 + q * 128 + (r & 7) * 16);
        bf16x8 wf[90];
        auto rdw = [&](int i) {
            const int kc = i / 5, n = i % 5;
            wf[i] = n < 4 ? LDS_FRAG((kc < 9 ? pw : pw2) + (kc % 9) * 4608 + n * 1024) : LDS_FRAG((kc < 9 ? ph2 : ph3) + (kc % 9) * 4608);
        };
#pragma unroll
        for (int i = 0; i < WO_W; ++i) rdw(i);
#pragma unroll
        for (int n = 0; n < 5; ++n) v[n] = pin(v[n]);
        const auto rO = RSRC(o + (size_t)rt * 18432);
        const ProjOut po = proj_out(wl + L_XB, wl + L_STB, rt, cg, lane);
        asm volatile("" :: "s"(rO));
        WAIT_L(0);
        __builtin_amdgcn_s_setprio(REL_PRIO);
        BARX();
        STAMP(7);
        bf16x8 af[18];
        auto ldo = [&](int kc) { af[kc] = __builtin_bit_cast(bf16x8, __builtin_amdgcn_raw_buffer_load_b128(rO, lane * 16 + (kc & 3) * 1024, (kc >> 2) * 4096, 16)); };
#pragma unroll
        for (int kc = 0; kc < WO_PRE; ++kc) { ldo(kc); SCHED0(); }
        static_for<90>([&](auto ic) {
            constexpr int i = ic.value, kc = i / 5, n = i % 5, j = WO_W + 2 * (i / 3) + i % 3;
            MFMA_T(wf[i], af[kc], v[n]);
            SCHED0();
            if (i % 3 < 2 && j < 90) rdw(j);
            if (i % 3 == 2 && (i / 3) % 2 == 0 && i / 6 + WO_PRE < 18) ldo(i / 6 + WO_PRE);
            SCHED0();
        });
        asm volatile("s_nop 4" : "+v"(v[0]), "+v"(v[1]));
        STAMP(8);
        proj_store(v, po);
        __builtin_amdgcn_s_setprio(0);
    }
}

DEVI f32x2 gelu2(f32x2 v) {
    const f32x2 w = v * (v * v * -0.10294324f + -2.3022082f);
    const f32x2 d = (f32x2){__builtin_amdgcn_exp2f(w.x), __builtin_amdgcn_exp2f(w.y)} + 1.0f;
    return v * (f32x2){__builtin_amdgcn_rcpf(d.x), __builtin_amdgcn_rcpf(d.y)};
}
DEVI f32x4 gelu4(f32x4 v) { const f32x2 a = gelu2(v.xy), b = gelu2(v.zw); return (f32x4){a.x, a.y, b.x, b.y}; }
DEVI void rowsum16x8(const f32x4 a, const f32x4 b, float& o0, float& o1) {
    float t0, t1, t2, t3;
    asm volatile(
        "s_nop 1\n\t"
        "v_add_f32_dpp %2, %6, %6 row_ror:8 row_mask:0xf bank_mask:0x3\n\t"
        "v_add_f32_dpp %3, %7, %7 row_ror:8 row_mask:0xf bank_mask:0x3\n\t"
        "v_add_f32_dpp %4, %10, %10 row_ror:8 row_mask:0xf bank_mask:0x3\n\t"
        "v_add_f32_dpp %5, %11, %11 row_ror:8 row_mask:0xf bank_mask:0x3\n\t"
        "v_add_f32_dpp %2, %8, %8 row_ror:8 row_mask:0xf bank_mask:0xc\n\t"
        "v_add_f32_dpp %3, %9, %9 row_ror:8 row_mask:0xf bank_mask:0xc\n\t"
        "v_add_f32_dpp %4, %12, %12 row_ror:8 row_mask:0xf bank_mask:0xc\n\t"
        "v_add_f32_dpp %5, %13, %13 row_ror:8 row_mask:0xf bank_mask:0xc\n\t"
        "v_add_f32_dpp %0, %2, %2 row_half_mirror row_mask:0xf bank_mask:0x5\n\t"
        "s_nop 0\n\t"
        "v_add_f32_dpp %1, %4, %4 row_half_mirror row_mask:0xf bank_mask:0x5\n\t"
        "v_add_f32_dpp %0, %3, %3 row_half_mirror row_mask:0xf bank_mask:0xa\n\t"
        "v_add_f32_dpp %1, %5, %5 row_half_mirror row_mask:0xf bank_mask:0xa\n\t"
        "s_nop 0\n\t"
        "v_add_f32_dpp %0, %0, %0 quad_perm:[1,0,3,2] row_mask:0xf bank_mask:0xf\n\t"
        "v_add_f32_dpp %1, %1, %1 quad_perm:[1,0,3,2] row_mask:0xf bank_mask:0xf\n\t"
        "s_nop 0\n\t"
        "v_add_f32_dpp %0, %0, %0 quad_perm:[2,3,0,1] row_mask:0xf bank_mask:0xf\n\t"
        "v_add_f32_dpp %1, %1, %1 quad_perm:[2,3,0,1] row_mask:0xf bank_mask:0xf\n\t"
        : "=&v"(o0), "=&v"(o1), "=&v"(t0), "=&v"(t1), "=&v"(t2), "=&v"(t3)
        : "v"(a[0]), "v"(a[1]), "v"(a[2]), "v"(a[3]), "v"(b[0]), "v"(b[1]), "v"(b[2]), "v"(b[3]));
}

#define K5_PRE 8
#define RH_MAP 0
#define NET_LAYERS 2
constexpr int G_STAGE = 34816;
constexpr int K5_STAGE = 18432, K5_NS = 8;

constexpr int y_load_at(int slot) {
    int next = K5_PRE;
    for (int s = 0;; ++s) { const bool go = next < 18 + 2 * (s / 3); if (s == slot) return go ? next : -1; next += go; }
}

struct GTile { f32x4 a[4][4], e[2]; };
struct GFrag { bf16x8 a[2][4], b[2][5]; const LAS char *pa[2][2], *pb, *p8; int slot; };
DEVI void g_rd(GFrag& f, int kk, int i) {
    if (i < 4) f.a[kk][i] = LDS_FRAG(f.pa[kk][i >> 1] + (i & 1) * 2048);
    else if (i < 8) f.b[kk][i - 4] = LDS_FRAG(f.pb + (kk * 9 + i - 4) * 1024);
    else f.b[kk][4] = LDS_FRAG(f.p8 + kk * 9216);
}
template <bool ROWS_IN_REGS>
DEVI void g_chunk(GTile& c, GFrag& f, int kk, int d) {
#pragma unroll
    for (int i = 0; i < 18; ++i) {
        const int n = i >> 2, m = i & 3;
        f32x4& e = n < 4 ? c.a[m][n] : c.e[m & 1];
        e = ROWS_IN_REGS ? MFMA(f.a[kk][m], f.b[kk][n], e) : MFMA(f.b[kk][n], f.a[kk][m], e);
        SCHED0();
        if (i < 9) g_rd(f, kk ^ 1, i);
        if (kk == 0) {
            if (i >= 10 && i < 14) f.pa[(i - 10) >> 1][i & 1] = opaque(f.pa[(i - 10) >> 1][i & 1] + d);
            if (i == 14) f.pb = opaque(f.pb + d);
            if (i == 15) f.p8 = opaque(f.p8 + d);
        }
        SCHED0();
    }
}
template <int NS, bool ROWS_IN_REGS>
DEVI void g_steps(GTile& c, GFrag& f, int n) {
    LOOP_ALIGN();
#pragma unroll 1
    for (int t = 0; t < n; ++t) {
        const int d = NS > 0 ? (f.slot == NS - 1 ? -(NS - 1) * G_STAGE : G_STAGE) : (f.slot == 0 ? -(NS + 1) * G_STAGE : -G_STAGE);
        f.slot = NS > 0 ? (f.slot == NS - 1 ? 0 : f.slot + 1) : (f.slot == 0 ? -(NS + 1) : f.slot - 1);
        SCHED0();
        g_chunk<ROWS_IN_REGS>(c, f, 0, d);
        WAIT_L0();
        BAR();
        SCHED0();
        g_chunk<ROWS_IN_REGS>(c, f, 1, 0);
    }
}
DEVI void keep4(const f32x4 (&c)[4]) { asm volatile("" :: "v"(c[0]), "v"(c[1]), "v"(c[2]), "v"(c[3])); }
template <int D, int DB, bool FIRST, class HOOK>
DEVI void g_step_u(GTile& c, GFrag& f, const f32x4 (&c0)[4], HOOK&& hook) {
    static_for<36>([&](auto ic) {
        constexpr int kk = ic.value / 18, i = ic.value % 18, n = i >> 2, m = i & 3;
        if (i == 0) { WAIT_L(0); if (kk) BAR(); SCHED0(); }
        f32x4& e = n < 4 ? c.a[m][n] : c.e[m & 1];
        if (FIRST && kk == 0) asm volatile("v_mfma_f32_16x16x32_bf16 %0, %1, %2, %3" : "=&v"(e) : "v"(f.b[kk][n]), "v"(f.a[kk][m]), "v"(c0[m]));
        else MFMA_T(f.b[kk][n], f.a[kk][m], e);
        SCHED0();
        if (i < 9) g_rd(f, kk ^ 1, i);
        if (kk == 0) {
            if (i >= 10 && i < 14) f.pa[(i - 10) >> 1][i & 1] = opaque(f.pa[(i - 10) >> 1][i & 1] + D);
            if (i == 14) f.pb = opaque(f.pb + DB);
            if (i == 15) f.p8 = opaque(f.p8 + DB);
        }
        if (FIRST && ic.value == 20) keep4(c0);
        hook(ic);
        SCHED0();
    });
}
#define MF(d, a, b) "v_mfma_f32_16x16x32_bf16 " d ", " a ", " b ", " d "\n\t"
template <int D, int DB>
DEVI void g_step_gelu(GTile& c, GFrag& f, f32x4& z, float r, float c1) {
#pragma unroll
    for (int kk = 0; kk < 2; ++kk) {
        float va = z[2 * kk], vb = z[2 * kk + 1], ta, tb;
        WAIT_L(0); if (kk) BAR(); SCHED0();
#pragma unroll
        for (int i = 0; i < 9; ++i) {
            MFMA_T(f.b[kk][i >> 2], f.a[kk][i & 3], c.a[i & 3][i >> 2]);
            if (i == 5) asm volatile("v_mul_f32 %0, %0, %1" : "+v"(va) : "v"(r));
            if (i == 6) asm volatile("v_mul_f32 %0, %0, %1" : "+v"(vb) : "v"(r));
            if (i == 7) asm volatile("v_mul_f32 %0, %1, %1" : "=v"(ta) : "v"(va));
            if (i == 8) asm volatile("v_mul_f32 %0, %1, %1" : "=v"(tb) : "v"(vb));
            SCHED0();
            g_rd(f, kk ^ 1, i);
            SCHED0();
        }
        asm volatile(MF("%0", "%5", "%6") "v_fmaak_f32 %3, %3, %11, 0xc0135761\n\tv_fmaak_f32 %4, %4, %11, 0xc0135761\n\t"
                     MF("%1", "%5", "%7") "v_mul_f32 %3, %3, %9\n\tv_mul_f32 %4, %4, %10\n\t" MF("%2", "%5", "%8") "v_exp_f32 %3, %3"
                     : "+v"(c.a[1][2]), "+v"(c.a[2][2]), "+v"(c.a[3][2]), "+v"(ta), "+v"(tb)
                     : "v"(f.b[kk][2]), "v"(f.a[kk][1]), "v"(f.a[kk][2]), "v"(f.a[kk][3]), "v"(va), "v"(vb), "v"(c1));
        SCHED0(); f.pa[kk][0] = opaque(f.pa[kk][0] + D); SCHED0();
        asm volatile(MF("%0", "%6", "%7") "v_exp_f32 %5, %5\n\t" MF("%1", "%6", "%8") "v_add_f32 %4, 1.0, %4\n\tv_add_f32 %5, 1.0, %5\n\t"
                     MF("%2", "%6", "%9") "v_rcp_f32 %4, %4\n\t" MF("%3", "%6", "%10") "v_rcp_f32 %5, %5"
                     : "+v"(c.a[0][3]), "+v"(c.a[1][3]), "+v"(c.a[2][3]), "+v"(c.a[3][3]), "+v"(ta), "+v"(tb)
                     : "v"(f.b[kk][3]), "v"(f.a[kk][0]), "v"(f.a[kk][1]), "v"(f.a[kk][2]), "v"(f.a[kk][3]));
        SCHED0(); f.pa[kk][1] = opaque(f.pa[kk][1] + D); SCHED0();
        asm volatile(MF("%0", "%4", "%5") "v_mul_f32 %2, %2, %7\n\tv_mul_f32 %3, %3, %8\n\t" MF("%1", "%4", "%6")
                     : "+v"(c.e[0]), "+v"(c.e[1]), "+v"(va), "+v"(vb) : "v"(f.b[kk][4]), "v"(f.a[kk][0]), "v"(f.a[kk][1]), "v"(ta), "v"(tb));
        SCHED0();
        if (kk == 0) { f.pb = opaque(f.pb + DB); f.p8 = opaque(f.p8 + DB); }
        SCHED0();
        z[2 * kk] = va; z[2 * kk + 1] = vb;
    }
}
DEVI void g_fill(GTile& c, const float (&e)[4]) {
#pragma unroll
    for (int m = 0; m < 4; ++m) {
#pragma unroll
        for (int n = 0; n < 4; ++n) c.a[m][n] = pin((f32x4){e[m], e[m], e[m], e[m]});
        if (m < 2) c.e[m] = pin((f32x4){e[m], e[m], e[m], e[m]});
    }
}

#undef STAMP_SEC
#define STAMP_SEC 1
DEVI void gmlp_body(LAS char* lds, int wm, int wn, int pm, int cg, char* z2t, char* y, const u16* __restrict__ bsp STAMP_PARAM) {
    const int mlo = wm * 4 + wn * 2, mhi = wm * 4 + (1 - wn) * 2;
    const float zero4[4] = {0.f, 0.f, 0.f, 0.f};
    GTile z2, z1, acc;
    GFrag f;
    {
        const int lane = lane_id_fresh();
#pragma unroll
        for (int kk = 0; kk < 2; ++kk) { f.pa[kk][0] = lds + mlo * 2048 + kk * 1024 + lane * 16; f.pa[kk][1] = lds + mhi * 2048 + kk * 1024 + lane * 16; }
        f.pb = lds + 16384 + wn * 4096 + lane * 16; f.p8 = lds + 16384 + 8192 + lane * 16; f.slot = 0;
    }
    g_fill(z2, zero4); g_fill(z1, zero4);
#pragma unroll
    for (int i = 0; i < 4; ++i) f.pa[i >> 1][i & 1] = opaque(f.pa[i >> 1][i & 1]);
    f.pb = opaque(f.pb); f.p8 = opaque(f.p8);
    SCHED0();
    WAIT_V(0); BARX();
    BAR();
    BARX();
    STAMP(2);
#pragma unroll
    for (int i = 0; i < 9; ++i) g_rd(f, 0, i);
    g_steps<4, true>(z2, f, 9);
    STAMP(3);
    {
        const int lane = lane_id_fresh(), r = lane & 15, q = lane >> 4;
        f32x4 ra[4];
#pragma unroll
        for (int m = 0; m < 4; ++m) ra[m] = *(const LAS f32x4*)(lds + G_TAB + ((m < 2 ? mlo : mhi) + (m & 1)) * 64 + q * 16);
        SCHED0();
#pragma unroll
        for (int m = 0; m < 4; ++m) {
            f32x4 s1, s2;
#pragma unroll
            for (int n = 0; n < 4; ++n) {
                const f32x4 g = gelu4(z2.a[m][n] * ra[m]);
                z2.a[m][n] = g;
                if (n == 0) { s1 = g; s2 = g * g; } else { s1 += g; s2 += g * g; }
            }
            if (m < 2) { const f32x4 g = gelu4(z2.e[m] * ra[m]); z2.e[m] = g; s1 += g; s2 += g * g; }
            float u0, u1;
            rowsum16x8(s1, s2, u0, u1);
            if ((r & 3) == 0) *(LAS f32x2*)(lds + G_PART + wn * 1024 + ((m < 2 ? mlo : mhi) + (m & 1)) * 128 + (q * 4 + (r >> 2)) * 8) = (f32x2){u0, u1};
            SCHED0();
        }
    }
    WAIT_L0(); BAR();
    STAMP(4);
    g_steps<4, false>(z1, f, 4);
    unsigned b0, b1;
    f32x4 c0[4];
    g_step_u<G_STAGE, G_STAGE, false>(z1, f, c0, [&](auto ic) {
        if (ic.value != 18) return;
        STAMP(5);
        const int lane = lane_id_fresh(), q = lane >> 4;
        b0 = *(const unsigned*)(bsp + pm * 128 + mlo * 16 + 2 * (lane & 15)); b1 = *(const unsigned*)(bsp + pm * 128 + mhi * 16 + 2 * (lane & 15));
#pragma unroll
        for (int g = 0; g < 2; ++g) {
            const auto rz2 = RSRC(z2t + (size_t)pm * 589824 + cg * 36864 + ((g ? mhi : mlo) >> 1) * 9216);
            const LAS char* tab = lds + G_TAB + (g ? mhi : mlo) * 64 + q * 16;
            LAS char* own = lds + (wm ? G_OWN : G_STAGE + 16384) + (g ^ wn) * 9216 + lane * 16;
            f32x4 ra[2], rb[2];
#pragma unroll
            for (int T = 0; T < 2; ++T) { ra[T] = *(const LAS f32x4*)(tab + 512 + T * 64); rb[T] = *(const LAS f32x4*)(tab + 1024 + T * 64); }
#pragma unroll
            for (int n = 0; n < (g ? 4 : 5); ++n) {
                const f32x4 v0 = (n < 4 ? z2.a[2 * g][n & 3] : z2.e[0]) * ra[0] + rb[0], v1 = (n < 4 ? z2.a[2 * g + 1][n & 3] : z2.e[1]) * ra[1] + rb[1];
                const u32x4 o = {pack2(v0[0], v1[0]), pack2(v0[1], v1[1]), pack2(v0[2], v1[2]), pack2(v0[3], v1[3])};
                ST16(rz2, lane * 16, n < 4 ? wn * 4096 + n * 1024 : 8192, o, 16);
                *(LAS u32x4*)(own + (n < 4 ? wn * 4096 + n * 1024 : 8192)) = o;
            }
        }
        STAMP(6);
    });
    static_for<3>([&](auto sc) {
        constexpr int D = sc.value == 0 ? G_STAGE : sc.value == 1 ? -3 * G_STAGE : 2 * G_STAGE;
        g_step_u<D, D, false>(z1, f, c0, [&](auto ic) { if (ic.value == 17 && sc.value == 1) WAIT_V(0); });
    });
    float ra[4], c1;
    {
        int lane, da[2];
        g_step_u<-G_STAGE, -G_STAGE, false>(z1, f, c0, [&](auto ic) {
            constexpr int i = ic.value;
            if (i == 9) lane = pin(lane_id_fresh());
            if (i == 14 || i == 15) da[i & 1] = pin((lane & 15) * 128 + (((4 * (i & 1) + (lane >> 4)) ^ (lane & 7)) * 16) - lane * 16 - (i & 1) * 1024);
            if (i == 16 || i == 17) f.pa[0][i & 1] = opaque(f.pa[0][i & 1] + da[0]);
            if (i == 27 || i == 28) f.pa[1][i & 1] = opaque(f.pa[1][i & 1] + da[1]);
            if (i >= 29 && i < 33) {
                constexpr int m = (i - 29) & 3;
                const float e = m & 1 ? bfhi(m < 2 ? b0 : b1) : bflo(m < 2 ? b0 : b1);
                c0[m] = pin((f32x4){e, e, e, e});
            }
            if (i == 33) {
#pragma unroll
                for (int m = 0; m < 4; ++m) ra[m] = *(const LAS float*)(lds + G_TAB + ((m < 2 ? mlo : mhi) + (m & 1)) * 64 + (lane & 15) * 4);
                c1 = pin(-0.10294324f);
            }
        });
    }
    STAMP(7);
    g_step_u<2 * G_STAGE, G_OWN - G_STAGE - 16384, true>(acc, f, c0, [](auto) {});
    static_for<18>([&](auto sc) {
        constexpr int st = sc.value, m = st < 16 ? st >> 2 : st & 1;
        constexpr int D = st == 0 ? -3 * G_STAGE : st % 3 == 1 ? 2 * G_STAGE : -G_STAGE;
        g_step_gelu<D, st == 0 ? 16384 - G_OWN : D>(acc, f, st < 16 ? z1.a[m][st & 3] : z1.e[m], ra[m], c1);
    });
    f.slot = 1;
    STAMP(9);
    g_steps<-3, false>(acc, f, 13);
    STAMP(10);
    const int lane = lane_id_fresh();
#pragma unroll
    for (int g = 0; g < 2; ++g) {
        const auto ry = RSRC(y + (size_t)(pm * 8 + (g ? mhi : mlo)) * 73728 + cg * 4608);
#pragma unroll
        for (int T = 0; T < 2; ++T) {
            const int m = 2 * g + T;
#pragma unroll
            for (int p = 0; p < 2; ++p) {
                const f32x4 a = acc.a[m][2 * p] * z1.a[m][2 * p], c = acc.a[m][2 * p + 1] * z1.a[m][2 * p + 1];
                ST16(ry, lane * 16 + wn * 2048 + p * 1024, T * 73728, ((u32x4){pack2(a[0], a[1]), pack2(a[2], a[3]), pack2(c[0], c[1]), pack2(c[2], c[3])}), 16);
            }
            if (g == 0) {
                const f32x4 a = acc.e[T] * z1.e[T];
                ST8(ry, (lane >> 5) * 256 + (lane & 15) * 16 + ((lane >> 4) & 1) * 8, T * 73728 + 4096, ((u32x2){pack2(a[0], a[1]), pack2(a[2], a[3])}), 16);
            }
        }
    }
}

#define NET_IDS \
    int b = blockIdx.x, wid = wid8 & 3; \
    asm volatile("" : "+s"(b), "+s"(wid));     \
    const int xcd = b & 7, idx = b >> 3; \
    const int pa = (xcd >> 1) * 4 + (idx & 3), hp = (xcd & 1) * 8 + (idx >> 2); \
    const int pm = (xcd >> 2) * 8 + (idx & 7), cg = (xcd & 3) * 4 + (idx >> 3); \
    const int rh = RH_MAP ? xcd * 4 + (idx & 3) : (xcd >> 1) * 8 + (idx & 7), c5 = RH_MAP ? idx >> 2 : (xcd & 1) * 4 + (idx >> 3); \
    const int lead = (idx + (idx >> 3)) & 3; \
    auto kord = [&](int t, int n) { \
        const int k = t + ((int)(0x1F000000u << (((t - lead) & 3) * 4)) >> 28); \
        return (unsigned)k < (unsigned)n ? k : t; \
    }; \
    auto kord5 = [&](int t) { \
        const int k = t + ((int)(0x1F000000u << (((t - (RH_MAP ? c5 : lead)) & 3) * 4)) >> 28); \
        return (unsigned)k < 18u ? k : t; \
    };

__global__ __launch_bounds__(512, 2) TRACE_ATTR void k_net(char* ws, const u16* wsp, const u16* bsp, char* out DBG_ARG) {
    asm volatile("gm_code_begin:" :: "s"(ws), "s"(wsp), "s"(bsp), "s"(out));
    extern __shared__ __attribute__((aligned(16))) char smem[];
    LAS char* lds = (LAS char*)smem;
    const char *twin = ws + OFF_TWIN, *twout = ws + OFF_TWOUT;
    STAMP_DECL;
    const int wid8 = __builtin_amdgcn_readfirstlane(threadIdx.x >> 6);
    if (wid8 >= 4) {
        lds_word_set(lds + G_WORD + (threadIdx.x & 15) * 4, 0u);
        WAIT_L0();
        u32x4 wr[5][9];
#pragma unroll 1
      for (int layer = 0; layer < NET_LAYERS; ++layer) {
        NET_IDS
        TRACE_BEGIN;
        char* wl = ws + OFF_L0 + layer * SZ_L;
        const unsigned fl = layer * F_LAYER;
        const char *xin = wl + L_XB, *st = wl + L_STB, *z2t = wl + (layer ? L_Y : L_Z2T);
        unsigned long long* zst = (unsigned long long*)(ws + OFF_ZST + layer * SZ_ZST);
        const int lane = lane_id_fresh(), l3 = lane >> 3;
        const unsigned vo = lane * 16;
        const unsigned l0 = (unsigned)(size_t)lds, lA = l0 + wid * 4096, lB = l0 + 16384 + wid * 4608;
        const auto rX = RSRC(xin + (size_t)(pm * 8 + 2 * wid) * 18432), rI = RSRC(twin + (size_t)cg * (18 * 18432) + wid * 4608);
        auto issue1B = [&](int g, int slot) { const int k = kord(g < 9 ? g : (g < 13 ? g - 4 : g - 13), 9); dma5<false>(lB + slot * G_STAGE, rI, (g < 9 ? 9 + k : k) * 18432, vo); };
        auto issue1A = [&](int g, int slot) { dma4<true>(lA + slot * G_STAGE, rX, kord(g < 9 ? g : g - 13, 9) * 2048, vo, vo, vo + 16384, vo + 16384); };
        auto issue1 = [&](int g, int slot) { issue1B(g, slot); issue1A(g, slot); };
        const auto rA = RSRC((const char*)wsp + (size_t)(pm * 128 + wid * 32) * 4096), rB = RSRC(z2t + cg * 36864 + wid * 4608);
        const unsigned voA = (unsigned)(2 * l3 * 4096 + ((lane & 7) ^ l3) * 16);
        auto issueB = [&](int k, int slot) { dma5<true>(lB + slot * G_STAGE, rB, (k >> 1) * 589824 + (k & 1) * 18432, vo); };
        const auto rW = RSRC(twout + (size_t)c5 * (72 * 4608));
        auto issue5 = [&](int t) { dma5<false>(l0 + ((t + 6) & 7) * K5_STAGE + wid * 4608, rW, kord5(t) * K5_STAGE + wid * 4608, vo); };
        auto load5 = [&](int p, int i) {
            const int t0 = p ? 2 * p + 6 : 4, ow = kord5(t0 + i) * K5_STAGE + wid * 4096;
            asm volatile("s_nop 4\n\tbuffer_load_dwordx4 %0, %4, %5, %6 offen\n\tbuffer_load_dwordx4 %1, %4, %5, %6 offen offset:1024\n\t"
                         "buffer_load_dwordx4 %2, %4, %5, %6 offen offset:2048\n\tbuffer_load_dwordx4 %3, %4, %5, %6 offen offset:3072"
                         : "+v"(wr[p][4 * i]), "+v"(wr[p][4 * i + 1]), "+v"(wr[p][4 * i + 2]), "+v"(wr[p][4 * i + 3]) : "v"(vo), "s"(rW), "s"(ow) : "memory");
            if (i) {
                const int oe = kord5(t0 + (wid >> 1)) * K5_STAGE + 16384 + (wid & 1) * 1024;
                asm volatile("s_nop 4\n\tbuffer_load_dwordx4 %0, %1, %2, %3 offen" : "+v"(wr[p][8]) : "v"(vo), "s"(rW), "s"(oe) : "memory");
            }
        };
        auto put5 = [&](int w0, int w1) {
#pragma unroll
            for (int w = w0; w < w1; ++w) {
                const int p = w / 9, i = w % 9 - 1;
                LAS char* sl = lds + (((p ? 2 * p + 6 : 4) + 6) & 7) * K5_STAGE + lane * 16;
                if (i < 0) *(LAS u32x4*)(sl + (wid >> 1) * K5_STAGE + 16384 + (wid & 1) * 1024) = wr[p][8];
                else *(LAS u32x4*)(sl + (i >> 2) * K5_STAGE + wid * 4096 + (i & 3) * 1024) = wr[p][i];
            }
            WAIT_L0();
        };
        const int row = wid * 32 + (lane & 31);
        auto step1 = [&](int t) { BAR(); issue1(t + 4, t & 3); };
        const char* ps;
        {
            unsigned fo, fi;
            attn_producer(lds, ws, wl, fl, layer, pa, hp, wid, [&] {
#pragma unroll
                for (int i = 0; i < 2; ++i)
                    asm volatile("s_nop 4\n\tbuffer_load_dwordx4 %0, %4, %5, %6 offen\n\tbuffer_load_dwordx4 %1, %4, %5, %6 offen offset:1024\n\t"
                                 "buffer_load_dwordx4 %2, %4, %5, %6 offen offset:2048\n\tbuffer_load_dwordx4 %3, %4, %5, %6 offen offset:3072"
                                 : "+v"(wr[0][4 * i]), "+v"(wr[0][4 * i + 1]), "+v"(wr[0][4 * i + 2]), "+v"(wr[0][4 * i + 3]) : "v"(vo), "s"(RSRC(wsp)), "s"(b * 32768 + wid * 8192 + i * 4096) : "memory");
            }, [&] {
                issue1B(0, 0); issue1B(1, 1);
                fo = flag_out(fl + F_X1, 256, pa, hp, lane); fi = flag_in(fl + F_X1, 256, pm * 16 + cg, 16, lane);
                ps = pin(st + (size_t)(pm * 128 + wid * 32) * 64 + lane * 16);
            });
            BARX();
            if (wid == 0) flag_raise(ws, fo);
            issue1B(2, 2); issue1B(3, 3);
            asm volatile("" :: "s"(rX));
            if (wid == 0) flags_wait<X1_FIRST>(ws, fi);
            BARX();
        }
        issue1A(0, 0); issue1A(1, 1); issue1A(2, 2);
        WAIT_V(8); BAR();
        issue1A(3, 3);
        {
            f32x4 sp[3];
            WAIT_V(8); step1(0);
            WAIT_V(13); step1(1);
            WAIT_V(18); step1(2);
            WAIT_V(18); step1(3);
            WAIT_V(18); step1(4);
            WAIT_V(18); BAR(); issue1B(9, 1); side_load(sp, ps, ps); if (layer == 0) load5(3, 0);
            WAIT_IF(layer, 17, 21); BAR(); issue1B(10, 2); if (layer == 0) load5(3, 1);
            WAIT_IF(layer, 5, 14); BARX();
            side_fence(sp);
#pragma unroll
            for (int i = 0; i < 2; ++i) lds_word_set(lds + G_TAB + (wid * 32 + i * 16 + (lane >> 2)) * 4, __float_as_uint(ln_rstd(sp[i])));
            issue1B(11, 3);
            WAIT_L0();
        }
        BAR(); issue1B(12, 0);
        if (layer == 1) {
            const auto rd = RSRC(out + (size_t)b * 9216 + wid * 2304);
            ST16(rd, vo, 0, ((u32x4){0u, 0u, 0u, 0u}), 16); ST16(rd, vo, 1024, ((u32x4){0u, 0u, 0u, 0u}), 16);
            if (lane < 16) ST16(rd, vo, 2048, ((u32x4){0u, 0u, 0u, 0u}), 16);
        }
        if (layer == 0) {
#pragma unroll
            for (int p = 0; p < 6; ++p) load5(p >> 1, p & 1);
        }
        WAIT_V(0);
        __builtin_amdgcn_s_setprio(3);
        const char* src = pin((const char*)zst + (size_t)(pm * 16 + (lane >> 4) * 4) * 1024 + wid * 256 + (lane & 15) * 16);
        const unsigned tw = pin(l0 + G_TAB + 512 + (lane >> 5) * 512 + (wid * 32 + (lane & 15) * 2 + ((lane >> 4) & 1)) * 4);
        const unsigned fz = flag_out(fl + F_Z2, 256, cg, pm, lane), fzi = pin(fl + F_Z2 + (cg * 16 + pm) * 256 + ((lane ^ pm) & 8) * 16 + (lane & 7) * 16);
        const unsigned vA[4] = {pin(voA), pin(voA + 16 * 4096 - 1024), pin(voA + 4096 - 2048), pin(voA + 17 * 4096 - 3072)};
        auto spA = [&](int so, unsigned la) { dma4<false>(la, rA, so, vA[0], vA[1], vA[2], vA[3]); };
        auto issueA = [&](int k, int slot) { spA(k * 128, lA + slot * G_STAGE); };
        {
            const LAS char* pp = opaque(lds + G_PART + row * 8);
            const auto rs = RSRC(zst + (size_t)(pm * 16 + cg) * 128);
            const unsigned vp = pin(lane < 32 ? (unsigned)row * 8u : 0x80000000u);
            asm volatile("" :: "s"(rs));
            BARX();
            const f32x2 a = *(const LAS f32x2*)pp, c = *(const LAS f32x2*)(pp + 1024);
            ST8(rs, vp, 0, ((u32x2){__float_as_uint(add1(a.x, c.x)), min(__float_as_uint(add1(a.y, c.y)), 0xfffffffeu)}), 16);
            SCHED0();
        }
        __builtin_amdgcn_s_setprio(0);
        {
            f32x4 g[4];
            float sm[4];
            auto gather = [&] {
                asm volatile("global_load_dwordx4 %0, %4, off sc1\n\tglobal_load_dwordx4 %1, %4, off offset:1024 sc1\n\t"
                             "global_load_dwordx4 %2, %4, off offset:2048 sc1\n\tglobal_load_dwordx4 %3, %4, off offset:3072 sc1"
                             : "=&v"(g[0]), "=&v"(g[1]), "=&v"(g[2]), "=&v"(g[3]) : "v"(src) : "memory");
            };
            auto total = [&] {
                asm volatile("" : "+v"(g[0]), "+v"(g[1]), "+v"(g[2]), "+v"(g[3]));
#pragma unroll
                for (int i = 0; i < 4; ++i) sm[i] = add1(add1(g[0][i], g[1][i]), add1(g[2][i], g[3][i]));
            };
            step1(9); step1(10);
            BARX(); gather(); issue1(15, 3);
            WAIT_V(22); BARX();
            __builtin_amdgcn_s_setprio(3);
            WAIT_V(9);
            total();
            if (__any(__builtin_isunordered(sm[1], sm[3]))) {
#pragma unroll 1
                for (unsigned spin = 0; spin < 400000u; ++spin) {
                    bool ok = true;
#pragma unroll
                    for (int i = 0; i < 4; ++i) ok &= __float_as_uint(g[i][1]) != 0xffffffffu && __float_as_uint(g[i][3]) != 0xffffffffu;
                    if (__all(ok)) break;
                    gather();
                    WAIT_V(0);
                    total();
                }
            }
            xq4_sum(sm);
            const bool odd = lane & 16;
            float mean;
            const float rs = rstd_of(odd ? sm[2] : sm[0], odd ? sm[3] : sm[1], 1.0f / 2304.0f, mean);
            asm volatile("ds_write_b32 %0, %1" :: "v"(tw), "v"(lane < 32 ? rs : -mean * rs) : "memory");
            __builtin_amdgcn_s_setprio(0);
            issue1B(16, 0);
            WAIT_L0();
        }
        BARX(); issue1A(16, 0); issueA(2 * pm, 1);
        auto kord2 = [&](int t) { const int j = kord(t - 2, 30); return ((j < 2 * (pm & 7) ? j : j + 2) + 2 * (pm & 8)) & 31; };
        unsigned seen;
        auto look = [&] { if (wid == 0) asm volatile("s_nop 4\n\tbuffer_load_dword %0, %1, %2, 0 offen sc1" : "+v"(seen) : "v"(fzi), "s"(RSRC(ws)) : "memory"); };
        auto need = [&](int n) {
            if (wid == 0) {
                asm volatile("" : "+v"(seen));
                if (__ballot(seen == 0u) & ((1ull << n) - 1ull)) flags_wait<0>(ws, lane < n ? fzi : fl + F_Z2 + (cg * 16 + pm) * 256 + pm * 16);
            }
        };
        auto issue = [&](int t, int sl) { const int k = kord2(t); issueB(k, sl); issueA(k, sl); };
        auto steps2 = [&](int t0, int t1) {
            int sl = (2 * t0 + 2) % 3 * G_STAGE, k = kord2(t0 + 3), ob = (k >> 1) * 589824 + (k & 1) * 18432;
            LOOP_ALIGN();
#pragma unroll 1
            for (int t = t0; t < t1; ++t) {
                WAIT_V(9); BARX();
                dma5<true>(lB + sl, rB, ob, vo);
                const int oa = pin_s(k * 128);
                const unsigned la = pin_s(lA + sl);
                k = pin_s(kord2(t + 4)); ob = pin_s((k >> 1) * 589824 + (k & 1) * 18432);
                sl = pin_s(sl == 0 ? 2 * G_STAGE : sl - G_STAGE);
                spA(oa, la);
            }
        };
        WAIT_V(13); BAR(); issue1(17, 2);
        WAIT_V(13); BARX();
        if (wid == 0) flag_raise(ws, fz);
        issueA(2 * pm + 1, 3); if (layer == 0) load5(4, 0);
        if (wid == 0) WAIT_IF(layer, 5, 9); else WAIT_IF(layer, 4, 8);
        BAR(); issueA(kord2(2), 0); if (layer == 0) load5(4, 1);
        BAR(); look(); issueA(kord2(3), 2);
        WAIT_V(4); need(3); BARX(); issueB(kord2(2), 0); issueB(kord2(3), 2); look();
        WAIT_IF(wid == 0, 6, 5); BAR(); issue(4, 1);
        WAIT_V(9); need(8);
        steps2(2, 11); look();
        WAIT_IF(wid == 0, 10, 9); BAR(); issue(14, 0);
        WAIT_V(9); need(16);
        steps2(12, 29);
        WAIT_V(9); BAR(); issue5(0); issue5(1); issue5(2);
        WAIT_V(15); BAR(); issue5(6); issue5(7);
        BAR(); issue5(3); put5(0, 9);
        {
            const unsigned fo = flag_out(fl + F_Y, 256, pm, cg, lane), fi = flag_in(fl + F_Y, 256, rh * 8 + c5, 16, lane);
            __builtin_amdgcn_s_setprio(3);
            WAIT_V(0); BARX();
            if (wid == 0) { flag_raise(ws, fo); flags_wait<Y_FIRST>(ws, fi); }
            __builtin_amdgcn_s_setprio(0);
            BARX();
        }
        BAR(); put5(9, 16);
        BAR(); put5(16, 23);
        BAR(); put5(23, 30);
        BAR(); put5(30, 36);
        BAR(); issue5(16); put5(36, 41);
        BAR(); issue5(17); put5(41, 45);
        WAIT_V(5); BAR();
        WAIT_V(0); BAR();
        if (layer == 0) { attn_issueB(lds, ws, hp, wid, lane, 0, 0); attn_issueB(lds, ws, hp, wid, lane, 1, 1); }
        BAR();
        if (layer == 0) {
            __builtin_amdgcn_s_setprio(3);
            const unsigned fo = flag_out(F_X2, 512, (rh >> 1) + wid, wid ? 16 + c5 : (rh & 1) * 8 + c5, wid == 0 || (wid == 1 && (rh & 1) && rh < 31) ? lane : 16);
            BARX();
            if (wid < 2) flag_raise(ws, fo);
        }
        TRACE_DUMP;
      }
        return;
    }
#pragma unroll 1
  for (int layer = 0; layer < NET_LAYERS; ++layer) {
    NET_IDS
    TRACE_BEGIN;
    char* wl = ws + OFF_L0 + layer * SZ_L;
    const unsigned fl = layer * F_LAYER;
    const char* xin = wl + L_XB;
    char* y = wl + (layer ? L_Z2T : L_Y);
#undef STAMP_SEC
#define STAMP_SEC 0
    {
    STAMP_BEGIN;
    attn_consumer(lds, wl, layer, pa, hp, wid STAMP_FWD);
    STAMP_END(1 + 5 * layer);
    }
#undef STAMP_SEC
#define STAMP_SEC 1
    STAMP_BEGIN;
    gmlp_body(lds, wid & 1, wid >> 1, pm, cg, wl + (layer ? L_Y : L_Z2T), y, bsp STAMP_FWD);
    {
        WAIT_V(0);
        BARX();
        STAMP(11);
        const int lane = lane_id_fresh(), r = lane & 15, q = lane >> 4;
        const int wk = wid >> 1, rt = rh * 4 + (wid & 1) * 2 + wk;
        const Res rx = proj_residual_load(xin, rt, c5, lane);
        const LAS char *pw[3], *ph[3];
#pragma unroll
        for (int i = 0; i < 3; ++i) {
            pw[i] = opaque(lds + 3 * i * K5_STAGE + wk * 4608 + lane * 16);
            ph[i] = opaque(lds + 3 * i * K5_STAGE + wk * 4608 + 4096 + q * 128 + (r & 7) * 16);
        }
        const auto rY = RSRC(y + (size_t)(rt & ~1) * 73728 + wk * 1024);
        const unsigned vy[2] = {pin((unsigned)(wk * 73728 + lane * 16)), pin((unsigned)((1 - wk) * 73728 + lane * 16))};
        int dk[4];
#pragma unroll
        for (int i = 0; i < 4; ++i) dk[i] = pin_s((kord5(4 + i) - 4 - i) * 4096);
        auto yoff = [&](int t) { return pin_s(t == 0 || t == 17 ? kord5(t) * 4096 : t * 4096 + dk[t & 3]); };
        bf16x8 af[5][4], wf[4][5];
        auto lda = [&](int so, int i) { return __builtin_bit_cast(bf16x8, __builtin_amdgcn_raw_buffer_load_b128(rY, vy[i & 1] + (i >> 1) * 2048, so, 16)); };
        auto rdw = [&](int t, int c, int n) {
            const int sl = (t + 6) & 7, o = (sl % 3) * K5_STAGE + c * 9216;
            wf[2 * (t & 1) + c][n] = n < 4 ? LDS_FRAG(pw[sl / 3] + o + n * 1024) : LDS_FRAG(ph[sl / 3] + o);
        };
#pragma unroll
        for (int n = 0; n < 10; ++n) rdw(0, n / 5, n % 5);
        int so[18];
#pragma unroll
        for (int i = 0; i < 5; ++i) so[i] = yoff(i);
        so[17] = yoff(17);
        f32x4 v[5], u[5];
        proj_residual(v, rx);
#pragma unroll
        for (int n = 0; n < 5; ++n) v[n] = pin(v[n]);
        LAS char* xo = (LAS char*)opaque(lds + wid * 4608 + lane * 16);
        LAS char* xo4 = (LAS char*)opaque(lds + wid * 4608 + 4096 + (lane & 31) * 16);
        const LAS char* xi = opaque(lds + (wid ^ 2) * 4608 + lane * 16);
        const LAS char* xi4 = opaque(lds + (wid ^ 2) * 4608 + 4096 + (lane & 31) * 16);
        const ProjOut po = layer ? proj_out_final(out, rt, c5, lane) : proj_out(wl + SZ_L + L_XA, wl + SZ_L + L_STA, rt, c5, lane);
        asm volatile("" :: "s"(rY));
        BARX();
        STAMP(12);
#pragma unroll
        for (int i = 0; i < K5_PRE; ++i) { af[i >> 2][i & 3] = lda(so[i >> 2], i & 3); SCHED0(); }
        static_for<18>([&](auto tc) {
            constexpr int t = tc.value;
            bf16x8 (&a)[4] = af[t % 5];
#pragma unroll
            for (int h = 0; h < 4; ++h) {
                const int c = t < 17 ? h >> 1 : h & 1, m = t < 17 ? h & 1 : 1 - (h >> 1);
                if (t < 17 ? m == 0 : h < 2) { if (t == 17 && h == 1) WAIT_L(0); else WAIT_L(5); }
#pragma unroll
                for (int n = 0; n < 5; ++n) {
                    f32x4& e = m ? u[n] : v[n];
                    if (t == 0 && h == 1) MFMA_0(wf[0][n], a[1], e); else MFMA_T(wf[2 * (t & 1) + c][n], a[2 * c + m], e);
                    SCHED0();
                    if (t < 17 && m == 0) rdw(t + 1, c, n);
                    if (t < 14 && m == 1 && !(n & 1)) { const int i = y_load_at(6 * t + 3 * c + n / 2); if (i >= 0) af[(i >> 2) % 5][i & 3] = lda(so[i >> 2], i & 3); }
                    if (t < 12 && h == 3 && n == 3) so[t + 5] = yoff(t + 5);
                    if (t == 17 && h * 5 + n >= 13 && h * 5 + n < 18) { const int i = h * 5 + n - 13; *(LAS f32x4*)(i < 4 ? xo + i * 1024 : xo4) = u[i]; }
                    SCHED0();
                }
            }
            if ((t & 1) && t < 17) BAR();
            SCHED0();
        });
        {
            WAIT_L0(); BARX();
#pragma unroll
            for (int n = 0; n < 5; ++n) u[n] = *(const LAS f32x4*)(n < 4 ? xi + n * 1024 : xi4);
            SCHED0();
            asm volatile("s_nop 1" : "+v"(v[0]), "+v"(v[1]), "+v"(v[2]), "+v"(v[3]), "+v"(v[4]));
#pragma unroll
            for (int n = 0; n < 5; ++n) v[n] += u[n];
        }
        STAMP(13);
        if (layer) proj_store<true>(v, po);
        else { proj_store(v, po); WAIT_V(0); BARX(); }
    }
    STAMP_END(3 + 5 * layer);
    TRACE_DUMP;
  }
}

typedef short s16x4 __attribute__((ext_vector_type(4)));
DEVI u32x4 tr_frag(const LAS char* p, int four_rows) {
    const u32x2 a = __builtin_bit_cast(u32x2, __builtin_amdgcn_ds_read_tr16_b64_v4i16((LAS s16x4*)p));
    const u32x2 b = __builtin_bit_cast(u32x2, __builtin_amdgcn_ds_read_tr16_b64_v4i16((LAS s16x4*)(p + four_rows)));
    return (u32x4){a.x, a.y, b.x, b.y};
}
DEVI unsigned centre2(float a, float b, f32x2& e, float mean) {
    const f32x2 t = (f32x2){a, b} + e;
    const unsigned r = pack2(t.x, t.y);
    e = (t - (f32x2){bflo(r), bfhi(r)}) - mean;
    return r;
}
#define PREP_AUX 16
struct Retile { u32x4 f[9]; };
DEVI void retile_in_make(Retile& o, int u, int kh, const LAS char* img, int lane) {
    const int j = u % 9, c = lane & 15, q = lane >> 4, p = lane & 3;
    const LAS char* src = img + (q * 8 + (c >> 2)) * 64 + (u & 1) * 32 + (j < 8 ? (p & 1) * 16 + (p >> 1) * 8 : p * 8);
    f32x2 sum = {0.f, 0.f};
#pragma unroll
    for (int i = 0; i < 18; ++i) {
        const u32x4 f = tr_frag(src + (i < 9 ? 9 - 9 * kh + i : 9 * kh + i - 9) * 2048, 256);
        if (i >= 9) o.f[i - 9] = f;
#pragma unroll
        for (int e = 0; e < 4; ++e) sum += (f32x2){bflo(f[e]), bfhi(f[e])};
    }
    const float mean = xq_sum(sum.x + sum.y) * (1.0f / 576.0f);
    f32x2 er[2] = {{-mean, -mean}, {-mean, -mean}};
#pragma unroll
    for (int i = 0; i < 9; ++i)
#pragma unroll
        for (int e = 0; e < 4; ++e) o.f[i][e] = centre2(bflo(o.f[i][e]), bfhi(o.f[i][e]), er[e & 1], mean);
}
DEVI void retile_in_store(const Retile& o, char* twin, int u, int kh, int lane) {
    const int half = u >= 144, g = u - half * 144, cg = g / 9, j = g - cg * 9, c = lane & 15, q = lane >> 4;
    const auto rd = RSRC(twin + (size_t)cg * (18 * 18432) + (half * 18 + kh * 9) * 9216);
    const unsigned vo = (unsigned)(q * 256 + (j < 8 ? ((j & ~1) + (c >> 3)) * 1024 + ((j & 1) * 8 + (c & 7)) * 16 : 8192 + c * 16));
#pragma unroll
    for (int i = 0; i < 9; ++i) ST16(rd, vo, i * 9216, o.f[i], PREP_AUX);
}
template <int PITCH>
DEVI void retile_out_make(Retile& o, const LAS char* img, int lane) {
    const int c = lane & 15, q = lane >> 4, p = lane & 3;
    const LAS char* src = img + (q * 8 + (c >> 2)) * PITCH;
#pragma unroll
    for (int i = 0; i < 8; ++i) o.f[i] = tr_frag(src + p * 16 + (i >> 2) * 144 + ((i >> 1) & 1) * 64 + (i & 1) * 8, 4 * PITCH);
    o.f[8] = tr_frag(src + 128 + (p >> 1) * 144 + (p & 1) * 8, 4 * PITCH);
}
DEVI void retile_out_store(const Retile& o, char* dst, int nkc, int kc, int w, int lane) {
    const int c = lane & 15, q = lane >> 4;
    const auto rd = RSRC(dst + (size_t)(2 * w * nkc + kc) * 4608);
#pragma unroll
    for (int i = 0; i < 8; ++i) ST16(rd, lane * 16 + (i & 3) * 1024, (i >> 2) * nkc * 4608, o.f[i], PREP_AUX);
    ST16(rd, (c >> 3) * nkc * 4608 + 4096 + q * 128 + (c & 7) * 16, 0, o.f[8], PREP_AUX);
}

constexpr int P_CODE = 64, P_CW = 64  , P_WIN = 144, P_WOUT = 72, P_WO = 18, P_X = 64, P_Z = 32;
constexpr int P_TOTAL = P_CW + P_WIN + P_WOUT + P_WO + P_X + P_Z + P_CODE;

DEVI void prep_body(const u16* __restrict__ x, const u16* __restrict__ wqkv, const u16* __restrict__ wo, const u16* __restrict__ w_in,
                    const u16* __restrict__ w_out, char* __restrict__ ws) {
    __shared__ __attribute__((aligned(16))) char smem[37376 + 512];
    LAS char* lds = (LAS char*)smem;
    const int tid = threadIdx.x;
    int u = blockIdx.x;
    if (u < P_CW) {
        const int st = (u & ~15) + 2 * (u & 7) + ((u >> 3) & 1);
        if (st >= 54) return;
        const u16* src = wqkv + st * 32;
        u32x4 v[9];
#pragma unroll
        for (int i = 0; i < 9; ++i) v[i] = *(const u32x4*)(src + (size_t)((tid >> 2) + 64 * i) * 1728 + (tid & 3) * 8);
        const int c = tid & 31;
        const int col = st * 32 + c, part = col / 576, cc = col % 576, hh = cc / 18, dim = cc % 18, h = hh & 1;
        const int tile = dim < 16 ? 3 * h + part : 6;
        const int sl = dim < 16 ? dim : (part == 2 ? 8 + 2 * h : 4 * h + 2 * part) + (dim - 16);
        char* dst = ws + OFF_TWQKV + pin((unsigned)((hh >> 1) * (18 * 7168) + tile * 1024 + sl * 16 + (tid >> 7) * 7168 + ((tid >> 5) & 3) * 256));
        float a[8];
#pragma unroll
        for (int j = 0; j < 8; ++j) a[j] = 0.f;
#pragma unroll
        for (int i = 0; i < 9; ++i) {
            *(LAS u32x4*)(lds + ((tid >> 2) + 64 * i) * 64 + (tid & 3) * 16) = v[i];
            a[0] += bflo(v[i].x); a[1] += bfhi(v[i].x); a[2] += bflo(v[i].y); a[3] += bfhi(v[i].y);
            a[4] += bflo(v[i].z); a[5] += bfhi(v[i].z); a[6] += bflo(v[i].w); a[7] += bfhi(v[i].w);
        }
#pragma unroll
        for (int j = 0; j < 8; ++j) {
            a[j] += __builtin_amdgcn_update_dpp(0.f, a[j], 0x124, 0xf, 0xf, true);
            a[j] += __builtin_amdgcn_update_dpp(0.f, a[j], 0x128, 0xf, 0xf, true);
            a[j] = xq_sum(a[j]);
        }
        LAS float* red = (LAS float*)(lds + 36864);
        if ((tid & 63) < 4) {
#pragma unroll
            for (int j = 0; j < 8; ++j) red[(tid >> 6) * 32 + (tid & 3) * 8 + j] = a[j];
        }
        __syncthreads();
        const float mean = ((red[c] + red[32 + c]) + (red[64 + c] + red[96 + c])) * (1.0f / 576.0f);
        f32x2 er[2] = {{-mean, -mean}, {-mean, -mean}};
#pragma unroll
        for (int i = 0; i < 9; ++i) {
            const LAS u16* p = (const LAS u16*)lds + ((tid >> 5) + 8 * i) * 256 + c;
            u32x4 o;
#pragma unroll
            for (int j = 0; j < 4; ++j) o[j] = centre2(__uint_as_float((unsigned)p[j * 64] << 16), __uint_as_float((unsigned)p[j * 64 + 32] << 16), er[j & 1], mean);
            *(u32x4*)(dst + i * 14336) = o;
        }
        return;
    }
    u -= P_CW;
    if (u < P_WIN + P_WOUT) {
        const int w = __builtin_amdgcn_readfirstlane(tid >> 6), lane = tid & 63;
        const bool isin = u < P_WIN;
        const int st = (u & ~15) + 2 * (u & 7) + ((u >> 3) & 1);
        const auto rs = RSRC(isin ? (const char*)(w_in + st * 32) : (const char*)w_out + (size_t)(u - P_WIN) * 36864);
        const unsigned vo = isin ? (unsigned)((lane >> 2) * 9216 + (lane & 3) * 16) : (unsigned)lane * 16u;
        const int step = isin ? 16 * 9216 : 1024;
#pragma unroll
        for (int j = 0; j < 9; ++j) BLDS(rs, lds + (j * 4 + w) * 1024, vo, (j * 4 + w) * step, 0);
        WAIT_V(0); BAR();
        Retile rt;
        if (isin) { retile_in_make(rt, st * 2 + (w & 1), w >> 1, lds, lane); retile_in_store(rt, ws + OFF_TWIN, st * 2 + (w & 1), w >> 1, lane); }
        else { retile_out_make<1152>(rt, lds + w * 288, lane); retile_out_store(rt, ws + OFF_TWOUT, 72, u - P_WIN, w, lane); }
        return;
    }
    u -= P_WIN + P_WOUT;
    if (u < P_WO) {
        const int w = __builtin_amdgcn_readfirstlane(tid >> 6);
        const int lane = tid & 63;
        const auto rs = RSRC(wo + w * 144);
#pragma unroll
        for (int j = 0; j < 9; ++j) {
            const int i = lane + 64 * j, rl = (i * 3641) >> 16, kp = u * 32 + rl;
            const int row = kp < 512 ? 18 * (kp >> 4) + (kp & 15) : 18 * ((kp - 512) >> 1) + 16 + (kp & 1);
            BLDS(rs, lds + w * 9216 + j * 1024, (unsigned)(row * 1152 + (i - rl * 18) * 16), 0, 0);
        }
        WAIT_V(0);
        Retile rt;
        retile_out_make<288>(rt, lds + w * 9216, lane);
        retile_out_store(rt, ws + OFF_TWO, 18, u, w, lane);
        return;
    }
    u -= P_WO;
    if (u < P_X) {
        const int rt = u * 2 + (tid >> 7), r = tid & 15, cg = (tid >> 4) & 7;
        const int sg = rt * 16 + r;
        u32x4 v[9];
#pragma unroll
        for (int i = 0; i < 9; ++i) v[i] = *(const u32x4*)(x + (size_t)u * 18432 + (tid + 256 * i) * 8);
#pragma unroll
        for (int i = 0; i < 9; ++i) { const int p = tid + 256 * i, row = (p * 3641) >> 18; *(LAS u32x4*)(lds + p * 16 + row * 16) = v[i]; }
        __syncthreads();
#pragma unroll
        for (int i = 0; i < 9; ++i) v[i] = *(const LAS u32x4*)(lds + (2 * r + (tid >> 7)) * 1168 + cg * 144 + i * 16);
        float sum = 0.f;
#pragma unroll
        for (int i = 0; i < 9; ++i) {
            sum += ((bflo(v[i].x) + bfhi(v[i].x)) + (bflo(v[i].y) + bfhi(v[i].y))) + ((bflo(v[i].z) + bfhi(v[i].z)) + (bflo(v[i].w) + bfhi(v[i].w)));
            ST16(RSRC(ws + OFF_L0 + L_XA), rt * 18432 + cg * 2304 + r * 16, i * 256, v[i], 16);
        }
        float m2 = 0.f;
#pragma unroll
        for (int i = 0; i < 9; ++i) {
            float d;
            d = bflo(v[i].x); m2 += d * d; d = bfhi(v[i].x); m2 += d * d; d = bflo(v[i].y); m2 += d * d; d = bfhi(v[i].y); m2 += d * d;
            d = bflo(v[i].z); m2 += d * d; d = bfhi(v[i].z); m2 += d * d; d = bflo(v[i].w); m2 += d * d; d = bfhi(v[i].w); m2 += d * d;
        }
        *(f32x2*)(ws + OFF_L0 + L_STA + (size_t)sg * 64 + cg * 8) = (f32x2){sum, m2};
        return;
    }
    u -= P_X;
    if (u >= P_Z) {
        u -= P_Z;
        unsigned long long c0;
        asm volatile("s_getpc_b64 s[40:41]\n\ts_add_u32 s40, s40, gm_code_begin@rel32@lo+4\n\ts_addc_u32 s41, s41, gm_code_begin@rel32@hi+12\n\ts_mov_b64 %0, s[40:41]"
                     : "=s"(c0) :: "s40", "s41", "scc");
        const unsigned long long pc = __builtin_amdgcn_s_getpc();
        const unsigned len = pc > c0 && pc - c0 < 262144ull ? (unsigned)(pc - c0) & ~15u : 0u;
        const auto rs = __builtin_amdgcn_make_buffer_rsrc((void*)(c0 & ~15ull), 0, len, 0x00020000);
        unsigned x = 0;
        for (unsigned o = ((u >> 3) * 256 + tid) * 16; o < len; o += 8 * 256 * 16) x ^= __builtin_amdgcn_raw_buffer_load_b128(rs, o, 0, 0)[0];
        asm volatile("" :: "v"(x));
        return;
    }
    {
        const auto rw = RSRC(ws);
        const u32x4 ones = {0xffffffffu, 0xffffffffu, 0xffffffffu, 0xffffffffu}, zero = {0u, 0u, 0u, 0u};
#pragma unroll
        for (int i = 0; i < 4; ++i) ST16(rw, (unsigned)OFF_ZST + (u * 1024 + i * 256 + tid) * 16, 0, ones, 16);
#pragma unroll
        for (int i = 0; i < 5; ++i) ST16(rw, (unsigned)OFF_CNT + (u * 1280 + i * 256 + tid) * 16, 0, zero, 16);
        ST16(rw, (unsigned)(OFF_L0 + L_STB) + (u * 256 + tid) * 16, 0, zero, 16);
#pragma unroll
        for (int i = 0; i < 2; ++i) ST16(rw, (unsigned)(OFF_L0 + L_O) + (u * 4 + i * 2 + (tid >> 7)) * 18432 + 16384 + (tid & 127) * 16, 0, zero, 16);
        if (tid < 144) {
            const int e = u * 144 + tid, f = e >> 4;
            *(u32x4*)(ws + OFF_TWQKV + (size_t)f * 7168 + 6144 + ((e >> 2) & 3) * 256 + (12 + (e & 3)) * 16) = (u32x4){0u, 0u, 0u, 0u};
        }
    }
}

__global__ __launch_bounds__(256) void k_prep(const u16* __restrict__ x, const u16* __restrict__ wqkv, const u16* __restrict__ wo,
                                              const u16* __restrict__ w_in, const u16* __restrict__ w_out, char* __restrict__ ws DBG_ARG) {
    asm volatile("" :: "s"(x), "s"(wqkv), "s"(wo), "s"(w_in), "s"(w_out), "s"(ws));
    prep_body(x, wqkv, wo, w_in, w_out, ws);
}

}

#define STOP_CHECK(n)

extern "C" void kernel_launch(void* const* d_in, const int* in_sizes, int n_in, void* d_out, int out_size, void* d_ws, size_t ws_size,
                              hipStream_t stream) {
    (void)in_sizes; (void)n_in; (void)out_size; (void)ws_size;
    const u16* x = (const u16*)d_in[0];
    const u16* wqkv = (const u16*)d_in[1];
    const u16* wo = (const u16*)d_in[2];
    const u16* w_in = (const u16*)d_in[4];
    const u16* w_sp = (const u16*)d_in[5];
    const u16* b_sp = (const u16*)d_in[6];
    const u16* w_out = (const u16*)d_in[7];
    char* ws = (char*)d_ws;
    (void)hipFuncSetAttribute((const void*)k_net, hipFuncAttributeMaxDynamicSharedMemorySize, NET_LDS);
    k_prep<<<P_TOTAL, 256, 0, stream>>>(x, wqkv, wo, w_in, w_out, ws DBG_LAUNCH(0));
    STOP_CHECK(1);
    k_net<<<256, 512, NET_LDS, stream>>>(ws, w_sp, b_sp, (char*)d_out DBG_LAUNCH(0));
}
```

```cpp
#include <hip/hip_runtime.h>
#include <stdint.h>

namespace {

typedef unsigned short u16;
typedef short bf16x8 __attribute__((ext_vector_type(8)));
typedef float f32x4 __attribute__((ext_vector_type(4)));
typedef float f32x2 __attribute__((ext_vector_type(2)));
typedef unsigned u32x4 __attribute__((ext_vector_type(4)));
typedef unsigned u32x2 __attribute__((ext_vector_type(2)));
typedef __bf16 bf16x2_t __attribute__((ext_vector_type(2)));
#define LAS __attribute__((address_space(3)))
#define DEVI __device__ __forceinline__

constexpr int S_ = 2048, D_ = 576, FH = 2304;

constexpr size_t SZ_TWQKV = (size_t)16 * 18 * 6 * 1024;
constexpr size_t SZ_TWT = (size_t)48 * 18 * 256;
constexpr size_t SZ_TWO = (size_t)8 * 18 * 4608;
constexpr size_t SZ_TWIN = (size_t)16 * 18 * 18 * 1024;
constexpr size_t SZ_TWOUT = (size_t)8 * 72 * 4608;
constexpr size_t SZ_X = (size_t)S_ * D_ * 2;
constexpr size_t SZ_Z = (size_t)S_ * FH * 2;
constexpr size_t SZ_ST = (size_t)S_ * 64;
constexpr size_t SZ_ZST = (size_t)16 * 16 * 128 * 8;
constexpr size_t OFF_TWQKV = 0;
constexpr size_t OFF_TWT = OFF_TWQKV + SZ_TWQKV;
constexpr size_t OFF_TWO = OFF_TWT + SZ_TWT;
constexpr size_t OFF_TWIN = OFF_TWO + SZ_TWO;
constexpr size_t OFF_TWOUT = OFF_TWIN + SZ_TWIN;
constexpr size_t OFF_CNT = OFF_TWOUT + SZ_TWOUT;
constexpr size_t SZ_CNT = 10 * 65536;
constexpr size_t OFF_FW = OFF_CNT + SZ_CNT;
constexpr size_t OFF_FL = OFF_FW + 262144;
constexpr size_t OFF_EP = OFF_FL + 4096;
constexpr size_t OFF_ZST = OFF_EP + 4096;
constexpr size_t OFF_L0 = OFF_ZST + 2 * SZ_ZST;
constexpr size_t L_XA = 0, L_STA = L_XA + SZ_X, L_O = L_STA + SZ_ST, L_XB = L_O + SZ_X, L_STB = L_XB + SZ_X, L_Z2T = L_STB + SZ_ST, L_Y = L_Z2T + SZ_Z;
constexpr size_t SZ_L = 0;
static_assert(OFF_L0 + L_Y + SZ_Z <= ((size_t)256 << 20), "workspace");
constexpr unsigned F_O = OFF_CNT, F_X1 = F_O + 65536, F_Z2 = F_O + 131072, F_Y = F_O + 196608, F_LAYER = 262144, F_X2 = F_O + 2 * F_LAYER;

DEVI unsigned pack2(float a, float b) { f32x2 v = {a, b}; bf16x2_t r = __builtin_convertvector(v, bf16x2_t); return __builtin_bit_cast(unsigned, r); }
DEVI float bflo(unsigned u) { return __uint_as_float(u << 16); }
DEVI float bfhi(unsigned u) { return __uint_as_float(u & 0xffff0000u); }
DEVI int slot2row(int sg) { return (sg & ~31) + 2 * (sg & 15) + ((sg >> 4) & 1); }
DEVI float fmax2(float a, float b) { return __builtin_amdgcn_fmed3f(a, b, __builtin_inff()); }
#define XQ4(OP, MID) \
    asm volatile("s_nop 1\n\tv_permlane32_swap_b32 %0, %1\n\tv_permlane32_swap_b32 %2, %3\n\t" OP " %0, %0, %1\n\t" OP " %2, %2, %3\n\t" \
                 "s_nop 1\n\tv_permlane16_swap_b32 %0, %2\n\t" OP " %0, %0, %2\n\t" MID \
                 "v_mov_b32 %2, %0\n\ts_nop 1\n\tv_permlane16_swap_b32 %0, %2\n\tv_mov_b32 %1, %0\n\tv_mov_b32 %3, %2\n\ts_nop 0\n\t" \
                 "v_permlane32_swap_b32 %0, %1\n\tv_permlane32_swap_b32 %2, %3" : "+v"(x[0]), "+v"(x[1]), "+v"(x[2]), "+v"(x[3]))
DEVI void xq4_max(float (&x)[4]) { XQ4("v_max_f32", ""); }
DEVI void xq4_sum(float (&x)[4]) { XQ4("v_add_f32", ""); }
DEVI void xq4_sum_rcp(float (&x)[4]) { XQ4("v_add_f32", "v_rcp_f32 %0, %0\n\ts_nop 0\n\t"); }
template <class T> DEVI T pin(T v) { asm volatile("" : "+v"(v)); return v; }
template <int V> struct IC { static constexpr int value = V; };
template <int N, int I = 0, class F> DEVI void static_for(F&& f) { if constexpr (I < N) { f(IC<I>{}); static_for<N, I + 1>(f); } }
template <class T> DEVI T* own_s(T* p) { T* r; asm volatile("s_mov_b64 %0, %1" : "=&s"(r) : "s"(p)); return r; }
DEVI int pin_s(int v) { asm volatile("" : "+s"(v)); return v; }
DEVI float xq_sum(float v) {
    auto a = __builtin_amdgcn_permlane32_swap(__float_as_uint(v), __float_as_uint(v), false, false);
    v = __uint_as_float(a[0]) + __uint_as_float(a[1]);
    auto b = __builtin_amdgcn_permlane16_swap(__float_as_uint(v), __float_as_uint(v), false, false);
    return __uint_as_float(b[0]) + __uint_as_float(b[1]);
}
#define BLDS(rs, l, vo, so, off) __builtin_amdgcn_raw_ptr_buffer_load_lds((rs), (LAS void*)(l), 16, (vo), (so), (off), 0)
#define BLDS1(rs, l, vo, so, off) __builtin_amdgcn_raw_ptr_buffer_load_lds((rs), (LAS void*)(l), 16, (vo), (so), (off), 16)
#define RSRC(p) __builtin_amdgcn_make_buffer_rsrc((void*)(p), 0, 0x7fffffff, 0x00020000)
#define ST16(rs, vo, so, v, aux) __builtin_amdgcn_raw_buffer_store_b128((v), (rs), (vo) + (so), 0, aux)
#define ST8(rs, vo, so, v, aux) __builtin_amdgcn_raw_buffer_store_b64((v), (rs), (vo), (so), aux)
#define WAIT_V(n) asm volatile("s_waitcnt vmcnt(%0)" ::"i"(n) : "memory")
#define WAIT_L0() asm volatile("s_waitcnt lgkmcnt(0)" ::: "memory")
#define BAR() do { asm volatile("" ::: "memory"); __builtin_amdgcn_s_barrier(); asm volatile("" ::: "memory"); } while (0)
#define BARX() do { __builtin_amdgcn_sched_barrier(0); BAR(); __builtin_amdgcn_sched_barrier(0); } while (0)
#define LOOP_ALIGN() asm volatile(".p2align 6")
#define SCHED0() __builtin_amdgcn_sched_barrier(0)
#define MFMA(a, b, c) __builtin_amdgcn_mfma_f32_16x16x32_bf16((a), (b), (c), 0, 0, 0)
#define MFMA_T(a, b, c) asm volatile("v_mfma_f32_16x16x32_bf16 %0, %1, %2, %0" : "+v"(c) : "v"(a), "v"(b))
#define MFMA_0(a, b, c) asm volatile("v_mfma_f32_16x16x32_bf16 %0, %1, %2, 0" : "=&v"(c) : "v"(a), "v"(b))
#define WAIT_L(n) __builtin_amdgcn_s_waitcnt(0xC07F | ((n) << 8))
#define LDS_FRAG(p) (*(const LAS bf16x8*)(p))
DEVI int lane_id_fresh() { int z = 0; asm volatile("" : "+v"(z)); return __builtin_amdgcn_mbcnt_hi(~0u, __builtin_amdgcn_mbcnt_lo(~0u, z)); }
DEVI const LAS char* opaque(const LAS char* p) { asm volatile("" : "+v"(p)); return p; }
#define DBG_ARG
#define DBG_LAUNCH(n)
#define STAMP(i)
#define STAMP_DECL
#define STAMP_BEGIN
#define STAMP_END(n)
#define STAMP_PARAM
#define STAMP_FWD

#define PST_PARAM
#define PST_FWD
#define PST_DECL
#define PST(i)
#define PST_DUMP(c, k)
#define TRACE_ATTR
#define TRACE_BEGIN
#define TRACE_DUMP

DEVI float add1(float a, float b) { float r; asm("v_add_f32 %0, %1, %2" : "=v"(r) : "v"(a), "v"(b)); return r; }
DEVI float rstd_of(float s, float q, float inv_n, float& mean) {
    float var;
    asm("v_mul_f32 %0, %2, %3\n\tv_mul_f32 %1, %2, %4\n\tv_fma_f32 %1, -%0, %0, %1" : "=&v"(mean), "=&v"(var) : "s"(inv_n), "v"(s), "v"(q));
    return __builtin_amdgcn_rsqf(fmax2(var, 0.f) + 1e-5f);
}
DEVI float ln_rstd(const f32x4 p) {
    float s, q, mean;
    asm("v_add_f32 %0, %2, %4\n\tv_add_f32 %1, %3, %5\n\ts_nop 0\n\t"
        "v_add_f32_dpp %0, %0, %0 quad_perm:[1,0,3,2] row_mask:0xf bank_mask:0xf\n\tv_add_f32_dpp %1, %1, %1 quad_perm:[1,0,3,2] row_mask:0xf bank_mask:0xf\n\ts_nop 0\n\t"
        "v_add_f32_dpp %0, %0, %0 quad_perm:[2,3,0,1] row_mask:0xf bank_mask:0xf\n\tv_add_f32_dpp %1, %1, %1 quad_perm:[2,3,0,1] row_mask:0xf bank_mask:0xf"
        : "=&v"(s), "=&v"(q) : "v"(p[0]), "v"(p[1]), "v"(p[2]), "v"(p[3]));
    return rstd_of(s, q, 1.0f / 576.0f, mean);
}

struct Res { u32x4 a, c; u32x2 e; };
DEVI Res proj_residual_load(const char* xin, int rt, int cg, int lane) {
    const auto rs = RSRC(xin + (size_t)rt * 18432 + cg * 2304);
    Res x;
    x.a = __builtin_amdgcn_raw_buffer_load_b128(rs, lane * 16, 0, 16); x.c = __builtin_amdgcn_raw_buffer_load_b128(rs, lane * 16 + 1024, 0, 16);
    x.e = __builtin_amdgcn_raw_buffer_load_b64(rs, 2048 + (lane & 15) * 16 + ((lane >> 4) & 1) * 8, 0, 16);
    return x;
}
DEVI void proj_residual(f32x4 (&v)[5], const Res& x) {
    v[0] = (f32x4){bflo(x.a.x), bfhi(x.a.x), bflo(x.a.y), bfhi(x.a.y)}; v[1] = (f32x4){bflo(x.a.z), bfhi(x.a.z), bflo(x.a.w), bfhi(x.a.w)};
    v[2] = (f32x4){bflo(x.c.x), bfhi(x.c.x), bflo(x.c.y), bfhi(x.c.y)}; v[3] = (f32x4){bflo(x.c.z), bfhi(x.c.z), bflo(x.c.w), bfhi(x.c.w)};
    v[4] = (f32x4){bflo(x.e.x), bfhi(x.e.x), bflo(x.e.y), bfhi(x.e.y)};
}
struct ProjOut { __amdgpu_buffer_rsrc_t x, st; unsigned v0, v1, v2, vs; };
DEVI ProjOut proj_out(char* xout, char* stout, int rt, int cg, int lane) {
    const int r = lane & 15, q = lane >> 4;
    ProjOut p = {RSRC(xout + (size_t)rt * 18432 + cg * 2304), RSRC(stout + (size_t)rt * 1024 + cg * 8),
                 pin((unsigned)lane * 16u), pin((unsigned)lane * 16u + 1024u), pin((unsigned)(2048 + r * 16 + (q & 1) * 8)), pin((unsigned)(r * 64 + (q >> 1) * 4))};
    asm volatile("" :: "s"(p.x), "s"(p.st));
    return p;
}
DEVI ProjOut proj_out_final(char* out, int rt, int cg, int lane) {
    const int q = lane >> 4;
    const unsigned o = (unsigned)(slot2row(rt * 16 + (lane & 15)) * 1152 + cg * 144);
    ProjOut p = {RSRC(out), RSRC(out), pin(o + q * 16), pin(o + 64 + q * 16), pin(o + 128 + (q & 1) * 8), 0u};
    return p;
}
template <bool FINAL = false>
DEVI void proj_store(f32x4 (&v)[5], const ProjOut& p) {
    ST16(p.x, p.v0, 0, ((u32x4){pack2(v[0][0], v[0][1]), pack2(v[0][2], v[0][3]), pack2(v[1][0], v[1][1]), pack2(v[1][2], v[1][3])}), FINAL ? 0 : 16);
    SCHED0();
    asm volatile("" : "+v"(v[2]), "+v"(v[3]), "+v"(v[4]));
    ST16(p.x, p.v1, 0, ((u32x4){pack2(v[2][0], v[2][1]), pack2(v[2][2], v[2][3]), pack2(v[3][0], v[3][1]), pack2(v[3][2], v[3][3])}), FINAL ? 0 : 16);
    SCHED0();
    ST8(p.x, p.v2, 0, ((u32x2){pack2(v[4][0], v[4][1]), pack2(v[4][2], v[4][3])}), FINAL ? 0 : 16);
    SCHED0();
    if (!FINAL) {
        const f32x4 s4 = ((v[0] + v[1]) + (v[2] + v[3])) + v[4] * 0.5f;
        const f32x4 q4 = ((v[0] * v[0] + v[1] * v[1]) + (v[2] * v[2] + v[3] * v[3])) + v[4] * v[4] * 0.5f;
        auto x = __builtin_amdgcn_permlane32_swap(__float_as_uint((s4[0] + s4[2]) + (s4[1] + s4[3])), __float_as_uint((q4[0] + q4[2]) + (q4[1] + q4[3])), false, false);
        const float t = __uint_as_float(x[0]) + __uint_as_float(x[1]);
        auto y = __builtin_amdgcn_permlane16_swap(__float_as_uint(t), __float_as_uint(t), false, false);
        __builtin_amdgcn_raw_buffer_store_b32(__float_as_uint(__uint_as_float(y[0]) + __uint_as_float(y[1])), p.st, p.vs, 0, 16);
        SCHED0();
    }
}

DEVI void side_load(f32x4 (&d)[3], const char* p, const char* p2) {
    asm volatile("global_load_dwordx4 %0, %3, off sc1\n\tglobal_load_dwordx4 %1, %3, off offset:1024 sc1\n\tglobal_load_dwordx4 %2, %4, off sc1"
                 : "=&v"(d[0]), "=&v"(d[1]), "=&v"(d[2]) : "v"(p), "v"(p2) : "memory");
}
DEVI void side_fence(f32x4 (&d)[3]) { asm volatile("" : "+v"(d[0]), "+v"(d[1]), "+v"(d[2])); }
DEVI void lds_word_set(LAS char* p, unsigned v) { asm volatile("ds_write_b32 %0, %1" :: "v"((unsigned)(size_t)p), "v"(v) : "memory"); }
#define DMA_I(v, off, sc) "buffer_load_dwordx4 " v ", %1, %2 offen offset:" off sc " lds\n\t"
#define DMA5(sc) asm volatile("s_mov_b32 m0, %0\n\ts_mov_b32 exec_hi, 0\n\ts_nop 2\n\t" DMA_I("%3", "0", sc) "s_mov_b32 exec_hi, -1\n\t" \
    DMA_I("%3", "512", sc) DMA_I("%3", "1536", sc) DMA_I("%3", "2560", sc) DMA_I("%3", "3584", sc) :: "s"(la), "s"(rs), "s"(so), "v"(vo) : "m0", "memory")
#define DMA4(sc) asm volatile("s_mov_b32 m0, %0\n\ts_nop 3\n\t" DMA_I("%3", "0", sc) DMA_I("%4", "1024", sc) DMA_I("%5", "2048", sc) DMA_I("%6", "3072", sc) \
    :: "s"(la), "s"(rs), "s"(so), "v"(v0), "v"(v1), "v"(v2), "v"(v3) : "m0", "memory")
template <bool SC1> DEVI void dma5(unsigned la, __amdgpu_buffer_rsrc_t rs, int so, unsigned vo) { if (SC1) DMA5(" sc1"); else DMA5(""); }
template <bool SC1> DEVI void dma4(unsigned la, __amdgpu_buffer_rsrc_t rs, int so, unsigned v0, unsigned v1, unsigned v2, unsigned v3) { if (SC1) DMA4(" sc1"); else DMA4(""); }
#define DMA2(sc) asm volatile("s_mov_b32 m0, %0\n\ts_nop 3\n\t" DMA_I("%3", "0", sc) DMA_I("%4", "1024", sc) :: "s"(la), "s"(rs), "s"(so), "v"(v0), "v"(v1) : "m0", "memory")
template <bool SC1> DEVI void dma2(unsigned la, __amdgpu_buffer_rsrc_t rs, int so, unsigned v0, unsigned v1) { if (SC1) DMA2(" sc1"); else DMA2(""); }
DEVI void dma1(unsigned la, __amdgpu_buffer_rsrc_t rs, int so, unsigned v0) { asm volatile("s_mov_b32 m0, %0\n\ts_nop 3\n\t" DMA_I("%3", "0", "") :: "s"(la), "s"(rs), "s"(so), "v"(v0) : "m0", "memory"); }
#define WAIT_IF(c, a, b) do { if (c) WAIT_V(a); else WAIT_V(b); } while (0)
template <int T0, int T1, int P, int S0 = 0, class ISSUE>
DEVI void ring_steps(ISSUE&& issue) {
    int slot = (T0 + S0) % 3;
    LOOP_ALIGN();
#pragma unroll 1
    for (int t = T0; t < T1; ++t) {
        WAIT_V(P); BAR();
        issue(t + 3, slot);
        slot = slot == 2 ? 0 : slot + 1;
    }
}

DEVI unsigned flag_out(unsigned use, int pitch, int group, int writer, int lane) { return pin(lane < 16 ? use + (group * 16 + lane) * pitch + writer * 16 : 0x80000000u); }
DEVI unsigned flag_in(unsigned use, int pitch, int reader, int n, int lane) { return pin(use + reader * pitch + (lane < n ? lane : 0) * 16); }
DEVI void flag_raise(const char* ws, unsigned vo) { ST16(RSRC(ws), vo, 0, ((u32x4){1u, 1u, 1u, 1u}), 16); SCHED0(); }
template <int FIRST>
DEVI void flags_wait(const char* ws, unsigned vo) {
    unsigned v, n = 400000u;
    asm volatile("s_nop 4\n\ts_sleep %4\n"
                 "1:\n\tbuffer_load_dword %0, %2, %3, 0 offen sc1\n\ts_waitcnt vmcnt(0)\n\tv_cmp_eq_u32 vcc, 0, %0\n\ts_cbranch_vccz 2f\n\t"
                 "s_sleep 1\n\ts_sub_u32 %1, %1, 1\n\ts_cbranch_scc0 1b\n2:"
                 : "=&v"(v), "+s"(n) : "v"(vo), "s"(RSRC(ws)), "i"(FIRST) : "vcc", "scc", "memory");
    SCHED0();
}
template <int FIRST>
DEVI void epoch_wait(const char* ws, unsigned vo, unsigned long long ep) {
    unsigned long long v; unsigned n = 400000u;
    asm volatile("s_nop 4\n\ts_sleep %5\n"
                 "1:\n\tbuffer_load_dwordx2 %0, %2, %3, 0 offen sc1\n\ts_waitcnt vmcnt(0)\n\tv_cmp_ne_u64 vcc, %4, %0\n\ts_cbranch_vccz 2f\n\t"
                 "s_sleep 1\n\ts_sub_u32 %1, %1, 1\n\ts_cbranch_scc0 1b\n2:"
                 : "=&v"(v), "+s"(n) : "v"(vo), "s"(RSRC(ws)), "s"(ep), "i"(FIRST) : "vcc", "scc", "memory");
    SCHED0();
}
DEVI unsigned lds_arrive(LAS char* p) {
    unsigned v;
    asm volatile("s_mov_b64 exec, 1\n\tds_add_rtn_u32 %0, %1, %2\n\ts_mov_b64 exec, -1\n\ts_waitcnt lgkmcnt(0)" : "=&v"(v) : "v"((unsigned)(size_t)p), "v"(1u) : "memory");
    return __builtin_amdgcn_readfirstlane(v);
}
DEVI void lds_count_wait(LAS char* p, unsigned n) {
#pragma unroll 1
    for (unsigned spin = 0; spin < 40000000u; ++spin) {
        unsigned v;
        asm volatile("ds_read_b32 %0, %1\n\ts_waitcnt lgkmcnt(0)" : "=v"(v) : "v"((unsigned)(size_t)p) : "memory");
        if (__builtin_amdgcn_readfirstlane(v) >= n) break;
        __builtin_amdgcn_s_sleep(1);
    }
}

constexpr int K1_STAGE = 32768;
constexpr int WO_OFF = 69632;
constexpr int G_OWN = 139264;
constexpr int G_PART = G_OWN + 18432;
constexpr int G_TAB = G_PART + 2048;
constexpr int K1_SCR = G_TAB + 1536;
constexpr int G_WORD = K1_SCR + 576;
constexpr int NET_LDS = G_WORD + 64;

#define WO_PRE 12
#define WO_W 33
#define REL_PRIO 0
#define WSP_PF 1
#define W_FIRST 6
#define O_FIRST 10
#define X1_FIRST 6
#define X2_FIRST 6
#define Y_FIRST 10
#define Z2_FIRST 10
DEVI void attn_issueB(LAS char* lds, const char* ws, int hp, int wid, int lane, int s, int slot) {
    const unsigned la = (unsigned)(size_t)lds + slot * K1_STAGE + 18432 + wid * 4096, vo = lane * 16;
    if (wid < 3) dma4<true>(la, RSRC(ws + OFF_TWQKV + (size_t)hp * (18 * 6144)), wid * 4096 + s * 12288, vo, vo, vo, vo);
    else {
        const int c = lane & 15;
        const unsigned vt = (unsigned)((c < 8 ? (c >> 1) & 1 : 2) * 4608 + (lane >> 4) * 64 + (c < 8 ? (c >> 2) * 2 + (c & 1) : c & 3) * 16 + 1024);
        dma2<true>(la, RSRC(ws + OFF_TWT + (size_t)hp * 13824 - 1024), s * 512, vt, vt + 256 - 1024);
    }
}
struct AttnX { __amdgpu_buffer_rsrc_t rs; unsigned v[4], vh[2]; int oA, oh, step; };
DEVI AttnX attn_x(const char* x0, const char* xa, int layer, int pm, int wid, int lane) {
    const int l3 = lane >> 3, pmh = pm > 0 ? pm : 1;
    const unsigned v = layer ? lane * 16 : 2 * l3 * 1152 + ((lane & 7) ^ l3) * 16 + 4096, d1 = layer ? 0 : 16 * 1152 - 1024, d2 = layer ? 16384 : 1152 - 2048;
    const unsigned vh = layer ? (l3 & 1) * 18432 + (lane >> 4) * 256 + (8 + (lane & 7)) * 16 : v + 16 * 1152;
    return {RSRC(layer ? xa : x0 - 4096), {pin(v), pin(v + d1), pin(v + d2), pin(v + d1 + d2)}, {pin(vh), pin(vh + (layer ? 0 : 1152 - 1024))},
            (pm * 8 + 2 * wid) * 18432, (pmh * 8 - 2) * 18432, layer ? 2048 : 128};
}
DEVI void attn_issueA(LAS char* lds, const AttnX& a, int wid, int s, int slot) {
    const unsigned la = (unsigned)(size_t)lds + slot * K1_STAGE;
    if (wid == 3) dma2<true>(la, a.rs, a.oh + s * a.step, a.vh[0], a.vh[1]);
    dma4<true>(la + 2048 + wid * 4096, a.rs, a.oA + s * a.step, a.v[0], a.v[1], a.v[2], a.v[3]);
}
template <class IDLE, class NEXT>
DEVI void attn_producer(LAS char* lds, const char* ws, const char* x0, const char* wl, unsigned fl, unsigned long long ep, int layer, int pm, int hp, int wid, IDLE&& idle8, NEXT&& next) {
    const int lane = lane_id_fresh();
    const int pmh = pm > 0 ? pm : 1;
    const unsigned vo = lane * 16;
    const AttnX ax = attn_x(x0, wl + L_XA, layer, pm, wid, lane);
    auto issue = [&](int s, int slot) { attn_issueA(lds, ax, wid, s, slot); attn_issueB(lds, ws, hp, wid, lane, s, slot); };
    const int hr = lane >> 2;
    const char* ps = pin(wl + L_STA + (size_t)(pm * 128 + wid * 32) * 64 + lane * 16);
    const char* psh = pin(wl + L_STA + (size_t)((pmh * 8 - 2 + (hr >> 3)) * 16 + 8 + (hr & 7)) * 64 + (lane & 3) * 16);
    if (layer == 0) {
        const unsigned fi = pin((unsigned)OFF_FW + (pm * 16 + hp) * 1024 + (lane < 6 ? lane : 0) * 128);
        if (wid == 0) epoch_wait<W_FIRST>(ws, fi, ep);
        BARX();
        attn_issueB(lds, ws, hp, wid, lane, 0, 0); attn_issueB(lds, ws, hp, wid, lane, 1, 1); issue(2, 2);
        WAIT_IF(wid == 3, 10, 12);
    } else {
        attn_issueB(lds, ws, hp, wid, lane, 2, 2);
        const unsigned fi = flag_in(F_X2, 512, pm * 16 + hp, pm > 0 ? 24 : 16, lane);
        if (wid == 0) flags_wait<X2_FIRST>(ws, fi);
        BARX();
#pragma unroll
        for (int i = 0; i < 3; ++i) attn_issueA(lds, ax, wid, i, i);
        WAIT_IF(wid == 3, 12, 8);
    }
    __builtin_amdgcn_s_setprio(0);
    BAR();
    if (layer) WAIT_IF(wid == 3, 6, 4);
    ring_steps<0, 4, 8>(issue);
    {
        unsigned long long done;
        const unsigned fd = (unsigned)OFF_FL + wid * 1024 + vo;
        if (layer == 0) asm volatile("s_nop 4\n\tbuffer_load_dwordx2 %0, %1, %2, 0 offen sc1" : "+v"(done) : "v"(fd), "s"(RSRC(ws)) : "memory");
        WAIT_IF(layer, 8, 9); BAR(); issue(7, 1);
        WAIT_V(8);
        if (layer == 0) { asm volatile("" : "+v"(done)); if (__any(done != ep)) epoch_wait<0>(ws, fd, ep); }
        BAR(); issue(8, 2);
    }
    {
        f32x4 sp[3];
        side_load(sp, ps, psh);
        WAIT_V(11); BAR();
        if (WSP_PF && layer == 0) { idle8(); WAIT_V(8); } else WAIT_V(0);
        BARX();
        side_fence(sp);
#pragma unroll
        for (int i = 0; i < 3; ++i) lds_word_set(lds + K1_SCR + (i < 2 ? 16 + wid * 32 + i * 16 + hr : hr) * 4, __float_as_uint(ln_rstd(sp[i])));
        WAIT_L0();
    }
    BAR();
    const auto rW = RSRC(ws + OFF_TWO + (size_t)(hp >> 1) * 82944);
#pragma unroll
    for (int i = 0; i < 5; ++i) {
        const int off = (i * 4 + wid) * 4096;
        LAS char* d = lds + WO_OFF + off;
        BLDS1(rW, d, vo, off, 0); BLDS1(rW, d, vo, off, 1024); BLDS1(rW, d, vo, off, 2048); BLDS1(rW, d, vo, off, 3072);
    }
    if (wid == 0) BLDS1(rW, lds + WO_OFF + 81920, vo, 81920, 0);
    next();
    const unsigned fo = flag_out(fl + F_O, 256, pm, hp, lane), fi = flag_in(fl + F_O, 256, pm * 16 + hp, 16, lane);
    WAIT_V(0);
    if (wid == 0) __builtin_amdgcn_s_setprio(3);
    BARX();
    if (wid == 0) { flag_raise(ws, fo); flags_wait<O_FIRST>(ws, fi); }
    __builtin_amdgcn_s_setprio(0);
    BARX();
}

#define STAMP_SEC 0
template <class DONE>
DEVI void attn_consumer(LAS char* lds, const char* x0, char* wl, int layer, int pm, int hp, int wid, DONE&& done STAMP_PARAM) {
    const int lane = lane_id_fresh(), r = lane & 15, q = lane >> 4;
    const char* xa = wl + L_XA;
    char* o = wl + L_O;
    const int T = wid & 1, h = wid >> 1;
    const LAS char *ph[2], *pa[2];
#pragma unroll
    for (int kk = 0; kk < 2; ++kk) {
        const int sw = ((4 * kk + q) ^ (r & 7)) * 16;
        ph[kk] = opaque(lds + (layer ? kk * 1024 + q * 256 + (r ^ (T ? 0 : 8)) * 16 : (r ^ (T ? 0 : 8)) * 128 + sw));
        pa[kk] = opaque(lds + T * 2048 + (layer ? kk * 1024 + lane * 16 : r * 128 + sw));
    }
    const LAS char* pb = lds + h * 3072 + lane * 16;
    const LAS char* pt = lds + lane * 16;
    f32x4 acc[5][4];
#pragma unroll
    for (int m = 0; m < 5; ++m)
#pragma unroll
        for (int n = 0; n < 4; ++n) if (m + n) acc[m][n] = pin((f32x4){0.f, 0.f, 0.f, 0.f});
    bf16x8 fa[2][5], fb[2][4];
    auto rd1 = [&](int kk, int i) {
        if (i < 3) fb[kk][i] = LDS_FRAG(pb + 18432 + kk * 6144 + i * 1024);
        else if (i == 3) fb[kk][3] = LDS_FRAG(pt + 30720 + kk * 1024);
        else if (i == 4) fa[kk][0] = LDS_FRAG(ph[kk]);
        else if (i < 9) fa[kk][i - 4] = LDS_FRAG(pa[kk] + 2048 + (i - 5) * 4096);
    };
    auto chunk = [&](int kk, int d) {
#pragma unroll
        for (int i = 1; i < 20; ++i) {
            const int m = i >> 2, n = i & 3;
            const bf16x8 &A = fa[kk][m], &B = fb[kk][n];
            acc[m][n] = n != 2 ? MFMA(B, A, acc[m][n]) : MFMA(A, B, acc[m][n]);
            SCHED0();
            if (i <= 9) rd1(kk ^ 1, i - 1);
            if (kk == 0) {
                if (i == 11 || i == 12) ph[i & 1] = opaque(ph[i & 1] + d);
                if (i == 13 || i == 14) pa[i & 1] = opaque(pa[i & 1] + d);
                if (i == 15) pb = opaque(pb + d);
                if (i == 16) pt = opaque(pt + d);
            }
            SCHED0();
        }
    };
    f32x4 mb, mb0;
#pragma unroll
    for (int j = 0; j < 4; ++j) {
        const bool ok = ((r - 4 * q - j) & 15) < 8;
        mb[j] = ok ? 0.f : -__builtin_inff();
        mb0[j] = (ok && (pm > 0 || 4 * q + j <= r)) ? 0.f : -__builtin_inff();
    }
    mb = pin(mb); mb0 = pin(mb0);
    const unsigned mp0 = pin((4 * q > r ? 0xffffu : 0u) | (4 * q + 1 > r ? 0xffff0000u : 0u)), mp1 = pin((4 * q + 2 > r ? 0xffffu : 0u) | (4 * q + 3 > r ? 0xffff0000u : 0u));
    const unsigned qtm = pin(q == h ? 0xffffffffu : 0u);
    const unsigned vom = pin((unsigned)(r * 16 + hp * 1024 + (2 * h + (q >> 1)) * 256 + (q & 1) * 8)), vot = pin((unsigned)(r * 16 + 16384 + (hp >> 1) * 256 + (hp & 1) * 8 + h * 4));
    const LAS char* tk0 = opaque(lds + K1_SCR + ((q * 4) ^ (T ? 0 : 8)) * 4);
    const LAS char* tk = opaque(lds + K1_SCR + ((1 + T) * 16 + q * 4) * 4);
    const LAS char* tq = opaque(lds + K1_SCR + ((1 + T) * 16 + r) * 4);
    pb = opaque(pb); pt = opaque(pt);
    STAMP(2);
    const int rt = pm * 8 + (hp & 1) * 4 + wid, cg = hp >> 1;
    const ProjOut pr = layer ? proj_out((char*)xa, (char*)xa, rt, cg, lane) : proj_out_final((char*)x0, rt, cg, lane);
    BAR();
    if (layer == 0) done();
    BARX();
    STAMP(3);
#pragma unroll
    for (int i = 0; i < 9; ++i) rd1(0, i);
    SCHED0();
    const Res rx = {__builtin_amdgcn_raw_buffer_load_b128(pr.x, pr.v0, 0, 16), __builtin_amdgcn_raw_buffer_load_b128(pr.x, pr.v1, 0, 16), __builtin_amdgcn_raw_buffer_load_b64(pr.x, pr.v2, 0, 16)};
    SCHED0();
    int slot = 0;
    LOOP_ALIGN();
#pragma unroll 1
    for (int t = 0; t < 9; ++t) {
        const int d = slot == 2 ? -2 * K1_STAGE : K1_STAGE;
        slot = slot == 2 ? 0 : slot + 1;
        SCHED0();
        chunk(0, d);
        WAIT_L0(); BAR();
        SCHED0();
        chunk(1, 0);
    }
    STAMP(4);
    u32x2 ts[5], tf[5];
    {
        LAS char* xb = lds + wid * 320;
#pragma unroll
        for (int m = 0; m < 5; ++m) ts[m] = (u32x2){pack2(acc[m][3][0], acc[m][3][1]), pack2(acc[m][3][2], acc[m][3][3])};
        if (q == 2) {
#pragma unroll
            for (int m = 0; m < 5; ++m) {
                const unsigned w = h ? ts[m].y : ts[m].x;
                *(LAS u16*)(xb + m * 64 + r * 2) = (u16)w; *(LAS u16*)(xb + m * 64 + 32 + r * 2) = (u16)(w >> 16);
            }
        }
        asm volatile("" ::: "memory");
#pragma unroll
        for (int m = 0; m < 5; ++m) tf[m] = *(const LAS u32x2*)(xb + m * 64 + (r & 1) * 32 + q * 8);
    }
    {
        f32x4 rk[5]; float rq[4];
        rk[0] = *(const LAS f32x4*)(tk0);
#pragma unroll
        for (int g = 0; g < 4; ++g) { rk[g + 1] = *(const LAS f32x4*)(tk + g * 128); rq[g] = *(const LAS float*)(tq + g * 128); }
        const f32x4 zero = {0.f, 0.f, 0.f, 0.f};
        u32x4 kf[5];
#pragma unroll
        for (int m = 0; m < 5; ++m) kf[m] = (u32x4){pack2(acc[m][1][0], acc[m][1][1]), pack2(acc[m][1][2], acc[m][1][3]), ts[m].y, 0u};
        f32x4 sp[4], sc[4];
#pragma unroll
        for (int g = 0; g < 4; ++g) {
            const u32x4 qf = {pack2(acc[g + 1][0][0], acc[g + 1][0][1]), pack2(acc[g + 1][0][2], acc[g + 1][0][3]), ts[g + 1].x & qtm, 0u};
            sp[g] = MFMA(__builtin_bit_cast(bf16x8, kf[g]), __builtin_bit_cast(bf16x8, qf), zero);
            sc[g] = MFMA(__builtin_bit_cast(bf16x8, kf[g + 1]), __builtin_bit_cast(bf16x8, qf), zero);
        }
        u32x2 vf[5];
#pragma unroll
        for (int m = 0; m < 5; ++m) vf[m] = (u32x2){pack2(acc[m][2][0], acc[m][2][1]), pack2(acc[m][2][2], acc[m][2][3])};
        f32x4 rs[4], t[4]; float mx[4], sm[4];
#pragma unroll
        for (int g = 0; g < 4; ++g) {
#pragma unroll
            for (int j = 0; j < 4; ++j) { const bool pv = 4 * q + j > r; rs[g][j] = pv ? rk[g][j] : rk[g + 1][j]; t[g][j] = pv ? sp[g][j] : sc[g][j]; }
            t[g] = (t[g] * rs[g]) * (rq[g] * (0.23570226039551584f * 1.4426950408889634f)) + (g == 0 ? mb0 : mb);
            mx[g] = fmaxf(fmaxf(t[g][0], t[g][1]), fmaxf(t[g][2], t[g][3]));
        }
        xq4_max(mx);
#pragma unroll
        for (int g = 0; g < 4; ++g) {
            t[g] = t[g] - mx[g];
#pragma unroll
            for (int j = 0; j < 4; ++j) t[g][j] = __builtin_amdgcn_exp2f(t[g][j]);
            sm[g] = (t[g][0] + t[g][2]) + (t[g][1] + t[g][3]);
        }
        f32x4 om[4], ot[4];
#pragma unroll
        for (int g = 0; g < 4; ++g) {
            const f32x4 p = t[g] * rs[g];
            const unsigned p01 = pack2(p[0], p[1]), p23 = pack2(p[2], p[3]);
            const u32x4 pf = {p01 & mp0, p23 & mp1, p01 & ~mp0, p23 & ~mp1};
            om[g] = MFMA(__builtin_bit_cast(bf16x8, ((u32x4){vf[g].x, vf[g].y, vf[g + 1].x, vf[g + 1].y})), __builtin_bit_cast(bf16x8, pf), zero);
            ot[g] = MFMA(__builtin_bit_cast(bf16x8, ((u32x4){tf[g].x, tf[g].y, tf[g + 1].x, tf[g + 1].y})), __builtin_bit_cast(bf16x8, pf), zero);
        }
        xq4_sum_rcp(sm);
        const auto ro = RSRC(o + (size_t)(pm * 8 + T) * 18432);
        unsigned tw2[4];
#pragma unroll
        for (int g = 0; g < 4; ++g) {
            const f32x4 v = om[g] * sm[g];
            __builtin_amdgcn_raw_buffer_store_b64((u32x2){pack2(v[0], v[1]), pack2(v[2], v[3])}, ro, vom, g * 36864, 16);
            const f32x2 w = ot[g].xy * sm[g];
            tw2[g] = pack2(w.x, w.y);
        }
        if (q == 2) {
#pragma unroll
            for (int g = 0; g < 4; ++g) __builtin_amdgcn_raw_buffer_store_b32(tw2[g], ro, vot, g * 36864, 16);
        }
    }
    STAMP(5);
    {
        WAIT_V(0); BARX();
        STAMP(6);
        f32x4 v[5];
        proj_residual(v, rx);
        const LAS char* pw = opaque(lds + WO_OFF + lane * 16);
        const LAS char* pw2 = opaque(lds + WO_OFF + 41472 + lane * 16);
        const LAS char* ph2 = opaque(lds + WO_OFF + 4096 + q * 128 + (r & 7) * 16);
        const LAS char* ph3 = opaque(lds + WO_OFF + 41472 + 4096 + q * 128 + (r & 7) * 16);
        bf16x8 wf[90];
        auto rdw = [&](int i) {
            const int kc = i / 5, n = i % 5;
            wf[i] = n < 4 ? LDS_FRAG((kc < 9 ? pw : pw2) + (kc % 9) * 4608 + n * 1024) : LDS_FRAG((kc < 9 ? ph2 : ph3) + (kc % 9) * 4608);
        };
#pragma unroll
        for (int i = 0; i < WO_W; ++i) rdw(i);
#pragma unroll
        for (int n = 0; n < 5; ++n) v[n] = pin(v[n]);
        const auto rO = RSRC(o + (size_t)rt * 18432);
        const ProjOut po = proj_out(wl + L_XB, wl + L_STB, rt, cg, lane);
        asm volatile("" :: "s"(rO));
        WAIT_L(0);
        __builtin_amdgcn_s_setprio(REL_PRIO);
        BARX();
        STAMP(7);
        bf16x8 af[18];
        auto ldo = [&](int kc) { af[kc] = __builtin_bit_cast(bf16x8, __builtin_amdgcn_raw_buffer_load_b128(rO, lane * 16 + (kc & 3) * 1024, (kc >> 2) * 4096, 16)); };
#pragma unroll
        for (int kc = 0; kc < WO_PRE; ++kc) { ldo(kc); SCHED0(); }
        static_for<90>([&](auto ic) {
            constexpr int i = ic.value, kc = i / 5, n = i % 5, j = WO_W + 2 * (i / 3) + i % 3;
            MFMA_T(wf[i], af[kc], v[n]);
            SCHED0();
            if (i % 3 < 2 && j < 90) rdw(j);
            if (i % 3 == 2 && (i / 3) % 2 == 0 && i / 6 + WO_PRE < 18) ldo(i / 6 + WO_PRE);
            SCHED0();
        });
        asm volatile("s_nop 4" : "+v"(v[0]), "+v"(v[1]));
        STAMP(8);
        proj_store(v, po);
        __builtin_amdgcn_s_setprio(0);
    }
}

DEVI f32x2 gelu2(f32x2 v) {
    const f32x2 w = v * (v * v * -0.10294324f + -2.3022082f);
    const f32x2 d = (f32x2){__builtin_amdgcn_exp2f(w.x), __builtin_amdgcn_exp2f(w.y)} + 1.0f;
    return v * (f32x2){__builtin_amdgcn_rcpf(d.x), __builtin_amdgcn_rcpf(d.y)};
}
DEVI f32x4 gelu4(f32x4 v) { const f32x2 a = gelu2(v.xy), b = gelu2(v.zw); return (f32x4){a.x, a.y, b.x, b.y}; }
DEVI void rowsum16x8(const f32x4 a, const f32x4 b, float& o0, float& o1) {
    float t0, t1, t2, t3;
    asm volatile(
        "s_nop 1\n\t"
        "v_add_f32_dpp %2, %6, %6 row_ror:8 row_mask:0xf bank_mask:0x3\n\t"
        "v_add_f32_dpp %3, %7, %7 row_ror:8 row_mask:0xf bank_mask:0x3\n\t"
        "v_add_f32_dpp %4, %10, %10 row_ror:8 row_mask:0xf bank_mask:0x3\n\t"
        "v_add_f32_dpp %5, %11, %11 row_ror:8 row_mask:0xf bank_mask:0x3\n\t"
        "v_add_f32_dpp %2, %8, %8 row_ror:8 row_mask:0xf bank_mask:0xc\n\t"
        "v_add_f32_dpp %3, %9, %9 row_ror:8 row_mask:0xf bank_mask:0xc\n\t"
        "v_add_f32_dpp %4, %12, %12 row_ror:8 row_mask:0xf bank_mask:0xc\n\t"
        "v_add_f32_dpp %5, %13, %13 row_ror:8 row_mask:0xf bank_mask:0xc\n\t"
        "v_add_f32_dpp %0, %2, %2 row_half_mirror row_mask:0xf bank_mask:0x5\n\t"
        "s_nop 0\n\t"
        "v_add_f32_dpp %1, %4, %4 row_half_mirror row_mask:0xf bank_mask:0x5\n\t"
        "v_add_f32_dpp %0, %3, %3 row_half_mirror row_mask:0xf bank_mask:0xa\n\t"
        "v_add_f32_dpp %1, %5, %5 row_half_mirror row_mask:0xf bank_mask:0xa\n\t"
        "s_nop 0\n\t"
        "v_add_f32_dpp %0, %0, %0 quad_perm:[1,0,3,2] row_mask:0xf bank_mask:0xf\n\t"
        "v_add_f32_dpp %1, %1, %1 quad_perm:[1,0,3,2] row_mask:0xf bank_mask:0xf\n\t"
        "s_nop 0\n\t"
        "v_add_f32_dpp %0, %0, %0 quad_perm:[2,3,0,1] row_mask:0xf bank_mask:0xf\n\t"
        "v_add_f32_dpp %1, %1, %1 quad_perm:[2,3,0,1] row_mask:0xf bank_mask:0xf\n\t"
        : "=&v"(o0), "=&v"(o1), "=&v"(t0), "=&v"(t1), "=&v"(t2), "=&v"(t3)
        : "v"(a[0]), "v"(a[1]), "v"(a[2]), "v"(a[3]), "v"(b[0]), "v"(b[1]), "v"(b[2]), "v"(b[3]));
}

#define K5_PRE 8
#define RH_MAP 0
#define NET_LAYERS 2
constexpr int G_STAGE = 34816;
constexpr int K5_STAGE = 18432, K5_NS = 8;

constexpr int y_load_at(int slot) {
    int next = K5_PRE;
    for (int s = 0;; ++s) { const bool go = next < 18 + 2 * (s / 3); if (s == slot) return go ? next : -1; next += go; }
}

struct GTile { f32x4 a[4][4], e[2]; };
struct GFrag { bf16x8 a[2][4], b[2][5]; const LAS char *pa[2][2], *pb, *p8; int slot; };
DEVI void g_rd(GFrag& f, int kk, int i) {
    if (i < 4) f.a[kk][i] = LDS_FRAG(f.pa[kk][i >> 1] + (i & 1) * 2048);
    else if (i < 8) f.b[kk][i - 4] = LDS_FRAG(f.pb + (kk * 9 + i - 4) * 1024);
    else f.b[kk][4] = LDS_FRAG(f.p8 + kk * 9216);
}
template <bool ROWS_IN_REGS>
DEVI void g_chunk(GTile& c, GFrag& f, int kk, int d) {
#pragma unroll
    for (int i = 0; i < 18; ++i) {
        const int n = i >> 2, m = i & 3;
        f32x4& e = n < 4 ? c.a[m][n] : c.e[m & 1];
        e = ROWS_IN_REGS ? MFMA(f.a[kk][m], f.b[kk][n], e) : MFMA(f.b[kk][n], f.a[kk][m], e);
        SCHED0();
        if (i < 9) g_rd(f, kk ^ 1, i);
        if (kk == 0) {
            if (i >= 10 && i < 14) f.pa[(i - 10) >> 1][i & 1] = opaque(f.pa[(i - 10) >> 1][i & 1] + d);
            if (i == 14) f.pb = opaque(f.pb + d);
            if (i == 15) f.p8 = opaque(f.p8 + d);
        }
        SCHED0();
    }
}
template <int NS, bool ROWS_IN_REGS>
DEVI void g_steps(GTile& c, GFrag& f, int n) {
    LOOP_ALIGN();
#pragma unroll 1
    for (int t = 0; t < n; ++t) {
        const int d = NS > 0 ? (f.slot == NS - 1 ? -(NS - 1) * G_STAGE : G_STAGE) : (f.slot == 0 ? -(NS + 1) * G_STAGE : -G_STAGE);
        f.slot = NS > 0 ? (f.slot == NS - 1 ? 0 : f.slot + 1) : (f.slot == 0 ? -(NS + 1) : f.slot - 1);
        SCHED0();
        g_chunk<ROWS_IN_REGS>(c, f, 0, d);
        WAIT_L0();
        BAR();
        SCHED0();
        g_chunk<ROWS_IN_REGS>(c, f, 1, 0);
    }
}
DEVI void keep4(const f32x4 (&c)[4]) { asm volatile("" :: "v"(c[0]), "v"(c[1]), "v"(c[2]), "v"(c[3])); }
template <int D, int DB, bool FIRST, class HOOK>
DEVI void g_step_u(GTile& c, GFrag& f, const f32x4 (&c0)[4], HOOK&& hook) {
    static_for<36>([&](auto ic) {
        constexpr int kk = ic.value / 18, i = ic.value % 18, n = i >> 2, m = i & 3;
        if (i == 0) { WAIT_L(0); if (kk) BAR(); SCHED0(); }
        f32x4& e = n < 4 ? c.a[m][n] : c.e[m & 1];
        if (FIRST && kk == 0) asm volatile("v_mfma_f32_16x16x32_bf16 %0, %1, %2, %3" : "=&v"(e) : "v"(f.b[kk][n]), "v"(f.a[kk][m]), "v"(c0[m]));
        else MFMA_T(f.b[kk][n], f.a[kk][m], e);
        SCHED0();
        if (i < 9) g_rd(f, kk ^ 1, i);
        if (kk == 0) {
            if (i >= 10 && i < 14) f.pa[(i - 10) >> 1][i & 1] = opaque(f.pa[(i - 10) >> 1][i & 1] + D);
            if (i == 14) f.pb = opaque(f.pb + DB);
            if (i == 15) f.p8 = opaque(f.p8 + DB);
        }
        if (FIRST && ic.value == 20) keep4(c0);
        hook(ic);
        SCHED0();
    });
}
#define MF(d, a, b) "v_mfma_f32_16x16x32_bf16 " d ", " a ", " b ", " d "\n\t"
template <int D, int DB>
DEVI void g_step_gelu(GTile& c, GFrag& f, f32x4& z, float r, float c1) {
#pragma unroll
    for (int kk = 0; kk < 2; ++kk) {
        float va = z[2 * kk], vb = z[2 * kk + 1], ta, tb;
        WAIT_L(0); if (kk) BAR(); SCHED0();
#pragma unroll
        for (int i = 0; i < 9; ++i) {
            MFMA_T(f.b[kk][i >> 2], f.a[kk][i & 3], c.a[i & 3][i >> 2]);
            if (i == 5) asm volatile("v_mul_f32 %0, %0, %1" : "+v"(va) : "v"(r));
            if (i == 6) asm volatile("v_mul_f32 %0, %0, %1" : "+v"(vb) : "v"(r));
            if (i == 7) asm volatile("v_mul_f32 %0, %1, %1" : "=v"(ta) : "v"(va));
            if (i == 8) asm volatile("v_mul_f32 %0, %1, %1" : "=v"(tb) : "v"(vb));
            SCHED0();
            g_rd(f, kk ^ 1, i);
            SCHED0();
        }
        asm volatile(MF("%0", "%5", "%6") "v_fmaak_f32 %3, %3, %11, 0xc0135761\n\tv_fmaak_f32 %4, %4, %11, 0xc0135761\n\t"
                     MF("%1", "%5", "%7") "v_mul_f32 %3, %3, %9\n\tv_mul_f32 %4, %4, %10\n\t" MF("%2", "%5", "%8") "v_exp_f32 %3, %3"
                     : "+v"(c.a[1][2]), "+v"(c.a[2][2]), "+v"(c.a[3][2]), "+v"(ta), "+v"(tb)
                     : "v"(f.b[kk][2]), "v"(f.a[kk][1]), "v"(f.a[kk][2]), "v"(f.a[kk][3]), "v"(va), "v"(vb), "v"(c1));
        SCHED0(); f.pa[kk][0] = opaque(f.pa[kk][0] + D); SCHED0();
        asm volatile(MF("%0", "%6", "%7") "v_exp_f32 %5, %5\n\t" MF("%1", "%6", "%8") "v_add_f32 %4, 1.0, %4\n\tv_add_f32 %5, 1.0, %5\n\t"
                     MF("%2", "%6", "%9") "v_rcp_f32 %4, %4\n\t" MF("%3", "%6", "%10") "v_rcp_f32 %5, %5"
                     : "+v"(c.a[0][3]), "+v"(c.a[1][3]), "+v"(c.a[2][3]), "+v"(c.a[3][3]), "+v"(ta), "+v"(tb)
                     : "v"(f.b[kk][3]), "v"(f.a[kk][0]), "v"(f.a[kk][1]), "v"(f.a[kk][2]), "v"(f.a[kk][3]));
        SCHED0(); f.pa[kk][1] = opaque(f.pa[kk][1] + D); SCHED0();
        asm volatile(MF("%0", "%4", "%5") "v_mul_f32 %2, %2, %7\n\tv_mul_f32 %3, %3, %8\n\t" MF("%1", "%4", "%6")
                     : "+v"(c.e[0]), "+v"(c.e[1]), "+v"(va), "+v"(vb) : "v"(f.b[kk][4]), "v"(f.a[kk][0]), "v"(f.a[kk][1]), "v"(ta), "v"(tb));
        SCHED0();
        if (kk == 0) { f.pb = opaque(f.pb + DB); f.p8 = opaque(f.p8 + DB); }
        SCHED0();
        z[2 * kk] = va; z[2 * kk + 1] = vb;
    }
}
DEVI void g_fill(GTile& c, const float (&e)[4]) {
#pragma unroll
    for (int m = 0; m < 4; ++m) {
#pragma unroll
        for (int n = 0; n < 4; ++n) c.a[m][n] = pin((f32x4){e[m], e[m], e[m], e[m]});
        if (m < 2) c.e[m] = pin((f32x4){e[m], e[m], e[m], e[m]});
    }
}

#undef STAMP_SEC
#define STAMP_SEC 1
DEVI void gmlp_body(LAS char* lds, int wm, int wn, int pm, int cg, char* z2t, char* y, const u16* __restrict__ bsp STAMP_PARAM) {
    const int mlo = wm * 4 + wn * 2, mhi = wm * 4 + (1 - wn) * 2;
    const float zero4[4] = {0.f, 0.f, 0.f, 0.f};
    GTile z2, z1, acc;
    GFrag f;
    {
        const int lane = lane_id_fresh();
#pragma unroll
        for (int kk = 0; kk < 2; ++kk) { f.pa[kk][0] = lds + mlo * 2048 + kk * 1024 + lane * 16; f.pa[kk][1] = lds + mhi * 2048 + kk * 1024 + lane * 16; }
        f.pb = lds + 16384 + wn * 4096 + lane * 16; f.p8 = lds + 16384 + 8192 + lane * 16; f.slot = 0;
    }
    g_fill(z2, zero4); g_fill(z1, zero4);
#pragma unroll
    for (int i = 0; i < 4; ++i) f.pa[i >> 1][i & 1] = opaque(f.pa[i >> 1][i & 1]);
    f.pb = opaque(f.pb); f.p8 = opaque(f.p8);
    SCHED0();
    WAIT_V(0); BARX();
    BAR();
    BARX();
    STAMP(2);
#pragma unroll
    for (int i = 0; i < 9; ++i) g_rd(f, 0, i);
    g_steps<4, true>(z2, f, 9);
    STAMP(3);
    {
        const int lane = lane_id_fresh(), r = lane & 15, q = lane >> 4;
        f32x4 ra[4];
#pragma unroll
        for (int m = 0; m < 4; ++m) ra[m] = *(const LAS f32x4*)(lds + G_TAB + ((m < 2 ? mlo : mhi) + (m & 1)) * 64 + q * 16);
        SCHED0();
#pragma unroll
        for (int m = 0; m < 4; ++m) {
            f32x4 s1, s2;
#pragma unroll
            for (int n = 0; n < 4; ++n) {
                const f32x4 g = gelu4(z2.a[m][n] * ra[m]);
                z2.a[m][n] = g;
                if (n == 0) { s1 = g; s2 = g * g; } else { s1 += g; s2 += g * g; }
            }
            if (m < 2) { const f32x4 g = gelu4(z2.e[m] * ra[m]); z2.e[m] = g; s1 += g; s2 += g * g; }
            float u0, u1;
            rowsum16x8(s1, s2, u0, u1);
            if ((r & 3) == 0) *(LAS f32x2*)(lds + G_PART + wn * 1024 + ((m < 2 ? mlo : mhi) + (m & 1)) * 128 + (q * 4 + (r >> 2)) * 8) = (f32x2){u0, u1};
            SCHED0();
        }
    }
    WAIT_L0(); BAR();
    STAMP(4);
    g_steps<4, false>(z1, f, 4);
    unsigned b0, b1;
    f32x4 c0[4];
    g_step_u<G_STAGE, G_STAGE, false>(z1, f, c0, [&](auto ic) {
        if (ic.value != 18) return;
        STAMP(5);
        const int lane = lane_id_fresh(), q = lane >> 4;
        b0 = __builtin_amdgcn_raw_buffer_load_b32(RSRC(bsp), (lane & 15) * 4, pm * 256 + mlo * 32, 0);
        b1 = __builtin_amdgcn_raw_buffer_load_b32(RSRC(bsp), (lane & 15) * 4, pm * 256 + mhi * 32, 0);
#pragma unroll
        for (int g = 0; g < 2; ++g) {
            const auto rz2 = RSRC(z2t + (size_t)pm * 589824 + cg * 36864 + ((g ? mhi : mlo) >> 1) * 9216);
            const LAS char* tab = lds + G_TAB + (g ? mhi : mlo) * 64 + q * 16;
            LAS char* own = lds + (wm ? G_OWN : G_STAGE + 16384) + (g ^ wn) * 9216 + lane * 16;
            f32x4 ra[2], rb[2];
#pragma unroll
            for (int T = 0; T < 2; ++T) { ra[T] = *(const LAS f32x4*)(tab + 512 + T * 64); rb[T] = *(const LAS f32x4*)(tab + 1024 + T * 64); }
#pragma unroll
            for (int n = 0; n < (g ? 4 : 5); ++n) {
                const f32x4 v0 = (n < 4 ? z2.a[2 * g][n & 3] : z2.e[0]) * ra[0] + rb[0], v1 = (n < 4 ? z2.a[2 * g + 1][n & 3] : z2.e[1]) * ra[1] + rb[1];
                const u32x4 o = {pack2(v0[0], v1[0]), pack2(v0[1], v1[1]), pack2(v0[2], v1[2]), pack2(v0[3], v1[3])};
                ST16(rz2, lane * 16, n < 4 ? wn * 4096 + n * 1024 : 8192, o, 16);
                *(LAS u32x4*)(own + (n < 4 ? wn * 4096 + n * 1024 : 8192)) = o;
            }
        }
        STAMP(6);
    });
    static_for<3>([&](auto sc) {
        constexpr int D = sc.value == 0 ? G_STAGE : sc.value == 1 ? -3 * G_STAGE : 2 * G_STAGE;
        g_step_u<D, D, false>(z1, f, c0, [&](auto ic) { if (ic.value == 17 && sc.value == 1) WAIT_V(0); });
    });
    float ra[4], c1;
    {
        int lane, da[2];
        g_step_u<-G_STAGE, -G_STAGE, false>(z1, f, c0, [&](auto ic) {
            constexpr int i = ic.value;
            if (i == 9) lane = pin(lane_id_fresh());
            if (i == 14 || i == 15) da[i & 1] = pin((lane & 15) * 128 + (((4 * (i & 1) + (lane >> 4)) ^ (lane & 7)) * 16) - lane * 16 - (i & 1) * 1024);
            if (i == 16 || i == 17) f.pa[0][i & 1] = opaque(f.pa[0][i & 1] + da[0]);
            if (i == 27 || i == 28) f.pa[1][i & 1] = opaque(f.pa[1][i & 1] + da[1]);
            if (i >= 29 && i < 33) {
                constexpr int m = (i - 29) & 3;
                const float e = m & 1 ? bfhi(m < 2 ? b0 : b1) : bflo(m < 2 ? b0 : b1);
                c0[m] = pin((f32x4){e, e, e, e});
            }
            if (i == 33) {
#pragma unroll
                for (int m = 0; m < 4; ++m) ra[m] = *(const LAS float*)(lds + G_TAB + ((m < 2 ? mlo : mhi) + (m & 1)) * 64 + (lane & 15) * 4);
                c1 = pin(-0.10294324f);
            }
        });
    }
    STAMP(7);
    g_step_u<2 * G_STAGE, G_OWN - G_STAGE - 16384, true>(acc, f, c0, [](auto) {});
    static_for<18>([&](auto sc) {
        constexpr int st = sc.value, m = st < 16 ? st >> 2 : st & 1;
        constexpr int D = st == 0 ? -3 * G_STAGE : st % 3 == 1 ? 2 * G_STAGE : -G_STAGE;
        g_step_gelu<D, st == 0 ? 16384 - G_OWN : D>(acc, f, st < 16 ? z1.a[m][st & 3] : z1.e[m], ra[m], c1);
    });
    f.slot = 1;
    STAMP(9);
    g_steps<-3, false>(acc, f, 13);
    STAMP(10);
    const int lane = lane_id_fresh();
#pragma unroll
    for (int g = 0; g < 2; ++g) {
        const auto ry = RSRC(y + (size_t)(pm * 8 + (g ? mhi : mlo)) * 73728 + cg * 4608);
#pragma unroll
        for (int T = 0; T < 2; ++T) {
            const int m = 2 * g + T;
#pragma unroll
            for (int p = 0; p < 2; ++p) {
                const f32x4 a = acc.a[m][2 * p] * z1.a[m][2 * p], c = acc.a[m][2 * p + 1] * z1.a[m][2 * p + 1];
                ST16(ry, lane * 16 + wn * 2048 + p * 1024, T * 73728, ((u32x4){pack2(a[0], a[1]), pack2(a[2], a[3]), pack2(c[0], c[1]), pack2(c[2], c[3])}), 16);
            }
            if (g == 0) {
                const f32x4 a = acc.e[T] * z1.e[T];
                ST8(ry, (lane >> 5) * 256 + (lane & 15) * 16 + ((lane >> 4) & 1) * 8, T * 73728 + 4096, ((u32x2){pack2(a[0], a[1]), pack2(a[2], a[3])}), 16);
            }
        }
    }
}

#define NET_IDS \
    int b = __builtin_amdgcn_readfirstlane(blockIdx.x), wid = wid8 & 3; \
    asm volatile("" : "+s"(b), "+s"(wid));     \
    const int xcd = b & 7, idx = b >> 3; \
    const int pa = (xcd >> 1) * 4 + (idx & 3), hp = (xcd & 1) * 8 + (idx >> 2); \
    const int pm = (xcd >> 2) * 8 + (idx & 7), cg = (xcd & 3) * 4 + (idx >> 3); \
    const int rh = RH_MAP ? xcd * 4 + (idx & 3) : (xcd >> 1) * 8 + (idx & 7), c5 = RH_MAP ? idx >> 2 : (xcd & 1) * 4 + (idx >> 3); \
    const int lead = (idx + (idx >> 3)) & 3; \
    auto kord = [&](int t, int n) { \
        const int k = t + ((int)(0x1F000000u << (((t - lead) & 3) * 4)) >> 28); \
        return (unsigned)k < (unsigned)n ? k : t; \
    }; \
    auto kord5 = [&](int t) { \
        const int k = t + ((int)(0x1F000000u << (((t - (RH_MAP ? c5 : lead)) & 3) * 4)) >> 28); \
        return (unsigned)k < 18u ? k : t; \
    };

typedef short s16x4 __attribute__((ext_vector_type(4)));
DEVI u32x4 tr_frag(const LAS char* p, int four_rows) {
    const u32x2 a = __builtin_bit_cast(u32x2, __builtin_amdgcn_ds_read_tr16_b64_v4i16((LAS s16x4*)p));
    const u32x2 b = __builtin_bit_cast(u32x2, __builtin_amdgcn_ds_read_tr16_b64_v4i16((LAS s16x4*)(p + four_rows)));
    return (u32x4){a.x, a.y, b.x, b.y};
}
DEVI float dot2(unsigned v, unsigned w, float acc) { return __builtin_amdgcn_fdot2_f32_bf16(__builtin_bit_cast(bf16x2_t, v), __builtin_bit_cast(bf16x2_t, w), acc, false); }
DEVI float sum2(unsigned v, float acc) { return dot2(v, 0x3f803f80u, acc); }
struct Sel { unsigned lo, hi, nlo, nhi; };
DEVI Sel make_sel() { return {(unsigned)pin_s(0x00003f80), (unsigned)pin_s(0x3f800000), (unsigned)pin_s(0x0000bf80), (unsigned)pin_s((int)0xbf800000u)}; }
DEVI unsigned centre2(unsigned v, f32x2& e, float mean, const Sel& k) {
    const float a = dot2(k.lo, v, e.x), b = dot2(k.hi, v, e.y);
    const unsigned r = pack2(a, b);
    e = (f32x2){dot2(k.nlo, r, a), dot2(k.nhi, r, b)} - mean;
    return r;
}
struct Retile { u32x4 f[9]; };
template <int PITCH>
DEVI void retile_out_make(Retile& o, const LAS char* img, int lane) {
    const int c = lane & 15, q = lane >> 4, p = lane & 3;
    const LAS char* src = img + (q * 8 + (c >> 2)) * PITCH;
#pragma unroll
    for (int i = 0; i < 8; ++i) o.f[i] = tr_frag(src + p * 16 + (i >> 2) * 144 + ((i >> 1) & 1) * 64 + (i & 1) * 8, 4 * PITCH);
    o.f[8] = tr_frag(src + 128 + (p >> 1) * 144 + (p & 1) * 8, 4 * PITCH);
}
DEVI void retile_out_store(const Retile& o, char* dst, int nkc, int kc, int w, int lane) {
    const int c = lane & 15, q = lane >> 4;
    const auto rd = RSRC(dst + (size_t)(2 * w * nkc + kc) * 4608);
#pragma unroll
    for (int i = 0; i < 8; ++i) ST16(rd, lane * 16 + (i & 3) * 1024, (i >> 2) * nkc * 4608, o.f[i], 16);
    ST16(rd, (c >> 3) * nkc * 4608 + 4096 + q * 128 + (c & 7) * 16, 0, o.f[8], 16);
}

DEVI float dpp_ror(float v, int n) { return n == 8 ? __builtin_amdgcn_update_dpp(0.f, v, 0x128, 0xf, 0xf, true) : n == 4 ? __builtin_amdgcn_update_dpp(0.f, v, 0x124, 0xf, 0xf, true)
                                          : n == 2 ? __builtin_amdgcn_update_dpp(0.f, v, 0x122, 0xf, 0xf, true) : __builtin_amdgcn_update_dpp(0.f, v, 0x121, 0xf, 0xf, true); }
DEVI unsigned long long epoch_of(const char* ws) {
    SCHED0();
    const unsigned long long ep = *(const __attribute__((address_space(4))) unsigned long long*)(ws + pin_s((int)OFF_EP)) + 1ull;
    asm volatile("" :: "s"(ep));
    return ep;
}
DEVI void prologue_done(LAS char* lds, char* ws, unsigned long long ep, int b, int lane) {
    if (lds_arrive(lds + G_WORD + 8) == 7) ST16(RSRC(ws), lane ? 0x80000000u : (unsigned)OFF_FL + b * 16, 0, ((u32x4){(unsigned)ep, (unsigned)(ep >> 32), 0u, 0u}), 16);
}
DEVI void centre_frag(u32x4& f, f32x2 (&er)[4], float mean, const Sel& k) {
#pragma unroll
    for (int e = 0; e < 4; ++e) f[e] = centre2(f[e], er[e], mean, k);
}
DEVI unsigned long long prologue(LAS char* lds, char* ws, const char* x0, const char* wqkv, const u16* wo, const u16* w_in, const u16* w_out, int b, int pa, int wid8 PST_PARAM) {
    PST_DECL;
    const int lane = lane_id_fresh(), c = lane & 15, q = lane >> 4, w = wid8 & 3;
    const bool stg = wid8 >= 4, wjob = b < 96 && wid8 > 4, ijob = b >= 112;
    const unsigned l0 = (unsigned)(size_t)lds;
    LAS char* img = lds + 3 * K1_STAGE;
    LAS float* red = (LAS float*)(lds + G_PART);
    const int head = (b & 7) * 4 + ((b >> 3) & 3), part = b >> 5, hp = head >> 1, odd = head & 1;
    const int c0 = part * 1152 + hp * 72 + odd * 40;
    const int ui = b - 112, st = (ui & ~15) + 2 * (ui & 7) + ((ui >> 3) & 1);
    if (wjob) {
        const auto rs = __builtin_amdgcn_make_buffer_rsrc((void*)wqkv, 0, 576 * 3456, 0x00020000);
        unsigned v[3];
#pragma unroll
        for (int i = 0; i < 3; ++i) { const int e = i * 64 + lane, row = (e * 21846) >> 16; v[i] = row * (3456 - 48) + lane * 16 + (w - 1) * 192 * 3456 + (c0 & ~15); }
#pragma unroll
        for (int i = 0; i < 3; ++i)
            asm volatile("s_mov_b32 m0, %0\n\ts_nop 3\n\t" DMA_I("%3", "0", "") DMA_I("%4", "1024", "") DMA_I("%5", "2048", "")
                         :: "s"(l0 + 2 * K1_STAGE + (w - 1) * 9216 + i * 3072), "s"(rs), "s"(0), "v"(v[0] + i * 64 * 3456), "v"(v[1] + i * 64 * 3456), "v"(v[2] + i * 64 * 3456) : "m0", "memory");
    }
    if (ijob) {
        const unsigned v = (lane >> 2) * 9216 + (lane & 3) * 16, d = 16 * 9216 - 1024;
        const auto rs = RSRC((const char*)(w_in + st * 32) + (size_t)wid8 * 72 * 9216);
        dma4<false>(l0 + 3 * K1_STAGE + wid8 * 4608, rs, 0, v, v + d, v + 2 * d, v + 3 * d);
        asm volatile("s_mov_b32 m0, %0\n\ts_mov_b32 exec_hi, 0\n\ts_nop 2\n\t" DMA_I("%3", "0", "") "s_mov_b32 exec_hi, -1"
                     :: "s"(l0 + 3 * K1_STAGE + wid8 * 4608 + 4096), "s"(rs), "s"(64 * 9216), "v"(v) : "m0", "memory");
    }
    if (wid8 == 4) { lds_word_set(lds + G_WORD + c * 4, 0u); WAIT_L0(); }
    if (!ijob) BAR();
    u32x4 xv[3];
    if (!stg) {
        if (b < 72) {
            const auto rs = RSRC((const char*)w_out + (size_t)b * 36864);
#pragma unroll
            for (int j = 0; j < 9; ++j) dma1(l0 + 3 * K1_STAGE + (j * 4 + w) * 1024, rs, (j * 4 + w) * 1024, lane * 16);
        } else if (b < 90) {
            const auto rs = RSRC(wo + w * 144);
#pragma unroll
            for (int j = 0; j < 9; ++j) {
                const int i = lane + 64 * j, rl = (i * 3641) >> 16, kp = (b - 72) * 32 + rl;
                const int row = kp < 512 ? 18 * (kp >> 4) + (kp & 15) + 2 * ((kp >> 4) & 1) : 36 * ((kp - 512) >> 2) + 16 + (kp & 3);
                dma1(l0 + 3 * K1_STAGE + w * 9216 + j * 1024, rs, 0, (unsigned)(row * 1152 + (i - rl * 18) * 16));
            }
        }
        {
            const unsigned v = (lane >> 5) * 2304 + (lane & 31) * 16;
            asm volatile("s_nop 4\n\tbuffer_load_dwordx4 %0, %3, %5, 0 offen\n\tbuffer_load_dwordx4 %1, %3, %5, 0 offen offset:512\n\tbuffer_load_dwordx4 %2, %4, %5, 0 offen offset:1024"
                         : "+v"(xv[0]), "+v"(xv[1]), "+v"(xv[2]) : "v"(v), "v"(lane & 24 ? 0x80000000u : v), "s"(RSRC(x0 + (size_t)slot2row(b * 8 + w * 2) * 1152)) : "memory");
        }
        const auto rw = RSRC(ws);
        const unsigned t = w * 64 + lane, z = w < 2 ? 0xffffffffu : 0u;
        ST16(rw, w < 2 ? (unsigned)OFF_ZST + b * 2048 + t * 16 : w == 2 ? (unsigned)(OFF_L0 + L_O) + (b >> 1) * 18432 + 16384 + (b & 1) * 1024 + lane * 16
                                                                        : lane < 32 ? (unsigned)(OFF_L0 + L_STB) + b * 512 + lane * 16 : 0x80000000u, 0, ((u32x4){z, z, z, z}), 16);
        ST16(rw, t < 160 ? (unsigned)OFF_CNT + b * 2560 + t * 16 : 0x80000000u, 0, ((u32x4){0u, 0u, 0u, 0u}), 16);
    }
    if (wid8 == 4) {
        unsigned long long pc; int d0, len;
        asm volatile("s_getpc_b64 %0\n1:\n\ts_mov_b32 %1, gm_code_begin-1b\n\ts_mov_b32 %2, gm_code_end1-gm_code_begin\n\ts_max_i32 %2, %2, gm_code_end2-gm_code_begin"
                     : "=s"(pc), "=s"(d0), "=s"(len) :: "scc");
        const auto rs = __builtin_amdgcn_make_buffer_rsrc((void*)((pc + d0) & ~15ull), 0, len, 0x00020000);
        const unsigned v = lane * 16 + (b >> 3) * 4096;
        dma4<false>(l0 + 2 * K1_STAGE + 27648, rs, 0, v, v, v, v);
    }
    PST(1);
    const unsigned long long ep = epoch_of(ws);
    const Sel k = make_sel();
    const AttnX ax = attn_x(x0, x0, 0, pa, w, lane);
    auto first_x = [&] { attn_issueA(lds, ax, w, 0, 0); attn_issueA(lds, ax, w, 1, 1); };
    if (ijob) { if (wid8 == 4) WAIT_V(4); else if (stg) WAIT_V(0); else WAIT_V(2); BAR(); }
    PST(2);
    if (ijob) {
        const int u = st * 2 + (wid8 & 1), kq = wid8 >> 1, k0 = (kq * 9 + 1) >> 1, half = u >= 144, g = u - half * 144, cg = g / 9, j = g - cg * 9, p = lane & 3;
        const LAS char* src = img + (k0 * 32 + q * 8 + (c >> 2)) * 64 + (wid8 & 1) * 32 + (j < 8 ? (p & 1) * 16 + (p >> 1) * 8 : p * 8);
        u32x4 f[5];
#pragma unroll
        for (int i = 0; i < 5; ++i) f[i] = tr_frag(src + (i < 4 || !(kq & 1) ? i : 0) * 2048, 256);
        float sm[2] = {0.f, 0.f}, s4 = 0.f;
#pragma unroll
        for (int e = 0; e < 16; ++e) sm[e & 1] = sum2(f[e >> 2][e & 3], sm[e & 1]);
#pragma unroll
        for (int e = 0; e < 4; ++e) s4 = sum2(f[4][e], s4);
        const float t = xq_sum(sm[0] + sm[1] + (kq & 1 ? 0.f : s4));
        if (lane < 16) red[wid8 * 16 + lane] = t;
        const auto rd = RSRC(ws + OFF_TWIN + (size_t)cg * (18 * 18432) + (half * 18 + k0) * 9216);
        const unsigned vo = pin((unsigned)(q * 256 + (j < 8 ? ((j & ~1) + (c >> 3)) * 1024 + ((j & 1) * 8 + (c & 7)) * 16 : 8192 + c * 16)));
        WAIT_L0(); BAR();
        PST(3);
        const LAS float* rr = red + (wid8 & 1) * 16 + c;
        const float mean = ((rr[0] + rr[32]) + (rr[64] + rr[96])) * (1.0f / 576.0f);
        f32x2 er[4] = {{-mean, -mean}, {-mean, -mean}, {-mean, -mean}, {-mean, -mean}};
#pragma unroll
        for (int i = 0; i < 5; ++i) if (i < 4 || !(kq & 1)) { centre_frag(f[i], er, mean, k); ST16(rd, vo, i * 9216, f[i], 16); }
    }
    if (wjob) {
        WAIT_V(0);
        PST(3);
        const LAS char* src = lds + 2 * K1_STAGE + (w - 1) * 9216 + (q * 8 + (c >> 2)) * 48 + (c0 & 15);
        u32x4 f[6], g[2];
        float sm[2] = {0.f, 0.f}, sg = 0.f;
#pragma unroll
        for (int i = 0; i < 6; ++i) f[i] = tr_frag(src + (c & 3) * 8 + i * 1536, 192);
#pragma unroll
        for (int i = 0; i < 2; ++i) g[i] = tr_frag(src + 32 - odd * 8 + (3 * i + min(c & 3, 2)) * 1536, 192);
#pragma unroll
        for (int e = 0; e < 24; ++e) sm[e & 1] = sum2(f[e >> 2][e & 3], sm[e & 1]);
#pragma unroll
        for (int e = 0; e < 8; ++e) sg = sum2(g[e >> 2][e & 3], sg);
        sg = c < 12 ? sg : 0.f;
        sg += dpp_ror(sg, 4); sg += dpp_ror(sg, 8);
        const float t0 = xq_sum(sm[0] + sm[1]), t1 = xq_sum(sg);
        if (lane < 32) red[(w - 1) * 32 + lane] = q ? t1 : t0;
        const auto rd = RSRC(ws + OFF_TWQKV + (size_t)(hp * 18 + (w - 1) * 6) * 6144 + (3 * odd + part) * 1024);
        const auto rt = RSRC(ws + OFF_TWT + (size_t)((hp * 3 + part) * 18 + (w - 1) * 6) * 256);
        const unsigned vt = pin(c < 12 ? (unsigned)((c >> 2) * 256 + q * 64 + (c & 3) * 16) : 0x80000000u);
        const unsigned fo = pin((unsigned)OFF_FW + ((lane >> 3) * 16 + hp) * 1024 + (3 * odd + part) * 128 + (lane & 7) * 16);
        if (lds_arrive(lds + G_WORD + 12) < 2) lds_count_wait(lds + G_WORD + 12, 3);
        PST(4);
        const float mean = ((red[c] + red[32 + c]) + red[64 + c]) * (1.0f / 576.0f), mt = ((red[16 + c] + red[48 + c]) + red[80 + c]) * (1.0f / 576.0f);
        f32x2 er[4] = {{-mean, -mean}, {-mean, -mean}, {-mean, -mean}, {-mean, -mean}};
#pragma unroll
        for (int i = 0; i < 6; ++i) { centre_frag(f[i], er, mean, k); ST16(rd, lane * 16, i * 6144, f[i], 16); }
        if (!odd) {
            f32x2 et[4] = {{-mt, -mt}, {-mt, -mt}, {-mt, -mt}, {-mt, -mt}};
#pragma unroll
            for (int i = 0; i < 2; ++i) { centre_frag(g[i], et, mt, k); ST16(rt, vt, i * 768, g[i], 16); }
        }
        PST(5);
        WAIT_V(0);
        PST(6);
        if (lds_arrive(lds + G_WORD + 4) == 2) {
            const u32x4 e4 = {(unsigned)ep, (unsigned)(ep >> 32), (unsigned)ep, (unsigned)(ep >> 32)};
            ST16(RSRC(ws), fo, 0, e4, 16); ST16(RSRC(ws), fo + 131072, 0, e4, 16);
        }
    }
    if (!stg && b < 90) {
        WAIT_V(2);
        PST(3);
        if (lds_arrive(lds + G_WORD) < 3) lds_count_wait(lds + G_WORD, 4);
        PST(4);
        Retile rt;
        if (b < 72) { retile_out_make<1152>(rt, img + w * 288, lane); retile_out_store(rt, ws + OFF_TWOUT, 72, b, w, lane); }
        else { retile_out_make<288>(rt, img + w * 9216, lane); retile_out_store(rt, ws + OFF_TWO, 18, b - 72, w, lane); }
    }
    if (stg) {
        if (ijob) WAIT_V(0);
        if (b < 96 && w == 0) lds_count_wait(lds + G_WORD + 4, 1);
        first_x();
        prologue_done(lds, ws, ep, b, lane);
    } else {
        if (b >= 90 && !ijob) WAIT_V(2);
        asm volatile("" : "+v"(xv[0]), "+v"(xv[1]), "+v"(xv[2]));
        float s = 0.f, q2 = 0.f;
#pragma unroll
        for (int e = 0; e < 12; ++e) { s = sum2(xv[e >> 2][e & 3], s); q2 = dot2(xv[e >> 2][e & 3], xv[e >> 2][e & 3], q2); }
#pragma unroll
        for (int n = 8; n >= 1; n >>= 1) { s += dpp_ror(s, n); q2 += dpp_ror(q2, n); }
        auto y = __builtin_amdgcn_permlane16_swap(__float_as_uint(s), __float_as_uint(q2), false, false);
        const unsigned t = __float_as_uint(__uint_as_float(y[0]) + __uint_as_float(y[1]));
        auto z = __builtin_amdgcn_permlane16_swap(t, t, false, false);
        ST16(RSRC(ws + OFF_L0 + L_STA + (size_t)(b * 8 + w * 2) * 64), lane & 28 ? 0x80000000u : (unsigned)((lane >> 5) * 64 + c * 16), 0,
             ((u32x4){c ? 0u : z[0], c ? 0u : z[1], 0u, 0u}), 16);
    }
    PST(7);
    PST_DUMP(wid8 == 0 || wid8 == 4 || wid8 == 5, wid8 ? wid8 : 2);
    return ep;
}

__global__ __launch_bounds__(512, 2) TRACE_ATTR void k_net(char* ws, const u16* wsp, const u16* bsp, char* out, const char* x0, const char* wqkv, const u16* wo,
                                                             const u16* w_in, const u16* w_out DBG_ARG) {
    asm volatile("gm_code_begin:" :: "s"(ws), "s"(wsp), "s"(bsp), "s"(out), "s"(x0), "s"(wqkv), "s"(wo), "s"(w_in), "s"(w_out));
    ws = own_s(ws); wsp = own_s(wsp); bsp = own_s(bsp); out = own_s(out); x0 = own_s(x0); wqkv = own_s(wqkv); wo = own_s(wo); w_in = own_s(w_in);
    extern __shared__ __attribute__((aligned(16))) char smem[];
    LAS char* lds = (LAS char*)smem;
    const char *twin = ws + OFF_TWIN, *twout = ws + OFF_TWOUT;
    STAMP_DECL;
    const int wid8 = __builtin_amdgcn_readfirstlane(threadIdx.x >> 6);
    if (wid8 > 4 && blockIdx.x < 96) __builtin_amdgcn_s_setprio(3);
    unsigned long long ep;
    { NET_IDS ep = prologue(lds, ws, x0, wqkv, wo, w_in, w_out, b, pa, wid8 PST_FWD); }
    if (wid8 >= 4) {
        u32x4 wr[5][9];
#pragma unroll 1
      for (int layer = 0; layer < NET_LAYERS; ++layer) {
        NET_IDS
        TRACE_BEGIN;
        char* wl = ws + OFF_L0 + layer * SZ_L;
        const unsigned fl = layer * F_LAYER;
        const char *xin = wl + L_XB, *st = wl + L_STB, *z2t = wl + (layer ? L_Y : L_Z2T);
        unsigned long long* zst = (unsigned long long*)(ws + OFF_ZST + layer * SZ_ZST);
        const int lane = lane_id_fresh(), l3 = lane >> 3;
        const unsigned vo = lane * 16;
        const unsigned l0 = (unsigned)(size_t)lds, lA = l0 + wid * 4096, lB = l0 + 16384 + wid * 4608;
        const auto rX = RSRC(xin + (size_t)(pm * 8 + 2 * wid) * 18432), rI = RSRC(twin + (size_t)cg * (18 * 18432) + wid * 4608);
        auto issue1B = [&](int g, int slot) { const int k = kord(g < 9 ? g : (g < 13 ? g - 4 : g - 13), 9); dma5<true>(lB + slot * G_STAGE, rI, (g < 9 ? 9 + k : k) * 18432, vo); };
        auto issue1A = [&](int g, int slot) { dma4<true>(lA + slot * G_STAGE, rX, kord(g < 9 ? g : g - 13, 9) * 2048, vo, vo, vo + 16384, vo + 16384); };
        auto issue1 = [&](int g, int slot) { issue1B(g, slot); issue1A(g, slot); };
        const auto rA = RSRC((const char*)wsp + (size_t)(pm * 128 + wid * 32) * 4096), rB = RSRC(z2t + cg * 36864 + wid * 4608);
        const unsigned voA = (unsigned)(2 * l3 * 4096 + ((lane & 7) ^ l3) * 16);
        auto issueB = [&](int k, int slot) { dma5<true>(lB + slot * G_STAGE, rB, (k >> 1) * 589824 + (k & 1) * 18432, vo); };
        const auto rW = RSRC(twout + (size_t)c5 * (72 * 4608));
        auto issue5 = [&](int t) { dma5<true>(l0 + ((t + 6) & 7) * K5_STAGE + wid * 4608, rW, kord5(t) * K5_STAGE + wid * 4608, vo); };
        auto load5 = [&](int p, int i) {
            const int t0 = p ? 2 * p + 6 : 4, ow = kord5(t0 + i) * K5_STAGE + wid * 4096;
            asm volatile("s_nop 4\n\tbuffer_load_dwordx4 %0, %4, %5, %6 offen sc1\n\tbuffer_load_dwordx4 %1, %4, %5, %6 offen offset:1024 sc1\n\t"
                         "buffer_load_dwordx4 %2, %4, %5, %6 offen offset:2048 sc1\n\tbuffer_load_dwordx4 %3, %4, %5, %6 offen offset:3072 sc1"
                         : "+v"(wr[p][4 * i]), "+v"(wr[p][4 * i + 1]), "+v"(wr[p][4 * i + 2]), "+v"(wr[p][4 * i + 3]) : "v"(vo), "s"(rW), "s"(ow) : "memory");
            if (i) {
                const int oe = kord5(t0 + (wid >> 1)) * K5_STAGE + 16384 + (wid & 1) * 1024;
                asm volatile("s_nop 4\n\tbuffer_load_dwordx4 %0, %1, %2, %3 offen sc1" : "+v"(wr[p][8]) : "v"(vo), "s"(rW), "s"(oe) : "memory");
            }
        };
        auto put5 = [&](int w0, int w1) {
#pragma unroll
            for (int w = w0; w < w1; ++w) {
                const int p = w / 9, i = w % 9 - 1;
                LAS char* sl = lds + (((p ? 2 * p + 6 : 4) + 6) & 7) * K5_STAGE + lane * 16;
                if (i < 0) *(LAS u32x4*)(sl + (wid >> 1) * K5_STAGE + 16384 + (wid & 1) * 1024) = wr[p][8];
                else *(LAS u32x4*)(sl + (i >> 2) * K5_STAGE + wid * 4096 + (i & 3) * 1024) = wr[p][i];
            }
            WAIT_L0();
        };
        const int row = wid * 32 + (lane & 31);
        auto step1 = [&](int t) { BAR(); issue1(t + 4, t & 3); };
        const char* ps;
        {
            unsigned fo, fi;
            attn_producer(lds, ws, x0, wl, fl, ep, layer, pa, hp, wid, [&] {
#pragma unroll
                for (int i = 0; i < 2; ++i)
                    asm volatile("s_nop 4\n\tbuffer_load_dwordx4 %0, %4, %5, %6 offen\n\tbuffer_load_dwordx4 %1, %4, %5, %6 offen offset:1024\n\t"
                                 "buffer_load_dwordx4 %2, %4, %5, %6 offen offset:2048\n\tbuffer_load_dwordx4 %3, %4, %5, %6 offen offset:3072"
                                 : "+v"(wr[0][4 * i]), "+v"(wr[0][4 * i + 1]), "+v"(wr[0][4 * i + 2]), "+v"(wr[0][4 * i + 3]) : "v"(vo), "s"(RSRC(wsp)), "s"(b * 32768 + wid * 8192 + i * 4096) : "memory");
            }, [&] {
                issue1B(0, 0); issue1B(1, 1);
                fo = flag_out(fl + F_X1, 256, pa, hp, lane); fi = flag_in(fl + F_X1, 256, pm * 16 + cg, 16, lane);
                ps = pin(st + (size_t)(pm * 128 + wid * 32) * 64 + lane * 16);
            });
            BARX();
            if (wid == 0) flag_raise(ws, fo);
            issue1B(2, 2); issue1B(3, 3);
            asm volatile("" :: "s"(rX));
            if (wid == 0) flags_wait<X1_FIRST>(ws, fi);
            BARX();
        }
        issue1A(0, 0); issue1A(1, 1); issue1A(2, 2);
        WAIT_V(8); BAR();
        issue1A(3, 3);
        {
            f32x4 sp[3];
            WAIT_V(8); step1(0);
            WAIT_V(13); step1(1);
            WAIT_V(18); step1(2);
            WAIT_V(18); step1(3);
            WAIT_V(18); step1(4);
            WAIT_V(18); BAR(); issue1B(9, 1); side_load(sp, ps, ps); if (layer == 0) load5(3, 0);
            WAIT_IF(layer, 17, 21); BAR(); issue1B(10, 2); if (layer == 0) load5(3, 1);
            WAIT_IF(layer, 5, 14); BARX();
            side_fence(sp);
#pragma unroll
            for (int i = 0; i < 2; ++i) lds_word_set(lds + G_TAB + (wid * 32 + i * 16 + (lane >> 2)) * 4, __float_as_uint(ln_rstd(sp[i])));
            issue1B(11, 3);
            WAIT_L0();
        }
        BAR(); issue1B(12, 0);
        if (layer == 1) {
            const auto rd = RSRC(out + (size_t)b * 9216 + wid * 2304);
            ST16(rd, vo, 0, ((u32x4){0u, 0u, 0u, 0u}), 16); ST16(rd, vo, 1024, ((u32x4){0u, 0u, 0u, 0u}), 16);
            if (lane < 16) ST16(rd, vo, 2048, ((u32x4){0u, 0u, 0u, 0u}), 16);
        }
        if (layer == 0) {
#pragma unroll
            for (int p = 0; p < 6; ++p) load5(p >> 1, p & 1);
        }
        WAIT_V(0);
        __builtin_amdgcn_s_setprio(3);
        const char* src = pin((const char*)zst + (size_t)(pm * 16 + (lane >> 4) * 4) * 1024 + wid * 256 + (lane & 15) * 16);
        const unsigned tw = pin(l0 + G_TAB + 512 + (lane >> 5) * 512 + (wid * 32 + (lane & 15) * 2 + ((lane >> 4) & 1)) * 4);
        const unsigned fz = flag_out(fl + F_Z2, 256, cg, pm, lane), fzi = pin(fl + F_Z2 + (cg * 16 + pm) * 256 + ((lane ^ pm) & 8) * 16 + (lane & 7) * 16);
        const unsigned vA[4] = {pin(voA), pin(voA + 16 * 4096 - 1024), pin(voA + 4096 - 2048), pin(voA + 17 * 4096 - 3072)};
        auto spA = [&](int so, unsigned la) { dma4<false>(la, rA, so, vA[0], vA[1], vA[2], vA[3]); };
        auto issueA = [&](int k, int slot) { spA(k * 128, lA + slot * G_STAGE); };
        {
            const LAS char* pp = opaque(lds + G_PART + row * 8);
            const auto rs = RSRC(zst + (size_t)(pm * 16 + cg) * 128);
            const unsigned vp = pin(lane < 32 ? (unsigned)row * 8u : 0x80000000u);
            asm volatile("" :: "s"(rs));
            BARX();
            const f32x2 a = *(const LAS f32x2*)pp, c = *(const LAS f32x2*)(pp + 1024);
            ST8(rs, vp, 0, ((u32x2){__float_as_uint(add1(a.x, c.x)), min(__float_as_uint(add1(a.y, c.y)), 0xfffffffeu)}), 16);
            SCHED0();
        }
        __builtin_amdgcn_s_setprio(0);
        {
            f32x4 g[4];
            float sm[4];
            auto gather = [&] {
                asm volatile("global_load_dwordx4 %0, %4, off sc1\n\tglobal_load_dwordx4 %1, %4, off offset:1024 sc1\n\t"
                             "global_load_dwordx4 %2, %4, off offset:2048 sc1\n\tglobal_load_dwordx4 %3, %4, off offset:3072 sc1"
                             : "=&v"(g[0]), "=&v"(g[1]), "=&v"(g[2]), "=&v"(g[3]) : "v"(src) : "memory");
            };
            auto total = [&] {
                asm volatile("" : "+v"(g[0]), "+v"(g[1]), "+v"(g[2]), "+v"(g[3]));
#pragma unroll
                for (int i = 0; i < 4; ++i) sm[i] = add1(add1(g[0][i], g[1][i]), add1(g[2][i], g[3][i]));
            };
            step1(9); step1(10);
            BARX(); gather(); issue1(15, 3);
            WAIT_V(22); BARX();
            __builtin_amdgcn_s_setprio(3);
            WAIT_V(9);
            total();
            if (__any(__builtin_isunordered(sm[1], sm[3]))) {
#pragma unroll 1
                for (unsigned spin = 0; spin < 400000u; ++spin) {
                    bool ok = true;
#pragma unroll
                    for (int i = 0; i < 4; ++i) ok &= __float_as_uint(g[i][1]) != 0xffffffffu && __float_as_uint(g[i][3]) != 0xffffffffu;
                    if (__all(ok)) break;
                    gather();
                    WAIT_V(0);
                    total();
                }
            }
            xq4_sum(sm);
            const bool odd = lane & 16;
            float mean;
            const float rs = rstd_of(odd ? sm[2] : sm[0], odd ? sm[3] : sm[1], 1.0f / 2304.0f, mean);
            asm volatile("ds_write_b32 %0, %1" :: "v"(tw), "v"(lane < 32 ? rs : -mean * rs) : "memory");
            __builtin_amdgcn_s_setprio(0);
            issue1B(16, 0);
            WAIT_L0();
        }
        BARX(); issue1A(16, 0); issueA(2 * pm, 1);
        auto kord2 = [&](int t) { const int j = kord(t - 2, 30); return ((j < 2 * (pm & 7) ? j : j + 2) + 2 * (pm & 8)) & 31; };
        unsigned seen;
        auto look = [&] { if (wid == 0) asm volatile("s_nop 4\n\tbuffer_load_dword %0, %1, %2, 0 offen sc1" : "+v"(seen) : "v"(fzi), "s"(RSRC(ws)) : "memory"); };
        auto need = [&](int n) {
            if (wid == 0) {
                asm volatile("" : "+v"(seen));
                if (__ballot(seen == 0u) & ((1ull << n) - 1ull)) flags_wait<0>(ws, lane < n ? fzi : fl + F_Z2 + (cg * 16 + pm) * 256 + pm * 16);
            }
        };
        auto issue = [&](int t, int sl) { const int k = kord2(t); issueB(k, sl); issueA(k, sl); };
        auto steps2 = [&](int t0, int t1) {
            int sl = (2 * t0 + 2) % 3 * G_STAGE, k = kord2(t0 + 3), ob = (k >> 1) * 589824 + (k & 1) * 18432;
            LOOP_ALIGN();
#pragma unroll 1
            for (int t = t0; t < t1; ++t) {
                WAIT_V(9); BARX();
                dma5<true>(lB + sl, rB, ob, vo);
                const int oa = pin_s(k * 128);
                const unsigned la = pin_s(lA + sl);
                k = pin_s(kord2(t + 4)); ob = pin_s((k >> 1) * 589824 + (k & 1) * 18432);
                sl = pin_s(sl == 0 ? 2 * G_STAGE : sl - G_STAGE);
                spA(oa, la);
            }
        };
        WAIT_V(13); BAR(); issue1(17, 2);
        WAIT_V(13); BARX();
        if (wid == 0) flag_raise(ws, fz);
        issueA(2 * pm + 1, 3); if (layer == 0) load5(4, 0);
        if (wid == 0) WAIT_IF(layer, 5, 9); else WAIT_IF(layer, 4, 8);
        BAR(); issueA(kord2(2), 0); if (layer == 0) load5(4, 1);
        BAR(); look(); issueA(kord2(3), 2);
        WAIT_V(4); need(3); BARX(); issueB(kord2(2), 0); issueB(kord2(3), 2); look();
        WAIT_IF(wid == 0, 6, 5); BAR(); issue(4, 1);
        WAIT_V(9); need(8);
        steps2(2, 11); look();
        WAIT_IF(wid == 0, 10, 9); BAR(); issue(14, 0);
        WAIT_V(9); need(16);
        steps2(12, 29);
        WAIT_V(9); BAR(); issue5(0); issue5(1); issue5(2);
        WAIT_V(15); BAR(); issue5(6); issue5(7);
        BAR(); issue5(3); put5(0, 9);
        {
            const unsigned fo = flag_out(fl + F_Y, 256, pm, cg, lane), fi = flag_in(fl + F_Y, 256, rh * 8 + c5, 16, lane);
            __builtin_amdgcn_s_setprio(3);
            WAIT_V(0); BARX();
            if (wid == 0) { flag_raise(ws, fo); flags_wait<Y_FIRST>(ws, fi); }
            __builtin_amdgcn_s_setprio(0);
            BARX();
        }
        BAR(); put5(9, 16);
        BAR(); put5(16, 23);
        BAR(); put5(23, 30);
        BAR(); put5(30, 36);
        BAR(); issue5(16); put5(36, 41);
        BAR(); issue5(17); put5(41, 45);
        WAIT_V(5); BAR();
        WAIT_V(0); BAR();
        if (layer == 0) { attn_issueB(lds, ws, hp, wid, lane, 0, 0); attn_issueB(lds, ws, hp, wid, lane, 1, 1); }
        BAR();
        if (layer == 0) {
            __builtin_amdgcn_s_setprio(3);
            const unsigned fo = flag_out(F_X2, 512, (rh >> 1) + wid, wid ? 16 + c5 : (rh & 1) * 8 + c5, wid == 0 || (wid == 1 && (rh & 1) && rh < 31) ? lane : 16);
            BARX();
            if (wid < 2) flag_raise(ws, fo);
        }
        TRACE_DUMP;
      }
        if (blockIdx.x == 0 && wid8 == 4) ST8(RSRC(ws), lane_id_fresh() ? 0x80000000u : (unsigned)OFF_EP, 0, ((u32x2){(unsigned)ep, (unsigned)(ep >> 32)}), 16);
        asm volatile("gm_code_end1:");
        return;
    }
#pragma unroll 1
  for (int layer = 0; layer < NET_LAYERS; ++layer) {
    NET_IDS
    TRACE_BEGIN;
    char* wl = ws + OFF_L0 + layer * SZ_L;
    const unsigned fl = layer * F_LAYER;
    const char* xin = wl + L_XB;
    char* y = wl + (layer ? L_Z2T : L_Y);
#undef STAMP_SEC
#define STAMP_SEC 0
    {
    STAMP_BEGIN;
    attn_consumer(lds, x0, wl, layer, pa, hp, wid, [&] { WAIT_V(0); prologue_done(lds, ws, ep, b, lane_id_fresh()); } STAMP_FWD);
    STAMP_END(1 + 5 * layer);
    }
#undef STAMP_SEC
#define STAMP_SEC 1
    STAMP_BEGIN;
    gmlp_body(lds, wid & 1, wid >> 1, pm, cg, wl + (layer ? L_Y : L_Z2T), y, bsp STAMP_FWD);
    {
        WAIT_V(0);
        BARX();
        STAMP(11);
        const int lane = lane_id_fresh(), r = lane & 15, q = lane >> 4;
        const int wk = wid >> 1, rt = rh * 4 + (wid & 1) * 2 + wk;
        const Res rx = proj_residual_load(xin, rt, c5, lane);
        const LAS char *pw[3], *ph[3];
#pragma unroll
        for (int i = 0; i < 3; ++i) {
            pw[i] = opaque(lds + 3 * i * K5_STAGE + wk * 4608 + lane * 16);
            ph[i] = opaque(lds + 3 * i * K5_STAGE + wk * 4608 + 4096 + q * 128 + (r & 7) * 16);
        }
        const auto rY = RSRC(y + (size_t)(rt & ~1) * 73728 + wk * 1024);
        const unsigned vy[2] = {pin((unsigned)(wk * 73728 + lane * 16)), pin((unsigned)((1 - wk) * 73728 + lane * 16))};
        int dk[4];
#pragma unroll
        for (int i = 0; i < 4; ++i) dk[i] = pin_s((kord5(4 + i) - 4 - i) * 4096);
        auto yoff = [&](int t) { return pin_s(t == 0 || t == 17 ? kord5(t) * 4096 : t * 4096 + dk[t & 3]); };
        bf16x8 af[5][4], wf[4][5];
        auto lda = [&](int so, int i) { return __builtin_bit_cast(bf16x8, __builtin_amdgcn_raw_buffer_load_b128(rY, vy[i & 1] + (i >> 1) * 2048, so, 16)); };
        auto rdw = [&](int t, int c, int n) {
            const int sl = (t + 6) & 7, o = (sl % 3) * K5_STAGE + c * 9216;
            wf[2 * (t & 1) + c][n] = n < 4 ? LDS_FRAG(pw[sl / 3] + o + n * 1024) : LDS_FRAG(ph[sl / 3] + o);
        };
#pragma unroll
        for (int n = 0; n < 10; ++n) rdw(0, n / 5, n % 5);
        int so[18];
#pragma unroll
        for (int i = 0; i < 5; ++i) so[i] = yoff(i);
        so[17] = yoff(17);
        f32x4 v[5], u[5];
        proj_residual(v, rx);
#pragma unroll
        for (int n = 0; n < 5; ++n) v[n] = pin(v[n]);
        LAS char* xo = (LAS char*)opaque(lds + wid * 4608 + lane * 16);
        LAS char* xo4 = (LAS char*)opaque(lds + wid * 4608 + 4096 + (lane & 31) * 16);
        const LAS char* xi = opaque(lds + (wid ^ 2) * 4608 + lane * 16);
        const LAS char* xi4 = opaque(lds + (wid ^ 2) * 4608 + 4096 + (lane & 31) * 16);
        const ProjOut po = layer ? proj_out_final(out, rt, c5, lane) : proj_out(wl + SZ_L + L_XA, wl + SZ_L + L_STA, rt, c5, lane);
        asm volatile("" :: "s"(rY));
        BARX();
        STAMP(12);
#pragma unroll
        for (int i = 0; i < K5_PRE; ++i) { af[i >> 2][i & 3] = lda(so[i >> 2], i & 3); SCHED0(); }
        static_for<18>([&](auto tc) {
            constexpr int t = tc.value;
            bf16x8 (&a)[4] = af[t % 5];
#pragma unroll
            for (int h = 0; h < 4; ++h) {
                const int c = t < 17 ? h >> 1 : h & 1, m = t < 17 ? h & 1 : 1 - (h >> 1);
                if (t < 17 ? m == 0 : h < 2) { if (t == 17 && h == 1) WAIT_L(0); else WAIT_L(5); }
#pragma unroll
                for (int n = 0; n < 5; ++n) {
                    f32x4& e = m ? u[n] : v[n];
                    if (t == 0 && h == 1) MFMA_0(wf[0][n], a[1], e); else MFMA_T(wf[2 * (t & 1) + c][n], a[2 * c + m], e);
                    SCHED0();
                    if (t < 17 && m == 0) rdw(t + 1, c, n);
                    if (t < 14 && m == 1 && !(n & 1)) { const int i = y_load_at(6 * t + 3 * c + n / 2); if (i >= 0) af[(i >> 2) % 5][i & 3] = lda(so[i >> 2], i & 3); }
                    if (t < 12 && h == 3 && n == 3) so[t + 5] = yoff(t + 5);
                    if (t == 17 && h * 5 + n >= 13 && h * 5 + n < 18) { const int i = h * 5 + n - 13; *(LAS f32x4*)(i < 4 ? xo + i * 1024 : xo4) = u[i]; }
                    SCHED0();
                }
            }
            if ((t & 1) && t < 17) BAR();
            SCHED0();
        });
        {
            WAIT_L0(); BARX();
#pragma unroll
            for (int n = 0; n < 5; ++n) u[n] = *(const LAS f32x4*)(n < 4 ? xi + n * 1024 : xi4);
            SCHED0();
            asm volatile("s_nop 1" : "+v"(v[0]), "+v"(v[1]), "+v"(v[2]), "+v"(v[3]), "+v"(v[4]));
#pragma unroll
            for (int n = 0; n < 5; ++n) v[n] += u[n];
        }
        STAMP(13);
        if (layer) proj_store<true>(v, po);
        else { proj_store(v, po); WAIT_V(0); BARX(); }
    }
    STAMP_END(3 + 5 * layer);
    TRACE_DUMP;
  }
  asm volatile("gm_code_end2:");
}

}

#define STOP_CHECK(n)

extern "C" void kernel_launch(void* const* d_in, const int* in_sizes, int n_in, void* d_out, int out_size, void* d_ws, size_t ws_size,
                              hipStream_t stream) {
    (void)in_sizes; (void)n_in; (void)out_size; (void)ws_size;
    const u16* x = (const u16*)d_in[0];
    const u16* wqkv = (const u16*)d_in[1];
    const u16* wo = (const u16*)d_in[2];
    const u16* w_in = (const u16*)d_in[4];
    const u16* w_sp = (const u16*)d_in[5];
    const u16* b_sp = (const u16*)d_in[6];
    const u16* w_out = (const u16*)d_in[7];
    char* ws = (char*)d_ws;
    (void)hipFuncSetAttribute((const void*)k_net, hipFuncAttributeMaxDynamicSharedMemorySize, NET_LDS);
    k_net<<<256, 512, NET_LDS, stream>>>(ws, w_sp, b_sp, (char*)d_out, (const char*)x, (const char*)wqkv, wo, w_in, w_out DBG_LAUNCH(0));
}
```

```cpp
#include <hip/hip_runtime.h>
#include <stdint.h>

namespace {

typedef unsigned short u16;
typedef short bf16x8 __attribute__((ext_vector_type(8)));
typedef float f32x4 __attribute__((ext_vector_type(4)));
typedef float f32x2 __attribute__((ext_vector_type(2)));
typedef unsigned u32x4 __attribute__((ext_vector_type(4)));
typedef unsigned u32x2 __attribute__((ext_vector_type(2)));
typedef __bf16 bf16x2_t __attribute__((ext_vector_type(2)));
#define LAS __attribute__((address_space(3)))
#define DEVI __device__ __forceinline__

constexpr int S_ = 2048, D_ = 576, FH = 2304;

constexpr size_t SZ_TWQKV = (size_t)16 * 18 * 6 * 1024;
constexpr size_t SZ_TWT = (size_t)48 * 18 * 256;
constexpr size_t SZ_TWO = (size_t)8 * 18 * 4608;
constexpr size_t SZ_TWIN = (size_t)16 * 18 * 18 * 1024;
constexpr size_t SZ_TWOUT = (size_t)8 * 72 * 4608;
constexpr size_t SZ_X = (size_t)S_ * D_ * 2;
constexpr size_t SZ_Z = (size_t)S_ * FH * 2;
constexpr size_t SZ_ST = (size_t)S_ * 64;
constexpr size_t SZ_ZST = (size_t)16 * 16 * 128 * 8;
constexpr size_t OFF_TWQKV = 0;
constexpr size_t OFF_TWT = OFF_TWQKV + SZ_TWQKV;
constexpr size_t OFF_TWO = OFF_TWT + SZ_TWT;
constexpr size_t OFF_TWIN = OFF_TWO + SZ_TWO;
constexpr size_t OFF_TWOUT = OFF_TWIN + SZ_TWIN;
constexpr size_t OFF_CNT = OFF_TWOUT + SZ_TWOUT;
constexpr size_t SZ_CNT = 10 * 65536;
constexpr size_t OFF_FW = OFF_CNT + SZ_CNT;
constexpr size_t OFF_FL = OFF_FW + 262144;
constexpr size_t OFF_EP = OFF_FL + 4096;
constexpr size_t OFF_FL2 = OFF_EP + 4096;
constexpr size_t OFF_ZST = OFF_FL2 + 8192;
constexpr size_t OFF_L0 = OFF_ZST + 2 * SZ_ZST;
constexpr size_t L_XA = 0, L_STA = L_XA + SZ_X, L_O = L_STA + SZ_ST, L_XB = L_O + SZ_X, L_STB = L_XB + SZ_X, L_Z2T = L_STB + SZ_ST, L_Y = L_Z2T + SZ_Z;
constexpr size_t SZ_L = 0;
static_assert(OFF_L0 + L_Y + SZ_Z <= ((size_t)256 << 20), "workspace");
constexpr unsigned F_O = OFF_CNT, F_X1 = F_O + 65536, F_Z2 = F_O + 131072, F_Y = F_O + 196608, F_LAYER = 262144, F_X2 = F_O + 2 * F_LAYER;

DEVI unsigned pack2(float a, float b) { f32x2 v = {a, b}; bf16x2_t r = __builtin_convertvector(v, bf16x2_t); return __builtin_bit_cast(unsigned, r); }
DEVI float bflo(unsigned u) { return __uint_as_float(u << 16); }
DEVI float bfhi(unsigned u) { return __uint_as_float(u & 0xffff0000u); }
DEVI int slot2row(int sg) { return (sg & ~31) + 2 * (sg & 15) + ((sg >> 4) & 1); }
DEVI float fmax2(float a, float b) { return __builtin_amdgcn_fmed3f(a, b, __builtin_inff()); }
#define XQ4(OP, MID) \
    asm volatile("s_nop 1\n\tv_permlane32_swap_b32 %0, %1\n\tv_permlane32_swap_b32 %2, %3\n\t" OP " %0, %0, %1\n\t" OP " %2, %2, %3\n\t" \
                 "s_nop 1\n\tv_permlane16_swap_b32 %0, %2\n\t" OP " %0, %0, %2\n\t" MID \
                 "v_mov_b32 %2, %0\n\ts_nop 1\n\tv_permlane16_swap_b32 %0, %2\n\tv_mov_b32 %1, %0\n\tv_mov_b32 %3, %2\n\ts_nop 0\n\t" \
                 "v_permlane32_swap_b32 %0, %1\n\tv_permlane32_swap_b32 %2, %3" : "+v"(x[0]), "+v"(x[1]), "+v"(x[2]), "+v"(x[3]))
DEVI void xq4_max(float (&x)[4]) { XQ4("v_max_f32", ""); }
DEVI void xq4_sum(float (&x)[4]) { XQ4("v_add_f32", ""); }
DEVI void xq4_sum_rcp(float (&x)[4]) { XQ4("v_add_f32", "v_rcp_f32 %0, %0\n\ts_nop 0\n\t"); }
template <class T> DEVI T pin(T v) { asm volatile("" : "+v"(v)); return v; }
template <int V> struct IC { static constexpr int value = V; };
template <int N, int I = 0, class F> DEVI void static_for(F&& f) { if constexpr (I < N) { f(IC<I>{}); static_for<N, I + 1>(f); } }
template <class T> DEVI T* own_s(T* p) { T* r; asm volatile("s_mov_b64 %0, %1" : "=&s"(r) : "s"(p)); return r; }
DEVI int pin_s(int v) { asm volatile("" : "+s"(v)); return v; }
DEVI float xq_sum(float v) {
    auto a = __builtin_amdgcn_permlane32_swap(__float_as_uint(v), __float_as_uint(v), false, false);
    v = __uint_as_float(a[0]) + __uint_as_float(a[1]);
    auto b = __builtin_amdgcn_permlane16_swap(__float_as_uint(v), __float_as_uint(v), false, false);
    return __uint_as_float(b[0]) + __uint_as_float(b[1]);
}
#define RSRC(p) __builtin_amdgcn_make_buffer_rsrc((void*)(p), 0, 0x7fffffff, 0x00020000)
#define ST16(rs, vo, so, v, aux) __builtin_amdgcn_raw_buffer_store_b128((v), (rs), (vo) + (so), 0, aux)
#define ST8(rs, vo, so, v, aux) __builtin_amdgcn_raw_buffer_store_b64((v), (rs), (vo), (so), aux)
#define WAIT_V(n) asm volatile("s_waitcnt vmcnt(%0)" ::"i"(n) : "memory")
#define WAIT_L0() asm volatile("s_waitcnt lgkmcnt(0)" ::: "memory")
#define BAR() do { asm volatile("" ::: "memory"); __builtin_amdgcn_s_barrier(); asm volatile("" ::: "memory"); } while (0)
#define BARX() do { __builtin_amdgcn_sched_barrier(0); BAR(); __builtin_amdgcn_sched_barrier(0); } while (0)
#define LOOP_ALIGN() asm volatile(".p2align 6")
#define SCHED0() __builtin_amdgcn_sched_barrier(0)
#define MFMA(a, b, c) __builtin_amdgcn_mfma_f32_16x16x32_bf16((a), (b), (c), 0, 0, 0)
#define MFMA_T(a, b, c) asm volatile("v_mfma_f32_16x16x32_bf16 %0, %1, %2, %0" : "+v"(c) : "v"(a), "v"(b))
#define MFMA_0(a, b, c) asm volatile("v_mfma_f32_16x16x32_bf16 %0, %1, %2, 0" : "=&v"(c) : "v"(a), "v"(b))
#define WAIT_L(n) __builtin_amdgcn_s_waitcnt(0xC07F | ((n) << 8))
#define LDS_FRAG(p) (*(const LAS bf16x8*)(p))
DEVI int lane_id_fresh() { int z = 0; asm volatile("" : "+v"(z)); return __builtin_amdgcn_mbcnt_hi(~0u, __builtin_amdgcn_mbcnt_lo(~0u, z)); }
DEVI const LAS char* opaque(const LAS char* p) { asm volatile("" : "+v"(p)); return p; }
#define DBG_ARG
#define DBG_LAUNCH(n)
#define STAMP(i)
#define STAMP_DECL
#define STAMP_BEGIN
#define STAMP_END(n)
#define STAMP_PARAM
#define STAMP_FWD

#define PST_PARAM
#define PST_FWD
#define PST_DECL
#define PST(i)
#define PST_DUMP(c, k)
#define TRACE_ATTR
#define TRACE_BEGIN
#define TRACE_DUMP

DEVI float add1(float a, float b) { float r; asm("v_add_f32 %0, %1, %2" : "=v"(r) : "v"(a), "v"(b)); return r; }
DEVI float rstd_of(float s, float q, float inv_n, float& mean) {
    float var;
    asm("v_mul_f32 %0, %2, %3\n\tv_mul_f32 %1, %2, %4\n\tv_fma_f32 %1, -%0, %0, %1" : "=&v"(mean), "=&v"(var) : "s"(inv_n), "v"(s), "v"(q));
    return __builtin_amdgcn_rsqf(fmax2(var, 0.f) + 1e-5f);
}
DEVI float ln_rstd(const f32x4 p) {
    float s, q, mean;
    asm("v_add_f32 %0, %2, %4\n\tv_add_f32 %1, %3, %5\n\ts_nop 0\n\t"
        "v_add_f32_dpp %0, %0, %0 quad_perm:[1,0,3,2] row_mask:0xf bank_mask:0xf\n\tv_add_f32_dpp %1, %1, %1 quad_perm:[1,0,3,2] row_mask:0xf bank_mask:0xf\n\ts_nop 0\n\t"
        "v_add_f32_dpp %0, %0, %0 quad_perm:[2,3,0,1] row_mask:0xf bank_mask:0xf\n\tv_add_f32_dpp %1, %1, %1 quad_perm:[2,3,0,1] row_mask:0xf bank_mask:0xf"
        : "=&v"(s), "=&v"(q) : "v"(p[0]), "v"(p[1]), "v"(p[2]), "v"(p[3]));
    return rstd_of(s, q, 1.0f / 576.0f, mean);
}

struct Res { u32x4 a, c; u32x2 e; };
DEVI Res proj_residual_load(const char* xin, int rt, int cg, int lane) {
    const auto rs = RSRC(xin + (size_t)rt * 18432 + cg * 2304);
    Res x;
    x.a = __builtin_amdgcn_raw_buffer_load_b128(rs, lane * 16, 0, 16); x.c = __builtin_amdgcn_raw_buffer_load_b128(rs, lane * 16 + 1024, 0, 16);
    x.e = __builtin_amdgcn_raw_buffer_load_b64(rs, 2048 + (lane & 15) * 16 + ((lane >> 4) & 1) * 8, 0, 16);
    return x;
}
DEVI void proj_residual(f32x4 (&v)[5], const Res& x) {
    v[0] = (f32x4){bflo(x.a.x), bfhi(x.a.x), bflo(x.a.y), bfhi(x.a.y)}; v[1] = (f32x4){bflo(x.a.z), bfhi(x.a.z), bflo(x.a.w), bfhi(x.a.w)};
    v[2] = (f32x4){bflo(x.c.x), bfhi(x.c.x), bflo(x.c.y), bfhi(x.c.y)}; v[3] = (f32x4){bflo(x.c.z), bfhi(x.c.z), bflo(x.c.w), bfhi(x.c.w)};
    v[4] = (f32x4){bflo(x.e.x), bfhi(x.e.x), bflo(x.e.y), bfhi(x.e.y)};
}
struct ProjOut { __amdgpu_buffer_rsrc_t x, st; unsigned v0, v1, v2, vs; };
DEVI ProjOut proj_out(char* xout, char* stout, int rt, int cg, int lane) {
    const int r = lane & 15, q = lane >> 4;
    ProjOut p = {RSRC(xout + (size_t)rt * 18432 + cg * 2304), RSRC(stout + (size_t)rt * 1024 + cg * 8),
                 pin((unsigned)lane * 16u), pin((unsigned)lane * 16u + 1024u), pin((unsigned)(2048 + r * 16 + (q & 1) * 8)), pin((unsigned)(r * 64 + (q >> 1) * 4))};
    asm volatile("" :: "s"(p.x), "s"(p.st));
    return p;
}
DEVI ProjOut proj_out_final(char* out, int rt, int cg, int lane) {
    const int q = lane >> 4;
    const unsigned o = (unsigned)(slot2row(rt * 16 + (lane & 15)) * 1152 + cg * 144);
    ProjOut p = {RSRC(out), RSRC(out), pin(o + q * 16), pin(o + 64 + q * 16), pin(o + 128 + (q & 1) * 8), 0u};
    return p;
}
template <bool FINAL = false>
DEVI void proj_store(f32x4 (&v)[5], const ProjOut& p) {
    ST16(p.x, p.v0, 0, ((u32x4){pack2(v[0][0], v[0][1]), pack2(v[0][2], v[0][3]), pack2(v[1][0], v[1][1]), pack2(v[1][2], v[1][3])}), FINAL ? 0 : 16);
    SCHED0();
    asm volatile("" : "+v"(v[2]), "+v"(v[3]), "+v"(v[4]));
    ST16(p.x, p.v1, 0, ((u32x4){pack2(v[2][0], v[2][1]), pack2(v[2][2], v[2][3]), pack2(v[3][0], v[3][1]), pack2(v[3][2], v[3][3])}), FINAL ? 0 : 16);
    SCHED0();
    ST8(p.x, p.v2, 0, ((u32x2){pack2(v[4][0], v[4][1]), pack2(v[4][2], v[4][3])}), FINAL ? 0 : 16);
    SCHED0();
    if (!FINAL) {
        const f32x4 s4 = ((v[0] + v[1]) + (v[2] + v[3])) + v[4] * 0.5f;
        const f32x4 q4 = ((v[0] * v[0] + v[1] * v[1]) + (v[2] * v[2] + v[3] * v[3])) + v[4] * v[4] * 0.5f;
        auto x = __builtin_amdgcn_permlane32_swap(__float_as_uint((s4[0] + s4[2]) + (s4[1] + s4[3])), __float_as_uint((q4[0] + q4[2]) + (q4[1] + q4[3])), false, false);
        const float t = __uint_as_float(x[0]) + __uint_as_float(x[1]);
        auto y = __builtin_amdgcn_permlane16_swap(__float_as_uint(t), __float_as_uint(t), false, false);
        __builtin_amdgcn_raw_buffer_store_b32(__float_as_uint(__uint_as_float(y[0]) + __uint_as_float(y[1])), p.st, p.vs, 0, 16);
        SCHED0();
    }
}

DEVI void side_load(f32x4 (&d)[3], const char* p, const char* p2) {
    asm volatile("global_load_dwordx4 %0, %3, off sc1\n\tglobal_load_dwordx4 %1, %3, off offset:1024 sc1\n\tglobal_load_dwordx4 %2, %4, off sc1"
                 : "=&v"(d[0]), "=&v"(d[1]), "=&v"(d[2]) : "v"(p), "v"(p2) : "memory");
}
DEVI void side_fence(f32x4 (&d)[3]) { asm volatile("" : "+v"(d[0]), "+v"(d[1]), "+v"(d[2])); }
DEVI void lds_word_set(LAS char* p, unsigned v) { asm volatile("ds_write_b32 %0, %1" :: "v"((unsigned)(size_t)p), "v"(v) : "memory"); }
#define DMA_I(v, off, sc) "buffer_load_dwordx4 " v ", %1, %2 offen offset:" off sc " lds\n\t"
#define DMA5(sc) asm volatile("s_mov_b32 m0, %0\n\ts_mov_b32 exec_hi, 0\n\ts_nop 2\n\t" DMA_I("%3", "0", sc) "s_mov_b32 exec_hi, -1\n\t" \
    DMA_I("%3", "512", sc) DMA_I("%3", "1536", sc) DMA_I("%3", "2560", sc) DMA_I("%3", "3584", sc) :: "s"(la), "s"(rs), "s"(so), "v"(vo) : "memory")
#define DMA4(sc) asm volatile("s_mov_b32 m0, %0\n\ts_nop 3\n\t" DMA_I("%3", "0", sc) DMA_I("%4", "1024", sc) DMA_I("%5", "2048", sc) DMA_I("%6", "3072", sc) \
    :: "s"(la), "s"(rs), "s"(so), "v"(v0), "v"(v1), "v"(v2), "v"(v3) : "memory")
template <bool SC1> DEVI void dma5(unsigned la, __amdgpu_buffer_rsrc_t rs, int so, unsigned vo) { if (SC1) DMA5(" sc1"); else DMA5(""); }
template <bool SC1> DEVI void dma4(unsigned la, __amdgpu_buffer_rsrc_t rs, int so, unsigned v0, unsigned v1, unsigned v2, unsigned v3) { if (SC1) DMA4(" sc1"); else DMA4(""); }
#define DMA2(sc) asm volatile("s_mov_b32 m0, %0\n\ts_nop 3\n\t" DMA_I("%3", "0", sc) DMA_I("%4", "1024", sc) :: "s"(la), "s"(rs), "s"(so), "v"(v0), "v"(v1) : "memory")
template <bool SC1> DEVI void dma2(unsigned la, __amdgpu_buffer_rsrc_t rs, int so, unsigned v0, unsigned v1) { if (SC1) DMA2(" sc1"); else DMA2(""); }
DEVI void dma1(unsigned la, __amdgpu_buffer_rsrc_t rs, int so, unsigned v0) { asm volatile("s_mov_b32 m0, %0\n\ts_nop 3\n\t" DMA_I("%3", "0", "") :: "s"(la), "s"(rs), "s"(so), "v"(v0) : "memory"); }
#define WAIT_IF(c, a, b) do { if (c) WAIT_V(a); else WAIT_V(b); } while (0)
template <int T0, int T1, int P, int S0 = 0, class ISSUE>
DEVI void ring_steps(ISSUE&& issue) {
    int slot = (T0 + S0) % 3;
    LOOP_ALIGN();
#pragma unroll 1
    for (int t = T0; t < T1; ++t) {
        WAIT_V(P); BAR();
        issue(t + 3, slot);
        slot = slot == 2 ? 0 : slot + 1;
    }
}

DEVI unsigned flag_out(unsigned use, int pitch, int group, int writer, int lane) { return pin(lane < 16 ? use + (group * 16 + lane) * pitch + writer * 16 : 0x80000000u); }
DEVI unsigned flag_in(unsigned use, int pitch, int reader, int n, int lane) { return pin(use + reader * pitch + (lane < n ? lane : 0) * 16); }
DEVI void flag_raise(const char* ws, unsigned vo) { ST16(RSRC(ws), vo, 0, ((u32x4){1u, 1u, 1u, 1u}), 16); SCHED0(); }
template <int FIRST>
DEVI void flags_wait(const char* ws, unsigned vo) {
    unsigned v, n = 400000u;
    asm volatile("s_nop 4\n\ts_sleep %4\n"
                 "1:\n\tbuffer_load_dword %0, %2, %3, 0 offen sc1\n\ts_waitcnt vmcnt(0)\n\tv_cmp_eq_u32 vcc, 0, %0\n\ts_cbranch_vccz 2f\n\t"
                 "s_sleep 1\n\ts_sub_u32 %1, %1, 1\n\ts_cbranch_scc0 1b\n2:"
                 : "=&v"(v), "+s"(n) : "v"(vo), "s"(RSRC(ws)), "i"(FIRST) : "vcc", "scc", "memory");
    SCHED0();
}
template <int FIRST>
DEVI void epoch_wait(const char* ws, unsigned vo, unsigned long long ep) {
    unsigned long long v; unsigned n = 400000u;
    asm volatile("s_nop 4\n\ts_sleep %5\n"
                 "1:\n\tbuffer_load_dwordx2 %0, %2, %3, 0 offen sc1\n\ts_waitcnt vmcnt(0)\n\tv_cmp_ne_u64 vcc, %4, %0\n\ts_cbranch_vccz 2f\n\t"
                 "s_sleep 1\n\ts_sub_u32 %1, %1, 1\n\ts_cbranch_scc0 1b\n2:"
                 : "=&v"(v), "+s"(n) : "v"(vo), "s"(RSRC(ws)), "s"(ep), "i"(FIRST) : "vcc", "scc", "memory");
    SCHED0();
}
DEVI unsigned lds_arrive(LAS char* p) {
    unsigned v;
    asm volatile("s_mov_b64 exec, 1\n\tds_add_rtn_u32 %0, %1, %2\n\ts_mov_b64 exec, -1\n\ts_waitcnt lgkmcnt(0)" : "=&v"(v) : "v"((unsigned)(size_t)p), "v"(1u) : "memory");
    return __builtin_amdgcn_readfirstlane(v);
}
DEVI void lds_count_wait(LAS char* p, unsigned n) {
#pragma unroll 1
    for (unsigned spin = 0; spin < 40000000u; ++spin) {
        unsigned v;
        asm volatile("ds_read_b32 %0, %1\n\ts_waitcnt lgkmcnt(0)" : "=v"(v) : "v"((unsigned)(size_t)p) : "memory");
        if (__builtin_amdgcn_readfirstlane(v) >= n) break;
    }
}

constexpr int K1_STAGE = 32768;
constexpr int WO_OFF = 69632;
constexpr int G_OWN = 139264;
constexpr int G_PART = G_OWN + 18432;
constexpr int G_TAB = G_PART + 2048;
constexpr int K1_SCR = G_TAB + 1536;
constexpr int G_WORD = K1_SCR + 576;
constexpr int NET_LDS = G_WORD + 64;

#define WO_PRE 12
#define WO_W 33
#define REL_PRIO 0
#define WSP_PF 1
#define W_FIRST 2
#define W_ALONE 12
#define I_SLEEP 12
#define OJOB_SLEEP 20
#define O_FIRST 10
#define X1_FIRST 6
#define X2_FIRST 6
#define Y_FIRST 10
#define Z2_FIRST 10
DEVI void attn_issueB(LAS char* lds, const char* ws, int hp, int wid, int lane, int s, int slot) {
    const unsigned la = (unsigned)(size_t)lds + slot * K1_STAGE + 18432 + wid * 4096, vo = lane * 16;
    if (wid < 3) dma4<true>(la, RSRC(ws + OFF_TWQKV + (size_t)hp * (18 * 6144)), wid * 4096 + s * 12288, vo, vo, vo, vo);
    else {
        const int c = lane & 15;
        const unsigned vt = (unsigned)((c < 8 ? (c >> 1) & 1 : 2) * 4608 + (lane >> 4) * 64 + (c < 8 ? (c >> 2) * 2 + (c & 1) : c & 3) * 16 + 1024);
        dma2<true>(la, RSRC(ws + OFF_TWT + (size_t)hp * 13824 - 1024), s * 512, vt, vt + 256 - 1024);
    }
}
struct AttnX { __amdgpu_buffer_rsrc_t rs; unsigned v[4], vh[2]; int oA, oh, step; };
DEVI AttnX attn_x(const char* x0, const char* xa, int layer, int pm, int wid, int lane) {
    const int l3 = lane >> 3, pmh = pm > 0 ? pm : 1;
    const unsigned v = layer ? lane * 16 : 2 * l3 * 1152 + ((lane & 7) ^ l3) * 16 + 4096, d1 = layer ? 0 : 16 * 1152 - 1024, d2 = layer ? 16384 : 1152 - 2048;
    const unsigned vh = layer ? (l3 & 1) * 18432 + (lane >> 4) * 256 + (8 + (lane & 7)) * 16 : v + 16 * 1152;
    return {RSRC(layer ? xa : x0 - 4096), {pin(v), pin(v + d1), pin(v + d2), pin(v + d1 + d2)}, {pin(vh), pin(vh + (layer ? 0 : 1152 - 1024))},
            (pm * 8 + 2 * wid) * 18432, (pmh * 8 - 2) * 18432, layer ? 2048 : 128};
}
DEVI void attn_issueA(LAS char* lds, const AttnX& a, int wid, int s, int slot) {
    const unsigned la = (unsigned)(size_t)lds + slot * K1_STAGE;
    if (wid == 3) dma2<true>(la, a.rs, a.oh + s * a.step, a.vh[0], a.vh[1]);
    dma4<true>(la + 2048 + wid * 4096, a.rs, a.oA + s * a.step, a.v[0], a.v[1], a.v[2], a.v[3]);
}
template <class DONE, class IDLE, class NEXT>
DEVI void attn_producer(LAS char* lds, const char* ws, const char* x0, const char* wl, unsigned fl, unsigned long long ep, int layer, int pm, int hp, int wid, DONE&& done, IDLE&& idle8, NEXT&& next) {
    const int lane = lane_id_fresh();
    const int pmh = pm > 0 ? pm : 1;
    const unsigned vo = lane * 16;
    const AttnX ax = attn_x(x0, wl + L_XA, layer, pm, wid, lane);
    auto issue = [&](int s, int slot) { attn_issueA(lds, ax, wid, s, slot); attn_issueB(lds, ws, hp, wid, lane, s, slot); };
    const int hr = lane >> 2;
    const char* ps = pin(wl + L_STA + (size_t)(pm * 128 + wid * 32) * 64 + lane * 16);
    const char* psh = pin(wl + L_STA + (size_t)((pmh * 8 - 2 + (hr >> 3)) * 16 + 8 + (hr & 7)) * 64 + (lane & 3) * 16);
    if (layer == 0) {
        const unsigned fi = pin((unsigned)OFF_FW + (pm * 16 + hp) * 1024 + (lane < 6 ? lane : 0) * 128);
        if (wid == 0) epoch_wait<W_FIRST>(ws, fi, ep);
        BARX();
        attn_issueB(lds, ws, hp, wid, lane, 0, 0); attn_issueB(lds, ws, hp, wid, lane, 1, 1);
        WAIT_IF(wid == 3, 2, 4);
    } else {
        attn_issueB(lds, ws, hp, wid, lane, 2, 2);
        const unsigned fi = flag_in(F_X2, 512, pm * 16 + hp, pm > 0 ? 24 : 16, lane);
        if (wid == 0) flags_wait<X2_FIRST>(ws, fi);
        BARX();
#pragma unroll
        for (int i = 0; i < 3; ++i) attn_issueA(lds, ax, wid, i, i);
        WAIT_IF(wid == 3, 12, 8);
    }
    __builtin_amdgcn_s_setprio(0);
    BAR();
    if (layer) WAIT_IF(wid == 3, 6, 4); else { done(); issue(2, 2); }
    ring_steps<0, 4, 8>(issue);
    {
        unsigned long long done;
        const unsigned fd = (unsigned)OFF_FL + wid * 1024 + vo;
        if (layer == 0) asm volatile("s_nop 4\n\tbuffer_load_dwordx2 %0, %1, %2, 0 offen sc1" : "+v"(done) : "v"(fd), "s"(RSRC(ws)) : "memory");
        WAIT_IF(layer, 8, 9); BAR(); issue(7, 1);
        WAIT_V(8);
        if (layer == 0) { asm volatile("" : "+v"(done)); if (__any(done != ep)) epoch_wait<0>(ws, fd, ep); }
        BAR(); issue(8, 2);
    }
    {
        f32x4 sp[3];
        side_load(sp, ps, psh);
        WAIT_V(11); BAR();
        if (WSP_PF && layer == 0) { idle8(); WAIT_V(8); } else WAIT_V(0);
        BARX();
        side_fence(sp);
#pragma unroll
        for (int i = 0; i < 3; ++i) lds_word_set(lds + K1_SCR + (i < 2 ? 16 + wid * 32 + i * 16 + hr : hr) * 4, __float_as_uint(ln_rstd(sp[i])));
        WAIT_L0();
    }
    BAR();
    const auto rW = RSRC(ws + OFF_TWO + (size_t)(hp >> 1) * 82944);
#pragma unroll
    for (int i = 0; i < 5; ++i) dma4<true>((unsigned)(size_t)lds + WO_OFF + (i * 4 + wid) * 4096, rW, (i * 4 + wid) * 4096, vo, vo, vo, vo);
    if (wid == 0) dma2<true>((unsigned)(size_t)lds + WO_OFF + 80896, rW, 80896, vo, vo);
    next();
    const unsigned fo = flag_out(fl + F_O, 256, pm, hp, lane), fi = flag_in(fl + F_O, 256, pm * 16 + hp, 16, lane);
    WAIT_V(0);
    if (wid == 0) __builtin_amdgcn_s_setprio(3);
    BARX();
    if (wid == 0) { flag_raise(ws, fo); flags_wait<O_FIRST>(ws, fi); }
    __builtin_amdgcn_s_setprio(0);
    BARX();
}

#define STAMP_SEC 0
template <class DONE>
DEVI void attn_consumer(LAS char* lds, const char* x0, char* wl, int layer, int pm, int hp, int wid, DONE&& done STAMP_PARAM) {
    const int lane = lane_id_fresh(), r = lane & 15, q = lane >> 4;
    const char* xa = wl + L_XA;
    char* o = wl + L_O;
    const int T = wid & 1, h = wid >> 1;
    const LAS char *ph[2], *pa[2];
#pragma unroll
    for (int kk = 0; kk < 2; ++kk) {
        const int sw = ((4 * kk + q) ^ (r & 7)) * 16;
        ph[kk] = opaque(lds + (layer ? kk * 1024 + q * 256 + (r ^ (T ? 0 : 8)) * 16 : (r ^ (T ? 0 : 8)) * 128 + sw));
        pa[kk] = opaque(lds + T * 2048 + (layer ? kk * 1024 + lane * 16 : r * 128 + sw));
    }
    const LAS char* pb = lds + h * 3072 + lane * 16;
    const LAS char* pt = lds + lane * 16;
    f32x4 acc[5][4];
#pragma unroll
    for (int m = 0; m < 5; ++m)
#pragma unroll
        for (int n = 0; n < 4; ++n) if (m + n) acc[m][n] = pin((f32x4){0.f, 0.f, 0.f, 0.f});
    bf16x8 fa[2][5], fb[2][4];
    auto rd1 = [&](int kk, int i) {
        if (i < 3) fb[kk][i] = LDS_FRAG(pb + 18432 + kk * 6144 + i * 1024);
        else if (i == 3) fb[kk][3] = LDS_FRAG(pt + 30720 + kk * 1024);
        else if (i == 4) fa[kk][0] = LDS_FRAG(ph[kk]);
        else if (i < 9) fa[kk][i - 4] = LDS_FRAG(pa[kk] + 2048 + (i - 5) * 4096);
    };
    auto chunk = [&](int kk, int d) {
#pragma unroll
        for (int i = 1; i < 20; ++i) {
            const int m = i >> 2, n = i & 3;
            const bf16x8 &A = fa[kk][m], &B = fb[kk][n];
            acc[m][n] = n != 2 ? MFMA(B, A, acc[m][n]) : MFMA(A, B, acc[m][n]);
            SCHED0();
            if (i <= 9) rd1(kk ^ 1, i - 1);
            if (kk == 0) {
                if (i == 11 || i == 12) ph[i & 1] = opaque(ph[i & 1] + d);
                if (i == 13 || i == 14) pa[i & 1] = opaque(pa[i & 1] + d);
                if (i == 15) pb = opaque(pb + d);
                if (i == 16) pt = opaque(pt + d);
            }
            SCHED0();
        }
    };
    f32x4 mb, mb0;
#pragma unroll
    for (int j = 0; j < 4; ++j) {
        const bool ok = ((r - 4 * q - j) & 15) < 8;
        mb[j] = ok ? 0.f : -__builtin_inff();
        mb0[j] = (ok && (pm > 0 || 4 * q + j <= r)) ? 0.f : -__builtin_inff();
    }
    mb = pin(mb); mb0 = pin(mb0);
    const unsigned mp0 = pin((4 * q > r ? 0xffffu : 0u) | (4 * q + 1 > r ? 0xffff0000u : 0u)), mp1 = pin((4 * q + 2 > r ? 0xffffu : 0u) | (4 * q + 3 > r ? 0xffff0000u : 0u));
    const unsigned qtm = pin(q == h ? 0xffffffffu : 0u);
    const unsigned vom = pin((unsigned)(r * 16 + hp * 1024 + (2 * h + (q >> 1)) * 256 + (q & 1) * 8)), vot = pin((unsigned)(r * 16 + 16384 + (hp >> 1) * 256 + (hp & 1) * 8 + h * 4));
    const LAS char* tk0 = opaque(lds + K1_SCR + ((q * 4) ^ (T ? 0 : 8)) * 4);
    const LAS char* tk = opaque(lds + K1_SCR + ((1 + T) * 16 + q * 4) * 4);
    const LAS char* tq = opaque(lds + K1_SCR + ((1 + T) * 16 + r) * 4);
    pb = opaque(pb); pt = opaque(pt);
    STAMP(2);
    const int rt = pm * 8 + (hp & 1) * 4 + wid, cg = hp >> 1;
    const ProjOut pr = layer ? proj_out((char*)xa, (char*)xa, rt, cg, lane) : proj_out_final((char*)x0, rt, cg, lane);
    BAR();
    if (layer == 0) done();
    BARX();
    STAMP(3);
#pragma unroll
    for (int i = 0; i < 9; ++i) rd1(0, i);
    SCHED0();
    const Res rx = {__builtin_amdgcn_raw_buffer_load_b128(pr.x, pr.v0, 0, 16), __builtin_amdgcn_raw_buffer_load_b128(pr.x, pr.v1, 0, 16), __builtin_amdgcn_raw_buffer_load_b64(pr.x, pr.v2, 0, 16)};
    SCHED0();
    int slot = 0;
    LOOP_ALIGN();
#pragma unroll 1
    for (int t = 0; t < 9; ++t) {
        const int d = slot == 2 ? -2 * K1_STAGE : K1_STAGE;
        slot = slot == 2 ? 0 : slot + 1;
        SCHED0();
        chunk(0, d);
        WAIT_L0(); BAR();
        SCHED0();
        chunk(1, 0);
    }
    STAMP(4);
    u32x2 ts[5], tf[5];
    {
        LAS char* xb = lds + wid * 320;
#pragma unroll
        for (int m = 0; m < 5; ++m) ts[m] = (u32x2){pack2(acc[m][3][0], acc[m][3][1]), pack2(acc[m][3][2], acc[m][3][3])};
        if (q == 2) {
#pragma unroll
            for (int m = 0; m < 5; ++m) {
                const unsigned w = h ? ts[m].y : ts[m].x;
                *(LAS u16*)(xb + m * 64 + r * 2) = (u16)w; *(LAS u16*)(xb + m * 64 + 32 + r * 2) = (u16)(w >> 16);
            }
        }
        asm volatile("" ::: "memory");
#pragma unroll
        for (int m = 0; m < 5; ++m) tf[m] = *(const LAS u32x2*)(xb + m * 64 + (r & 1) * 32 + q * 8);
    }
    {
        f32x4 rk[5]; float rq[4];
        rk[0] = *(const LAS f32x4*)(tk0);
#pragma unroll
        for (int g = 0; g < 4; ++g) { rk[g + 1] = *(const LAS f32x4*)(tk + g * 128); rq[g] = *(const LAS float*)(tq + g * 128); }
        const f32x4 zero = {0.f, 0.f, 0.f, 0.f};
        u32x4 kf[5];
#pragma unroll
        for (int m = 0; m < 5; ++m) kf[m] = (u32x4){pack2(acc[m][1][0], acc[m][1][1]), pack2(acc[m][1][2], acc[m][1][3]), ts[m].y, 0u};
        f32x4 sp[4], sc[4];
#pragma unroll
        for (int g = 0; g < 4; ++g) {
            const u32x4 qf = {pack2(acc[g + 1][0][0], acc[g + 1][0][1]), pack2(acc[g + 1][0][2], acc[g + 1][0][3]), ts[g + 1].x & qtm, 0u};
            sp[g] = MFMA(__builtin_bit_cast(bf16x8, kf[g]), __builtin_bit_cast(bf16x8, qf), zero);
            sc[g] = MFMA(__builtin_bit_cast(bf16x8, kf[g + 1]), __builtin_bit_cast(bf16x8, qf), zero);
        }
        u32x2 vf[5];
#pragma unroll
        for (int m = 0; m < 5; ++m) vf[m] = (u32x2){pack2(acc[m][2][0], acc[m][2][1]), pack2(acc[m][2][2], acc[m][2][3])};
        f32x4 rs[4], t[4]; float mx[4], sm[4];
#pragma unroll
        for (int g = 0; g < 4; ++g) {
#pragma unroll
            for (int j = 0; j < 4; ++j) { const bool pv = 4 * q + j > r; rs[g][j] = pv ? rk[g][j] : rk[g + 1][j]; t[g][j] = pv ? sp[g][j] : sc[g][j]; }
            t[g] = (t[g] * rs[g]) * (rq[g] * (0.23570226039551584f * 1.4426950408889634f)) + (g == 0 ? mb0 : mb);
            mx[g] = fmaxf(fmaxf(t[g][0], t[g][1]), fmaxf(t[g][2], t[g][3]));
        }
        xq4_max(mx);
#pragma unroll
        for (int g = 0; g < 4; ++g) {
            t[g] = t[g] - mx[g];
#pragma unroll
            for (int j = 0; j < 4; ++j) t[g][j] = __builtin_amdgcn_exp2f(t[g][j]);
            sm[g] = (t[g][0] + t[g][2]) + (t[g][1] + t[g][3]);
        }
        f32x4 om[4], ot[4];
#pragma unroll
        for (int g = 0; g < 4; ++g) {
            const f32x4 p = t[g] * rs[g];
            const unsigned p01 = pack2(p[0], p[1]), p23 = pack2(p[2], p[3]);
            const u32x4 pf = {p01 & mp0, p23 & mp1, p01 & ~mp0, p23 & ~mp1};
            om[g] = MFMA(__builtin_bit_cast(bf16x8, ((u32x4){vf[g].x, vf[g].y, vf[g + 1].x, vf[g + 1].y})), __builtin_bit_cast(bf16x8, pf), zero);
            ot[g] = MFMA(__builtin_bit_cast(bf16x8, ((u32x4){tf[g].x, tf[g].y, tf[g + 1].x, tf[g + 1].y})), __builtin_bit_cast(bf16x8, pf), zero);
        }
        xq4_sum_rcp(sm);
        const auto ro = RSRC(o + (size_t)(pm * 8 + T) * 18432);
        unsigned tw2[4];
#pragma unroll
        for (int g = 0; g < 4; ++g) {
            const f32x4 v = om[g] * sm[g];
            __builtin_amdgcn_raw_buffer_store_b64((u32x2){pack2(v[0], v[1]), pack2(v[2], v[3])}, ro, vom, g * 36864, 16);
            const f32x2 w = ot[g].xy * sm[g];
            tw2[g] = pack2(w.x, w.y);
        }
        if (q == 2) {
#pragma unroll
            for (int g = 0; g < 4; ++g) __builtin_amdgcn_raw_buffer_store_b32(tw2[g], ro, vot, g * 36864, 16);
        }
    }
    STAMP(5);
    {
        WAIT_V(0); BARX();
        STAMP(6);
        f32x4 v[5];
        proj_residual(v, rx);
        const LAS char* pw = opaque(lds + WO_OFF + lane * 16);
        const LAS char* pw2 = opaque(lds + WO_OFF + 41472 + lane * 16);
        const LAS char* ph2 = opaque(lds + WO_OFF + 4096 + q * 128 + (r & 7) * 16);
        const LAS char* ph3 = opaque(lds + WO_OFF + 41472 + 4096 + q * 128 + (r & 7) * 16);
        bf16x8 wf[90];
        auto rdw = [&](int i) {
            const int kc = i / 5, n = i % 5;
            wf[i] = n < 4 ? LDS_FRAG((kc < 9 ? pw : pw2) + (kc % 9) * 4608 + n * 1024) : LDS_FRAG((kc < 9 ? ph2 : ph3) + (kc % 9) * 4608);
        };
#pragma unroll
        for (int i = 0; i < WO_W; ++i) rdw(i);
#pragma unroll
        for (int n = 0; n < 5; ++n) v[n] = pin(v[n]);
        const auto rO = RSRC(o + (size_t)rt * 18432);
        const ProjOut po = proj_out(wl + L_XB, wl + L_STB, rt, cg, lane);
        asm volatile("" :: "s"(rO));
        WAIT_L(0);
        __builtin_amdgcn_s_setprio(REL_PRIO);
        BARX();
        STAMP(7);
        bf16x8 af[18];
        auto ldo = [&](int kc) { af[kc] = __builtin_bit_cast(bf16x8, __builtin_amdgcn_raw_buffer_load_b128(rO, lane * 16 + (kc & 3) * 1024, (kc >> 2) * 4096, 16)); };
#pragma unroll
        for (int kc = 0; kc < WO_PRE; ++kc) { ldo(kc); SCHED0(); }
        static_for<90>([&](auto ic) {
            constexpr int i = ic.value, kc = i / 5, n = i % 5, j = WO_W + 2 * (i / 3) + i % 3;
            MFMA_T(wf[i], af[kc], v[n]);
            SCHED0();
            if (i % 3 < 2 && j < 90) rdw(j);
            if (i % 3 == 2 && (i / 3) % 2 == 0 && i / 6 + WO_PRE < 18) ldo(i / 6 + WO_PRE);
            SCHED0();
        });
        asm volatile("s_nop 4" : "+v"(v[0]), "+v"(v[1]));
        STAMP(8);
        proj_store(v, po);
        __builtin_amdgcn_s_setprio(0);
    }
}

DEVI f32x2 gelu2(f32x2 v) {
    const f32x2 w = v * (v * v * -0.10294324f + -2.3022082f);
    const f32x2 d = (f32x2){__builtin_amdgcn_exp2f(w.x), __builtin_amdgcn_exp2f(w.y)} + 1.0f;
    return v * (f32x2){__builtin_amdgcn_rcpf(d.x), __builtin_amdgcn_rcpf(d.y)};
}
DEVI f32x4 gelu4(f32x4 v) { const f32x2 a = gelu2(v.xy), b = gelu2(v.zw); return (f32x4){a.x, a.y, b.x, b.y}; }
DEVI void rowsum16x8(const f32x4 a, const f32x4 b, float& o0, float& o1) {
    float t0, t1, t2, t3;
    asm volatile(
        "s_nop 1\n\t"
        "v_add_f32_dpp %2, %6, %6 row_ror:8 row_mask:0xf bank_mask:0x3\n\t"
        "v_add_f32_dpp %3, %7, %7 row_ror:8 row_mask:0xf bank_mask:0x3\n\t"
        "v_add_f32_dpp %4, %10, %10 row_ror:8 row_mask:0xf bank_mask:0x3\n\t"
        "v_add_f32_dpp %5, %11, %11 row_ror:8 row_mask:0xf bank_mask:0x3\n\t"
        "v_add_f32_dpp %2, %8, %8 row_ror:8 row_mask:0xf bank_mask:0xc\n\t"
        "v_add_f32_dpp %3, %9, %9 row_ror:8 row_mask:0xf bank_mask:0xc\n\t"
        "v_add_f32_dpp %4, %12, %12 row_ror:8 row_mask:0xf bank_mask:0xc\n\t"
        "v_add_f32_dpp %5, %13, %13 row_ror:8 row_mask:0xf bank_mask:0xc\n\t"
        "v_add_f32_dpp %0, %2, %2 row_half_mirror row_mask:0xf bank_mask:0x5\n\t"
        "s_nop 0\n\t"
        "v_add_f32_dpp %1, %4, %4 row_half_mirror row_mask:0xf bank_mask:0x5\n\t"
        "v_add_f32_dpp %0, %3, %3 row_half_mirror row_mask:0xf bank_mask:0xa\n\t"
        "v_add_f32_dpp %1, %5, %5 row_half_mirror row_mask:0xf bank_mask:0xa\n\t"
        "s_nop 0\n\t"
        "v_add_f32_dpp %0, %0, %0 quad_perm:[1,0,3,2] row_mask:0xf bank_mask:0xf\n\t"
        "v_add_f32_dpp %1, %1, %1 quad_perm:[1,0,3,2] row_mask:0xf bank_mask:0xf\n\t"
        "s_nop 0\n\t"
        "v_add_f32_dpp %0, %0, %0 quad_perm:[2,3,0,1] row_mask:0xf bank_mask:0xf\n\t"
        "v_add_f32_dpp %1, %1, %1 quad_perm:[2,3,0,1] row_mask:0xf bank_mask:0xf\n\t"
        : "=&v"(o0), "=&v"(o1), "=&v"(t0), "=&v"(t1), "=&v"(t2), "=&v"(t3)
        : "v"(a[0]), "v"(a[1]), "v"(a[2]), "v"(a[3]), "v"(b[0]), "v"(b[1]), "v"(b[2]), "v"(b[3]));
}

#define K5_PRE 8
#define RH_MAP 0
#define NET_LAYERS 2
constexpr int G_STAGE = 34816;
constexpr int K5_STAGE = 18432, K5_NS = 8;

constexpr int y_load_at(int slot) {
    int next = K5_PRE;
    for (int s = 0;; ++s) { const bool go = next < 18 + 2 * (s / 3); if (s == slot) return go ? next : -1; next += go; }
}

struct GTile { f32x4 a[4][4], e[2]; };
struct GFrag { bf16x8 a[2][4], b[2][5]; const LAS char *pa[2][2], *pb, *p8; int slot; };
DEVI void g_rd(GFrag& f, int kk, int i) {
    if (i < 4) f.a[kk][i] = LDS_FRAG(f.pa[kk][i >> 1] + (i & 1) * 2048);
    else if (i < 8) f.b[kk][i - 4] = LDS_FRAG(f.pb + (kk * 9 + i - 4) * 1024);
    else f.b[kk][4] = LDS_FRAG(f.p8 + kk * 9216);
}
template <bool ROWS_IN_REGS>
DEVI void g_chunk(GTile& c, GFrag& f, int kk, int d) {
#pragma unroll
    for (int i = 0; i < 18; ++i) {
        const int n = i >> 2, m = i & 3;
        f32x4& e = n < 4 ? c.a[m][n] : c.e[m & 1];
        e = ROWS_IN_REGS ? MFMA(f.a[kk][m], f.b[kk][n], e) : MFMA(f.b[kk][n], f.a[kk][m], e);
        SCHED0();
        if (i < 9) g_rd(f, kk ^ 1, i);
        if (kk == 0) {
            if (i >= 10 && i < 14) f.pa[(i - 10) >> 1][i & 1] = opaque(f.pa[(i - 10) >> 1][i & 1] + d);
            if (i == 14) f.pb = opaque(f.pb + d);
            if (i == 15) f.p8 = opaque(f.p8 + d);
        }
        SCHED0();
    }
}
template <int NS, bool ROWS_IN_REGS>
DEVI void g_steps(GTile& c, GFrag& f, int n) {
    LOOP_ALIGN();
#pragma unroll 1
    for (int t = 0; t < n; ++t) {
        const int d = NS > 0 ? (f.slot == NS - 1 ? -(NS - 1) * G_STAGE : G_STAGE) : (f.slot == 0 ? -(NS + 1) * G_STAGE : -G_STAGE);
        f.slot = NS > 0 ? (f.slot == NS - 1 ? 0 : f.slot + 1) : (f.slot == 0 ? -(NS + 1) : f.slot - 1);
        SCHED0();
        g_chunk<ROWS_IN_REGS>(c, f, 0, d);
        WAIT_L0();
        BAR();
        SCHED0();
        g_chunk<ROWS_IN_REGS>(c, f, 1, 0);
    }
}
DEVI void keep4(const f32x4 (&c)[4]) { asm volatile("" :: "v"(c[0]), "v"(c[1]), "v"(c[2]), "v"(c[3])); }
template <int D, int DB, bool FIRST, class HOOK>
DEVI void g_step_u(GTile& c, GFrag& f, const f32x4 (&c0)[4], HOOK&& hook) {
    static_for<36>([&](auto ic) {
        constexpr int kk = ic.value / 18, i = ic.value % 18, n = i >> 2, m = i & 3;
        if (i == 0) { WAIT_L(0); if (kk) BAR(); SCHED0(); }
        f32x4& e = n < 4 ? c.a[m][n] : c.e[m & 1];
        if (FIRST && kk == 0) asm volatile("v_mfma_f32_16x16x32_bf16 %0, %1, %2, %3" : "=&v"(e) : "v"(f.b[kk][n]), "v"(f.a[kk][m]), "v"(c0[m]));
        else MFMA_T(f.b[kk][n], f.a[kk][m], e);
        SCHED0();
        if (i < 9) g_rd(f, kk ^ 1, i);
        if (kk == 0) {
            if (i >= 10 && i < 14) f.pa[(i - 10) >> 1][i & 1] = opaque(f.pa[(i - 10) >> 1][i & 1] + D);
            if (i == 14) f.pb = opaque(f.pb + DB);
            if (i == 15) f.p8 = opaque(f.p8 + DB);
        }
        if (FIRST && ic.value == 20) keep4(c0);
        hook(ic);
        SCHED0();
    });
}
#define MF(d, a, b) "v_mfma_f32_16x16x32_bf16 " d ", " a ", " b ", " d "\n\t"
template <int D, int DB>
DEVI void g_step_gelu(GTile& c, GFrag& f, f32x4& z, float r, float c1) {
#pragma unroll
    for (int kk = 0; kk < 2; ++kk) {
        float va = z[2 * kk], vb = z[2 * kk + 1], ta, tb;
        WAIT_L(0); if (kk) BAR(); SCHED0();
#pragma unroll
        for (int i = 0; i < 9; ++i) {
            MFMA_T(f.b[kk][i >> 2], f.a[kk][i & 3], c.a[i & 3][i >> 2]);
            if (i == 5) asm volatile("v_mul_f32 %0, %0, %1" : "+v"(va) : "v"(r));
            if (i == 6) asm volatile("v_mul_f32 %0, %0, %1" : "+v"(vb) : "v"(r));
            if (i == 7) asm volatile("v_mul_f32 %0, %1, %1" : "=v"(ta) : "v"(va));
            if (i == 8) asm volatile("v_mul_f32 %0, %1, %1" : "=v"(tb) : "v"(vb));
            SCHED0();
            g_rd(f, kk ^ 1, i);
            SCHED0();
        }
        asm volatile(MF("%0", "%5", "%6") "v_fmaak_f32 %3, %3, %11, 0xc0135761\n\tv_fmaak_f32 %4, %4, %11, 0xc0135761\n\t"
                     MF("%1", "%5", "%7") "v_mul_f32 %3, %3, %9\n\tv_mul_f32 %4, %4, %10\n\t" MF("%2", "%5", "%8") "v_exp_f32 %3, %3"
                     : "+v"(c.a[1][2]), "+v"(c.a[2][2]), "+v"(c.a[3][2]), "+v"(ta), "+v"(tb)
                     : "v"(f.b[kk][2]), "v"(f.a[kk][1]), "v"(f.a[kk][2]), "v"(f.a[kk][3]), "v"(va), "v"(vb), "v"(c1));
        SCHED0(); f.pa[kk][0] = opaque(f.pa[kk][0] + D); SCHED0();
        asm volatile(MF("%0", "%6", "%7") "v_exp_f32 %5, %5\n\t" MF("%1", "%6", "%8") "v_add_f32 %4, 1.0, %4\n\tv_add_f32 %5, 1.0, %5\n\t"
                     MF("%2", "%6", "%9") "v_rcp_f32 %4, %4\n\t" MF("%3", "%6", "%10") "v_rcp_f32 %5, %5"
                     : "+v"(c.a[0][3]), "+v"(c.a[1][3]), "+v"(c.a[2][3]), "+v"(c.a[3][3]), "+v"(ta), "+v"(tb)
                     : "v"(f.b[kk][3]), "v"(f.a[kk][0]), "v"(f.a[kk][1]), "v"(f.a[kk][2]), "v"(f.a[kk][3]));
        SCHED0(); f.pa[kk][1] = opaque(f.pa[kk][1] + D); SCHED0();
        asm volatile(MF("%0", "%4", "%5") "v_mul_f32 %2, %2, %7\n\tv_mul_f32 %3, %3, %8\n\t" MF("%1", "%4", "%6")
                     : "+v"(c.e[0]), "+v"(c.e[1]), "+v"(va), "+v"(vb) : "v"(f.b[kk][4]), "v"(f.a[kk][0]), "v"(f.a[kk][1]), "v"(ta), "v"(tb));
        SCHED0();
        if (kk == 0) { f.pb = opaque(f.pb + DB); f.p8 = opaque(f.p8 + DB); }
        SCHED0();
        z[2 * kk] = va; z[2 * kk + 1] = vb;
    }
}
DEVI void g_fill(GTile& c, const float (&e)[4]) {
#pragma unroll
    for (int m = 0; m < 4; ++m) {
#pragma unroll
        for (int n = 0; n < 4; ++n) c.a[m][n] = pin((f32x4){e[m], e[m], e[m], e[m]});
        if (m < 2) c.e[m] = pin((f32x4){e[m], e[m], e[m], e[m]});
    }
}

#undef STAMP_SEC
#define STAMP_SEC 1
DEVI void gmlp_body(LAS char* lds, int wm, int wn, int pm, int cg, char* z2t, char* y, const u16* __restrict__ bsp STAMP_PARAM) {
    const int mlo = wm * 4 + wn * 2, mhi = wm * 4 + (1 - wn) * 2;
    const float zero4[4] = {0.f, 0.f, 0.f, 0.f};
    GTile z2, z1, acc;
    GFrag f;
    {
        const int lane = lane_id_fresh();
#pragma unroll
        for (int kk = 0; kk < 2; ++kk) { f.pa[kk][0] = lds + mlo * 2048 + kk * 1024 + lane * 16; f.pa[kk][1] = lds + mhi * 2048 + kk * 1024 + lane * 16; }
        f.pb = lds + 16384 + wn * 4096 + lane * 16; f.p8 = lds + 16384 + 8192 + lane * 16; f.slot = 0;
    }
    g_fill(z2, zero4); g_fill(z1, zero4);
#pragma unroll
    for (int i = 0; i < 4; ++i) f.pa[i >> 1][i & 1] = opaque(f.pa[i >> 1][i & 1]);
    f.pb = opaque(f.pb); f.p8 = opaque(f.p8);
    SCHED0();
    WAIT_V(0); BARX();
    BAR();
    BARX();
    STAMP(2);
#pragma unroll
    for (int i = 0; i < 9; ++i) g_rd(f, 0, i);
    g_steps<4, true>(z2, f, 9);
    STAMP(3);
    {
        const int lane = lane_id_fresh(), r = lane & 15, q = lane >> 4;
        f32x4 ra[4];
#pragma unroll
        for (int m = 0; m < 4; ++m) ra[m] = *(const LAS f32x4*)(lds + G_TAB + ((m < 2 ? mlo : mhi) + (m & 1)) * 64 + q * 16);
        SCHED0();
#pragma unroll
        for (int m = 0; m < 4; ++m) {
            f32x4 s1, s2;
#pragma unroll
            for (int n = 0; n < 4; ++n) {
                const f32x4 g = gelu4(z2.a[m][n] * ra[m]);
                z2.a[m][n] = g;
                if (n == 0) { s1 = g; s2 = g * g; } else { s1 += g; s2 += g * g; }
            }
            if (m < 2) { const f32x4 g = gelu4(z2.e[m] * ra[m]); z2.e[m] = g; s1 += g; s2 += g * g; }
            float u0, u1;
            rowsum16x8(s1, s2, u0, u1);
            if ((r & 3) == 0) *(LAS f32x2*)(lds + G_PART + wn * 1024 + ((m < 2 ? mlo : mhi) + (m & 1)) * 128 + (q * 4 + (r >> 2)) * 8) = (f32x2){u0, u1};
            SCHED0();
        }
    }
    WAIT_L0(); BAR();
    STAMP(4);
    g_steps<4, false>(z1, f, 4);
    unsigned b0, b1;
    f32x4 c0[4];
    g_step_u<G_STAGE, G_STAGE, false>(z1, f, c0, [&](auto ic) {
        if (ic.value != 18) return;
        STAMP(5);
        const int lane = lane_id_fresh(), q = lane >> 4;
        b0 = __builtin_amdgcn_raw_buffer_load_b32(RSRC(bsp), (lane & 15) * 4, pm * 256 + mlo * 32, 0);
        b1 = __builtin_amdgcn_raw_buffer_load_b32(RSRC(bsp), (lane & 15) * 4, pm * 256 + mhi * 32, 0);
#pragma unroll
        for (int g = 0; g < 2; ++g) {
            const auto rz2 = RSRC(z2t + (size_t)pm * 589824 + cg * 36864 + ((g ? mhi : mlo) >> 1) * 9216);
            const LAS char* tab = lds + G_TAB + (g ? mhi : mlo) * 64 + q * 16;
            LAS char* own = lds + (wm ? G_OWN : G_STAGE + 16384) + (g ^ wn) * 9216 + lane * 16;
            f32x4 ra[2], rb[2];
#pragma unroll
            for (int T = 0; T < 2; ++T) { ra[T] = *(const LAS f32x4*)(tab + 512 + T * 64); rb[T] = *(const LAS f32x4*)(tab + 1024 + T * 64); }
#pragma unroll
            for (int n = 0; n < (g ? 4 : 5); ++n) {
                const f32x4 v0 = (n < 4 ? z2.a[2 * g][n & 3] : z2.e[0]) * ra[0] + rb[0], v1 = (n < 4 ? z2.a[2 * g + 1][n & 3] : z2.e[1]) * ra[1] + rb[1];
                const u32x4 o = {pack2(v0[0], v1[0]), pack2(v0[1], v1[1]), pack2(v0[2], v1[2]), pack2(v0[3], v1[3])};
                ST16(rz2, lane * 16, n < 4 ? wn * 4096 + n * 1024 : 8192, o, 16);
                *(LAS u32x4*)(own + (n < 4 ? wn * 4096 + n * 1024 : 8192)) = o;
            }
        }
        STAMP(6);
    });
    static_for<3>([&](auto sc) {
        constexpr int D = sc.value == 0 ? G_STAGE : sc.value == 1 ? -3 * G_STAGE : 2 * G_STAGE;
        g_step_u<D, D, false>(z1, f, c0, [&](auto ic) { if (ic.value == 17 && sc.value == 1) WAIT_V(0); });
    });
    float ra[4], c1;
    {
        int lane, da[2];
        g_step_u<-G_STAGE, -G_STAGE, false>(z1, f, c0, [&](auto ic) {
            constexpr int i = ic.value;
            if (i == 9) lane = pin(lane_id_fresh());
            if (i == 14 || i == 15) da[i & 1] = pin((lane & 15) * 128 + (((4 * (i & 1) + (lane >> 4)) ^ (lane & 7)) * 16) - lane * 16 - (i & 1) * 1024);
            if (i == 16 || i == 17) f.pa[0][i & 1] = opaque(f.pa[0][i & 1] + da[0]);
            if (i == 27 || i == 28) f.pa[1][i & 1] = opaque(f.pa[1][i & 1] + da[1]);
            if (i >= 29 && i < 33) {
                constexpr int m = (i - 29) & 3;
                const float e = m & 1 ? bfhi(m < 2 ? b0 : b1) : bflo(m < 2 ? b0 : b1);
                c0[m] = pin((f32x4){e, e, e, e});
            }
            if (i == 33) {
#pragma unroll
                for (int m = 0; m < 4; ++m) ra[m] = *(const LAS float*)(lds + G_TAB + ((m < 2 ? mlo : mhi) + (m & 1)) * 64 + (lane & 15) * 4);
                c1 = pin(-0.10294324f);
            }
        });
    }
    STAMP(7);
    g_step_u<2 * G_STAGE, G_OWN - G_STAGE - 16384, true>(acc, f, c0, [](auto) {});
    static_for<18>([&](auto sc) {
        constexpr int st = sc.value, m = st < 16 ? st >> 2 : st & 1;
        constexpr int D = st == 0 ? -3 * G_STAGE : st % 3 == 1 ? 2 * G_STAGE : -G_STAGE;
        g_step_gelu<D, st == 0 ? 16384 - G_OWN : D>(acc, f, st < 16 ? z1.a[m][st & 3] : z1.e[m], ra[m], c1);
    });
    f.slot = 1;
    STAMP(9);
    g_steps<-3, false>(acc, f, 13);
    STAMP(10);
    const int lane = lane_id_fresh();
#pragma unroll
    for (int g = 0; g < 2; ++g) {
        const auto ry = RSRC(y + (size_t)(pm * 8 + (g ? mhi : mlo)) * 73728 + cg * 4608);
#pragma unroll
        for (int T = 0; T < 2; ++T) {
            const int m = 2 * g + T;
#pragma unroll
            for (int p = 0; p < 2; ++p) {
                const f32x4 a = acc.a[m][2 * p] * z1.a[m][2 * p], c = acc.a[m][2 * p + 1] * z1.a[m][2 * p + 1];
                ST16(ry, lane * 16 + wn * 2048 + p * 1024, T * 73728, ((u32x4){pack2(a[0], a[1]), pack2(a[2], a[3]), pack2(c[0], c[1]), pack2(c[2], c[3])}), 16);
            }
            if (g == 0) {
                const f32x4 a = acc.e[T] * z1.e[T];
                ST8(ry, (lane >> 5) * 256 + (lane & 15) * 16 + ((lane >> 4) & 1) * 8, T * 73728 + 4096, ((u32x2){pack2(a[0], a[1]), pack2(a[2], a[3])}), 16);
            }
        }
    }
}

#define NET_IDS \
    int b = __builtin_amdgcn_readfirstlane(blockIdx.x), wid = wid8 & 3; \
    asm volatile("" : "+s"(b), "+s"(wid));     \
    const int xcd = b & 7, idx = b >> 3; \
    const int pa = (xcd >> 1) * 4 + (idx & 3), hp = (xcd & 1) * 8 + (idx >> 2); \
    const int pm = (xcd >> 2) * 8 + (idx & 7), cg = (xcd & 3) * 4 + (idx >> 3); \
    const int rh = RH_MAP ? xcd * 4 + (idx & 3) : (xcd >> 1) * 8 + (idx & 7), c5 = RH_MAP ? idx >> 2 : (xcd & 1) * 4 + (idx >> 3); \
    const int lead = (idx + (idx >> 3)) & 3; \
    auto kord = [&](int t, int n) { \
        const int k = t + ((int)(0x1F000000u << (((t - lead) & 3) * 4)) >> 28); \
        return (unsigned)k < (unsigned)n ? k : t; \
    }; \
    auto kord5 = [&](int t) { \
        const int k = t + ((int)(0x1F000000u << (((t - (RH_MAP ? c5 : lead)) & 3) * 4)) >> 28); \
        return (unsigned)k < 18u ? k : t; \
    };

typedef short s16x4 __attribute__((ext_vector_type(4)));
DEVI u32x4 tr_frag(const LAS char* p, int four_rows) {
    const u32x2 a = __builtin_bit_cast(u32x2, __builtin_amdgcn_ds_read_tr16_b64_v4i16((LAS s16x4*)p));
    const u32x2 b = __builtin_bit_cast(u32x2, __builtin_amdgcn_ds_read_tr16_b64_v4i16((LAS s16x4*)(p + four_rows)));
    return (u32x4){a.x, a.y, b.x, b.y};
}
DEVI float dot2(unsigned v, unsigned w, float acc) { return __builtin_amdgcn_fdot2_f32_bf16(__builtin_bit_cast(bf16x2_t, v), __builtin_bit_cast(bf16x2_t, w), acc, false); }
DEVI float sum2(unsigned v, float acc) { return dot2(v, 0x3f803f80u, acc); }
struct Sel { unsigned lo, hi, nlo, nhi; };
DEVI Sel make_sel() { return {(unsigned)pin_s(0x00003f80), (unsigned)pin_s(0x3f800000), (unsigned)pin_s(0x0000bf80), (unsigned)pin_s((int)0xbf800000u)}; }
DEVI unsigned centre2(unsigned v, f32x2& e, float mean, const Sel& k) {
    const float a = dot2(k.lo, v, e.x), b = dot2(k.hi, v, e.y);
    const unsigned r = pack2(a, b);
    e = (f32x2){dot2(k.nlo, r, a), dot2(k.nhi, r, b)} - mean;
    return r;
}
typedef u32x4 Retile[9];
template <int PITCH>
DEVI void retile_out_make(Retile& o, const LAS char* img, int lane) {
    const int c = lane & 15, q = lane >> 4, p = lane & 3;
    const LAS char* src = img + (q * 8 + (c >> 2)) * PITCH;
#pragma unroll
    for (int i = 0; i < 8; ++i) o[i] = tr_frag(src + p * 16 + (i >> 2) * 144 + ((i >> 1) & 1) * 64 + (i & 1) * 8, 4 * PITCH);
    o[8] = tr_frag(src + 128 + (p >> 1) * 144 + (p & 1) * 8, 4 * PITCH);
}
DEVI void retile_out_store(const Retile& o, char* dst, int nkc, int kc, int w, int lane) {
    const int c = lane & 15, q = lane >> 4;
    const auto rd = RSRC(dst + (size_t)(2 * w * nkc + kc) * 4608);
#pragma unroll
    for (int i = 0; i < 8; ++i) ST16(rd, lane * 16 + (i & 3) * 1024, (i >> 2) * nkc * 4608, o[i], 16);
    ST16(rd, (c >> 3) * nkc * 4608 + 4096 + q * 128 + (c & 7) * 16, 0, o[8], 16);
}

DEVI float dpp_ror(float v, int n) { return n == 8 ? __builtin_amdgcn_update_dpp(0.f, v, 0x128, 0xf, 0xf, true) : n == 4 ? __builtin_amdgcn_update_dpp(0.f, v, 0x124, 0xf, 0xf, true)
                                          : n == 2 ? __builtin_amdgcn_update_dpp(0.f, v, 0x122, 0xf, 0xf, true) : __builtin_amdgcn_update_dpp(0.f, v, 0x121, 0xf, 0xf, true); }
DEVI unsigned long long epoch_of(const char* ws) {
    SCHED0();
    const unsigned long long ep = *(const __attribute__((address_space(4))) unsigned long long*)(ws + pin_s((int)OFF_EP)) + 1ull;
    asm volatile("" :: "s"(ep));
    return ep;
}
DEVI void prologue_done(LAS char* lds, char* ws, unsigned long long ep, int b, int lane) {
    if (lds_arrive(lds + G_WORD + 8) == 7) ST16(RSRC(ws), lane ? 0x80000000u : (unsigned)OFF_FL + b * 16, 0, ((u32x4){(unsigned)ep, (unsigned)(ep >> 32), 0u, 0u}), 16);
}
DEVI void centre_frag(u32x4& f, f32x2 (&er)[4], float mean, const Sel& k) {
#pragma unroll
    for (int e = 0; e < 4; ++e) f[e] = centre2(f[e], er[e], mean, k);
}
DEVI unsigned long long prologue(LAS char* lds, char* ws, const char* x0, const char* wqkv, const u16* wo, const u16* w_in, int b, int pa, int wid8 PST_PARAM) {
    PST_DECL;
    const int lane = lane_id_fresh(), c = lane & 15, q = lane >> 4, w = wid8 & 3, jw = w - 1 + 3 * (wid8 >> 2);
    const bool wjob = b >= 16 && b < 112 && w, ijob = b >= 112 && w, ojob = b < 9;
    const unsigned l0 = (unsigned)(size_t)lds;
    LAS char* img = lds + 2 * K1_STAGE;
    LAS float* red = (LAS float*)(lds + G_PART);
    const int uw = b - 16, head = (uw & 7) * 4 + ((uw >> 3) & 3), part = uw >> 5, hp = head >> 1, odd = head & 1;
    const int c0 = part * 1152 + hp * 72 + odd * 40;
    const int ui = b - 112, st = (ui & ~15) + 2 * (ui & 7) + ((ui >> 3) & 1);
    if (wjob) {
        const auto rs = __builtin_amdgcn_make_buffer_rsrc((void*)wqkv, 0, 576 * 3456, 0x00020000);
        unsigned v[5];
#pragma unroll
        for (int i = 0; i < 5; ++i) v[i] = (((i * 64 + lane) * 21846) >> 16) * (3456 - 48) + lane * 16 + jw * 96 * 3456 + (c0 & ~15);
        dma4<false>(l0 + 2 * K1_STAGE + jw * 6144, rs, 0, v[0], v[1], v[2], v[3]);
        dma1(l0 + 2 * K1_STAGE + jw * 6144 + 4096, rs, 0, lane < 32 ? v[4] + 4096 : 0x80000000u);
    }
    if (ijob) {
        if (I_SLEEP) __builtin_amdgcn_s_sleep(I_SLEEP);
        const unsigned v = (lane >> 2) * 9216 + (lane & 3) * 16, d = 16 * 9216 - 1024;
        const auto rs = RSRC((const char*)(w_in + st * 32) + (size_t)jw * 96 * 9216);
        dma4<false>(l0 + 2 * K1_STAGE + jw * 6144, rs, 0, v, v + d, v + 2 * d, v + 3 * d);
        dma2<false>(l0 + 2 * K1_STAGE + jw * 6144 + 4096, rs, 64 * 9216, v, v + d);
    }
    if (ojob) {
        const auto rs = RSRC(wo + w * 144);
#pragma unroll
        for (int j = 0; j < 9; ++j) {
            const int i = lane + 64 * j, rl = (i * 3641) >> 16, kp = (b * 2 + (wid8 >> 2)) * 32 + rl;
            const int row = kp < 512 ? 18 * (kp >> 4) + (kp & 15) + 2 * ((kp >> 4) & 1) : 36 * ((kp - 512) >> 2) + 16 + (kp & 3);
            dma1(l0 + 2 * K1_STAGE + wid8 * 9216 + j * 1024, rs, 0, (unsigned)(row * 1152 + (i - rl * 18) * 16));
        }
    }
    if (wid8 == 4) { lds_word_set(lds + G_WORD + c * 4, 0u); WAIT_L0(); }
    BAR();
    if (W_ALONE && wid8 == 4 && b >= 16 && b < 112) lds_count_wait(lds + G_WORD + 12, W_ALONE);
    if (wid8 == 4) {
        AttnX ax = attn_x(x0, x0, 0, pa, 0, lane);
#pragma unroll
        for (int s = 0; s < 2; ++s)
#pragma unroll
            for (int i = 0; i < 4; ++i) { ax.oA = (pa * 8 + 2 * i) * 18432; attn_issueA(lds, ax, i, s, s); }
    }
    u32x4 xv[9];
    if (wid8 == 0) {
        const unsigned v = (lane >> 3) * 2304 + (lane & 7) * 16;
        const auto rx = RSRC(x0 + (size_t)slot2row(b * 8) * 1152);
#pragma unroll
        for (int i = 0; i < 9; ++i) asm volatile("s_nop 4\n\tbuffer_load_dwordx4 %0, %1, %2, 0 offen offset:%3" : "+v"(xv[i]) : "v"(v), "s"(rx), "i"(i * 128) : "memory");
        const auto rw = RSRC(ws);
        const u32x4 ones = {~0u, ~0u, ~0u, ~0u}, zero = {0u, 0u, 0u, 0u};
        const unsigned vo = lane * 16;
        ST16(rw, vo, (unsigned)OFF_ZST + b * 2048, ones, 16); ST16(rw, vo, (unsigned)OFF_ZST + b * 2048 + 1024, ones, 16);
        ST16(rw, vo, (unsigned)OFF_CNT + b * 2560, zero, 16); ST16(rw, vo, (unsigned)OFF_CNT + b * 2560 + 1024, zero, 16);
        ST16(rw, vo, lane < 32 ? (unsigned)OFF_CNT + b * 2560 + 2048 : (unsigned)(OFF_L0 + L_STB) + b * 512 - 512, zero, 16);
        ST16(rw, vo, (unsigned)(OFF_L0 + L_O) + (b >> 1) * 18432 + 16384 + (b & 1) * 1024, zero, 16);
        ST16(rw, b < 8 ? vo : 0x80000000u, (unsigned)OFF_FL2 + b * 1024, zero, 16);
    }
    PST(1);
    const unsigned long long ep = epoch_of(ws);
    const Sel k = make_sel();
    u32x4 f[6];
    unsigned vd, vd2;
    __amdgpu_buffer_rsrc_t rd, rd2;
    if (wjob || ijob) {
        const LAS char *s0, *s1;
        if (ijob) {
            int o[2];
#pragma unroll
            for (int h = 0; h < 2; ++h) {
                const int u = st * 2 + h, half = u >= 144, g = u - half * 144, cg = g / 9, j = g - cg * 9, p = lane & 3;
                o[h] = h * 32 + (j < 8 ? (p & 1) * 16 + (p >> 1) * 8 : p * 8);
                (h ? rd2 : rd) = RSRC(ws + OFF_TWIN + (size_t)cg * (18 * 18432) + (half * 18 + jw * 3) * 9216);
                (h ? vd2 : vd) = q * 256 + (j < 8 ? ((j & ~1) + (c >> 3)) * 1024 + ((j & 1) * 8 + (c & 7)) * 16 : 8192 + c * 16);
            }
            s0 = img + jw * 6144 + (q * 8 + (c >> 2)) * 64 + o[0]; s1 = s0 + (o[1] - o[0]);
        } else {
            s0 = img + jw * 6144 + (q * 8 + (c >> 2)) * 48 + (c0 & 15); s1 = s0 + 32 - odd * 8 + min(c & 3, 2) * 1536; s0 += (c & 3) * 8;
            rd = RSRC(ws + OFF_TWQKV + (size_t)(hp * 18 + jw * 3) * 6144 + (3 * odd + part) * 1024); vd = lane * 16;
            rd2 = RSRC(ws + OFF_TWT + (size_t)((hp * 3 + part) * 18 + jw * 3) * 256); vd2 = c < 12 && !odd ? (unsigned)((c >> 2) * 256 + q * 64 + (c & 3) * 16) : 0x80000000u;
        }
        s0 = opaque(s0); s1 = opaque(s1); vd = pin(vd); vd2 = pin(vd2);
        asm volatile("" :: "s"(rd), "s"(rd2));
        WAIT_V(0);
        PST(2);
        float sm[4] = {0.f, 0.f, 0.f, 0.f};
        if (ijob) {
#pragma unroll
            for (int i = 0; i < 6; ++i) f[i] = tr_frag((i < 3 ? s0 : s1) + (i % 3) * 2048, 256);
#pragma unroll
            for (int e = 0; e < 24; ++e) sm[(e >= 12) * 2 + (e & 1)] = sum2(f[e >> 2][e & 3], sm[(e >= 12) * 2 + (e & 1)]);
            sm[2] += sm[3];
        } else {
#pragma unroll
            for (int i = 0; i < 4; ++i) f[i] = tr_frag(i < 3 ? s0 + i * 1536 : s1, 192);
#pragma unroll
            for (int e = 0; e < 16; ++e) sm[(e >= 12) * 2 + (e & 1)] = sum2(f[e >> 2][e & 3], sm[(e >= 12) * 2 + (e & 1)]);
            sm[2] = c < 12 ? sm[2] + sm[3] : 0.f;
            sm[2] += dpp_ror(sm[2], 4); sm[2] += dpp_ror(sm[2], 8);
        }
        auto x = __builtin_amdgcn_permlane16_swap(__float_as_uint(sm[0] + sm[1]), __float_as_uint(sm[2]), false, false);
        red[jw * 64 + lane] = __uint_as_float(x[0]) + __uint_as_float(x[1]);
        if (lds_arrive(lds + G_WORD + 12) < 5) lds_count_wait(lds + G_WORD + 12, 6);
        PST(3);
        float t = 0.f;
#pragma unroll
        for (int i = 0; i < 6; ++i) t += red[i * 64 + lane];
        x = __builtin_amdgcn_permlane32_swap(__float_as_uint(t), __float_as_uint(t), false, false);
        t = (__uint_as_float(x[0]) + __uint_as_float(x[1])) * (1.0f / 576.0f);
        auto y = __builtin_amdgcn_permlane16_swap(__float_as_uint(t), __float_as_uint(t), false, false);
        const float m0 = __uint_as_float(y[0]), m1 = __uint_as_float(y[1]);
        f32x2 e0[4] = {{-m0, -m0}, {-m0, -m0}, {-m0, -m0}, {-m0, -m0}}, e1[4] = {{-m1, -m1}, {-m1, -m1}, {-m1, -m1}, {-m1, -m1}};
        const unsigned dk = ijob ? 9216 : 6144;
#pragma unroll
        for (int i = 0; i < 3; ++i) { centre_frag(f[i], e0, m0, k); ST16(rd, vd, i * dk, f[i], 16); }
#pragma unroll
        for (int i = 0; i < 3; ++i) if (i == 0 || ijob) { centre_frag(f[3 + i], e1, m1, k); ST16(rd2, vd2, i * 9216, f[3 + i], 16); }
        PST(4);
        if (wjob) {
            if (W_ALONE > 6) lds_arrive(lds + G_WORD + 12);
            const unsigned fo = pin((unsigned)OFF_FW + ((lane >> 3) * 16 + hp) * 1024 + (3 * odd + part) * 128 + (lane & 7) * 16);
            WAIT_V(0);
            PST(5);
            if (lds_arrive(lds + G_WORD + 4) == 5) {
                const u32x4 e4 = {(unsigned)ep, (unsigned)(ep >> 32), (unsigned)ep, (unsigned)(ep >> 32)};
                ST16(RSRC(ws), fo, 0, e4, 16); ST16(RSRC(ws), fo + 131072, 0, e4, 16);
            }
        }
    }
    if (wid8 == 0) {
        WAIT_V(7);
        asm volatile("" : "+v"(xv[0]), "+v"(xv[1]), "+v"(xv[2]), "+v"(xv[3]), "+v"(xv[4]), "+v"(xv[5]), "+v"(xv[6]), "+v"(xv[7]), "+v"(xv[8]));
        float s = 0.f, q2 = 0.f;
#pragma unroll
        for (int e = 0; e < 36; ++e) { s = sum2(xv[e >> 2][e & 3], s); q2 = dot2(xv[e >> 2][e & 3], xv[e >> 2][e & 3], q2); }
        ST8(RSRC(ws + OFF_L0 + L_STA + (size_t)b * 512), lane * 8, 0, ((u32x2){__float_as_uint(s), __float_as_uint(q2)}), 16);
    }
    if (ojob) {
        if (wid8) WAIT_V(0);
        Retile rt;
        retile_out_make<288>(rt, img + wid8 * 9216, lane); retile_out_store(rt, ws + OFF_TWO, 18, b * 2 + (wid8 >> 2), w, lane);
    }
    PST(7);
    PST_DUMP(wid8 == 0 || wid8 == 4 || wid8 == 5, wid8 ? wid8 : 2);
    return ep;
}

__global__ __launch_bounds__(512, 2) TRACE_ATTR void k_net(char* ws, const u16* wsp, const u16* bsp, char* out, const char* x0, const char* wqkv, const u16* wo,
                                                             const u16* w_in, const u16* w_out DBG_ARG) {
    extern __shared__ __attribute__((aligned(16))) char smem[];
    LAS char* lds = (LAS char*)smem;
    asm volatile("gm_code_begin:");
    if (blockIdx.x < 16) {
        unsigned long long pc; int d0, len;
        asm volatile("s_getpc_b64 %0\n1:\n\ts_mov_b32 %1, gm_code_begin-1b\n\ts_mov_b32 %2, gm_code_end1-gm_code_begin\n\ts_max_i32 %2, %2, gm_code_end2-gm_code_begin"
                     : "=s"(pc), "=s"(d0), "=s"(len) :: "scc");
        const auto rs = __builtin_amdgcn_make_buffer_rsrc((void*)((pc + d0) & ~15ull), 0, len, 0x00020000);
        dma1((unsigned)(size_t)lds + G_OWN, rs, 0, (threadIdx.x * 2 + (blockIdx.x >> 3)) * 128);
    }
    asm volatile("" :: "s"(ws), "s"(wsp), "s"(bsp), "s"(out), "s"(x0), "s"(wqkv), "s"(wo), "s"(w_in), "s"(w_out));
    ws = own_s(ws); wsp = own_s(wsp); bsp = own_s(bsp); out = own_s(out); x0 = own_s(x0); wqkv = own_s(wqkv); wo = own_s(wo); w_in = own_s(w_in); w_out = own_s(w_out);
    const char *twin = ws + OFF_TWIN, *twout = ws + OFF_TWOUT;
    STAMP_DECL;
    const int wid8 = __builtin_amdgcn_readfirstlane(threadIdx.x >> 6);
    unsigned long long ep;
    { NET_IDS ep = prologue(lds, ws, x0, wqkv, wo, w_in, b, pa, wid8 PST_FWD); }
    if (wid8 >= 4) {
        u32x4 wr[5][9];
#pragma unroll 1
      for (int layer = 0; layer < NET_LAYERS; ++layer) {
        NET_IDS
        TRACE_BEGIN;
        char* wl = ws + OFF_L0 + layer * SZ_L;
        const unsigned fl = layer * F_LAYER;
        const char *xin = wl + L_XB, *st = wl + L_STB, *z2t = wl + (layer ? L_Y : L_Z2T);
        unsigned long long* zst = (unsigned long long*)(ws + OFF_ZST + layer * SZ_ZST);
        const int lane = lane_id_fresh(), l3 = lane >> 3;
        const unsigned vo = lane * 16;
        const unsigned l0 = (unsigned)(size_t)lds, lA = l0 + wid * 4096, lB = l0 + 16384 + wid * 4608;
        const auto rX = RSRC(xin + (size_t)(pm * 8 + 2 * wid) * 18432), rI = RSRC(twin + (size_t)cg * (18 * 18432) + wid * 4608);
        auto issue1B = [&](int g, int slot) { const int k = kord(g < 9 ? g : (g < 13 ? g - 4 : g - 13), 9); dma5<true>(lB + slot * G_STAGE, rI, (g < 9 ? 9 + k : k) * 18432, vo); };
        auto issue1A = [&](int g, int slot) { dma4<true>(lA + slot * G_STAGE, rX, kord(g < 9 ? g : g - 13, 9) * 2048, vo, vo, vo + 16384, vo + 16384); };
        auto issue1 = [&](int g, int slot) { issue1B(g, slot); issue1A(g, slot); };
        const auto rA = RSRC((const char*)wsp + (size_t)(pm * 128 + wid * 32) * 4096), rB = RSRC(z2t + cg * 36864 + wid * 4608);
        const unsigned voA = (unsigned)(2 * l3 * 4096 + ((lane & 7) ^ l3) * 16);
        auto issueB = [&](int k, int slot) { dma5<true>(lB + slot * G_STAGE, rB, (k >> 1) * 589824 + (k & 1) * 18432, vo); };
        const auto rW = RSRC(twout + (size_t)c5 * (72 * 4608));
        auto issue5 = [&](int t) { dma5<true>(l0 + ((t + 6) & 7) * K5_STAGE + wid * 4608, rW, kord5(t) * K5_STAGE + wid * 4608, vo); };
        auto load5 = [&](int p, int i) {
            const int t0 = p ? 2 * p + 6 : 4, ow = kord5(t0 + i) * K5_STAGE + wid * 4096;
            asm volatile("s_nop 4\n\tbuffer_load_dwordx4 %0, %4, %5, %6 offen sc1\n\tbuffer_load_dwordx4 %1, %4, %5, %6 offen offset:1024 sc1\n\t"
                         "buffer_load_dwordx4 %2, %4, %5, %6 offen offset:2048 sc1\n\tbuffer_load_dwordx4 %3, %4, %5, %6 offen offset:3072 sc1"
                         : "+v"(wr[p][4 * i]), "+v"(wr[p][4 * i + 1]), "+v"(wr[p][4 * i + 2]), "+v"(wr[p][4 * i + 3]) : "v"(vo), "s"(rW), "s"(ow) : "memory");
            if (i) {
                const int oe = kord5(t0 + (wid >> 1)) * K5_STAGE + 16384 + (wid & 1) * 1024;
                asm volatile("s_nop 4\n\tbuffer_load_dwordx4 %0, %1, %2, %3 offen sc1" : "+v"(wr[p][8]) : "v"(vo), "s"(rW), "s"(oe) : "memory");
            }
        };
        auto put5 = [&](int w0, int w1) {
#pragma unroll
            for (int w = w0; w < w1; ++w) {
                const int p = w / 9, i = w % 9 - 1;
                LAS char* sl = lds + (((p ? 2 * p + 6 : 4) + 6) & 7) * K5_STAGE + lane * 16;
                if (i < 0) *(LAS u32x4*)(sl + (wid >> 1) * K5_STAGE + 16384 + (wid & 1) * 1024) = wr[p][8];
                else *(LAS u32x4*)(sl + (i >> 2) * K5_STAGE + wid * 4096 + (i & 3) * 1024) = wr[p][i];
            }
            WAIT_L0();
        };
        const int row = wid * 32 + (lane & 31);
        auto step1 = [&](int t) { BAR(); issue1(t + 4, t & 3); };
        const char* ps;
        {
            unsigned fo, fi;
            const int ojob = layer || !wid ? 288 : b + (wid - 1) * 256, oimg = wid == 1 ? 2048 : G_STAGE;
            attn_producer(lds, ws, x0, wl, fl, ep, layer, pa, hp, wid, [&] { prologue_done(lds, ws, ep, b, lane); }, [&] {
#pragma unroll
                for (int i = 0; i < 2; ++i)
                    asm volatile("s_nop 4\n\tbuffer_load_dwordx4 %0, %4, %5, %6 offen\n\tbuffer_load_dwordx4 %1, %4, %5, %6 offen offset:1024\n\t"
                                 "buffer_load_dwordx4 %2, %4, %5, %6 offen offset:2048\n\tbuffer_load_dwordx4 %3, %4, %5, %6 offen offset:3072"
                                 : "+v"(wr[0][4 * i]), "+v"(wr[0][4 * i + 1]), "+v"(wr[0][4 * i + 2]), "+v"(wr[0][4 * i + 3]) : "v"(vo), "s"(RSRC(wsp)), "s"(b * 32768 + wid * 8192 + i * 4096) : "memory");
            }, [&] {
                issue1B(0, 0); issue1B(1, 1);
                fo = flag_out(fl + F_X1, 256, pa, hp, lane); fi = flag_in(fl + F_X1, 256, pm * 16 + cg, 16, lane);
                ps = pin(st + (size_t)(pm * 128 + wid * 32) * 64 + lane * 16);
                if (ojob < 288) {
                    const auto rs = RSRC(w_out + (size_t)(ojob >> 2) * 32 * 576 + (ojob & 3) * 144);
#pragma unroll
                    for (int j = 0; j < 9; ++j) { const int i = lane + 64 * j; dma1(l0 + oimg + j * 1024, rs, 0, (unsigned)(((i * 3641) >> 16) * (1152 - 288) + i * 16)); }
                }
            });
            if (ojob < 288) {
                Retile& rt = wr[1];
                retile_out_make<288>(rt, lds + oimg, lane);
                __builtin_amdgcn_s_sleep(OJOB_SLEEP);
                retile_out_store(rt, ws + OFF_TWOUT, 72, ojob >> 2, ojob & 3, lane);
                WAIT_V(0);
                flag_raise(ws, lane ? 0x80000000u : (unsigned)OFF_FL2 + ojob * 16);
            }
            BARX();
            if (wid == 0) flag_raise(ws, fo);
            issue1B(2, 2); issue1B(3, 3);
            asm volatile("" :: "s"(rX));
            if (wid == 0) flags_wait<X1_FIRST>(ws, fi);
            BARX();
        }
        issue1A(0, 0); issue1A(1, 1); issue1A(2, 2);
        WAIT_V(8); BAR();
        issue1A(3, 3);
        {
            f32x4 sp[3];
            WAIT_V(8); step1(0);
            WAIT_V(13); step1(1);
            WAIT_V(18); step1(2);
            WAIT_V(18); step1(3);
            unsigned got[2];
            const unsigned fd = (unsigned)OFF_FL2 + wid * 1024 + vo, fd2 = (unsigned)OFF_FL2 + 4096 + (lane & 31) * 16;
            if (layer == 0) asm volatile("s_nop 4\n\tbuffer_load_dword %0, %2, %4, 0 offen sc1\n\tbuffer_load_dword %1, %3, %4, 0 offen sc1" : "+v"(got[0]), "+v"(got[1]) : "v"(fd), "v"(fd2), "s"(RSRC(ws)) : "memory");
            WAIT_IF(layer, 18, 20); step1(4);
            if (layer == 0) {
                WAIT_V(9);
                asm volatile("" : "+v"(got[0]), "+v"(got[1]));
                if (__any((got[0] & got[1]) == 0u)) { flags_wait<0>(ws, fd); flags_wait<0>(ws, fd2); }
            }
            WAIT_V(18); BAR(); issue1B(9, 1); side_load(sp, ps, ps); if (layer == 0) load5(3, 0);
            WAIT_IF(layer, 17, 21); BAR(); issue1B(10, 2); if (layer == 0) load5(3, 1);
            WAIT_IF(layer, 5, 14); BARX();
            side_fence(sp);
#pragma unroll
            for (int i = 0; i < 2; ++i) lds_word_set(lds + G_TAB + (wid * 32 + i * 16 + (lane >> 2)) * 4, __float_as_uint(ln_rstd(sp[i])));
            issue1B(11, 3);
            WAIT_L0();
        }
        BAR(); issue1B(12, 0);
        if (layer == 1) {
            const auto rd = RSRC(out + (size_t)b * 9216 + wid * 2304);
            ST16(rd, vo, 0, ((u32x4){0u, 0u, 0u, 0u}), 16); ST16(rd, vo, 1024, ((u32x4){0u, 0u, 0u, 0u}), 16);
            if (lane < 16) ST16(rd, vo, 2048, ((u32x4){0u, 0u, 0u, 0u}), 16);
        }
        if (layer == 0) {
#pragma unroll
            for (int p = 0; p < 6; ++p) load5(p >> 1, p & 1);
        }
        WAIT_V(0);
        __builtin_amdgcn_s_setprio(3);
        const char* src = pin((const char*)zst + (size_t)(pm * 16 + (lane >> 4) * 4) * 1024 + wid * 256 + (lane & 15) * 16);
        const unsigned tw = pin(l0 + G_TAB + 512 + (lane >> 5) * 512 + (wid * 32 + (lane & 15) * 2 + ((lane >> 4) & 1)) * 4);
        const unsigned fz = flag_out(fl + F_Z2, 256, cg, pm, lane), fzi = pin(fl + F_Z2 + (cg * 16 + pm) * 256 + ((lane ^ pm) & 8) * 16 + (lane & 7) * 16);
        const unsigned vA[4] = {pin(voA), pin(voA + 16 * 4096 - 1024), pin(voA + 4096 - 2048), pin(voA + 17 * 4096 - 3072)};
        auto spA = [&](int so, unsigned la) { dma4<false>(la, rA, so, vA[0], vA[1], vA[2], vA[3]); };
        auto issueA = [&](int k, int slot) { spA(k * 128, lA + slot * G_STAGE); };
        {
            const LAS char* pp = opaque(lds + G_PART + row * 8);
            const auto rs = RSRC(zst + (size_t)(pm * 16 + cg) * 128);
            const unsigned vp = pin(lane < 32 ? (unsigned)row * 8u : 0x80000000u);
            asm volatile("" :: "s"(rs));
            BARX();
            const f32x2 a = *(const LAS f32x2*)pp, c = *(const LAS f32x2*)(pp + 1024);
            ST8(rs, vp, 0, ((u32x2){__float_as_uint(add1(a.x, c.x)), min(__float_as_uint(add1(a.y, c.y)), 0xfffffffeu)}), 16);
            SCHED0();
        }
        __builtin_amdgcn_s_setprio(0);
        {
            f32x4 g[4];
            float sm[4];
            auto gather = [&] {
                asm volatile("global_load_dwordx4 %0, %4, off sc1\n\tglobal_load_dwordx4 %1, %4, off offset:1024 sc1\n\t"
                             "global_load_dwordx4 %2, %4, off offset:2048 sc1\n\tglobal_load_dwordx4 %3, %4, off offset:3072 sc1"
                             : "=&v"(g[0]), "=&v"(g[1]), "=&v"(g[2]), "=&v"(g[3]) : "v"(src) : "memory");
            };
            auto total = [&] {
                asm volatile("" : "+v"(g[0]), "+v"(g[1]), "+v"(g[2]), "+v"(g[3]));
#pragma unroll
                for (int i = 0; i < 4; ++i) sm[i] = add1(add1(g[0][i], g[1][i]), add1(g[2][i], g[3][i]));
            };
            step1(9); step1(10);
            BARX(); gather(); issue1(15, 3);
            WAIT_V(22); BARX();
            __builtin_amdgcn_s_setprio(3);
            WAIT_V(9);
            total();
            if (__any(__builtin_isunordered(sm[1], sm[3]))) {
#pragma unroll 1
                for (unsigned spin = 0; spin < 400000u; ++spin) {
                    bool ok = true;
#pragma unroll
                    for (int i = 0; i < 4; ++i) ok &= __float_as_uint(g[i][1]) != 0xffffffffu && __float_as_uint(g[i][3]) != 0xffffffffu;
                    if (__all(ok)) break;
                    gather();
                    WAIT_V(0);
                    total();
                }
            }
            xq4_sum(sm);
            const bool odd = lane & 16;
            float mean;
            const float rs = rstd_of(odd ? sm[2] : sm[0], odd ? sm[3] : sm[1], 1.0f / 2304.0f, mean);
            asm volatile("ds_write_b32 %0, %1" :: "v"(tw), "v"(lane < 32 ? rs : -mean * rs) : "memory");
            __builtin_amdgcn_s_setprio(0);
            issue1B(16, 0);
            WAIT_L0();
        }
        BARX(); issue1A(16, 0); issueA(2 * pm, 1);
        auto kord2 = [&](int t) { const int j = kord(t - 2, 30); return ((j < 2 * (pm & 7) ? j : j + 2) + 2 * (pm & 8)) & 31; };
        unsigned seen;
        auto look = [&] { if (wid == 0) asm volatile("s_nop 4\n\tbuffer_load_dword %0, %1, %2, 0 offen sc1" : "+v"(seen) : "v"(fzi), "s"(RSRC(ws)) : "memory"); };
        auto need = [&](int n) {
            if (wid == 0) {
                asm volatile("" : "+v"(seen));
                if (__ballot(seen == 0u) & ((1ull << n) - 1ull)) flags_wait<0>(ws, lane < n ? fzi : fl + F_Z2 + (cg * 16 + pm) * 256 + pm * 16);
            }
        };
        auto issue = [&](int t, int sl) { const int k = kord2(t); issueB(k, sl); issueA(k, sl); };
        auto steps2 = [&](int t0, int t1) {
            int sl = (2 * t0 + 2) % 3 * G_STAGE, k = kord2(t0 + 3), ob = (k >> 1) * 589824 + (k & 1) * 18432;
            LOOP_ALIGN();
#pragma unroll 1
            for (int t = t0; t < t1; ++t) {
                WAIT_V(9); BARX();
                dma5<true>(lB + sl, rB, ob, vo);
                const int oa = pin_s(k * 128);
                const unsigned la = pin_s(lA + sl);
                k = pin_s(kord2(t + 4)); ob = pin_s((k >> 1) * 589824 + (k & 1) * 18432);
                sl = pin_s(sl == 0 ? 2 * G_STAGE : sl - G_STAGE);
                spA(oa, la);
            }
        };
        WAIT_V(13); BAR(); issue1(17, 2);
        WAIT_V(13); BARX();
        if (wid == 0) flag_raise(ws, fz);
        issueA(2 * pm + 1, 3); if (layer == 0) load5(4, 0);
        if (wid == 0) WAIT_IF(layer, 5, 9); else WAIT_IF(layer, 4, 8);
        BAR(); issueA(kord2(2), 0); if (layer == 0) load5(4, 1);
        BAR(); look(); issueA(kord2(3), 2);
        WAIT_V(4); need(3); BARX(); issueB(kord2(2), 0); issueB(kord2(3), 2); look();
        WAIT_IF(wid == 0, 6, 5); BAR(); issue(4, 1);
        WAIT_V(9); need(8);
        steps2(2, 11); look();
        WAIT_IF(wid == 0, 10, 9); BAR(); issue(14, 0);
        WAIT_V(9); need(16);
        steps2(12, 29);
        WAIT_V(9); BAR(); issue5(0); issue5(1); issue5(2);
        WAIT_V(15); BAR(); issue5(6); issue5(7);
        BAR(); issue5(3); put5(0, 9);
        {
            const unsigned fo = flag_out(fl + F_Y, 256, pm, cg, lane), fi = flag_in(fl + F_Y, 256, rh * 8 + c5, 16, lane);
            __builtin_amdgcn_s_setprio(3);
            WAIT_V(0); BARX();
            if (wid == 0) { flag_raise(ws, fo); flags_wait<Y_FIRST>(ws, fi); }
            __builtin_amdgcn_s_setprio(0);
            BARX();
        }
        BAR(); put5(9, 16);
        BAR(); put5(16, 23);
        BAR(); put5(23, 30);
        BAR(); put5(30, 36);
        BAR(); issue5(16); put5(36, 41);
        BAR(); issue5(17); put5(41, 45);
        WAIT_V(5); BAR();
        WAIT_V(0); BAR();
        if (layer == 0) { attn_issueB(lds, ws, hp, wid, lane, 0, 0); attn_issueB(lds, ws, hp, wid, lane, 1, 1); }
        BAR();
        if (layer == 0) {
            __builtin_amdgcn_s_setprio(3);
            const unsigned fo = flag_out(F_X2, 512, (rh >> 1) + wid, wid ? 16 + c5 : (rh & 1) * 8 + c5, wid == 0 || (wid == 1 && (rh & 1) && rh < 31) ? lane : 16);
            BARX();
            if (wid < 2) flag_raise(ws, fo);
        }
        TRACE_DUMP;
      }
        if (blockIdx.x == 0 && wid8 == 4) ST8(RSRC(ws), lane_id_fresh() ? 0x80000000u : (unsigned)OFF_EP, 0, ((u32x2){(unsigned)ep, (unsigned)(ep >> 32)}), 16);
        asm volatile("gm_code_end1:");
        return;
    }
#pragma unroll 1
  for (int layer = 0; layer < NET_LAYERS; ++layer) {
    NET_IDS
    TRACE_BEGIN;
    char* wl = ws + OFF_L0 + layer * SZ_L;
    const unsigned fl = layer * F_LAYER;
    const char* xin = wl + L_XB;
    char* y = wl + (layer ? L_Z2T : L_Y);
#undef STAMP_SEC
#define STAMP_SEC 0
    {
    STAMP_BEGIN;
    attn_consumer(lds, x0, wl, layer, pa, hp, wid, [&] { WAIT_V(0); prologue_done(lds, ws, ep, b, lane_id_fresh()); } STAMP_FWD);
    STAMP_END(1 + 5 * layer);
    }
#undef STAMP_SEC
#define STAMP_SEC 1
    STAMP_BEGIN;
    gmlp_body(lds, wid & 1, wid >> 1, pm, cg, wl + (layer ? L_Y : L_Z2T), y, bsp STAMP_FWD);
    {
        WAIT_V(0);
        BARX();
        STAMP(11);
        const int lane = lane_id_fresh(), r = lane & 15, q = lane >> 4;
        const int wk = wid >> 1, rt = rh * 4 + (wid & 1) * 2 + wk;
        const Res rx = proj_residual_load(xin, rt, c5, lane);
        const LAS char *pw[3], *ph[3];
#pragma unroll
        for (int i = 0; i < 3; ++i) {
            pw[i] = opaque(lds + 3 * i * K5_STAGE + wk * 4608 + lane * 16);
            ph[i] = opaque(lds + 3 * i * K5_STAGE + wk * 4608 + 4096 + q * 128 + (r & 7) * 16);
        }
        const auto rY = RSRC(y + (size_t)(rt & ~1) * 73728 + wk * 1024);
        const unsigned vy[2] = {pin((unsigned)(wk * 73728 + lane * 16)), pin((unsigned)((1 - wk) * 73728 + lane * 16))};
        int dk[4];
#pragma unroll
        for (int i = 0; i < 4; ++i) dk[i] = pin_s((kord5(4 + i) - 4 - i) * 4096);
        auto yoff = [&](int t) { return pin_s(t == 0 || t == 17 ? kord5(t) * 4096 : t * 4096 + dk[t & 3]); };
        bf16x8 af[5][4], wf[4][5];
        auto lda = [&](int so, int i) { return __builtin_bit_cast(bf16x8, __builtin_amdgcn_raw_buffer_load_b128(rY, vy[i & 1] + (i >> 1) * 2048, so, 16)); };
        auto rdw = [&](int t, int c, int n) {
            const int sl = (t + 6) & 7, o = (sl % 3) * K5_STAGE + c * 9216;
            wf[2 * (t & 1) + c][n] = n < 4 ? LDS_FRAG(pw[sl / 3] + o + n * 1024) : LDS_FRAG(ph[sl / 3] + o);
        };
#pragma unroll
        for (int n = 0; n < 10; ++n) rdw(0, n / 5, n % 5);
        int so[18];
#pragma unroll
        for (int i = 0; i < 5; ++i) so[i] = yoff(i);
        so[17] = yoff(17);
        f32x4 v[5], u[5];
        proj_residual(v, rx);
#pragma unroll
        for (int n = 0; n < 5; ++n) v[n] = pin(v[n]);
        LAS char* xo = (LAS char*)opaque(lds + wid * 4608 + lane * 16);
        LAS char* xo4 = (LAS char*)opaque(lds + wid * 4608 + 4096 + (lane & 31) * 16);
        const LAS char* xi = opaque(lds + (wid ^ 2) * 4608 + lane * 16);
        const LAS char* xi4 = opaque(lds + (wid ^ 2) * 4608 + 4096 + (lane & 31) * 16);
        const ProjOut po = layer ? proj_out_final(out, rt, c5, lane) : proj_out(wl + SZ_L + L_XA, wl + SZ_L + L_STA, rt, c5, lane);
        asm volatile("" :: "s"(rY));
        BARX();
        STAMP(12);
#pragma unroll
        for (int i = 0; i < K5_PRE; ++i) { af[i >> 2][i & 3] = lda(so[i >> 2], i & 3); SCHED0(); }
        static_for<18>([&](auto tc) {
            constexpr int t = tc.value;
            bf16x8 (&a)[4] = af[t % 5];
#pragma unroll
            for (int h = 0; h < 4; ++h) {
                const int c = t < 17 ? h >> 1 : h & 1, m = t < 17 ? h & 1 : 1 - (h >> 1);
                if (t < 17 ? m == 0 : h < 2) { if (t == 17 && h == 1) WAIT_L(0); else WAIT_L(5); }
#pragma unroll
                for (int n = 0; n < 5; ++n) {
                    f32x4& e = m ? u[n] : v[n];
                    if (t == 0 && h == 1) MFMA_0(wf[0][n], a[1], e); else MFMA_T(wf[2 * (t & 1) + c][n], a[2 * c + m], e);
                    SCHED0();
                    if (t < 17 && m == 0) rdw(t + 1, c, n);
                    if (t < 14 && m == 1 && !(n & 1)) { const int i = y_load_at(6 * t + 3 * c + n / 2); if (i >= 0) af[(i >> 2) % 5][i & 3] = lda(so[i >> 2], i & 3); }
                    if (t < 12 && h == 3 && n == 3) so[t + 5] = yoff(t + 5);
                    if (t == 17 && h * 5 + n >= 13 && h * 5 + n < 18) { const int i = h * 5 + n - 13; *(LAS f32x4*)(i < 4 ? xo + i * 1024 : xo4) = u[i]; }
                    SCHED0();
                }
            }
            if ((t & 1) && t < 17) BAR();
            SCHED0();
        });
        {
            WAIT_L0(); BARX();
#pragma unroll
            for (int n = 0; n < 5; ++n) u[n] = *(const LAS f32x4*)(n < 4 ? xi + n * 1024 : xi4);
            SCHED0();
            asm volatile("s_nop 1" : "+v"(v[0]), "+v"(v[1]), "+v"(v[2]), "+v"(v[3]), "+v"(v[4]));
#pragma unroll
            for (int n = 0; n < 5; ++n) v[n] += u[n];
        }
        STAMP(13);
        if (layer) proj_store<true>(v, po);
        else { proj_store(v, po); WAIT_V(0); BARX(); }
    }
    STAMP_END(3 + 5 * layer);
    TRACE_DUMP;
  }
  asm volatile("gm_code_end2:");
}

}

#define STOP_CHECK(n)

extern "C" void kernel_launch(void* const* d_in, const int* in_sizes, int n_in, void* d_out, int out_size, void* d_ws, size_t ws_size,
                              hipStream_t stream) {
    (void)in_sizes; (void)n_in; (void)out_size; (void)ws_size;
    const u16* x = (const u16*)d_in[0];
    const u16* wqkv = (const u16*)d_in[1];
    const u16* wo = (const u16*)d_in[2];
    const u16* w_in = (const u16*)d_in[4];
    const u16* w_sp = (const u16*)d_in[5];
    const u16* b_sp = (const u16*)d_in[6];
    const u16* w_out = (const u16*)d_in[7];
    char* ws = (char*)d_ws;
    (void)hipFuncSetAttribute((const void*)k_net, hipFuncAttributeMaxDynamicSharedMemorySize, NET_LDS);
    k_net<<<256, 512, NET_LDS, stream>>>(ws, w_sp, b_sp, (char*)d_out, (const char*)x, (const char*)wqkv, wo, w_in, w_out DBG_LAUNCH(0));
}
```

```cpp
#include <hip/hip_runtime.h>
#include <stdint.h>

namespace {

typedef unsigned short u16;
typedef short bf16x8 __attribute__((ext_vector_type(8)));
typedef float f32x4 __attribute__((ext_vector_type(4)));
typedef float f32x2 __attribute__((ext_vector_type(2)));
typedef unsigned u32x4 __attribute__((ext_vector_type(4)));
typedef unsigned u32x2 __attribute__((ext_vector_type(2)));
typedef __bf16 bf16x2_t __attribute__((ext_vector_type(2)));
#define LAS __attribute__((address_space(3)))
#define DEVI __device__ __forceinline__

constexpr int S_ = 2048, D_ = 576, FH = 2304;

constexpr size_t SZ_TWQKV = (size_t)16 * 18 * 6 * 1024;
constexpr size_t SZ_TWT = (size_t)48 * 18 * 256;
constexpr size_t SZ_TWO = (size_t)8 * 18 * 4608;
constexpr size_t SZ_TWIN = (size_t)16 * 18 * 18 * 1024;
constexpr size_t SZ_TWOUT = (size_t)8 * 72 * 4608;
constexpr size_t SZ_X = (size_t)S_ * D_ * 2;
constexpr size_t SZ_Z = (size_t)S_ * FH * 2;
constexpr size_t SZ_ST = (size_t)S_ * 64;
constexpr size_t SZ_ZST = (size_t)16 * 16 * 128 * 8;
constexpr size_t OFF_TWQKV = 0;
constexpr size_t OFF_TWT = OFF_TWQKV + SZ_TWQKV;
constexpr size_t OFF_TWO = OFF_TWT + SZ_TWT;
constexpr size_t OFF_TWIN = OFF_TWO + SZ_TWO;
constexpr size_t OFF_TWOUT = OFF_TWIN + SZ_TWIN;
constexpr size_t OFF_CNT = OFF_TWOUT + SZ_TWOUT;
constexpr size_t SZ_CNT = 10 * 65536;
constexpr size_t OFF_FW = OFF_CNT + SZ_CNT;
constexpr size_t OFF_FL = OFF_FW + 262144;
constexpr size_t OFF_EP = OFF_FL + 4096;
constexpr size_t OFF_FL2 = OFF_EP + 4096;
constexpr size_t OFF_ZST = OFF_FL2 + 8192;
constexpr size_t OFF_L0 = OFF_ZST + 2 * SZ_ZST;
constexpr size_t L_XA = 0, L_STA = L_XA + SZ_X, L_O = L_STA + SZ_ST, L_XB = L_O + SZ_X, L_STB = L_XB + SZ_X, L_Z2T = L_STB + SZ_ST, L_Y = L_Z2T + SZ_Z;
constexpr size_t SZ_L = 0;
static_assert(OFF_L0 + L_Y + SZ_Z <= ((size_t)256 << 20), "workspace");
constexpr unsigned F_O = OFF_CNT, F_X1 = F_O + 65536, F_Z2 = F_O + 131072, F_Y = F_O + 196608, F_LAYER = 262144, F_X2 = F_O + 2 * F_LAYER;

DEVI unsigned pack2(float a, float b) { f32x2 v = {a, b}; bf16x2_t r = __builtin_convertvector(v, bf16x2_t); return __builtin_bit_cast(unsigned, r); }
DEVI float bflo(unsigned u) { return __uint_as_float(u << 16); }
DEVI float bfhi(unsigned u) { return __uint_as_float(u & 0xffff0000u); }
DEVI int slot2row(int sg) { return (sg & ~31) + 2 * (sg & 15) + ((sg >> 4) & 1); }
DEVI float fmax2(float a, float b) { return __builtin_amdgcn_fmed3f(a, b, __builtin_inff()); }
#define XQ4(OP, MID) \
    asm volatile("s_nop 1\n\tv_permlane32_swap_b32 %0, %1\n\tv_permlane32_swap_b32 %2, %3\n\t" OP " %0, %0, %1\n\t" OP " %2, %2, %3\n\t" \
                 "s_nop 1\n\tv_permlane16_swap_b32 %0, %2\n\t" OP " %0, %0, %2\n\t" MID \
                 "v_mov_b32 %2, %0\n\ts_nop 1\n\tv_permlane16_swap_b32 %0, %2\n\tv_mov_b32 %1, %0\n\tv_mov_b32 %3, %2\n\ts_nop 0\n\t" \
                 "v_permlane32_swap_b32 %0, %1\n\tv_permlane32_swap_b32 %2, %3" : "+v"(x[0]), "+v"(x[1]), "+v"(x[2]), "+v"(x[3]))
DEVI void xq4_max(float (&x)[4]) { XQ4("v_max_f32", ""); }
DEVI void xq4_sum(float (&x)[4]) { XQ4("v_add_f32", ""); }
DEVI void xq4_sum_rcp(float (&x)[4]) { XQ4("v_add_f32", "v_rcp_f32 %0, %0\n\ts_nop 0\n\t"); }
template <class T> DEVI T pin(T v) { asm volatile("" : "+v"(v)); return v; }
template <int V> struct IC { static constexpr int value = V; };
template <int N, int I = 0, class F> DEVI void static_for(F&& f) { if constexpr (I < N) { f(IC<I>{}); static_for<N, I + 1>(f); } }
template <class T> DEVI T* own_s(T* p) { T* r; asm volatile("s_mov_b64 %0, %1" : "=&s"(r) : "s"(p)); return r; }
DEVI int pin_s(int v) { asm volatile("" : "+s"(v)); return v; }
DEVI float xq_sum(float v) {
    auto a = __builtin_amdgcn_permlane32_swap(__float_as_uint(v), __float_as_uint(v), false, false);
    v = __uint_as_float(a[0]) + __uint_as_float(a[1]);
    auto b = __builtin_amdgcn_permlane16_swap(__float_as_uint(v), __float_as_uint(v), false, false);
    return __uint_as_float(b[0]) + __uint_as_float(b[1]);
}
#define RSRC(p) __builtin_amdgcn_make_buffer_rsrc((void*)(p), 0, 0x7fffffff, 0x00020000)
#define ST16(rs, vo, so, v, aux) __builtin_amdgcn_raw_buffer_store_b128((v), (rs), (vo) + (so), 0, aux)
#define ST8(rs, vo, so, v, aux) __builtin_amdgcn_raw_buffer_store_b64((v), (rs), (vo), (so), aux)
#define WAIT_V(n) asm volatile("s_waitcnt vmcnt(%0)" ::"i"(n) : "memory")
#define WAIT_L0() asm volatile("s_waitcnt lgkmcnt(0)" ::: "memory")
#define BAR() do { asm volatile("" ::: "memory"); __builtin_amdgcn_s_barrier(); asm volatile("" ::: "memory"); } while (0)
#define BARX() do { __builtin_amdgcn_sched_barrier(0); BAR(); __builtin_amdgcn_sched_barrier(0); } while (0)
#define LOOP_ALIGN() asm volatile(".p2align 6")
#define SCHED0() __builtin_amdgcn_sched_barrier(0)
#define MFMA(a, b, c) __builtin_amdgcn_mfma_f32_16x16x32_bf16((a), (b), (c), 0, 0, 0)
#define MFMA_T(a, b, c) asm volatile("v_mfma_f32_16x16x32_bf16 %0, %1, %2, %0" : "+v"(c) : "v"(a), "v"(b))
#define MFMA_0(a, b, c) asm volatile("v_mfma_f32_16x16x32_bf16 %0, %1, %2, 0" : "=&v"(c) : "v"(a), "v"(b))
#define WAIT_L(n) __builtin_amdgcn_s_waitcnt(0xC07F | ((n) << 8))
#define LDS_FRAG(p) (*(const LAS bf16x8*)(p))
DEVI int lane_id_fresh() { int z = 0; asm volatile("" : "+v"(z)); return __builtin_amdgcn_mbcnt_hi(~0u, __builtin_amdgcn_mbcnt_lo(~0u, z)); }
DEVI const LAS char* opaque(const LAS char* p) { asm volatile("" : "+v"(p)); return p; }
#define DBG_ARG
#define DBG_LAUNCH(n)
#define STAMP(i)
#define STAMP_DECL
#define STAMP_BEGIN
#define STAMP_END(n)
#define STAMP_PARAM
#define STAMP_FWD

#define PST_PARAM
#define PST_FWD
#define PST_DECL
#define PST(i)
#define PST_DUMP(c, k)
#define TRACE_ATTR
#define TRACE_BEGIN
#define TRACE_DUMP

DEVI float add1(float a, float b) { float r; asm("v_add_f32 %0, %1, %2" : "=v"(r) : "v"(a), "v"(b)); return r; }
DEVI float rstd_of(float s, float q, float inv_n, float& mean) {
    float var;
    asm("v_mul_f32 %0, %2, %3\n\tv_mul_f32 %1, %2, %4\n\tv_fma_f32 %1, -%0, %0, %1" : "=&v"(mean), "=&v"(var) : "s"(inv_n), "v"(s), "v"(q));
    return __builtin_amdgcn_rsqf(fmax2(var, 0.f) + 1e-5f);
}
DEVI float ln_rstd(const f32x4 p) {
    float s, q, mean;
    asm("v_add_f32 %0, %2, %4\n\tv_add_f32 %1, %3, %5\n\ts_nop 0\n\t"
        "v_add_f32_dpp %0, %0, %0 quad_perm:[1,0,3,2] row_mask:0xf bank_mask:0xf\n\tv_add_f32_dpp %1, %1, %1 quad_perm:[1,0,3,2] row_mask:0xf bank_mask:0xf\n\ts_nop 0\n\t"
        "v_add_f32_dpp %0, %0, %0 quad_perm:[2,3,0,1] row_mask:0xf bank_mask:0xf\n\tv_add_f32_dpp %1, %1, %1 quad_perm:[2,3,0,1] row_mask:0xf bank_mask:0xf"
        : "=&v"(s), "=&v"(q) : "v"(p[0]), "v"(p[1]), "v"(p[2]), "v"(p[3]));
    return rstd_of(s, q, 1.0f / 576.0f, mean);
}

struct Res { u32x4 a, c; u32x2 e; };
DEVI Res proj_residual_load(const char* xin, int rt, int cg, int lane) {
    const auto rs = RSRC(xin + (size_t)rt * 18432 + cg * 2304);
    Res x;
    x.a = __builtin_amdgcn_raw_buffer_load_b128(rs, lane * 16, 0, 16); x.c = __builtin_amdgcn_raw_buffer_load_b128(rs, lane * 16 + 1024, 0, 16);
    x.e = __builtin_amdgcn_raw_buffer_load_b64(rs, 2048 + (lane & 15) * 16 + ((lane >> 4) & 1) * 8, 0, 16);
    return x;
}
DEVI void proj_residual(f32x4 (&v)[5], const Res& x) {
    v[0] = (f32x4){bflo(x.a.x), bfhi(x.a.x), bflo(x.a.y), bfhi(x.a.y)}; v[1] = (f32x4){bflo(x.a.z), bfhi(x.a.z), bflo(x.a.w), bfhi(x.a.w)};
    v[2] = (f32x4){bflo(x.c.x), bfhi(x.c.x), bflo(x.c.y), bfhi(x.c.y)}; v[3] = (f32x4){bflo(x.c.z), bfhi(x.c.z), bflo(x.c.w), bfhi(x.c.w)};
    v[4] = (f32x4){bflo(x.e.x), bfhi(x.e.x), bflo(x.e.y), bfhi(x.e.y)};
}
struct ProjOut { __amdgpu_buffer_rsrc_t x, st; unsigned v0, v1, v2, vs; };
DEVI ProjOut proj_out(char* xout, char* stout, int rt, int cg, int lane) {
    const int r = lane & 15, q = lane >> 4;
    ProjOut p = {RSRC(xout + (size_t)rt * 18432 + cg * 2304), RSRC(stout + (size_t)rt * 1024 + cg * 8),
                 pin((unsigned)lane * 16u), pin((unsigned)lane * 16u + 1024u), pin((unsigned)(2048 + r * 16 + (q & 1) * 8)), pin((unsigned)(r * 64 + (q >> 1) * 4))};
    asm volatile("" :: "s"(p.x), "s"(p.st));
    return p;
}
DEVI ProjOut proj_out_final(char* out, int rt, int cg, int lane) {
    const int q = lane >> 4;
    const unsigned o = (unsigned)(slot2row(rt * 16 + (lane & 15)) * 1152 + cg * 144);
    ProjOut p = {RSRC(out), RSRC(out), pin(o + q * 16), pin(o + 64 + q * 16), pin(o + 128 + (q & 1) * 8), 0u};
    return p;
}
template <bool FINAL = false>
DEVI void proj_store(f32x4 (&v)[5], const ProjOut& p) {
    ST16(p.x, p.v0, 0, ((u32x4){pack2(v[0][0], v[0][1]), pack2(v[0][2], v[0][3]), pack2(v[1][0], v[1][1]), pack2(v[1][2], v[1][3])}), FINAL ? 0 : 16);
    SCHED0();
    asm volatile("" : "+v"(v[2]), "+v"(v[3]), "+v"(v[4]));
    ST16(p.x, p.v1, 0, ((u32x4){pack2(v[2][0], v[2][1]), pack2(v[2][2], v[2][3]), pack2(v[3][0], v[3][1]), pack2(v[3][2], v[3][3])}), FINAL ? 0 : 16);
    SCHED0();
    ST8(p.x, p.v2, 0, ((u32x2){pack2(v[4][0], v[4][1]), pack2(v[4][2], v[4][3])}), FINAL ? 0 : 16);
    SCHED0();
    if (!FINAL) {
        const f32x4 s4 = ((v[0] + v[1]) + (v[2] + v[3])) + v[4] * 0.5f;
        const f32x4 q4 = ((v[0] * v[0] + v[1] * v[1]) + (v[2] * v[2] + v[3] * v[3])) + v[4] * v[4] * 0.5f;
        auto x = __builtin_amdgcn_permlane32_swap(__float_as_uint((s4[0] + s4[2]) + (s4[1] + s4[3])), __float_as_uint((q4[0] + q4[2]) + (q4[1] + q4[3])), false, false);
        const float t = __uint_as_float(x[0]) + __uint_as_float(x[1]);
        auto y = __builtin_amdgcn_permlane16_swap(__float_as_uint(t), __float_as_uint(t), false, false);
        __builtin_amdgcn_raw_buffer_store_b32(__float_as_uint(__uint_as_float(y[0]) + __uint_as_float(y[1])), p.st, p.vs, 0, 16);
        SCHED0();
    }
}

DEVI void side_load(f32x4 (&d)[3], const char* p, const char* p2) {
    asm volatile("global_load_dwordx4 %0, %3, off sc1\n\tglobal_load_dwordx4 %1, %3, off offset:1024 sc1\n\tglobal_load_dwordx4 %2, %4, off sc1"
                 : "=&v"(d[0]), "=&v"(d[1]), "=&v"(d[2]) : "v"(p), "v"(p2) : "memory");
}
DEVI void side_fence(f32x4 (&d)[3]) { asm volatile("" : "+v"(d[0]), "+v"(d[1]), "+v"(d[2])); }
DEVI void lds_word_set(LAS char* p, unsigned v) { asm volatile("ds_write_b32 %0, %1" :: "v"((unsigned)(size_t)p), "v"(v) : "memory"); }
#define DMA_I(v, off, sc) "buffer_load_dwordx4 " v ", %1, %2 offen offset:" off sc " lds\n\t"
#define DMA5(sc) asm volatile("s_mov_b32 m0, %0\n\ts_mov_b32 exec_hi, 0\n\ts_nop 2\n\t" DMA_I("%3", "0", sc) "s_mov_b32 exec_hi, -1\n\t" \
    DMA_I("%3", "512", sc) DMA_I("%3", "1536", sc) DMA_I("%3", "2560", sc) DMA_I("%3", "3584", sc) :: "s"(la), "s"(rs), "s"(so), "v"(vo) : "memory")
#define DMA4(sc) asm volatile("s_mov_b32 m0, %0\n\ts_nop 3\n\t" DMA_I("%3", "0", sc) DMA_I("%4", "1024", sc) DMA_I("%5", "2048", sc) DMA_I("%6", "3072", sc) \
    :: "s"(la), "s"(rs), "s"(so), "v"(v0), "v"(v1), "v"(v2), "v"(v3) : "memory")
template <bool SC1> DEVI void dma5(unsigned la, __amdgpu_buffer_rsrc_t rs, int so, unsigned vo) { if (SC1) DMA5(" sc1"); else DMA5(""); }
template <bool SC1> DEVI void dma4(unsigned la, __amdgpu_buffer_rsrc_t rs, int so, unsigned v0, unsigned v1, unsigned v2, unsigned v3) { if (SC1) DMA4(" sc1"); else DMA4(""); }
#define DMA2(sc) asm volatile("s_mov_b32 m0, %0\n\ts_nop 3\n\t" DMA_I("%3", "0", sc) DMA_I("%4", "1024", sc) :: "s"(la), "s"(rs), "s"(so), "v"(v0), "v"(v1) : "memory")
template <bool SC1> DEVI void dma2(unsigned la, __amdgpu_buffer_rsrc_t rs, int so, unsigned v0, unsigned v1) { if (SC1) DMA2(" sc1"); else DMA2(""); }
DEVI void dma1(unsigned la, __amdgpu_buffer_rsrc_t rs, int so, unsigned v0) { asm volatile("s_mov_b32 m0, %0\n\ts_nop 3\n\t" DMA_I("%3", "0", "") :: "s"(la), "s"(rs), "s"(so), "v"(v0) : "memory"); }
#define WAIT_IF(c, a, b) do { if (c) WAIT_V(a); else WAIT_V(b); } while (0)
template <int T0, int T1, int P, int S0 = 0, class ISSUE>
DEVI void ring_steps(ISSUE&& issue) {
    int slot = (T0 + S0) % 3;
    LOOP_ALIGN();
#pragma unroll 1
    for (int t = T0; t < T1; ++t) {
        WAIT_V(P); BAR();
        issue(t + 3, slot);
        slot = slot == 2 ? 0 : slot + 1;
    }
}

DEVI unsigned flag_out(unsigned use, int pitch, int group, int writer, int lane) { return pin(lane < 16 ? use + (group * 16 + lane) * pitch + writer * 16 : 0x80000000u); }
DEVI unsigned flag_in(unsigned use, int pitch, int reader, int n, int lane) { return pin(use + reader * pitch + (lane < n ? lane : 0) * 16); }
DEVI void flag_raise(const char* ws, unsigned vo) { ST16(RSRC(ws), vo, 0, ((u32x4){1u, 1u, 1u, 1u}), 16); SCHED0(); }
template <int FIRST>
DEVI void flags_wait(const char* ws, unsigned vo) {
    unsigned v, n = 400000u;
    asm volatile("s_nop 4\n\ts_sleep %4\n"
                 "1:\n\tbuffer_load_dword %0, %2, %3, 0 offen sc1\n\ts_waitcnt vmcnt(0)\n\tv_cmp_eq_u32 vcc, 0, %0\n\ts_cbranch_vccz 2f\n\t"
                 "s_sleep 1\n\ts_sub_u32 %1, %1, 1\n\ts_cbranch_scc0 1b\n2:"
                 : "=&v"(v), "+s"(n) : "v"(vo), "s"(RSRC(ws)), "i"(FIRST) : "vcc", "scc", "memory");
    SCHED0();
}
template <int FIRST>
DEVI void epoch_wait(const char* ws, unsigned vo, unsigned long long ep) {
    unsigned long long v; unsigned n = 400000u;
    asm volatile("s_nop 4\n\ts_sleep %5\n"
                 "1:\n\tbuffer_load_dwordx2 %0, %2, %3, 0 offen sc1\n\ts_waitcnt vmcnt(0)\n\tv_cmp_ne_u64 vcc, %4, %0\n\ts_cbranch_vccz 2f\n\t"
                 "s_sleep 1\n\ts_sub_u32 %1, %1, 1\n\ts_cbranch_scc0 1b\n2:"
                 : "=&v"(v), "+s"(n) : "v"(vo), "s"(RSRC(ws)), "s"(ep), "i"(FIRST) : "vcc", "scc", "memory");
    SCHED0();
}
DEVI unsigned lds_arrive(LAS char* p) {
    unsigned v;
    asm volatile("s_mov_b64 exec, 1\n\tds_add_rtn_u32 %0, %1, %2\n\ts_mov_b64 exec, -1\n\ts_waitcnt lgkmcnt(0)" : "=&v"(v) : "v"((unsigned)(size_t)p), "v"(1u) : "memory");
    return __builtin_amdgcn_readfirstlane(v);
}
DEVI void lds_count_wait(LAS char* p, unsigned n) {
#pragma unroll 1
    for (unsigned spin = 0; spin < 40000000u; ++spin) {
        unsigned v;
        asm volatile("ds_read_b32 %0, %1\n\ts_waitcnt lgkmcnt(0)" : "=v"(v) : "v"((unsigned)(size_t)p) : "memory");
        if (__builtin_amdgcn_readfirstlane(v) >= n) break;
    }
}

constexpr int K1_STAGE = 32768;
constexpr int WO_OFF = 69632;
constexpr int G_OWN = 139264;
constexpr int G_PART = G_OWN + 18432;
constexpr int G_TAB = G_PART + 2048;
constexpr int K1_SCR = G_TAB + 1536;
constexpr int G_WORD = K1_SCR + 576;
constexpr int NET_LDS = G_WORD + 64;

#define WO_PRE 9
#define WO_W 33
#define REL_PRIO 0
#define W_FIRST 2
#define W_ALONE 12
#define I_SLEEP 12
#define OJOB_SLEEP 20
#define PACE0 1
#define PACE __builtin_amdgcn_s_sleep(1)
#define POLLW 0
#define WO_EV 4
#define O_FIRST 10
#define X1_FIRST 6
#define X2_FIRST 6
#define Y_FIRST 10
#define Z2_FIRST 10
DEVI unsigned attn_vb(int wid, int lane) {
    const int c = lane & 15;
    return pin((unsigned)(wid < 3 ? lane * 16 : (c < 8 ? (c >> 1) & 1 : 2) * 4608 + (lane >> 4) * 64 + (c < 8 ? (c >> 2) * 2 + (c & 1) : c & 3) * 16));
}
DEVI void attn_issueB(LAS char* lds, const char* ws, int hp, int wid, unsigned vb, int s, int slot) {
    const unsigned la = (unsigned)(size_t)lds + slot * K1_STAGE + 18432 + wid * 4096;
    if (wid < 3) dma4<true>(la, RSRC(ws), (int)OFF_TWQKV + hp * (18 * 6144) + wid * 4096 + s * 12288, vb, vb, vb, vb);
    else asm volatile("s_mov_b32 m0, %0\n\ts_nop 3\n\tbuffer_load_dwordx4 %4, %1, %2 offen sc1 lds\n\tbuffer_load_dwordx4 %4, %1, %3 offen offset:1024 sc1 lds"
                      :: "s"(la), "s"(RSRC(ws)), "s"((int)OFF_TWT + hp * 13824 + s * 512), "s"((int)OFF_TWT + hp * 13824 + s * 512 + 256 - 1024), "v"(vb) : "memory");
}
struct AttnX { __amdgpu_buffer_rsrc_t rs; unsigned v[4], vh[2]; int oA, oh, step; };
DEVI AttnX attn_x(const char* x0, const char* xa, int layer, int pm, int wid, int lane) {
    const int l3 = lane >> 3, pmh = pm > 0 ? pm : 1;
    const unsigned v = layer ? lane * 16 : 2 * l3 * 1152 + ((lane & 7) ^ l3) * 16 + 4096, d1 = layer ? 0 : 16 * 1152 - 1024, d2 = layer ? 16384 : 1152 - 2048;
    const unsigned vh = layer ? (l3 & 1) * 18432 + (lane >> 4) * 256 + (8 + (lane & 7)) * 16 : v + 16 * 1152;
    return {RSRC(layer ? xa : x0 - 4096), {pin(v), pin(v + d1), pin(v + d2), pin(v + d1 + d2)}, {pin(vh), pin(vh + (layer ? 0 : 1152 - 1024))},
            (pm * 8 + 2 * wid) * 18432, (pmh * 8 - 2) * 18432, layer ? 2048 : 128};
}
DEVI void attn_issueA(LAS char* lds, const AttnX& a, int wid, int s, int slot) {
    const unsigned la = (unsigned)(size_t)lds + slot * K1_STAGE;
    if (wid == 3) dma2<true>(la, a.rs, a.oh + s * a.step, a.vh[0], a.vh[1]);
    dma4<true>(la + 2048 + wid * 4096, a.rs, a.oA + s * a.step, a.v[0], a.v[1], a.v[2], a.v[3]);
}
template <class DONE, class NEXT>
DEVI void attn_producer(LAS char* lds, const char* ws, const char* x0, const char* wl, unsigned fl, unsigned long long ep, int layer, int pm, int hp, int wid, DONE&& done, NEXT&& next) {
    const int lane = lane_id_fresh();
    const int pmh = pm > 0 ? pm : 1;
    const unsigned vo = lane * 16;
    const AttnX ax = attn_x(x0, wl + L_XA, layer, pm, wid, lane);
    const unsigned vb = attn_vb(wid, lane), fd = pin((unsigned)OFF_FL + wid * 1024 + vo);
    auto issueB = [&](int s, int slot) { attn_issueB(lds, ws, hp, wid, vb, s, slot); };
    auto issue = [&](int s, int slot) { attn_issueA(lds, ax, wid, s, slot); issueB(s, slot); };
    const int hr = lane >> 2;
    const char* ps = pin(wl + L_STA + (size_t)(pm * 128 + wid * 32) * 64 + lane * 16);
    const char* psh = pin(wl + L_STA + (size_t)((pmh * 8 - 2 + (hr >> 3)) * 16 + 8 + (hr & 7)) * 64 + (lane & 3) * 16);
    if (layer == 0) {
        const unsigned fi = pin((unsigned)OFF_FW + (pm * 16 + hp) * 1024 + (lane < 6 ? lane : 0) * 128);
        WAIT_V(0); done();
        if (wid == 0) epoch_wait<W_FIRST>(ws, fi, ep);
        BARX();
        issueB(0, 0); issueB(1, 1); issue(2, 2);
        WAIT_IF(wid == 3, 10, 12);
    } else {
        issueB(2, 2);
        const unsigned fi = flag_in(F_X2, 512, pm * 16 + hp, pm > 0 ? 24 : 16, lane);
        if (wid == 0) flags_wait<X2_FIRST>(ws, fi);
        BARX();
#pragma unroll
        for (int i = 0; i < 3; ++i) attn_issueA(lds, ax, wid, i, i);
        WAIT_IF(wid == 3, 12, 8);
    }
    __builtin_amdgcn_s_setprio(0);
    BAR();
    if (layer) WAIT_IF(wid == 3, 6, 4);
    ring_steps<0, 4, 8>(issue);
    {
        unsigned long long done;
        if (layer == 0) asm volatile("s_nop 4\n\tbuffer_load_dwordx2 %0, %1, %2, 0 offen sc1" : "+v"(done) : "v"(fd), "s"(RSRC(ws)) : "memory");
        WAIT_IF(layer, 8, 9); BAR(); issue(7, 1);
        WAIT_V(8);
        if (layer == 0) { asm volatile("" : "+v"(done)); if (__any(done != ep)) epoch_wait<0>(ws, fd, ep); }
        BAR(); issue(8, 2);
    }
    const auto rW = RSRC(ws + OFF_TWO + (size_t)(hp >> 1) * 82944 + wid * 4096);
    auto issueW = [&](int i) { dma4<true>((unsigned)(size_t)lds + WO_OFF + (i * 4 + wid) * 4096, rW, i * 16384, vo, vo, vo, vo); };
    {
        f32x4 sp[3];
        side_load(sp, ps, psh);
        WAIT_V(11); BAR(); issueW(2); issueW(3);
        WAIT_V(8); BARX();
        side_fence(sp);
#pragma unroll
        for (int i = 0; i < 3; ++i) lds_word_set(lds + K1_SCR + (i < 2 ? 16 + wid * 32 + i * 16 + hr : hr) * 4, __float_as_uint(ln_rstd(sp[i])));
        WAIT_L0();
    }
    BAR();
    next(0);
    issueW(0); issueW(1); issueW(4);
    if (wid == 0) dma2<true>((unsigned)(size_t)lds + WO_OFF + 80896, rW, 80896, vo, vo);
    next(1);
    const unsigned fo = flag_out(fl + F_O, 256, pm, hp, lane), fi = flag_in(fl + F_O, 256, pm * 16 + hp, 16, lane);
    WAIT_V(0);
    if (wid == POLLW) __builtin_amdgcn_s_setprio(3);
    BARX();
    if (wid == POLLW) { flag_raise(ws, fo); flags_wait<O_FIRST>(ws, fi); }
    __builtin_amdgcn_s_setprio(0);
    BARX();
}

#define STAMP_SEC 0
template <class DONE>
DEVI void attn_consumer(LAS char* lds, const char* x0, char* wl, int layer, int pm, int hp, int wid, DONE&& done STAMP_PARAM) {
    const int lane = lane_id_fresh(), r = lane & 15, q = lane >> 4;
    const char* xa = wl + L_XA;
    char* o = wl + L_O;
    const int T = wid & 1, h = wid >> 1;
    const LAS char *ph[2], *pa[2];
#pragma unroll
    for (int kk = 0; kk < 2; ++kk) {
        const int sw = ((4 * kk + q) ^ (r & 7)) * 16;
        ph[kk] = opaque(lds + (layer ? kk * 1024 + q * 256 + (r ^ (T ? 0 : 8)) * 16 : (r ^ (T ? 0 : 8)) * 128 + sw));
        pa[kk] = opaque(lds + T * 2048 + (layer ? kk * 1024 + lane * 16 : r * 128 + sw));
    }
    const LAS char* pb = lds + h * 3072 + lane * 16;
    const LAS char* pt = lds + lane * 16;
    f32x4 acc[5][4];
#pragma unroll
    for (int m = 0; m < 5; ++m)
#pragma unroll
        for (int n = 0; n < 4; ++n) if (m + n) acc[m][n] = pin((f32x4){0.f, 0.f, 0.f, 0.f});
    bf16x8 fa[2][5], fb[2][4];
    auto rd1 = [&](int kk, int i) {
        if (i < 3) fb[kk][i] = LDS_FRAG(pb + 18432 + kk * 6144 + i * 1024);
        else if (i == 3) fb[kk][3] = LDS_FRAG(pt + 30720 + kk * 1024);
        else if (i == 4) fa[kk][0] = LDS_FRAG(ph[kk]);
        else if (i < 9) fa[kk][i - 4] = LDS_FRAG(pa[kk] + 2048 + (i - 5) * 4096);
    };
    f32x4 mb, mb0;
#pragma unroll
    for (int j = 0; j < 4; ++j) {
        const bool ok = ((r - 4 * q - j) & 15) < 8;
        mb[j] = ok ? 0.f : -__builtin_inff();
        mb0[j] = (ok && (pm > 0 || 4 * q + j <= r)) ? 0.f : -__builtin_inff();
    }
    mb = pin(mb); mb0 = pin(mb0);
    const unsigned mp0 = pin((4 * q > r ? 0xffffu : 0u) | (4 * q + 1 > r ? 0xffff0000u : 0u)), mp1 = pin((4 * q + 2 > r ? 0xffffu : 0u) | (4 * q + 3 > r ? 0xffff0000u : 0u));
    const unsigned qtm = pin(q == h ? 0xffffffffu : 0u);
    const unsigned vom = pin((unsigned)(r * 16 + hp * 1024 + (2 * h + (q >> 1)) * 256 + (q & 1) * 8)), vot = pin((unsigned)(r * 16 + 16384 + (hp >> 1) * 256 + (hp & 1) * 8 + h * 4));
    const LAS char* tk0 = opaque(lds + K1_SCR + ((q * 4) ^ (T ? 0 : 8)) * 4);
    const LAS char* tk = opaque(lds + K1_SCR + ((1 + T) * 16 + q * 4) * 4);
    const LAS char* tq = opaque(lds + K1_SCR + ((1 + T) * 16 + r) * 4);
    pb = opaque(pb); pt = opaque(pt);
    STAMP(2);
    const int rt = pm * 8 + (hp & 1) * 4 + wid, cg = hp >> 1;
    const ProjOut pr = layer ? proj_out((char*)xa, (char*)xa, rt, cg, lane) : proj_out_final((char*)x0, rt, cg, lane);
    BAR();
    if (layer == 0) done();
    BARX();
    STAMP(3);
#pragma unroll
    for (int i = 0; i < 9; ++i) rd1(0, i);
    SCHED0();
    const Res rx = {__builtin_amdgcn_raw_buffer_load_b128(pr.x, pr.v0, 0, 16), __builtin_amdgcn_raw_buffer_load_b128(pr.x, pr.v1, 0, 16), __builtin_amdgcn_raw_buffer_load_b64(pr.x, pr.v2, 0, 16)};
    SCHED0();
    int slot = 0, left = 9;
    LOOP_ALIGN();
#pragma unroll 1
    do {
        int d;
#pragma unroll
        for (int kk = 0; kk < 2; ++kk) {
            WAIT_L(0);
            if (kk) BAR();
            SCHED0();
#pragma unroll
            for (int i = 1; i < 20; ++i) {
                const int m = i >> 2, n = i & 3;
                const bf16x8 &A = fa[kk][m], &B = fb[kk][n];
                acc[m][n] = n != 2 ? MFMA(B, A, acc[m][n]) : MFMA(A, B, acc[m][n]);
                SCHED0();
                if (i <= 9) rd1(kk ^ 1, i - 1);
                if (kk == 0) {
                    if (i == 10) d = pin_s(slot == 2 ? -2 * K1_STAGE : K1_STAGE);
                    if (i == 11 || i == 12) ph[i & 1] = opaque(ph[i & 1] + d);
                    if (i == 13 || i == 14) pa[i & 1] = opaque(pa[i & 1] + d);
                    if (i == 15) pb = opaque(pb + d);
                    if (i == 16) pt = opaque(pt + d);
                } else {
                    if (i == 11) slot = pin_s(slot == 2 ? 0 : slot + 1);
                    if (i == 13) left = pin_s(left - 1);
                }
                SCHED0();
            }
        }
    } while (left);
    STAMP(4);
    u32x2 ts[5], tf[5];
    {
        LAS char* xb = lds + wid * 320;
#pragma unroll
        for (int m = 0; m < 5; ++m) ts[m] = (u32x2){pack2(acc[m][3][0], acc[m][3][1]), pack2(acc[m][3][2], acc[m][3][3])};
        if (q == 2) {
#pragma unroll
            for (int m = 0; m < 5; ++m) {
                const unsigned w = h ? ts[m].y : ts[m].x;
                *(LAS u16*)(xb + m * 64 + r * 2) = (u16)w; *(LAS u16*)(xb + m * 64 + 32 + r * 2) = (u16)(w >> 16);
            }
        }
        asm volatile("" ::: "memory");
#pragma unroll
        for (int m = 0; m < 5; ++m) tf[m] = *(const LAS u32x2*)(xb + m * 64 + (r & 1) * 32 + q * 8);
    }
    {
        f32x4 rk[5]; float rq[4];
        rk[0] = *(const LAS f32x4*)(tk0);
#pragma unroll
        for (int g = 0; g < 4; ++g) { rk[g + 1] = *(const LAS f32x4*)(tk + g * 128); rq[g] = *(const LAS float*)(tq + g * 128); }
        const f32x4 zero = {0.f, 0.f, 0.f, 0.f};
        u32x4 kf[5];
#pragma unroll
        for (int m = 0; m < 5; ++m) kf[m] = (u32x4){pack2(acc[m][1][0], acc[m][1][1]), pack2(acc[m][1][2], acc[m][1][3]), ts[m].y, 0u};
        f32x4 sp[4], sc[4];
#pragma unroll
        for (int g = 0; g < 4; ++g) {
            const u32x4 qf = {pack2(acc[g + 1][0][0], acc[g + 1][0][1]), pack2(acc[g + 1][0][2], acc[g + 1][0][3]), ts[g + 1].x & qtm, 0u};
            sp[g] = MFMA(__builtin_bit_cast(bf16x8, kf[g]), __builtin_bit_cast(bf16x8, qf), zero);
            sc[g] = MFMA(__builtin_bit_cast(bf16x8, kf[g + 1]), __builtin_bit_cast(bf16x8, qf), zero);
        }
        u32x2 vf[5];
#pragma unroll
        for (int m = 0; m < 5; ++m) vf[m] = (u32x2){pack2(acc[m][2][0], acc[m][2][1]), pack2(acc[m][2][2], acc[m][2][3])};
        f32x4 rs[4], t[4]; float mx[4], sm[4];
#pragma unroll
        for (int g = 0; g < 4; ++g) {
#pragma unroll
            for (int j = 0; j < 4; ++j) { const bool pv = 4 * q + j > r; rs[g][j] = pv ? rk[g][j] : rk[g + 1][j]; t[g][j] = pv ? sp[g][j] : sc[g][j]; }
            t[g] = (t[g] * rs[g]) * (rq[g] * (0.23570226039551584f * 1.4426950408889634f)) + (g == 0 ? mb0 : mb);
            mx[g] = fmaxf(fmaxf(t[g][0], t[g][1]), fmaxf(t[g][2], t[g][3]));
        }
        xq4_max(mx);
#pragma unroll
        for (int g = 0; g < 4; ++g) {
            t[g] = t[g] - mx[g];
#pragma unroll
            for (int j = 0; j < 4; ++j) t[g][j] = __builtin_amdgcn_exp2f(t[g][j]);
            sm[g] = (t[g][0] + t[g][2]) + (t[g][1] + t[g][3]);
        }
        f32x4 om[4], ot[4];
#pragma unroll
        for (int g = 0; g < 4; ++g) {
            const f32x4 p = t[g] * rs[g];
            const unsigned p01 = pack2(p[0], p[1]), p23 = pack2(p[2], p[3]);
            const u32x4 pf = {p01 & mp0, p23 & mp1, p01 & ~mp0, p23 & ~mp1};
            om[g] = MFMA(__builtin_bit_cast(bf16x8, ((u32x4){vf[g].x, vf[g].y, vf[g + 1].x, vf[g + 1].y})), __builtin_bit_cast(bf16x8, pf), zero);
            ot[g] = MFMA(__builtin_bit_cast(bf16x8, ((u32x4){tf[g].x, tf[g].y, tf[g + 1].x, tf[g + 1].y})), __builtin_bit_cast(bf16x8, pf), zero);
        }
        xq4_sum_rcp(sm);
        const auto ro = RSRC(o + (size_t)(pm * 8 + T) * 18432);
        unsigned tw2[4];
#pragma unroll
        for (int g = 0; g < 4; ++g) {
            const f32x4 v = om[g] * sm[g];
            __builtin_amdgcn_raw_buffer_store_b64((u32x2){pack2(v[0], v[1]), pack2(v[2], v[3])}, ro, vom, g * 36864, 16);
            const f32x2 w = ot[g].xy * sm[g];
            tw2[g] = pack2(w.x, w.y);
        }
        if (q == 2) {
#pragma unroll
            for (int g = 0; g < 4; ++g) __builtin_amdgcn_raw_buffer_store_b32(tw2[g], ro, vot, g * 36864, 16);
        }
    }
    STAMP(5);
    {
        WAIT_V(0); BARX();
        STAMP(6);
        f32x4 v[5];
        proj_residual(v, rx);
        const LAS char* pw = opaque(lds + WO_OFF + lane * 16);
        const LAS char* pw2 = opaque(lds + WO_OFF + 41472 + lane * 16);
        const LAS char* ph2 = opaque(lds + WO_OFF + 4096 + q * 128 + (r & 7) * 16);
        const LAS char* ph3 = opaque(lds + WO_OFF + 41472 + 4096 + q * 128 + (r & 7) * 16);
        bf16x8 wf[90];
        auto rdw = [&](int i) {
            const int kc = i / 5, n = i % 5;
            wf[i] = n < 4 ? LDS_FRAG((kc < 9 ? pw : pw2) + (kc % 9) * 4608 + n * 1024) : LDS_FRAG((kc < 9 ? ph2 : ph3) + (kc % 9) * 4608);
        };
#pragma unroll
        for (int i = 0; i < WO_W; ++i) rdw(i);
#pragma unroll
        for (int n = 0; n < 5; ++n) v[n] = pin(v[n]);
        const auto rO = RSRC(o + (size_t)rt * 18432);
        const ProjOut po = proj_out(wl + L_XB, wl + L_STB, rt, cg, lane);
        asm volatile("" :: "s"(rO));
        WAIT_L(0);
        __builtin_amdgcn_s_setprio(REL_PRIO);
        BARX();
        STAMP(7);
        bf16x8 af[18];
        auto ldo = [&](int kc) { af[kc] = __builtin_bit_cast(bf16x8, __builtin_amdgcn_raw_buffer_load_b128(rO, lane * 16 + (kc & 3) * 1024, (kc >> 2) * 4096, 16)); };
#pragma unroll
        for (int kc = 0; kc < WO_PRE; ++kc) { ldo(kc); SCHED0(); if (kc >= PACE0 && kc + 1 < WO_PRE) PACE; SCHED0(); }
        static_for<90>([&](auto ic) {
            constexpr int i = ic.value, kc = i / 5, n = i % 5, j = WO_W + 2 * (i / 3) + i % 3;
            MFMA_T(wf[i], af[kc], v[n]);
            SCHED0();
            if (i % 3 < 2 && j < 90) rdw(j);
            if (i % WO_EV == 2 && i / WO_EV + WO_PRE < 18) ldo(i / WO_EV + WO_PRE);
            SCHED0();
        });
        asm volatile("s_nop 4" : "+v"(v[0]), "+v"(v[1]));
        STAMP(8);
        proj_store(v, po);
        __builtin_amdgcn_s_setprio(0);
    }
}

DEVI f32x2 gelu2(f32x2 v) {
    const f32x2 w = v * (v * v * -0.10294324f + -2.3022082f);
    const f32x2 d = (f32x2){__builtin_amdgcn_exp2f(w.x), __builtin_amdgcn_exp2f(w.y)} + 1.0f;
    return v * (f32x2){__builtin_amdgcn_rcpf(d.x), __builtin_amdgcn_rcpf(d.y)};
}
DEVI f32x4 gelu4(f32x4 v) { const f32x2 a = gelu2(v.xy), b = gelu2(v.zw); return (f32x4){a.x, a.y, b.x, b.y}; }
DEVI void rowsum16x8(const f32x4 a, const f32x4 b, float& o0, float& o1) {
    float t0, t1, t2, t3;
    asm volatile(
        "s_nop 1\n\t"
        "v_add_f32_dpp %2, %6, %6 row_ror:8 row_mask:0xf bank_mask:0x3\n\t"
        "v_add_f32_dpp %3, %7, %7 row_ror:8 row_mask:0xf bank_mask:0x3\n\t"
        "v_add_f32_dpp %4, %10, %10 row_ror:8 row_mask:0xf bank_mask:0x3\n\t"
        "v_add_f32_dpp %5, %11, %11 row_ror:8 row_mask:0xf bank_mask:0x3\n\t"
        "v_add_f32_dpp %2, %8, %8 row_ror:8 row_mask:0xf bank_mask:0xc\n\t"
        "v_add_f32_dpp %3, %9, %9 row_ror:8 row_mask:0xf bank_mask:0xc\n\t"
        "v_add_f32_dpp %4, %12, %12 row_ror:8 row_mask:0xf bank_mask:0xc\n\t"
        "v_add_f32_dpp %5, %13, %13 row_ror:8 row_mask:0xf bank_mask:0xc\n\t"
        "v_add_f32_dpp %0, %2, %2 row_half_mirror row_mask:0xf bank_mask:0x5\n\t"
        "s_nop 0\n\t"
        "v_add_f32_dpp %1, %4, %4 row_half_mirror row_mask:0xf bank_mask:0x5\n\t"
        "v_add_f32_dpp %0, %3, %3 row_half_mirror row_mask:0xf bank_mask:0xa\n\t"
        "v_add_f32_dpp %1, %5, %5 row_half_mirror row_mask:0xf bank_mask:0xa\n\t"
        "s_nop 0\n\t"
        "v_add_f32_dpp %0, %0, %0 quad_perm:[1,0,3,2] row_mask:0xf bank_mask:0xf\n\t"
        "v_add_f32_dpp %1, %1, %1 quad_perm:[1,0,3,2] row_mask:0xf bank_mask:0xf\n\t"
        "s_nop 0\n\t"
        "v_add_f32_dpp %0, %0, %0 quad_perm:[2,3,0,1] row_mask:0xf bank_mask:0xf\n\t"
        "v_add_f32_dpp %1, %1, %1 quad_perm:[2,3,0,1] row_mask:0xf bank_mask:0xf\n\t"
        : "=&v"(o0), "=&v"(o1), "=&v"(t0), "=&v"(t1), "=&v"(t2), "=&v"(t3)
        : "v"(a[0]), "v"(a[1]), "v"(a[2]), "v"(a[3]), "v"(b[0]), "v"(b[1]), "v"(b[2]), "v"(b[3]));
}

#define K5_PRE 9
#define RH_MAP 0
#define NET_LAYERS 2
constexpr int G_STAGE = 34816;
constexpr int K5_STAGE = 18432, K5_NS = 8;

constexpr int y_load_at(int slot) {
    int next = K5_PRE;
    for (int s = 0;; ++s) { const bool go = next < 18 + 2 * (s / 3); if (s == slot) return go ? next : -1; next += go; }
}

struct GTile { f32x4 a[4][4], e[2]; };
struct GFrag { bf16x8 a[2][4], b[2][5]; const LAS char *pa[2][2], *pb, *p8; int slot; };
DEVI void g_rd(GFrag& f, int kk, int i) {
    if (i < 4) f.a[kk][i] = LDS_FRAG(f.pa[kk][i >> 1] + (i & 1) * 2048);
    else if (i < 8) f.b[kk][i - 4] = LDS_FRAG(f.pb + (kk * 9 + i - 4) * 1024);
    else f.b[kk][4] = LDS_FRAG(f.p8 + kk * 9216);
}
template <int NS, int G, bool ROWS_IN_REGS>
DEVI void g_steps(GTile& c, GFrag& f, int n) {
    LOOP_ALIGN();
#pragma unroll 1
    do {
        int d;
#pragma unroll
        for (int kk = 0; kk < 2; ++kk) {
            WAIT_L(0);
            if (kk) BAR();
            SCHED0();
#pragma unroll
            for (int i = 0; i < 18; ++i) {
                const int nn = i >> 2, m = i & 3;
                f32x4& e = nn < 4 ? c.a[m][nn] : c.e[m & 1];
                e = ROWS_IN_REGS ? MFMA(f.a[kk][m], f.b[kk][nn], e) : MFMA(f.b[kk][nn], f.a[kk][m], e);
                SCHED0();
                if (i < 9) g_rd(f, kk ^ 1, i);
                if (kk == 0) {
                    if (i == 9) d = pin_s(f.slot == NS - 1 ? -(NS - 1) * G : G);
                    if (i >= 10 && i < 14) f.pa[(i - 10) >> 1][i & 1] = opaque(f.pa[(i - 10) >> 1][i & 1] + d);
                    if (i == 14) f.pb = opaque(f.pb + d);
                    if (i == 15) f.p8 = opaque(f.p8 + d);
                } else {
                    if (i == 10) f.slot = pin_s(f.slot == NS - 1 ? 0 : f.slot + 1);
                    if (i == 12) n = pin_s(n - 1);
                }
                SCHED0();
            }
        }
    } while (n);
}
DEVI void keep4(const f32x4 (&c)[4]) { asm volatile("" :: "v"(c[0]), "v"(c[1]), "v"(c[2]), "v"(c[3])); }
template <int D, int DB, bool FIRST, class HOOK>
DEVI void g_step_u(GTile& c, GFrag& f, const f32x4 (&c0)[4], HOOK&& hook) {
    static_for<36>([&](auto ic) {
        constexpr int kk = ic.value / 18, i = ic.value % 18, n = i >> 2, m = i & 3;
        if (i == 0) { WAIT_L(0); if (kk) BAR(); SCHED0(); }
        f32x4& e = n < 4 ? c.a[m][n] : c.e[m & 1];
        if (FIRST && kk == 0) asm volatile("v_mfma_f32_16x16x32_bf16 %0, %1, %2, %3" : "=&v"(e) : "v"(f.b[kk][n]), "v"(f.a[kk][m]), "v"(c0[m]));
        else MFMA_T(f.b[kk][n], f.a[kk][m], e);
        SCHED0();
        if (i < 9) g_rd(f, kk ^ 1, i);
        if (kk == 0) {
            if (i >= 10 && i < 14) f.pa[(i - 10) >> 1][i & 1] = opaque(f.pa[(i - 10) >> 1][i & 1] + D);
            if (i == 14) f.pb = opaque(f.pb + DB);
            if (i == 15) f.p8 = opaque(f.p8 + DB);
        }
        if (FIRST && ic.value == 20) keep4(c0);
        hook(ic);
        SCHED0();
    });
}
#define MF(d, a, b) "v_mfma_f32_16x16x32_bf16 " d ", " a ", " b ", " d "\n\t"
template <int D, int DB>
DEVI void g_step_gelu(GTile& c, GFrag& f, f32x4& z, float r, float c1) {
#pragma unroll
    for (int kk = 0; kk < 2; ++kk) {
        float va = z[2 * kk], vb = z[2 * kk + 1], ta, tb;
        WAIT_L(0); if (kk) BAR(); SCHED0();
#pragma unroll
        for (int i = 0; i < 9; ++i) {
            MFMA_T(f.b[kk][i >> 2], f.a[kk][i & 3], c.a[i & 3][i >> 2]);
            if (i == 5) asm volatile("v_mul_f32 %0, %0, %1" : "+v"(va) : "v"(r));
            if (i == 6) asm volatile("v_mul_f32 %0, %0, %1" : "+v"(vb) : "v"(r));
            if (i == 7) asm volatile("v_mul_f32 %0, %1, %1" : "=v"(ta) : "v"(va));
            if (i == 8) asm volatile("v_mul_f32 %0, %1, %1" : "=v"(tb) : "v"(vb));
            SCHED0();
            g_rd(f, kk ^ 1, i);
            SCHED0();
        }
        asm volatile(MF("%0", "%5", "%6") "v_fmaak_f32 %3, %3, %11, 0xc0135761\n\tv_fmaak_f32 %4, %4, %11, 0xc0135761\n\t"
                     MF("%1", "%5", "%7") "v_mul_f32 %3, %3, %9\n\tv_mul_f32 %4, %4, %10\n\t" MF("%2", "%5", "%8") "v_exp_f32 %3, %3"
                     : "+v"(c.a[1][2]), "+v"(c.a[2][2]), "+v"(c.a[3][2]), "+v"(ta), "+v"(tb)
                     : "v"(f.b[kk][2]), "v"(f.a[kk][1]), "v"(f.a[kk][2]), "v"(f.a[kk][3]), "v"(va), "v"(vb), "v"(c1));
        SCHED0(); f.pa[kk][0] = opaque(f.pa[kk][0] + D); SCHED0();
        asm volatile(MF("%0", "%6", "%7") "v_exp_f32 %5, %5\n\t" MF("%1", "%6", "%8") "v_add_f32 %4, 1.0, %4\n\tv_add_f32 %5, 1.0, %5\n\t"
                     MF("%2", "%6", "%9") "v_rcp_f32 %4, %4\n\t" MF("%3", "%6", "%10") "v_rcp_f32 %5, %5"
                     : "+v"(c.a[0][3]), "+v"(c.a[1][3]), "+v"(c.a[2][3]), "+v"(c.a[3][3]), "+v"(ta), "+v"(tb)
                     : "v"(f.b[kk][3]), "v"(f.a[kk][0]), "v"(f.a[kk][1]), "v"(f.a[kk][2]), "v"(f.a[kk][3]));
        SCHED0(); f.pa[kk][1] = opaque(f.pa[kk][1] + D); SCHED0();
        asm volatile(MF("%0", "%4", "%5") "v_mul_f32 %2, %2, %7\n\tv_mul_f32 %3, %3, %8\n\t" MF("%1", "%4", "%6")
                     : "+v"(c.e[0]), "+v"(c.e[1]), "+v"(va), "+v"(vb) : "v"(f.b[kk][4]), "v"(f.a[kk][0]), "v"(f.a[kk][1]), "v"(ta), "v"(tb));
        SCHED0();
        if (kk == 0) { f.pb = opaque(f.pb + DB); f.p8 = opaque(f.p8 + DB); }
        SCHED0();
        z[2 * kk] = va; z[2 * kk + 1] = vb;
    }
}
DEVI void g_fill(GTile& c, const float (&e)[4]) {
#pragma unroll
    for (int m = 0; m < 4; ++m) {
#pragma unroll
        for (int n = 0; n < 4; ++n) c.a[m][n] = pin((f32x4){e[m], e[m], e[m], e[m]});
        if (m < 2) c.e[m] = pin((f32x4){e[m], e[m], e[m], e[m]});
    }
}

#undef STAMP_SEC
#define STAMP_SEC 1
DEVI void gmlp_body(LAS char* lds, int wm, int wn, int pm, int cg, char* z2t, char* y, const u16* __restrict__ bsp STAMP_PARAM) {
    const int mlo = wm * 4 + wn * 2, mhi = wm * 4 + (1 - wn) * 2;
    const float zero4[4] = {0.f, 0.f, 0.f, 0.f};
    GTile z2, z1, acc;
    GFrag f;
    {
        const int lane = lane_id_fresh();
#pragma unroll
        for (int kk = 0; kk < 2; ++kk) { f.pa[kk][0] = lds + mlo * 2048 + kk * 1024 + lane * 16; f.pa[kk][1] = lds + mhi * 2048 + kk * 1024 + lane * 16; }
        f.pb = lds + 16384 + wn * 4096 + lane * 16; f.p8 = lds + 16384 + 8192 + lane * 16; f.slot = 0;
    }
    g_fill(z2, zero4); g_fill(z1, zero4);
#pragma unroll
    for (int i = 0; i < 4; ++i) f.pa[i >> 1][i & 1] = opaque(f.pa[i >> 1][i & 1]);
    f.pb = opaque(f.pb); f.p8 = opaque(f.p8);
    SCHED0();
    WAIT_V(0); BARX();
    BAR();
    BARX();
    STAMP(2);
#pragma unroll
    for (int i = 0; i < 9; ++i) g_rd(f, 0, i);
    g_steps<4, G_STAGE, true>(z2, f, 9);
    STAMP(3);
    {
        const int lane = lane_id_fresh(), r = lane & 15, q = lane >> 4;
        f32x4 ra[4];
#pragma unroll
        for (int m = 0; m < 4; ++m) ra[m] = *(const LAS f32x4*)(lds + G_TAB + ((m < 2 ? mlo : mhi) + (m & 1)) * 64 + q * 16);
        SCHED0();
#pragma unroll
        for (int m = 0; m < 4; ++m) {
            f32x4 s1, s2;
#pragma unroll
            for (int n = 0; n < 4; ++n) {
                const f32x4 g = gelu4(z2.a[m][n] * ra[m]);
                z2.a[m][n] = g;
                if (n == 0) { s1 = g; s2 = g * g; } else { s1 += g; s2 += g * g; }
            }
            if (m < 2) { const f32x4 g = gelu4(z2.e[m] * ra[m]); z2.e[m] = g; s1 += g; s2 += g * g; }
            float u0, u1;
            rowsum16x8(s1, s2, u0, u1);
            if ((r & 3) == 0) *(LAS f32x2*)(lds + G_PART + wn * 1024 + ((m < 2 ? mlo : mhi) + (m & 1)) * 128 + (q * 4 + (r >> 2)) * 8) = (f32x2){u0, u1};
            SCHED0();
        }
    }
    WAIT_L0(); BAR();
    STAMP(4);
    g_steps<4, G_STAGE, false>(z1, f, 4);
    unsigned b0, b1;
    f32x4 c0[4];
    g_step_u<G_STAGE, G_STAGE, false>(z1, f, c0, [&](auto ic) {
        if (ic.value != 18) return;
        STAMP(5);
        const int lane = lane_id_fresh(), q = lane >> 4;
        b0 = __builtin_amdgcn_raw_buffer_load_b32(RSRC(bsp), (lane & 15) * 4, pm * 256 + mlo * 32, 0);
        b1 = __builtin_amdgcn_raw_buffer_load_b32(RSRC(bsp), (lane & 15) * 4, pm * 256 + mhi * 32, 0);
#pragma unroll
        for (int g = 0; g < 2; ++g) {
            const auto rz2 = RSRC(z2t + (size_t)pm * 589824 + cg * 36864 + ((g ? mhi : mlo) >> 1) * 9216);
            const LAS char* tab = lds + G_TAB + (g ? mhi : mlo) * 64 + q * 16;
            LAS char* own = lds + (wm ? G_OWN : G_STAGE + 16384) + (g ^ wn) * 9216 + lane * 16;
            f32x4 ra[2], rb[2];
#pragma unroll
            for (int T = 0; T < 2; ++T) { ra[T] = *(const LAS f32x4*)(tab + 512 + T * 64); rb[T] = *(const LAS f32x4*)(tab + 1024 + T * 64); }
#pragma unroll
            for (int n = 0; n < (g ? 4 : 5); ++n) {
                const f32x4 v0 = (n < 4 ? z2.a[2 * g][n & 3] : z2.e[0]) * ra[0] + rb[0], v1 = (n < 4 ? z2.a[2 * g + 1][n & 3] : z2.e[1]) * ra[1] + rb[1];
                const u32x4 o = {pack2(v0[0], v1[0]), pack2(v0[1], v1[1]), pack2(v0[2], v1[2]), pack2(v0[3], v1[3])};
                ST16(rz2, lane * 16, n < 4 ? wn * 4096 + n * 1024 : 8192, o, 16);
                *(LAS u32x4*)(own + (n < 4 ? wn * 4096 + n * 1024 : 8192)) = o;
            }
        }
        STAMP(6);
    });
    static_for<3>([&](auto sc) {
        constexpr int D = sc.value == 0 ? G_STAGE : sc.value == 1 ? -3 * G_STAGE : 2 * G_STAGE;
        g_step_u<D, D, false>(z1, f, c0, [&](auto ic) { if (ic.value == 17 && sc.value == 1) WAIT_V(0); });
    });
    float ra[4], c1;
    {
        int lane, da[2];
        g_step_u<-G_STAGE, -G_STAGE, false>(z1, f, c0, [&](auto ic) {
            constexpr int i = ic.value;
            if (i == 9) lane = pin(lane_id_fresh());
            if (i == 14 || i == 15) da[i & 1] = pin((lane & 15) * 128 + (((4 * (i & 1) + (lane >> 4)) ^ (lane & 7)) * 16) - lane * 16 - (i & 1) * 1024);
            if (i == 16 || i == 17) f.pa[0][i & 1] = opaque(f.pa[0][i & 1] + da[0]);
            if (i == 27 || i == 28) f.pa[1][i & 1] = opaque(f.pa[1][i & 1] + da[1]);
            if (i >= 29 && i < 33) {
                constexpr int m = (i - 29) & 3;
                const float e = m & 1 ? bfhi(m < 2 ? b0 : b1) : bflo(m < 2 ? b0 : b1);
                c0[m] = pin((f32x4){e, e, e, e});
            }
            if (i == 33) {
#pragma unroll
                for (int m = 0; m < 4; ++m) ra[m] = *(const LAS float*)(lds + G_TAB + ((m < 2 ? mlo : mhi) + (m & 1)) * 64 + (lane & 15) * 4);
                c1 = pin(-0.10294324f);
            }
        });
    }
    STAMP(7);
    g_step_u<2 * G_STAGE, G_OWN - G_STAGE - 16384, true>(acc, f, c0, [](auto) {});
    static_for<18>([&](auto sc) {
        constexpr int st = sc.value, m = st < 16 ? st >> 2 : st & 1;
        constexpr int D = st == 0 ? -3 * G_STAGE : st % 3 == 1 ? 2 * G_STAGE : -G_STAGE;
        g_step_gelu<D, st == 0 ? 16384 - G_OWN : D>(acc, f, st < 16 ? z1.a[m][st & 3] : z1.e[m], ra[m], c1);
    });
    f.slot = 1;
    STAMP(9);
    g_steps<3, -G_STAGE, false>(acc, f, 13);
    STAMP(10);
    const int lane = lane_id_fresh();
#pragma unroll
    for (int g = 0; g < 2; ++g) {
        const auto ry = RSRC(y + (size_t)(pm * 8 + (g ? mhi : mlo)) * 73728 + cg * 4608);
#pragma unroll
        for (int T = 0; T < 2; ++T) {
            const int m = 2 * g + T;
#pragma unroll
            for (int p = 0; p < 2; ++p) {
                const f32x4 a = acc.a[m][2 * p] * z1.a[m][2 * p], c = acc.a[m][2 * p + 1] * z1.a[m][2 * p + 1];
                ST16(ry, lane * 16 + wn * 2048 + p * 1024, T * 73728, ((u32x4){pack2(a[0], a[1]), pack2(a[2], a[3]), pack2(c[0], c[1]), pack2(c[2], c[3])}), 16);
            }
            if (g == 0) {
                const f32x4 a = acc.e[T] * z1.e[T];
                ST8(ry, (lane >> 5) * 256 + (lane & 15) * 16 + ((lane >> 4) & 1) * 8, T * 73728 + 4096, ((u32x2){pack2(a[0], a[1]), pack2(a[2], a[3])}), 16);
            }
        }
    }
}

#define NET_IDS \
    int b = __builtin_amdgcn_readfirstlane(blockIdx.x), wid = wid8 & 3; \
    asm volatile("" : "+s"(b), "+s"(wid));     \
    const int xcd = b & 7, idx = b >> 3; \
    const int pa = (xcd >> 1) * 4 + (idx & 3), hp = (xcd & 1) * 8 + (idx >> 2); \
    const int pm = (xcd >> 2) * 8 + (idx & 7), cg = (xcd & 3) * 4 + (idx >> 3); \
    const int rh = RH_MAP ? xcd * 4 + (idx & 3) : (xcd >> 1) * 8 + (idx & 7), c5 = RH_MAP ? idx >> 2 : (xcd & 1) * 4 + (idx >> 3); \
    const int lead = (idx + (idx >> 3)) & 3; \
    auto kord = [&](int t, int n) { \
        const int k = t + ((int)(0x1F000000u << (((t - lead) & 3) * 4)) >> 28); \
        return (unsigned)k < (unsigned)n ? k : t; \
    }; \
    auto kord5 = [&](int t) { \
        const int k = t + ((int)(0x1F000000u << (((t - (RH_MAP ? c5 : lead)) & 3) * 4)) >> 28); \
        return (unsigned)k < 18u ? k : t; \
    };

typedef short s16x4 __attribute__((ext_vector_type(4)));
DEVI u32x4 tr_frag(const LAS char* p, int four_rows) {
    const u32x2 a = __builtin_bit_cast(u32x2, __builtin_amdgcn_ds_read_tr16_b64_v4i16((LAS s16x4*)p));
    const u32x2 b = __builtin_bit_cast(u32x2, __builtin_amdgcn_ds_read_tr16_b64_v4i16((LAS s16x4*)(p + four_rows)));
    return (u32x4){a.x, a.y, b.x, b.y};
}
DEVI float dot2(unsigned v, unsigned w, float acc) { return __builtin_amdgcn_fdot2_f32_bf16(__builtin_bit_cast(bf16x2_t, v), __builtin_bit_cast(bf16x2_t, w), acc, false); }
DEVI float sum2(unsigned v, float acc) { return dot2(v, 0x3f803f80u, acc); }
struct Sel { unsigned lo, hi, nlo, nhi; };
DEVI Sel make_sel() { return {(unsigned)pin_s(0x00003f80), (unsigned)pin_s(0x3f800000), (unsigned)pin_s(0x0000bf80), (unsigned)pin_s((int)0xbf800000u)}; }
DEVI unsigned centre2(unsigned v, f32x2& e, float mean, const Sel& k) {
    const float a = dot2(k.lo, v, e.x), b = dot2(k.hi, v, e.y);
    const unsigned r = pack2(a, b);
    e = (f32x2){dot2(k.nlo, r, a), dot2(k.nhi, r, b)} - mean;
    return r;
}
typedef u32x4 Retile[9];
template <int PITCH>
DEVI void retile_out_make(Retile& o, const LAS char* img, int lane) {
    const int c = lane & 15, q = lane >> 4, p = lane & 3;
    const LAS char* src = img + (q * 8 + (c >> 2)) * PITCH;
#pragma unroll
    for (int i = 0; i < 8; ++i) o[i] = tr_frag(src + p * 16 + (i >> 2) * 144 + ((i >> 1) & 1) * 64 + (i & 1) * 8, 4 * PITCH);
    o[8] = tr_frag(src + 128 + (p >> 1) * 144 + (p & 1) * 8, 4 * PITCH);
}
DEVI void retile_out_store(const Retile& o, char* dst, int nkc, int kc, int w, int lane) {
    const int c = lane & 15, q = lane >> 4;
    const auto rd = RSRC(dst + (size_t)(2 * w * nkc + kc) * 4608);
#pragma unroll
    for (int i = 0; i < 8; ++i) ST16(rd, lane * 16 + (i & 3) * 1024, (i >> 2) * nkc * 4608, o[i], 16);
    ST16(rd, (c >> 3) * nkc * 4608 + 4096 + q * 128 + (c & 7) * 16, 0, o[8], 16);
}

DEVI float dpp_ror(float v, int n) { return n == 8 ? __builtin_amdgcn_update_dpp(0.f, v, 0x128, 0xf, 0xf, true) : n == 4 ? __builtin_amdgcn_update_dpp(0.f, v, 0x124, 0xf, 0xf, true)
                                          : n == 2 ? __builtin_amdgcn_update_dpp(0.f, v, 0x122, 0xf, 0xf, true) : __builtin_amdgcn_update_dpp(0.f, v, 0x121, 0xf, 0xf, true); }
DEVI unsigned long long epoch_of(const char* ws) {
    SCHED0();
    const unsigned long long ep = *(const __attribute__((address_space(4))) unsigned long long*)(ws + pin_s((int)OFF_EP)) + 1ull;
    asm volatile("" :: "s"(ep));
    return ep;
}
DEVI void prologue_done(LAS char* lds, char* ws, unsigned long long ep, int b, int lane) {
    if (lds_arrive(lds + G_WORD + 8) == 7) ST16(RSRC(ws), lane ? 0x80000000u : (unsigned)OFF_FL + b * 16, 0, ((u32x4){(unsigned)ep, (unsigned)(ep >> 32), 0u, 0u}), 16);
}
DEVI void centre_frag(u32x4& f, f32x2 (&er)[4], float mean, const Sel& k) {
#pragma unroll
    for (int e = 0; e < 4; ++e) f[e] = centre2(f[e], er[e], mean, k);
}
DEVI unsigned long long prologue(LAS char* lds, char* ws, const char* x0, const char* wqkv, const u16* wo, const u16* w_in, int b, int pa, int wid8 PST_PARAM) {
    PST_DECL;
    const int lane = lane_id_fresh(), c = lane & 15, q = lane >> 4, w = wid8 & 3, jw = w - 1 + 3 * (wid8 >> 2);
    const bool wjob = b >= 16 && b < 112 && w, ijob = b >= 112 && w, ojob = b < 9;
    const unsigned l0 = (unsigned)(size_t)lds;
    LAS char* img = lds + 2 * K1_STAGE;
    LAS float* red = (LAS float*)(lds + G_PART);
    const int uw = b - 16, head = (uw & 7) * 4 + ((uw >> 3) & 3), part = uw >> 5, hp = head >> 1, odd = head & 1;
    const int c0 = part * 1152 + hp * 72 + odd * 40;
    const int ui = b - 112, st = (ui & ~15) + 2 * (ui & 7) + ((ui >> 3) & 1);
    if (wjob) {
        const auto rs = __builtin_amdgcn_make_buffer_rsrc((void*)wqkv, 0, 576 * 3456, 0x00020000);
        unsigned v[5];
#pragma unroll
        for (int i = 0; i < 5; ++i) v[i] = (((i * 64 + lane) * 21846) >> 16) * (3456 - 48) + lane * 16 + jw * 96 * 3456 + (c0 & ~15);
        dma4<false>(l0 + 2 * K1_STAGE + jw * 6144, rs, 0, v[0], v[1], v[2], v[3]);
        dma1(l0 + 2 * K1_STAGE + jw * 6144 + 4096, rs, 0, lane < 32 ? v[4] + 4096 : 0x80000000u);
    }
    if (ijob) {
        if (I_SLEEP) __builtin_amdgcn_s_sleep(I_SLEEP);
        const unsigned v = (lane >> 2) * 9216 + (lane & 3) * 16, d = 16 * 9216 - 1024;
        const auto rs = RSRC((const char*)(w_in + st * 32) + (size_t)jw * 96 * 9216);
        dma4<false>(l0 + 2 * K1_STAGE + jw * 6144, rs, 0, v, v + d, v + 2 * d, v + 3 * d);
        dma2<false>(l0 + 2 * K1_STAGE + jw * 6144 + 4096, rs, 64 * 9216, v, v + d);
    }
    if (ojob) {
        const auto rs = RSRC(wo + w * 144);
#pragma unroll
        for (int j = 0; j < 9; ++j) {
            const int i = lane + 64 * j, rl = (i * 3641) >> 16, kp = (b * 2 + (wid8 >> 2)) * 32 + rl;
            const int row = kp < 512 ? 18 * (kp >> 4) + (kp & 15) + 2 * ((kp >> 4) & 1) : 36 * ((kp - 512) >> 2) + 16 + (kp & 3);
            dma1(l0 + 2 * K1_STAGE + wid8 * 9216 + j * 1024, rs, 0, (unsigned)(row * 1152 + (i - rl * 18) * 16));
        }
    }
    if (wid8 == 4) { lds_word_set(lds + G_WORD + c * 4, 0u); WAIT_L0(); }
    BAR();
    if (W_ALONE && wid8 == 4 && b >= 16 && b < 112) lds_count_wait(lds + G_WORD + 12, W_ALONE);
    if (wid8 == 4) {
        AttnX ax = attn_x(x0, x0, 0, pa, 0, lane);
#pragma unroll
        for (int s = 0; s < 2; ++s)
#pragma unroll
            for (int i = 0; i < 4; ++i) { ax.oA = (pa * 8 + 2 * i) * 18432; attn_issueA(lds, ax, i, s, s); }
    }
    u32x4 xv[9];
    if (wid8 == 0) {
        const unsigned v = (lane >> 3) * 2304 + (lane & 7) * 16;
        const auto rx = RSRC(x0 + (size_t)slot2row(b * 8) * 1152);
#pragma unroll
        for (int i = 0; i < 9; ++i) asm volatile("s_nop 4\n\tbuffer_load_dwordx4 %0, %1, %2, 0 offen offset:%3" : "+v"(xv[i]) : "v"(v), "s"(rx), "i"(i * 128) : "memory");
        const auto rw = RSRC(ws);
        const u32x4 ones = {~0u, ~0u, ~0u, ~0u}, zero = {0u, 0u, 0u, 0u};
        const unsigned vo = lane * 16;
        ST16(rw, vo, (unsigned)OFF_ZST + b * 2048, ones, 16); ST16(rw, vo, (unsigned)OFF_ZST + b * 2048 + 1024, ones, 16);
        ST16(rw, vo, (unsigned)OFF_CNT + b * 2560, zero, 16); ST16(rw, vo, (unsigned)OFF_CNT + b * 2560 + 1024, zero, 16);
        ST16(rw, vo, lane < 32 ? (unsigned)OFF_CNT + b * 2560 + 2048 : (unsigned)(OFF_L0 + L_STB) + b * 512 - 512, zero, 16);
        ST16(rw, vo, (unsigned)(OFF_L0 + L_O) + (b >> 1) * 18432 + 16384 + (b & 1) * 1024, zero, 16);
        ST16(rw, b < 8 ? vo : 0x80000000u, (unsigned)OFF_FL2 + b * 1024, zero, 16);
    }
    PST(1);
    const unsigned long long ep = epoch_of(ws);
    const Sel k = make_sel();
    u32x4 f[6];
    unsigned vd, vd2;
    __amdgpu_buffer_rsrc_t rd, rd2;
    if (wjob || ijob) {
        const LAS char *s0, *s1;
        if (ijob) {
            int o[2];
#pragma unroll
            for (int h = 0; h < 2; ++h) {
                const int u = st * 2 + h, half = u >= 144, g = u - half * 144, cg = g / 9, j = g - cg * 9, p = lane & 3;
                o[h] = h * 32 + (j < 8 ? (p & 1) * 16 + (p >> 1) * 8 : p * 8);
                (h ? rd2 : rd) = RSRC(ws + OFF_TWIN + (size_t)cg * (18 * 18432) + (half * 18 + jw * 3) * 9216);
                (h ? vd2 : vd) = q * 256 + (j < 8 ? ((j & ~1) + (c >> 3)) * 1024 + ((j & 1) * 8 + (c & 7)) * 16 : 8192 + c * 16);
            }
            s0 = img + jw * 6144 + (q * 8 + (c >> 2)) * 64 + o[0]; s1 = s0 + (o[1] - o[0]);
        } else {
            s0 = img + jw * 6144 + (q * 8 + (c >> 2)) * 48 + (c0 & 15); s1 = s0 + 32 - odd * 8 + min(c & 3, 2) * 1536; s0 += (c & 3) * 8;
            rd = RSRC(ws + OFF_TWQKV + (size_t)(hp * 18 + jw * 3) * 6144 + (3 * odd + part) * 1024); vd = lane * 16;
            rd2 = RSRC(ws + OFF_TWT + (size_t)((hp * 3 + part) * 18 + jw * 3) * 256); vd2 = c < 12 && !odd ? (unsigned)((c >> 2) * 256 + q * 64 + (c & 3) * 16) : 0x80000000u;
        }
        s0 = opaque(s0); s1 = opaque(s1); vd = pin(vd); vd2 = pin(vd2);
        asm volatile("" :: "s"(rd), "s"(rd2));
        WAIT_V(0);
        PST(2);
        float sm[4] = {0.f, 0.f, 0.f, 0.f};
        if (ijob) {
#pragma unroll
            for (int i = 0; i < 6; ++i) f[i] = tr_frag((i < 3 ? s0 : s1) + (i % 3) * 2048, 256);
#pragma unroll
            for (int e = 0; e < 24; ++e) sm[(e >= 12) * 2 + (e & 1)] = sum2(f[e >> 2][e & 3], sm[(e >= 12) * 2 + (e & 1)]);
            sm[2] += sm[3];
        } else {
#pragma unroll
            for (int i = 0; i < 4; ++i) f[i] = tr_frag(i < 3 ? s0 + i * 1536 : s1, 192);
#pragma unroll
            for (int e = 0; e < 16; ++e) sm[(e >= 12) * 2 + (e & 1)] = sum2(f[e >> 2][e & 3], sm[(e >= 12) * 2 + (e & 1)]);
            sm[2] = c < 12 ? sm[2] + sm[3] : 0.f;
            sm[2] += dpp_ror(sm[2], 4); sm[2] += dpp_ror(sm[2], 8);
        }
        auto x = __builtin_amdgcn_permlane16_swap(__float_as_uint(sm[0] + sm[1]), __float_as_uint(sm[2]), false, false);
        red[jw * 64 + lane] = __uint_as_float(x[0]) + __uint_as_float(x[1]);
        if (lds_arrive(lds + G_WORD + 12) < 5) lds_count_wait(lds + G_WORD + 12, 6);
        PST(3);
        float t = 0.f;
#pragma unroll
        for (int i = 0; i < 6; ++i) t += red[i * 64 + lane];
        x = __builtin_amdgcn_permlane32_swap(__float_as_uint(t), __float_as_uint(t), false, false);
        t = (__uint_as_float(x[0]) + __uint_as_float(x[1])) * (1.0f / 576.0f);
        auto y = __builtin_amdgcn_permlane16_swap(__float_as_uint(t), __float_as_uint(t), false, false);
        const float m0 = __uint_as_float(y[0]), m1 = __uint_as_float(y[1]);
        f32x2 e0[4] = {{-m0, -m0}, {-m0, -m0}, {-m0, -m0}, {-m0, -m0}}, e1[4] = {{-m1, -m1}, {-m1, -m1}, {-m1, -m1}, {-m1, -m1}};
        const unsigned dk = ijob ? 9216 : 6144;
#pragma unroll
        for (int i = 0; i < 3; ++i) { centre_frag(f[i], e0, m0, k); ST16(rd, vd, i * dk, f[i], 16); }
#pragma unroll
        for (int i = 0; i < 3; ++i) if (i == 0 || ijob) { centre_frag(f[3 + i], e1, m1, k); ST16(rd2, vd2, i * 9216, f[3 + i], 16); }
        PST(4);
        if (wjob) {
            if (W_ALONE > 6) lds_arrive(lds + G_WORD + 12);
            const unsigned fo = pin((unsigned)OFF_FW + ((lane >> 3) * 16 + hp) * 1024 + (3 * odd + part) * 128 + (lane & 7) * 16);
            WAIT_V(0);
            PST(5);
            if (lds_arrive(lds + G_WORD + 4) == 5) {
                const u32x4 e4 = {(unsigned)ep, (unsigned)(ep >> 32), (unsigned)ep, (unsigned)(ep >> 32)};
                ST16(RSRC(ws), fo, 0, e4, 16); ST16(RSRC(ws), fo + 131072, 0, e4, 16);
            }
        }
    }
    if (wid8 == 0) {
        WAIT_V(7);
        asm volatile("" : "+v"(xv[0]), "+v"(xv[1]), "+v"(xv[2]), "+v"(xv[3]), "+v"(xv[4]), "+v"(xv[5]), "+v"(xv[6]), "+v"(xv[7]), "+v"(xv[8]));
        float s = 0.f, q2 = 0.f;
#pragma unroll
        for (int e = 0; e < 36; ++e) { s = sum2(xv[e >> 2][e & 3], s); q2 = dot2(xv[e >> 2][e & 3], xv[e >> 2][e & 3], q2); }
        ST8(RSRC(ws + OFF_L0 + L_STA + (size_t)b * 512), lane * 8, 0, ((u32x2){__float_as_uint(s), __float_as_uint(q2)}), 16);
    }
    if (ojob) {
        if (wid8) WAIT_V(0);
        Retile rt;
        retile_out_make<288>(rt, img + wid8 * 9216, lane); retile_out_store(rt, ws + OFF_TWO, 18, b * 2 + (wid8 >> 2), w, lane);
    }
    PST(7);
    PST_DUMP(wid8 == 0 || wid8 == 4 || wid8 == 5, wid8 ? wid8 : 2);
    return ep;
}

__global__ __launch_bounds__(512, 2) TRACE_ATTR void k_net(char* ws, const u16* wsp, const u16* bsp, char* out, const char* x0, const char* wqkv, const u16* wo,
                                                             const u16* w_in, const u16* w_out DBG_ARG) {
    extern __shared__ __attribute__((aligned(16))) char smem[];
    LAS char* lds = (LAS char*)smem;
    asm volatile("gm_code_begin:");
    if (blockIdx.x < 16) {
        unsigned long long pc; int d0, len;
        asm volatile("s_getpc_b64 %0\n1:\n\ts_mov_b32 %1, gm_code_begin-1b\n\ts_mov_b32 %2, gm_code_end1-gm_code_begin\n\ts_max_i32 %2, %2, gm_code_end2-gm_code_begin"
                     : "=s"(pc), "=s"(d0), "=s"(len) :: "scc");
        const auto rs = __builtin_amdgcn_make_buffer_rsrc((void*)((pc + d0) & ~15ull), 0, len, 0x00020000);
        dma1((unsigned)(size_t)lds + G_OWN, rs, 0, (threadIdx.x * 2 + (blockIdx.x >> 3)) * 128);
    }
    asm volatile("" :: "s"(ws), "s"(wsp), "s"(bsp), "s"(out), "s"(x0), "s"(wqkv), "s"(wo), "s"(w_in), "s"(w_out));
    ws = own_s(ws); wsp = own_s(wsp); bsp = own_s(bsp); out = own_s(out); x0 = own_s(x0); wqkv = own_s(wqkv); wo = own_s(wo); w_in = own_s(w_in); w_out = own_s(w_out);
    const char *twin = ws + OFF_TWIN, *twout = ws + OFF_TWOUT;
    STAMP_DECL;
    const int wid8 = __builtin_amdgcn_readfirstlane(threadIdx.x >> 6);
    unsigned long long ep;
    { NET_IDS ep = prologue(lds, ws, x0, wqkv, wo, w_in, b, pa, wid8 PST_FWD); }
    if (wid8 >= 4) {
        u32x4 wr[5][9];
#pragma unroll 1
      for (int layer = 0; layer < NET_LAYERS; ++layer) {
        NET_IDS
        TRACE_BEGIN;
        char* wl = ws + OFF_L0 + layer * SZ_L;
        const unsigned fl = layer * F_LAYER;
        const char *xin = wl + L_XB, *st = wl + L_STB, *z2t = wl + (layer ? L_Y : L_Z2T);
        unsigned long long* zst = (unsigned long long*)(ws + OFF_ZST + layer * SZ_ZST);
        const int lane = lane_id_fresh(), l3 = lane >> 3;
        const unsigned vo = lane * 16;
        const unsigned l0 = (unsigned)(size_t)lds, lA = l0 + wid * 4096, lB = l0 + 16384 + wid * 4608;
        const auto rX = RSRC(xin + (size_t)(pm * 8 + 2 * wid) * 18432), rI = RSRC(twin + (size_t)cg * (18 * 18432) + wid * 4608);
        auto issue1B = [&](int g, int slot) { const int k = kord(g < 9 ? g : (g < 13 ? g - 4 : g - 13), 9); dma5<true>(lB + slot * G_STAGE, rI, (g < 9 ? 9 + k : k) * 18432, vo); };
        auto issue1A = [&](int g, int slot) { dma4<true>(lA + slot * G_STAGE, rX, kord(g < 9 ? g : g - 13, 9) * 2048, vo, vo, vo + 16384, vo + 16384); };
        auto issue1 = [&](int g, int slot) { issue1B(g, slot); issue1A(g, slot); };
        const auto rA = RSRC((const char*)wsp + (size_t)(pm * 128 + wid * 32) * 4096), rB = RSRC(z2t + cg * 36864 + wid * 4608);
        const unsigned voA = (unsigned)(2 * l3 * 4096 + ((lane & 7) ^ l3) * 16);
        auto issueB = [&](int k, int slot) { dma5<true>(lB + slot * G_STAGE, rB, (k >> 1) * 589824 + (k & 1) * 18432, vo); };
        const auto rW = RSRC(twout + (size_t)c5 * (72 * 4608));
        auto issue5 = [&](int t) { dma5<true>(l0 + ((t + 6) & 7) * K5_STAGE + wid * 4608, rW, kord5(t) * K5_STAGE + wid * 4608, vo); };
        auto load5 = [&](int p, int i) {
            const int t0 = p ? 2 * p + 6 : 4, ow = kord5(t0 + i) * K5_STAGE + wid * 4096;
            asm volatile("s_nop 4\n\tbuffer_load_dwordx4 %0, %4, %5, %6 offen sc1\n\tbuffer_load_dwordx4 %1, %4, %5, %6 offen offset:1024 sc1\n\t"
                         "buffer_load_dwordx4 %2, %4, %5, %6 offen offset:2048 sc1\n\tbuffer_load_dwordx4 %3, %4, %5, %6 offen offset:3072 sc1"
                         : "+v"(wr[p][4 * i]), "+v"(wr[p][4 * i + 1]), "+v"(wr[p][4 * i + 2]), "+v"(wr[p][4 * i + 3]) : "v"(vo), "s"(rW), "s"(ow) : "memory");
            if (i) {
                const int oe = kord5(t0 + (wid >> 1)) * K5_STAGE + 16384 + (wid & 1) * 1024;
                asm volatile("s_nop 4\n\tbuffer_load_dwordx4 %0, %1, %2, %3 offen sc1" : "+v"(wr[p][8]) : "v"(vo), "s"(rW), "s"(oe) : "memory");
            }
        };
        auto put5 = [&](int w0, int w1) {
#pragma unroll
            for (int w = w0; w < w1; ++w) {
                const int p = w / 9, i = w % 9 == 4 ? -1 : w % 9 - (w % 9 > 4);
                LAS char* sl = lds + (((p ? 2 * p + 6 : 4) + 6) & 7) * K5_STAGE + lane * 16;
                if (i < 0) *(LAS u32x4*)(sl + (wid >> 1) * K5_STAGE + 16384 + (wid & 1) * 1024) = wr[p][8];
                else *(LAS u32x4*)(sl + (i >> 2) * K5_STAGE + wid * 4096 + (i & 3) * 1024) = wr[p][i];
            }
            WAIT_L0();
        };
        const int row = wid * 32 + (lane & 31);
        auto step1 = [&](int t) { BAR(); issue1(t + 4, t & 3); };
        const char* ps;
        unsigned fd, fd2;
        {
            unsigned fo, fi;
            const int ojob = layer || !wid ? 288 : b + (wid - 1) * 256, oimg = wid == 1 ? 2048 : G_STAGE;
            attn_producer(lds, ws, x0, wl, fl, ep, layer, pa, hp, wid, [&] { prologue_done(lds, ws, ep, b, lane); }, [&](int part) {
                if (part == 0 && ojob < 288) {
                    const auto rs = RSRC(w_out + (size_t)(ojob >> 2) * 32 * 576 + (ojob & 3) * 144);
#pragma unroll
                    for (int j = 0; j < 9; ++j) { const int i = lane + 64 * j; dma1(l0 + oimg + j * 1024, rs, 0, (unsigned)(((i * 3641) >> 16) * (1152 - 288) + i * 16)); }
                }
                if (part == 1) {
                    issue1B(0, 0); issue1B(1, 1);
                    fo = flag_out(fl + F_X1, 256, pa, hp, lane); fi = flag_in(fl + F_X1, 256, pm * 16 + cg, 16, lane);
                    ps = pin(st + (size_t)(pm * 128 + wid * 32) * 64 + lane * 16);
                    fd = pin((unsigned)OFF_FL2 + wid * 1024 + vo); fd2 = pin((unsigned)OFF_FL2 + 4096 + (lane & 31) * 16);
                }
            });
            if (ojob < 288) {
                Retile& rt = wr[1];
                retile_out_make<288>(rt, lds + oimg, lane);
                __builtin_amdgcn_s_sleep(OJOB_SLEEP);
                retile_out_store(rt, ws + OFF_TWOUT, 72, ojob >> 2, ojob & 3, lane);
                WAIT_V(0);
                flag_raise(ws, lane ? 0x80000000u : (unsigned)OFF_FL2 + ojob * 16);
            }
            BARX();
            if (wid == 0) flag_raise(ws, fo);
            issue1B(2, 2); issue1B(3, 3);
            asm volatile("" :: "s"(rX));
            if (wid == 0) flags_wait<X1_FIRST>(ws, fi);
            BARX();
        }
        issue1A(0, 0); issue1A(1, 1); issue1A(2, 2);
        WAIT_V(8); BAR();
        issue1A(3, 3);
        {
            f32x4 sp[3];
            WAIT_V(8); step1(0);
            WAIT_V(13); step1(1);
            WAIT_V(18); step1(2);
            unsigned got[2];
            if (layer == 0) asm volatile("s_nop 4\n\tbuffer_load_dword %0, %2, %4, 0 offen sc1\n\tbuffer_load_dword %1, %3, %4, 0 offen sc1" : "+v"(got[0]), "+v"(got[1]) : "v"(fd), "v"(fd2), "s"(RSRC(ws)) : "memory");
            WAIT_IF(layer, 18, 20); step1(3);
            WAIT_IF(layer, 18, 20); step1(4);
            WAIT_V(18);
            if (layer == 0) {
                asm volatile("" : "+v"(got[0]), "+v"(got[1]));
                if (__any((got[0] & got[1]) == 0u)) { flags_wait<0>(ws, fd); flags_wait<0>(ws, fd2); }
            }
            BAR(); issue1B(9, 1); side_load(sp, ps, ps); if (layer == 0) load5(3, 0);
            WAIT_IF(layer, 17, 21); BAR(); issue1B(10, 2); if (layer == 0) load5(3, 1);
            WAIT_IF(layer, 5, 14); BARX();
            side_fence(sp);
#pragma unroll
            for (int i = 0; i < 2; ++i) lds_word_set(lds + G_TAB + (wid * 32 + i * 16 + (lane >> 2)) * 4, __float_as_uint(ln_rstd(sp[i])));
            issue1B(11, 3);
            WAIT_L0();
        }
        BAR(); issue1B(12, 0);
        if (layer == 1) {
            const auto rd = RSRC(out + (size_t)b * 9216 + wid * 2304);
            ST16(rd, vo, 0, ((u32x4){0u, 0u, 0u, 0u}), 16); ST16(rd, vo, 1024, ((u32x4){0u, 0u, 0u, 0u}), 16);
            if (lane < 16) ST16(rd, vo, 2048, ((u32x4){0u, 0u, 0u, 0u}), 16);
        }
        if (layer == 0) {
#pragma unroll
            for (int p = 0; p < 6; ++p) load5(p >> 1, p & 1);
        }
        WAIT_V(0);
        __builtin_amdgcn_s_setprio(3);
        const char* src = pin((const char*)zst + (size_t)(pm * 16 + (lane >> 4) * 4) * 1024 + wid * 256 + (lane & 15) * 16);
        const unsigned tw = pin(l0 + G_TAB + 512 + (lane >> 5) * 512 + (wid * 32 + (lane & 15) * 2 + ((lane >> 4) & 1)) * 4);
        const unsigned fz = flag_out(fl + F_Z2, 256, cg, pm, lane), fzi = pin(fl + F_Z2 + (cg * 16 + pm) * 256 + ((lane ^ pm) & 8) * 16 + (lane & 7) * 16);
        const unsigned vA[4] = {pin(voA), pin(voA + 16 * 4096 - 1024), pin(voA + 4096 - 2048), pin(voA + 17 * 4096 - 3072)};
        auto spA = [&](int so, unsigned la) { dma4<false>(la, rA, so, vA[0], vA[1], vA[2], vA[3]); };
        auto issueA = [&](int k, int slot) { spA(k * 128, lA + slot * G_STAGE); };
        {
            const LAS char* pp = opaque(lds + G_PART + row * 8);
            const auto rs = RSRC(zst + (size_t)(pm * 16 + cg) * 128);
            const unsigned vp = pin(lane < 32 ? (unsigned)row * 8u : 0x80000000u);
            asm volatile("" :: "s"(rs));
            BARX();
            const f32x2 a = *(const LAS f32x2*)pp, c = *(const LAS f32x2*)(pp + 1024);
            ST8(rs, vp, 0, ((u32x2){__float_as_uint(add1(a.x, c.x)), min(__float_as_uint(add1(a.y, c.y)), 0xfffffffeu)}), 16);
            SCHED0();
        }
        __builtin_amdgcn_s_setprio(0);
        {
            f32x4 g[4];
            float sm[4];
            auto gather = [&] {
                asm volatile("global_load_dwordx4 %0, %4, off sc1\n\tglobal_load_dwordx4 %1, %4, off offset:1024 sc1\n\t"
                             "global_load_dwordx4 %2, %4, off offset:2048 sc1\n\tglobal_load_dwordx4 %3, %4, off offset:3072 sc1"
                             : "=&v"(g[0]), "=&v"(g[1]), "=&v"(g[2]), "=&v"(g[3]) : "v"(src) : "memory");
            };
            auto total = [&] {
                asm volatile("" : "+v"(g[0]), "+v"(g[1]), "+v"(g[2]), "+v"(g[3]));
#pragma unroll
                for (int i = 0; i < 4; ++i) sm[i] = add1(add1(g[0][i], g[1][i]), add1(g[2][i], g[3][i]));
            };
            step1(9); step1(10);
            BARX(); gather(); issue1(15, 3);
            WAIT_V(22); BARX();
            __builtin_amdgcn_s_setprio(3);
            WAIT_V(9);
            total();
            if (__any(__builtin_isunordered(sm[1], sm[3]))) {
#pragma unroll 1
                for (unsigned spin = 0; spin < 400000u; ++spin) {
                    bool ok = true;
#pragma unroll
                    for (int i = 0; i < 4; ++i) ok &= __float_as_uint(g[i][1]) != 0xffffffffu && __float_as_uint(g[i][3]) != 0xffffffffu;
                    if (__all(ok)) break;
                    gather();
                    WAIT_V(0);
                    total();
                }
            }
            xq4_sum(sm);
            const bool odd = lane & 16;
            float mean;
            const float rs = rstd_of(odd ? sm[2] : sm[0], odd ? sm[3] : sm[1], 1.0f / 2304.0f, mean);
            asm volatile("ds_write_b32 %0, %1" :: "v"(tw), "v"(lane < 32 ? rs : -mean * rs) : "memory");
            __builtin_amdgcn_s_setprio(0);
            issue1B(16, 0);
            WAIT_L0();
        }
        BARX(); issue1A(16, 0); issueA(2 * pm, 1);
        auto kord2 = [&](int t) { const int j = kord(t - 2, 30); return ((j < 2 * (pm & 7) ? j : j + 2) + 2 * (pm & 8)) & 31; };
        unsigned seen;
        auto look = [&] { if (wid == 0) asm volatile("s_nop 4\n\tbuffer_load_dword %0, %1, %2, 0 offen sc1" : "+v"(seen) : "v"(fzi), "s"(RSRC(ws)) : "memory"); };
        auto need = [&](int n) {
            if (wid == 0) {
                asm volatile("" : "+v"(seen));
                if (__ballot(seen == 0u) & ((1ull << n) - 1ull)) flags_wait<0>(ws, lane < n ? fzi : fl + F_Z2 + (cg * 16 + pm) * 256 + pm * 16);
            }
        };
        auto issue = [&](int t, int sl) { const int k = kord2(t); issueB(k, sl); issueA(k, sl); };
        auto steps2 = [&](int t0, int t1) {
            int sl = (2 * t0 + 2) % 3 * G_STAGE, k = kord2(t0 + 3), ob = (k >> 1) * 589824 + (k & 1) * 18432;
            LOOP_ALIGN();
#pragma unroll 1
            for (int t = t0; t < t1; ++t) {
                WAIT_V(9); BARX();
                dma5<true>(lB + sl, rB, ob, vo);
                const int oa = pin_s(k * 128);
                const unsigned la = pin_s(lA + sl);
                k = pin_s(kord2(t + 4)); ob = pin_s((k >> 1) * 589824 + (k & 1) * 18432);
                sl = pin_s(sl == 0 ? 2 * G_STAGE : sl - G_STAGE);
                spA(oa, la);
            }
        };
        WAIT_V(13); BAR(); issue1(17, 2);
        WAIT_V(13); BARX();
        if (wid == 0) flag_raise(ws, fz);
        issueA(2 * pm + 1, 3); if (layer == 0) load5(4, 0);
        if (wid == 0) WAIT_IF(layer, 5, 9); else WAIT_IF(layer, 4, 8);
        BAR(); issueA(kord2(2), 0); if (layer == 0) load5(4, 1);
        BAR(); look(); issueA(kord2(3), 2);
        WAIT_V(4); need(3); BARX(); issueB(kord2(2), 0); issueB(kord2(3), 2); look();
        WAIT_IF(wid == 0, 6, 5); BAR(); issue(4, 1);
        WAIT_V(9); need(8);
        steps2(2, 11); look();
        WAIT_IF(wid == 0, 10, 9); BAR(); issue(14, 0);
        WAIT_V(9); need(16);
        steps2(12, 29);
        WAIT_V(9); BAR(); issue5(0); issue5(1); issue5(2);
        WAIT_V(15); BAR(); issue5(6); issue5(7);
        BAR(); issue5(3); put5(0, 9);
        unsigned vb;
        {
            vb = attn_vb(wid, lane);
            const unsigned fo = flag_out(fl + F_Y, 256, pm, cg, lane), fi = flag_in(fl + F_Y, 256, rh * 8 + c5, 16, lane);
            __builtin_amdgcn_s_setprio(3);
            WAIT_V(0); BARX();
            if (wid == 0) { flag_raise(ws, fo); flags_wait<Y_FIRST>(ws, fi); }
            __builtin_amdgcn_s_setprio(0);
            BARX();
        }
        put5(9, 13);
        BAR(); put5(13, 18);
        BAR(); put5(18, 24);
        BAR(); put5(24, 30);
        BAR(); put5(30, 36);
        BAR(); issue5(16); put5(36, 41);
        BAR(); issue5(17); put5(41, 45);
        WAIT_V(5); BAR();
        WAIT_V(0); BAR();
        if (layer == 0) { attn_issueB(lds, ws, hp, wid, vb, 0, 0); attn_issueB(lds, ws, hp, wid, vb, 1, 1); }
        BAR();
        if (layer == 0) {
            __builtin_amdgcn_s_setprio(3);
            const unsigned fo = flag_out(F_X2, 512, (rh >> 1) + wid, wid ? 16 + c5 : (rh & 1) * 8 + c5, wid == 0 || (wid == 1 && (rh & 1) && rh < 31) ? lane : 16);
            BARX();
            if (wid < 2) flag_raise(ws, fo);
        }
        TRACE_DUMP;
      }
        if (blockIdx.x == 0 && wid8 == 4) ST8(RSRC(ws), lane_id_fresh() ? 0x80000000u : (unsigned)OFF_EP, 0, ((u32x2){(unsigned)ep, (unsigned)(ep >> 32)}), 16);
        asm volatile("gm_code_end1:");
        return;
    }
#pragma unroll 1
  for (int layer = 0; layer < NET_LAYERS; ++layer) {
    NET_IDS
    TRACE_BEGIN;
    char* wl = ws + OFF_L0 + layer * SZ_L;
    const unsigned fl = layer * F_LAYER;
    const char* xin = wl + L_XB;
    char* y = wl + (layer ? L_Z2T : L_Y);
#undef STAMP_SEC
#define STAMP_SEC 0
    {
    STAMP_BEGIN;
    attn_consumer(lds, x0, wl, layer, pa, hp, wid, [&] { WAIT_V(0); prologue_done(lds, ws, ep, b, lane_id_fresh()); } STAMP_FWD);
    STAMP_END(1 + 5 * layer);
    }
#undef STAMP_SEC
#define STAMP_SEC 1
    STAMP_BEGIN;
    gmlp_body(lds, wid & 1, wid >> 1, pm, cg, wl + (layer ? L_Y : L_Z2T), y, bsp STAMP_FWD);
    {
        WAIT_V(0);
        BARX();
        STAMP(11);
        const int lane = lane_id_fresh(), r = lane & 15, q = lane >> 4;
        const int wk = wid >> 1, rt = rh * 4 + (wid & 1) * 2 + wk;
        const Res rx = proj_residual_load(xin, rt, c5, lane);
        const LAS char *pw[3], *ph[3];
#pragma unroll
        for (int i = 0; i < 3; ++i) {
            pw[i] = opaque(lds + 3 * i * K5_STAGE + wk * 4608 + lane * 16);
            ph[i] = opaque(lds + 3 * i * K5_STAGE + wk * 4608 + 4096 + q * 128 + (r & 7) * 16);
        }
        const auto rY = RSRC(y + (size_t)(rt & ~1) * 73728 + wk * 1024);
        const unsigned vy[2] = {pin((unsigned)(wk * 73728 + lane * 16)), pin((unsigned)((1 - wk) * 73728 + lane * 16))};
        int dk[4];
#pragma unroll
        for (int i = 0; i < 4; ++i) dk[i] = pin_s((kord5(4 + i) - 4 - i) * 4096);
        auto yoff = [&](int t) { return pin_s(t == 0 || t == 17 ? kord5(t) * 4096 : t * 4096 + dk[t & 3]); };
        bf16x8 af[5][4], wf[4][5];
        auto lda = [&](int so, int i) { return __builtin_bit_cast(bf16x8, __builtin_amdgcn_raw_buffer_load_b128(rY, vy[i & 1] + (i >> 1) * 2048, so, 16)); };
        auto rdw = [&](int t, int c, int n) {
            const int sl = (t + 6) & 7, o = (sl % 3) * K5_STAGE + c * 9216;
            wf[2 * (t & 1) + c][n] = n < 4 ? LDS_FRAG(pw[sl / 3] + o + n * 1024) : LDS_FRAG(ph[sl / 3] + o);
        };
#pragma unroll
        for (int n = 0; n < 10; ++n) rdw(0, n / 5, n % 5);
        int so[18];
#pragma unroll
        for (int i = 0; i < 5; ++i) so[i] = yoff(i);
        so[17] = yoff(17);
        f32x4 v[5], u[5];
        proj_residual(v, rx);
#pragma unroll
        for (int n = 0; n < 5; ++n) v[n] = pin(v[n]);
        LAS char* xo = (LAS char*)opaque(lds + wid * 4608 + lane * 16);
        LAS char* xo4 = (LAS char*)opaque(lds + wid * 4608 + 4096 + (lane & 31) * 16);
        const LAS char* xi = opaque(lds + (wid ^ 2) * 4608 + lane * 16);
        const LAS char* xi4 = opaque(lds + (wid ^ 2) * 4608 + 4096 + (lane & 31) * 16);
        const ProjOut po = layer ? proj_out_final(out, rt, c5, lane) : proj_out(wl + SZ_L + L_XA, wl + SZ_L + L_STA, rt, c5, lane);
        asm volatile("" :: "s"(rY));
        WAIT_L(0);
        BARX();
        STAMP(12);
#pragma unroll
        for (int i = 0; i < K5_PRE; ++i) { af[i >> 2][i & 3] = lda(so[i >> 2], i & 3); SCHED0(); if (i >= PACE0 && i + 1 < K5_PRE) PACE; SCHED0(); }
        static_for<18>([&](auto tc) {
            constexpr int t = tc.value;
            bf16x8 (&a)[4] = af[t % 5];
#pragma unroll
            for (int h = 0; h < 4; ++h) {
                const int c = t < 17 ? h >> 1 : h & 1, m = t < 17 ? h & 1 : 1 - (h >> 1);
                if (t < 17 ? m == 0 : h < 2) { if (t == 17 && h == 1) WAIT_L(0); else WAIT_L(5); }
#pragma unroll
                for (int n = 0; n < 5; ++n) {
                    f32x4& e = m ? u[n] : v[n];
                    if (t == 0 && h == 1) MFMA_0(wf[0][n], a[1], e); else MFMA_T(wf[2 * (t & 1) + c][n], a[2 * c + m], e);
                    SCHED0();
                    if (t < 17 && m == 0) rdw(t + 1, c, n);
                    if (t < 14 && m == 1 && !(n & 1)) { const int i = y_load_at(6 * t + 3 * c + n / 2); if (i >= 0) af[(i >> 2) % 5][i & 3] = lda(so[i >> 2], i & 3); }
                    if (t < 12 && h == 3 && n == 3) so[t + 5] = yoff(t + 5);
                    if (t == 17 && h * 5 + n >= 13 && h * 5 + n < 18) { const int i = h * 5 + n - 13; *(LAS f32x4*)(i < 4 ? xo + i * 1024 : xo4) = u[i]; }
                    SCHED0();
                }
            }
            if ((t & 1) && t < 17) BAR();
            SCHED0();
        });
        {
            WAIT_L0(); BARX();
#pragma unroll
            for (int n = 0; n < 5; ++n) u[n] = *(const LAS f32x4*)(n < 4 ? xi + n * 1024 : xi4);
            SCHED0();
            asm volatile("s_nop 1" : "+v"(v[0]), "+v"(v[1]), "+v"(v[2]), "+v"(v[3]), "+v"(v[4]));
#pragma unroll
            for (int n = 0; n < 5; ++n) v[n] += u[n];
        }
        STAMP(13);
        if (layer) proj_store<true>(v, po);
        else { proj_store(v, po); WAIT_V(0); BARX(); }
    }
    STAMP_END(3 + 5 * layer);
    TRACE_DUMP;
  }
  asm volatile("gm_code_end2:");
}

}

#define STOP_CHECK(n)

extern "C" void kernel_launch(void* const* d_in, const int* in_sizes, int n_in, void* d_out, int out_size, void* d_ws, size_t ws_size,
                              hipStream_t stream) {
    (void)in_sizes; (void)n_in; (void)out_size; (void)ws_size;
    const u16* x = (const u16*)d_in[0];
    const u16* wqkv = (const u16*)d_in[1];
    const u16* wo = (const u16*)d_in[2];
    const u16* w_in = (const u16*)d_in[4];
    const u16* w_sp = (const u16*)d_in[5];
    const u16* b_sp = (const u16*)d_in[6];
    const u16* w_out = (const u16*)d_in[7];
    char* ws = (char*)d_ws;
    (void)hipFuncSetAttribute((const void*)k_net, hipFuncAttributeMaxDynamicSharedMemorySize, NET_LDS);
    k_net<<<256, 512, NET_LDS, stream>>>(ws, w_sp, b_sp, (char*)d_out, (const char*)x, (const char*)wqkv, wo, w_in, w_out DBG_LAUNCH(0));
}
```

```cpp
#include <hip/hip_runtime.h>
#include <stdint.h>

namespace {

typedef unsigned short u16;
typedef short bf16x8 __attribute__((ext_vector_type(8)));
typedef float f32x4 __attribute__((ext_vector_type(4)));
typedef float f32x2 __attribute__((ext_vector_type(2)));
typedef unsigned u32x4 __attribute__((ext_vector_type(4)));
typedef unsigned u32x2 __attribute__((ext_vector_type(2)));
typedef __bf16 bf16x2_t __attribute__((ext_vector_type(2)));
#define LAS __attribute__((address_space(3)))
#define DEVI __device__ __forceinline__

constexpr int S_ = 2048, D_ = 576, FH = 2304;

constexpr size_t SZ_TWQKV = (size_t)16 * 18 * 6 * 1024;
constexpr size_t SZ_TWT = (size_t)48 * 18 * 256;
constexpr size_t SZ_TWO = (size_t)8 * 18 * 4608;
constexpr size_t SZ_TWIN = (size_t)16 * 18 * 18 * 1024;
constexpr size_t SZ_TWOUT = (size_t)8 * 72 * 4608;
constexpr size_t SZ_X = (size_t)S_ * D_ * 2;
constexpr size_t SZ_Z = (size_t)S_ * FH * 2;
constexpr size_t SZ_ST = (size_t)S_ * 64;
constexpr size_t SZ_ZST = (size_t)16 * 16 * 128 * 8;
constexpr size_t OFF_TWQKV = 0;
constexpr size_t OFF_TWT = OFF_TWQKV + SZ_TWQKV;
constexpr size_t OFF_TWO = OFF_TWT + SZ_TWT;
constexpr size_t OFF_TWIN = OFF_TWO + SZ_TWO;
constexpr size_t OFF_TWOUT = OFF_TWIN + SZ_TWIN;
constexpr size_t OFF_CNT = OFF_TWOUT + SZ_TWOUT;
constexpr size_t SZ_CNT = 10 * 65536;
constexpr size_t OFF_FW = OFF_CNT + SZ_CNT;
constexpr size_t OFF_FL = OFF_FW + 262144;
constexpr size_t OFF_EP = OFF_FL + 4096;
constexpr size_t OFF_FL2 = OFF_EP + 4096;
constexpr size_t OFF_ZST = OFF_FL2 + 8192;
constexpr size_t OFF_L0 = OFF_ZST + 2 * SZ_ZST;
constexpr size_t L_XA = 0, L_STA = L_XA + SZ_X, L_O = L_STA + SZ_ST, L_XB = L_O + SZ_X, L_STB = L_XB + SZ_X, L_Z2T = L_STB + SZ_ST, L_Y = L_Z2T + SZ_Z;
constexpr size_t SZ_L = 0;
static_assert(OFF_L0 + L_Y + SZ_Z <= ((size_t)256 << 20), "workspace");
constexpr unsigned F_O = OFF_CNT, F_X1 = F_O + 65536, F_Z2 = F_O + 131072, F_Y = F_O + 196608, F_LAYER = 262144, F_X2 = F_O + 2 * F_LAYER;

DEVI unsigned pack2(float a, float b) { f32x2 v = {a, b}; bf16x2_t r = __builtin_convertvector(v, bf16x2_t); return __builtin_bit_cast(unsigned, r); }
DEVI float bflo(unsigned u) { return __uint_as_float(u << 16); }
DEVI float bfhi(unsigned u) { return __uint_as_float(u & 0xffff0000u); }
DEVI int slot2row(int sg) { return (sg & ~31) + 2 * (sg & 15) + ((sg >> 4) & 1); }
DEVI float fmax2(float a, float b) { return __builtin_amdgcn_fmed3f(a, b, __builtin_inff()); }
#define XQ4(OP, MID) \
    asm volatile("s_nop 1\n\tv_permlane32_swap_b32 %0, %1\n\tv_permlane32_swap_b32 %2, %3\n\t" OP " %0, %0, %1\n\t" OP " %2, %2, %3\n\t" \
                 "s_nop 1\n\tv_permlane16_swap_b32 %0, %2\n\t" OP " %0, %0, %2\n\t" MID \
                 "v_mov_b32 %2, %0\n\ts_nop 1\n\tv_permlane16_swap_b32 %0, %2\n\tv_mov_b32 %1, %0\n\tv_mov_b32 %3, %2\n\ts_nop 0\n\t" \
                 "v_permlane32_swap_b32 %0, %1\n\tv_permlane32_swap_b32 %2, %3" : "+v"(x[0]), "+v"(x[1]), "+v"(x[2]), "+v"(x[3]))
DEVI void xq4_max(float (&x)[4]) { XQ4("v_max_f32", ""); }
DEVI void xq4_sum(float (&x)[4]) { XQ4("v_add_f32", ""); }
DEVI void xq4_sum_rcp(float (&x)[4]) { XQ4("v_add_f32", "v_rcp_f32 %0, %0\n\ts_nop 0\n\t"); }
template <class T> DEVI T pin(T v) { asm volatile("" : "+v"(v)); return v; }
template <int V> struct IC { static constexpr int value = V; };
template <int N, int I = 0, class F> DEVI void static_for(F&& f) { if constexpr (I < N) { f(IC<I>{}); static_for<N, I + 1>(f); } }
template <class T> DEVI T* own_s(T* p) { T* r; asm volatile("s_mov_b64 %0, %1" : "=&s"(r) : "s"(p)); return r; }
DEVI int pin_s(int v) { asm volatile("" : "+s"(v)); return v; }
DEVI float xq_sum(float v) {
    auto a = __builtin_amdgcn_permlane32_swap(__float_as_uint(v), __float_as_uint(v), false, false);
    v = __uint_as_float(a[0]) + __uint_as_float(a[1]);
    auto b = __builtin_amdgcn_permlane16_swap(__float_as_uint(v), __float_as_uint(v), false, false);
    return __uint_as_float(b[0]) + __uint_as_float(b[1]);
}
#define RSRC(p) __builtin_amdgcn_make_buffer_rsrc((void*)(p), 0, 0x7fffffff, 0x00020000)
#define ST16(rs, vo, so, v, aux) __builtin_amdgcn_raw_buffer_store_b128((v), (rs), (vo) + (so), 0, aux)
#define ST8(rs, vo, so, v, aux) __builtin_amdgcn_raw_buffer_store_b64((v), (rs), (vo), (so), aux)
#define WAIT_V(n) asm volatile("s_waitcnt vmcnt(%0)" ::"i"(n) : "memory")
#define WAIT_L0() asm volatile("s_waitcnt lgkmcnt(0)" ::: "memory")
#define BAR() do { asm volatile("" ::: "memory"); __builtin_amdgcn_s_barrier(); asm volatile("" ::: "memory"); } while (0)
#define BARX() do { __builtin_amdgcn_sched_barrier(0); BAR(); __builtin_amdgcn_sched_barrier(0); } while (0)
#define LOOP_ALIGN() asm volatile(".p2align 6")
#define SCHED0() __builtin_amdgcn_sched_barrier(0)
#define MFMA(a, b, c) __builtin_amdgcn_mfma_f32_16x16x32_bf16((a), (b), (c), 0, 0, 0)
#define MFMA_T(a, b, c) asm volatile("v_mfma_f32_16x16x32_bf16 %0, %1, %2, %0" : "+v"(c) : "v"(a), "v"(b))
#define MFMA_0(a, b, c) asm volatile("v_mfma_f32_16x16x32_bf16 %0, %1, %2, 0" : "=&v"(c) : "v"(a), "v"(b))
#define WAIT_L(n) __builtin_amdgcn_s_waitcnt(0xC07F | ((n) << 8))
#define LDS_FRAG(p) (*(const LAS bf16x8*)(p))
DEVI int lane_id_fresh() { int z = 0; asm volatile("" : "+v"(z)); return __builtin_amdgcn_mbcnt_hi(~0u, __builtin_amdgcn_mbcnt_lo(~0u, z)); }
DEVI const LAS char* opaque(const LAS char* p) { asm volatile("" : "+v"(p)); return p; }
#define DBG_ARG
#define DBG_LAUNCH(n)
#define STAMP(i)
#define STAMP_DECL
#define STAMP_BEGIN
#define STAMP_END(n)
#define STAMP_PARAM
#define STAMP_FWD

#define PST_PARAM
#define PST_FWD
#define PST_DECL
#define PST(i)
#define PST_DUMP(c, k)
#define TRACE_ATTR
#define TRACE_BEGIN
#define TRACE_DUMP

DEVI float add1(float a, float b) { float r; asm("v_add_f32 %0, %1, %2" : "=v"(r) : "v"(a), "v"(b)); return r; }
DEVI float rstd_of(float s, float q, float inv_n, float& mean) {
    float var;
    asm("v_mul_f32 %0, %2, %3\n\tv_mul_f32 %1, %2, %4\n\tv_fma_f32 %1, -%0, %0, %1" : "=&v"(mean), "=&v"(var) : "s"(inv_n), "v"(s), "v"(q));
    return __builtin_amdgcn_rsqf(fmax2(var, 0.f) + 1e-5f);
}
DEVI float ln_rstd(const f32x4 p) {
    float s, q, mean;
    asm("v_add_f32 %0, %2, %4\n\tv_add_f32 %1, %3, %5\n\ts_nop 0\n\t"
        "v_add_f32_dpp %0, %0, %0 quad_perm:[1,0,3,2] row_mask:0xf bank_mask:0xf\n\tv_add_f32_dpp %1, %1, %1 quad_perm:[1,0,3,2] row_mask:0xf bank_mask:0xf\n\ts_nop 0\n\t"
        "v_add_f32_dpp %0, %0, %0 quad_perm:[2,3,0,1] row_mask:0xf bank_mask:0xf\n\tv_add_f32_dpp %1, %1, %1 quad_perm:[2,3,0,1] row_mask:0xf bank_mask:0xf"
        : "=&v"(s), "=&v"(q) : "v"(p[0]), "v"(p[1]), "v"(p[2]), "v"(p[3]));
    return rstd_of(s, q, 1.0f / 576.0f, mean);
}

struct Res { u32x4 a, c; u32x2 e; };
DEVI Res proj_residual_load(const char* xin, int rt, int cg, int lane) {
    const auto rs = RSRC(xin + (size_t)rt * 18432 + cg * 2304);
    Res x;
    x.a = __builtin_amdgcn_raw_buffer_load_b128(rs, lane * 16, 0, 16); x.c = __builtin_amdgcn_raw_buffer_load_b128(rs, lane * 16 + 1024, 0, 16);
    x.e = __builtin_amdgcn_raw_buffer_load_b64(rs, 2048 + (lane & 15) * 16 + ((lane >> 4) & 1) * 8, 0, 16);
    return x;
}
DEVI void proj_residual(f32x4 (&v)[5], const Res& x) {
    v[0] = (f32x4){bflo(x.a.x), bfhi(x.a.x), bflo(x.a.y), bfhi(x.a.y)}; v[1] = (f32x4){bflo(x.a.z), bfhi(x.a.z), bflo(x.a.w), bfhi(x.a.w)};
    v[2] = (f32x4){bflo(x.c.x), bfhi(x.c.x), bflo(x.c.y), bfhi(x.c.y)}; v[3] = (f32x4){bflo(x.c.z), bfhi(x.c.z), bflo(x.c.w), bfhi(x.c.w)};
    v[4] = (f32x4){bflo(x.e.x), bfhi(x.e.x), bflo(x.e.y), bfhi(x.e.y)};
}
struct ProjOut { __amdgpu_buffer_rsrc_t x, st; unsigned v0, v1, v2, vs; };
DEVI ProjOut proj_out(char* xout, char* stout, int rt, int cg, int lane) {
    const int r = lane & 15, q = lane >> 4;
    ProjOut p = {RSRC(xout + (size_t)rt * 18432 + cg * 2304), RSRC(stout + (size_t)rt * 1024 + cg * 8),
                 pin((unsigned)lane * 16u), pin((unsigned)lane * 16u + 1024u), pin((unsigned)(2048 + r * 16 + (q & 1) * 8)), pin((unsigned)(r * 64 + (q >> 1) * 4))};
    asm volatile("" :: "s"(p.x), "s"(p.st));
    return p;
}
DEVI ProjOut proj_out_final(char* out, int rt, int cg, int lane) {
    const int q = lane >> 4;
    const unsigned o = (unsigned)(slot2row(rt * 16 + (lane & 15)) * 1152 + cg * 144);
    ProjOut p = {RSRC(out), RSRC(out), pin(o + q * 16), pin(o + 64 + q * 16), pin(o + 128 + (q & 1) * 8), 0u};
    return p;
}
template <bool FINAL = false>
DEVI void proj_store(f32x4 (&v)[5], const ProjOut& p) {
    ST16(p.x, p.v0, 0, ((u32x4){pack2(v[0][0], v[0][1]), pack2(v[0][2], v[0][3]), pack2(v[1][0], v[1][1]), pack2(v[1][2], v[1][3])}), FINAL ? 0 : 16);
    SCHED0();
    asm volatile("" : "+v"(v[2]), "+v"(v[3]), "+v"(v[4]));
    ST16(p.x, p.v1, 0, ((u32x4){pack2(v[2][0], v[2][1]), pack2(v[2][2], v[2][3]), pack2(v[3][0], v[3][1]), pack2(v[3][2], v[3][3])}), FINAL ? 0 : 16);
    SCHED0();
    ST8(p.x, p.v2, 0, ((u32x2){pack2(v[4][0], v[4][1]), pack2(v[4][2], v[4][3])}), FINAL ? 0 : 16);
    SCHED0();
    if (!FINAL) {
        const f32x4 s4 = ((v[0] + v[1]) + (v[2] + v[3])) + v[4] * 0.5f;
        const f32x4 q4 = ((v[0] * v[0] + v[1] * v[1]) + (v[2] * v[2] + v[3] * v[3])) + v[4] * v[4] * 0.5f;
        auto x = __builtin_amdgcn_permlane32_swap(__float_as_uint((s4[0] + s4[2]) + (s4[1] + s4[3])), __float_as_uint((q4[0] + q4[2]) + (q4[1] + q4[3])), false, false);
        const float t = __uint_as_float(x[0]) + __uint_as_float(x[1]);
        auto y = __builtin_amdgcn_permlane16_swap(__float_as_uint(t), __float_as_uint(t), false, false);
        __builtin_amdgcn_raw_buffer_store_b32(__float_as_uint(__uint_as_float(y[0]) + __uint_as_float(y[1])), p.st, p.vs, 0, 16);
        SCHED0();
    }
}

DEVI void side_load(f32x4 (&d)[3], const char* p, const char* p2) {
    asm volatile("global_load_dwordx4 %0, %3, off sc1\n\tglobal_load_dwordx4 %1, %3, off offset:1024 sc1\n\tglobal_load_dwordx4 %2, %4, off sc1"
                 : "=&v"(d[0]), "=&v"(d[1]), "=&v"(d[2]) : "v"(p), "v"(p2) : "memory");
}
DEVI void side_fence(f32x4 (&d)[3]) { asm volatile("" : "+v"(d[0]), "+v"(d[1]), "+v"(d[2])); }
DEVI void lds_word_set(LAS char* p, unsigned v) { asm volatile("ds_write_b32 %0, %1" :: "v"((unsigned)(size_t)p), "v"(v) : "memory"); }
#define DMA_I(v, off, sc) "buffer_load_dwordx4 " v ", %1, %2 offen offset:" off sc " lds\n\t"
#define DMA5(sc) asm volatile("s_mov_b32 m0, %0\n\ts_mov_b32 exec_hi, 0\n\ts_nop 2\n\t" DMA_I("%3", "0", sc) "s_mov_b32 exec_hi, -1\n\t" \
    DMA_I("%3", "512", sc) DMA_I("%3", "1536", sc) DMA_I("%3", "2560", sc) DMA_I("%3", "3584", sc) :: "s"(la), "s"(rs), "s"(so), "v"(vo) : "memory")
#define DMA4(sc) asm volatile("s_mov_b32 m0, %0\n\ts_nop 3\n\t" DMA_I("%3", "0", sc) DMA_I("%4", "1024", sc) DMA_I("%5", "2048", sc) DMA_I("%6", "3072", sc) \
    :: "s"(la), "s"(rs), "s"(so), "v"(v0), "v"(v1), "v"(v2), "v"(v3) : "memory")
template <bool SC1> DEVI void dma5(unsigned la, __amdgpu_buffer_rsrc_t rs, int so, unsigned vo) { if (SC1) DMA5(" sc1"); else DMA5(""); }
template <bool SC1> DEVI void dma4(unsigned la, __amdgpu_buffer_rsrc_t rs, int so, unsigned v0, unsigned v1, unsigned v2, unsigned v3) { if (SC1) DMA4(" sc1"); else DMA4(""); }
#define DMA2(sc) asm volatile("s_mov_b32 m0, %0\n\ts_nop 3\n\t" DMA_I("%3", "0", sc) DMA_I("%4", "1024", sc) :: "s"(la), "s"(rs), "s"(so), "v"(v0), "v"(v1) : "memory")
template <bool SC1> DEVI void dma2(unsigned la, __amdgpu_buffer_rsrc_t rs, int so, unsigned v0, unsigned v1) { if (SC1) DMA2(" sc1"); else DMA2(""); }
DEVI void dma1(unsigned la, __amdgpu_buffer_rsrc_t rs, int so, unsigned v0) { asm volatile("s_mov_b32 m0, %0\n\ts_nop 3\n\t" DMA_I("%3", "0", "") :: "s"(la), "s"(rs), "s"(so), "v"(v0) : "memory"); }
#define WAIT_IF(c, a, b) do { if (c) WAIT_V(a); else WAIT_V(b); } while (0)
template <int T0, int T1, int P, int S0 = 0, class ISSUE>
DEVI void ring_steps(ISSUE&& issue) {
    int slot = (T0 + S0) % 3;
    LOOP_ALIGN();
#pragma unroll 1
    for (int t = T0; t < T1; ++t) {
        WAIT_V(P); BAR();
        issue(t + 3, slot);
        slot = slot == 2 ? 0 : slot + 1;
    }
}

DEVI unsigned flag_out(unsigned use, int pitch, int group, int writer, int lane) { return pin(lane < 16 ? use + (group * 16 + lane) * pitch + writer * 16 : 0x80000000u); }
DEVI unsigned flag_in(unsigned use, int pitch, int reader, int n, int lane) { return pin(use + reader * pitch + (lane < n ? lane : 0) * 16); }
DEVI void flag_raise(const char* ws, unsigned vo) { ST16(RSRC(ws), vo, 0, ((u32x4){1u, 1u, 1u, 1u}), 16); SCHED0(); }
template <int FIRST>
DEVI void flags_wait(const char* ws, unsigned vo) {
    unsigned v, n = 400000u;
    asm volatile("s_nop 4\n\ts_sleep %4\n"
                 "1:\n\tbuffer_load_dword %0, %2, %3, 0 offen sc1\n\ts_waitcnt vmcnt(0)\n\tv_cmp_eq_u32 vcc, 0, %0\n\ts_cbranch_vccz 2f\n\t"
                 "s_sleep 1\n\ts_sub_u32 %1, %1, 1\n\ts_cbranch_scc0 1b\n2:"
                 : "=&v"(v), "+s"(n) : "v"(vo), "s"(RSRC(ws)), "i"(FIRST) : "vcc", "scc", "memory");
    SCHED0();
}
template <int FIRST>
DEVI void epoch_wait(const char* ws, unsigned vo, unsigned long long ep) {
    unsigned long long v; unsigned n = 400000u;
    asm volatile("s_nop 4\n\ts_sleep %5\n"
                 "1:\n\tbuffer_load_dwordx2 %0, %2, %3, 0 offen sc1\n\ts_waitcnt vmcnt(0)\n\tv_cmp_ne_u64 vcc, %4, %0\n\ts_cbranch_vccz 2f\n\t"
                 "s_sleep 1\n\ts_sub_u32 %1, %1, 1\n\ts_cbranch_scc0 1b\n2:"
                 : "=&v"(v), "+s"(n) : "v"(vo), "s"(RSRC(ws)), "s"(ep), "i"(FIRST) : "vcc", "scc", "memory");
    SCHED0();
}
DEVI unsigned lds_arrive(LAS char* p) {
    unsigned v;
    asm volatile("s_mov_b64 exec, 1\n\tds_add_rtn_u32 %0, %1, %2\n\ts_mov_b64 exec, -1\n\ts_waitcnt lgkmcnt(0)" : "=&v"(v) : "v"((unsigned)(size_t)p), "v"(1u) : "memory");
    return __builtin_amdgcn_readfirstlane(v);
}
DEVI void lds_count_wait(LAS char* p, unsigned n) {
#pragma unroll 1
    for (unsigned spin = 0; spin < 40000000u; ++spin) {
        unsigned v;
        asm volatile("ds_read_b32 %0, %1\n\ts_waitcnt lgkmcnt(0)" : "=v"(v) : "v"((unsigned)(size_t)p) : "memory");
        if (__builtin_amdgcn_readfirstlane(v) >= n) break;
    }
}

constexpr int K1_STAGE = 32768;
constexpr int WO_OFF = 69632;
constexpr int G_OWN = 139264;
constexpr int G_PART = G_OWN + 18432;
constexpr int G_TAB = G_PART + 2048;
constexpr int K1_SCR = G_TAB + 1536;
constexpr int G_WORD = K1_SCR + 576;
constexpr int NET_LDS = G_WORD + 64;

#define WO_PRE 9
#define WO_W 33
#define REL_PRIO 0
#define W_FIRST 2
#define W_ALONE 12
#define I_SLEEP 12
#define OJOB_SLEEP 20
#define PACE0 1
#define PACE __builtin_amdgcn_s_sleep(1)
#define POLLW 0
#define WO_EV 4
#define WSP_PF 0
#define O_FIRST 10
#define X1_FIRST 6
#define X2_FIRST 6
#define Y_FIRST 10
#define Z2_FIRST 10
DEVI unsigned attn_vb(int wid, int lane) {
    const int c = lane & 15;
    return pin((unsigned)(wid < 3 ? lane * 16 : (c < 8 ? (c >> 1) & 1 : 2) * 4608 + (lane >> 4) * 64 + (c < 8 ? (c >> 2) * 2 + (c & 1) : c & 3) * 16));
}
DEVI void attn_issueB(LAS char* lds, const char* ws, int hp, int wid, unsigned vb, int s, int slot) {
    const unsigned la = (unsigned)(size_t)lds + slot * K1_STAGE + 18432 + wid * 4096;
    if (wid < 3) dma4<true>(la, RSRC(ws), (int)OFF_TWQKV + hp * (18 * 6144) + wid * 4096 + s * 12288, vb, vb, vb, vb);
    else asm volatile("s_mov_b32 m0, %0\n\ts_nop 3\n\tbuffer_load_dwordx4 %4, %1, %2 offen sc1 lds\n\tbuffer_load_dwordx4 %4, %1, %3 offen offset:1024 sc1 lds"
                      :: "s"(la), "s"(RSRC(ws)), "s"((int)OFF_TWT + hp * 13824 + s * 512), "s"((int)OFF_TWT + hp * 13824 + s * 512 + 256 - 1024), "v"(vb) : "memory");
}
struct AttnX { __amdgpu_buffer_rsrc_t rs; unsigned v[4], vh[2]; int oA, oh, step; };
DEVI AttnX attn_x(const char* x0, const char* xa, int layer, int pm, int wid, int lane) {
    const int l3 = lane >> 3, pmh = pm > 0 ? pm : 1;
    const unsigned v = layer ? lane * 16 : 2 * l3 * 1152 + ((lane & 7) ^ l3) * 16 + 4096, d1 = layer ? 0 : 16 * 1152 - 1024, d2 = layer ? 16384 : 1152 - 2048;
    const unsigned vh = layer ? (l3 & 1) * 18432 + (lane >> 4) * 256 + (8 + (lane & 7)) * 16 : v + 16 * 1152;
    return {RSRC(layer ? xa : x0 - 4096), {pin(v), pin(v + d1), pin(v + d2), pin(v + d1 + d2)}, {pin(vh), pin(vh + (layer ? 0 : 1152 - 1024))},
            (pm * 8 + 2 * wid) * 18432, (pmh * 8 - 2) * 18432, layer ? 2048 : 128};
}
DEVI void attn_issueA(LAS char* lds, const AttnX& a, int wid, int s, int slot) {
    const unsigned la = (unsigned)(size_t)lds + slot * K1_STAGE;
    if (wid == 3) dma2<true>(la, a.rs, a.oh + s * a.step, a.vh[0], a.vh[1]);
    dma4<true>(la + 2048 + wid * 4096, a.rs, a.oA + s * a.step, a.v[0], a.v[1], a.v[2], a.v[3]);
}
template <class DONE, class NEXT>
DEVI void attn_producer(LAS char* lds, const char* ws, const char* x0, const char* wl, unsigned fl, unsigned long long ep, int layer, int pm, int hp, int wid, DONE&& done, NEXT&& next) {
    const int lane = lane_id_fresh();
    const int pmh = pm > 0 ? pm : 1;
    const unsigned vo = lane * 16;
    const AttnX ax = attn_x(x0, wl + L_XA, layer, pm, wid, lane);
    const unsigned vb = attn_vb(wid, lane), fd = pin((unsigned)OFF_FL + wid * 1024 + vo);
    auto issueB = [&](int s, int slot) { attn_issueB(lds, ws, hp, wid, vb, s, slot); };
    auto issue = [&](int s, int slot) { attn_issueA(lds, ax, wid, s, slot); issueB(s, slot); };
    const int hr = lane >> 2;
    const char* ps = pin(wl + L_STA + (size_t)(pm * 128 + wid * 32) * 64 + lane * 16);
    const char* psh = pin(wl + L_STA + (size_t)((pmh * 8 - 2 + (hr >> 3)) * 16 + 8 + (hr & 7)) * 64 + (lane & 3) * 16);
    if (layer == 0) {
        const unsigned fi = pin((unsigned)OFF_FW + (pm * 16 + hp) * 1024 + (lane < 6 ? lane : 0) * 128);
        WAIT_V(0); done();
        if (wid == 0) epoch_wait<W_FIRST>(ws, fi, ep);
        BARX();
        issueB(0, 0); issueB(1, 1); issue(2, 2);
        WAIT_IF(wid == 3, 10, 12);
    } else {
        issueB(2, 2);
        const unsigned fi = flag_in(F_X2, 512, pm * 16 + hp, pm > 0 ? 24 : 16, lane);
        if (wid == 0) flags_wait<X2_FIRST>(ws, fi);
        BARX();
#pragma unroll
        for (int i = 0; i < 3; ++i) attn_issueA(lds, ax, wid, i, i);
        WAIT_IF(wid == 3, 12, 8);
    }
    __builtin_amdgcn_s_setprio(0);
    BAR();
    if (layer) WAIT_IF(wid == 3, 6, 4);
    ring_steps<0, 4, 8>(issue);
    {
        unsigned long long done;
        if (layer == 0) asm volatile("s_nop 4\n\tbuffer_load_dwordx2 %0, %1, %2, 0 offen sc1" : "+v"(done) : "v"(fd), "s"(RSRC(ws)) : "memory");
        WAIT_IF(layer, 8, 9); BAR(); issue(7, 1);
        WAIT_V(8);
        if (layer == 0) { asm volatile("" : "+v"(done)); if (__any(done != ep)) epoch_wait<0>(ws, fd, ep); }
        BAR(); issue(8, 2);
    }
    const auto rW = RSRC(ws + OFF_TWO + (size_t)(hp >> 1) * 82944 + wid * 4096);
    auto issueW = [&](int i) { dma4<true>((unsigned)(size_t)lds + WO_OFF + (i * 4 + wid) * 4096, rW, i * 16384, vo, vo, vo, vo); };
    {
        f32x4 sp[3];
        side_load(sp, ps, psh);
        WAIT_V(11); BAR(); issueW(2); issueW(3);
        WAIT_V(8); BARX();
        side_fence(sp);
#pragma unroll
        for (int i = 0; i < 3; ++i) lds_word_set(lds + K1_SCR + (i < 2 ? 16 + wid * 32 + i * 16 + hr : hr) * 4, __float_as_uint(ln_rstd(sp[i])));
        WAIT_L0();
    }
    BAR();
    next(0);
    issueW(0); issueW(1); issueW(4);
    if (wid == 0) dma2<true>((unsigned)(size_t)lds + WO_OFF + 80896, rW, 80896, vo, vo);
    next(1);
    const unsigned fo = flag_out(fl + F_O, 256, pm, hp, lane), fi = flag_in(fl + F_O, 256, pm * 16 + hp, 16, lane);
    WAIT_V(0);
    if (wid == POLLW) __builtin_amdgcn_s_setprio(3);
    BARX();
    if (wid == POLLW) { flag_raise(ws, fo); flags_wait<O_FIRST>(ws, fi); }
    __builtin_amdgcn_s_setprio(0);
    BARX();
}

#define STAMP_SEC 0
template <class DONE>
DEVI void attn_consumer(LAS char* lds, const char* x0, char* wl, int layer, int pm, int hp, int wid, DONE&& done STAMP_PARAM) {
    const int lane = lane_id_fresh(), r = lane & 15, q = lane >> 4;
    const char* xa = wl + L_XA;
    char* o = wl + L_O;
    const int T = wid & 1, h = wid >> 1;
    const LAS char *ph[2], *pa[2];
#pragma unroll
    for (int kk = 0; kk < 2; ++kk) {
        const int sw = ((4 * kk + q) ^ (r & 7)) * 16;
        ph[kk] = opaque(lds + (layer ? kk * 1024 + q * 256 + (r ^ (T ? 0 : 8)) * 16 : (r ^ (T ? 0 : 8)) * 128 + sw));
        pa[kk] = opaque(lds + T * 2048 + (layer ? kk * 1024 + lane * 16 : r * 128 + sw));
    }
    const LAS char* pb = lds + h * 3072 + lane * 16;
    const LAS char* pt = lds + lane * 16;
    f32x4 acc[5][4];
#pragma unroll
    for (int m = 0; m < 5; ++m)
#pragma unroll
        for (int n = 0; n < 4; ++n) if (m + n) acc[m][n] = pin((f32x4){0.f, 0.f, 0.f, 0.f});
    bf16x8 fa[2][5], fb[2][4];
    auto rd1 = [&](int kk, int i) {
        if (i < 3) fb[kk][i] = LDS_FRAG(pb + 18432 + kk * 6144 + i * 1024);
        else if (i == 3) fb[kk][3] = LDS_FRAG(pt + 30720 + kk * 1024);
        else if (i == 4) fa[kk][0] = LDS_FRAG(ph[kk]);
        else if (i < 9) fa[kk][i - 4] = LDS_FRAG(pa[kk] + 2048 + (i - 5) * 4096);
    };
    f32x4 mb, mb0;
#pragma unroll
    for (int j = 0; j < 4; ++j) {
        const bool ok = ((r - 4 * q - j) & 15) < 8;
        mb[j] = ok ? 0.f : -__builtin_inff();
        mb0[j] = (ok && (pm > 0 || 4 * q + j <= r)) ? 0.f : -__builtin_inff();
    }
    mb = pin(mb); mb0 = pin(mb0);
    const unsigned mp0 = pin((4 * q > r ? 0xffffu : 0u) | (4 * q + 1 > r ? 0xffff0000u : 0u)), mp1 = pin((4 * q + 2 > r ? 0xffffu : 0u) | (4 * q + 3 > r ? 0xffff0000u : 0u));
    const unsigned qtm = pin(q == h ? 0xffffffffu : 0u);
    const unsigned vom = pin((unsigned)(r * 16 + hp * 1024 + (2 * h + (q >> 1)) * 256 + (q & 1) * 8)), vot = pin((unsigned)(r * 16 + 16384 + (hp >> 1) * 256 + (hp & 1) * 8 + h * 4));
    const LAS char* tk0 = opaque(lds + K1_SCR + ((q * 4) ^ (T ? 0 : 8)) * 4);
    const LAS char* tk = opaque(lds + K1_SCR + ((1 + T) * 16 + q * 4) * 4);
    const LAS char* tq = opaque(lds + K1_SCR + ((1 + T) * 16 + r) * 4);
    pb = opaque(pb); pt = opaque(pt);
    STAMP(2);
    const int rt = pm * 8 + (hp & 1) * 4 + wid, cg = hp >> 1;
    const ProjOut pr = layer ? proj_out((char*)xa, (char*)xa, rt, cg, lane) : proj_out_final((char*)x0, rt, cg, lane);
    BAR();
    if (layer == 0) done();
    BARX();
    STAMP(3);
#pragma unroll
    for (int i = 0; i < 9; ++i) rd1(0, i);
    SCHED0();
    const Res rx = {__builtin_amdgcn_raw_buffer_load_b128(pr.x, pr.v0, 0, 16), __builtin_amdgcn_raw_buffer_load_b128(pr.x, pr.v1, 0, 16), __builtin_amdgcn_raw_buffer_load_b64(pr.x, pr.v2, 0, 16)};
    SCHED0();
    int slot = 0, left = 9;
    LOOP_ALIGN();
#pragma unroll 1
    do {
        int d;
#pragma unroll
        for (int kk = 0; kk < 2; ++kk) {
            WAIT_L(0);
            if (kk) BAR();
            SCHED0();
#pragma unroll
            for (int i = 1; i < 20; ++i) {
                const int m = i >> 2, n = i & 3;
                const bf16x8 &A = fa[kk][m], &B = fb[kk][n];
                acc[m][n] = n != 2 ? MFMA(B, A, acc[m][n]) : MFMA(A, B, acc[m][n]);
                SCHED0();
                if (i <= 9) rd1(kk ^ 1, i - 1);
                if (kk == 0) {
                    if (i == 10) d = pin_s(slot == 2 ? -2 * K1_STAGE : K1_STAGE);
                    if (i == 11 || i == 12) ph[i & 1] = opaque(ph[i & 1] + d);
                    if (i == 13 || i == 14) pa[i & 1] = opaque(pa[i & 1] + d);
                    if (i == 15) pb = opaque(pb + d);
                    if (i == 16) pt = opaque(pt + d);
                } else {
                    if (i == 11) slot = pin_s(slot == 2 ? 0 : slot + 1);
                    if (i == 13) left = pin_s(left - 1);
                }
                SCHED0();
            }
        }
    } while (left);
    STAMP(4);
    u32x2 ts[5], tf[5];
    {
        LAS char* xb = lds + wid * 320;
#pragma unroll
        for (int m = 0; m < 5; ++m) ts[m] = (u32x2){pack2(acc[m][3][0], acc[m][3][1]), pack2(acc[m][3][2], acc[m][3][3])};
        if (q == 2) {
#pragma unroll
            for (int m = 0; m < 5; ++m) {
                const unsigned w = h ? ts[m].y : ts[m].x;
                *(LAS u16*)(xb + m * 64 + r * 2) = (u16)w; *(LAS u16*)(xb + m * 64 + 32 + r * 2) = (u16)(w >> 16);
            }
        }
        asm volatile("" ::: "memory");
#pragma unroll
        for (int m = 0; m < 5; ++m) tf[m] = *(const LAS u32x2*)(xb + m * 64 + (r & 1) * 32 + q * 8);
    }
    {
        f32x4 rk[5]; float rq[4];
        rk[0] = *(const LAS f32x4*)(tk0);
#pragma unroll
        for (int g = 0; g < 4; ++g) { rk[g + 1] = *(const LAS f32x4*)(tk + g * 128); rq[g] = *(const LAS float*)(tq + g * 128); }
        const f32x4 zero = {0.f, 0.f, 0.f, 0.f};
        u32x4 kf[5];
#pragma unroll
        for (int m = 0; m < 5; ++m) kf[m] = (u32x4){pack2(acc[m][1][0], acc[m][1][1]), pack2(acc[m][1][2], acc[m][1][3]), ts[m].y, 0u};
        f32x4 sp[4], sc[4];
#pragma unroll
        for (int g = 0; g < 4; ++g) {
            const u32x4 qf = {pack2(acc[g + 1][0][0], acc[g + 1][0][1]), pack2(acc[g + 1][0][2], acc[g + 1][0][3]), ts[g + 1].x & qtm, 0u};
            sp[g] = MFMA(__builtin_bit_cast(bf16x8, kf[g]), __builtin_bit_cast(bf16x8, qf), zero);
            sc[g] = MFMA(__builtin_bit_cast(bf16x8, kf[g + 1]), __builtin_bit_cast(bf16x8, qf), zero);
        }
        u32x2 vf[5];
#pragma unroll
        for (int m = 0; m < 5; ++m) vf[m] = (u32x2){pack2(acc[m][2][0], acc[m][2][1]), pack2(acc[m][2][2], acc[m][2][3])};
        f32x4 rs[4], t[4]; float mx[4], sm[4];
#pragma unroll
        for (int g = 0; g < 4; ++g) {
#pragma unroll
            for (int j = 0; j < 4; ++j) { const bool pv = 4 * q + j > r; rs[g][j] = pv ? rk[g][j] : rk[g + 1][j]; t[g][j] = pv ? sp[g][j] : sc[g][j]; }
            t[g] = (t[g] * rs[g]) * (rq[g] * (0.23570226039551584f * 1.4426950408889634f)) + (g == 0 ? mb0 : mb);
            mx[g] = fmaxf(fmaxf(t[g][0], t[g][1]), fmaxf(t[g][2], t[g][3]));
        }
        xq4_max(mx);
#pragma unroll
        for (int g = 0; g < 4; ++g) {
            t[g] = t[g] - mx[g];
#pragma unroll
            for (int j = 0; j < 4; ++j) t[g][j] = __builtin_amdgcn_exp2f(t[g][j]);
            sm[g] = (t[g][0] + t[g][2]) + (t[g][1] + t[g][3]);
        }
        f32x4 om[4], ot[4];
#pragma unroll
        for (int g = 0; g < 4; ++g) {
            const f32x4 p = t[g] * rs[g];
            const unsigned p01 = pack2(p[0], p[1]), p23 = pack2(p[2], p[3]);
            const u32x4 pf = {p01 & mp0, p23 & mp1, p01 & ~mp0, p23 & ~mp1};
            om[g] = MFMA(__builtin_bit_cast(bf16x8, ((u32x4){vf[g].x, vf[g].y, vf[g + 1].x, vf[g + 1].y})), __builtin_bit_cast(bf16x8, pf), zero);
            ot[g] = MFMA(__builtin_bit_cast(bf16x8, ((u32x4){tf[g].x, tf[g].y, tf[g + 1].x, tf[g + 1].y})), __builtin_bit_cast(bf16x8, pf), zero);
        }
        xq4_sum_rcp(sm);
        const auto ro = RSRC(o + (size_t)(pm * 8 + T) * 18432);
        unsigned tw2[4];
#pragma unroll
        for (int g = 0; g < 4; ++g) {
            const f32x4 v = om[g] * sm[g];
            __builtin_amdgcn_raw_buffer_store_b64((u32x2){pack2(v[0], v[1]), pack2(v[2], v[3])}, ro, vom, g * 36864, 16);
            const f32x2 w = ot[g].xy * sm[g];
            tw2[g] = pack2(w.x, w.y);
        }
        if (q == 2) {
#pragma unroll
            for (int g = 0; g < 4; ++g) __builtin_amdgcn_raw_buffer_store_b32(tw2[g], ro, vot, g * 36864, 16);
        }
    }
    STAMP(5);
    {
        WAIT_V(0); BARX();
        STAMP(6);
        f32x4 v[5];
        proj_residual(v, rx);
        const LAS char* pw = opaque(lds + WO_OFF + lane * 16);
        const LAS char* pw2 = opaque(lds + WO_OFF + 41472 + lane * 16);
        const LAS char* ph2 = opaque(lds + WO_OFF + 4096 + q * 128 + (r & 7) * 16);
        const LAS char* ph3 = opaque(lds + WO_OFF + 41472 + 4096 + q * 128 + (r & 7) * 16);
        bf16x8 wf[90];
        auto rdw = [&](int i) {
            const int kc = i / 5, n = i % 5;
            wf[i] = n < 4 ? LDS_FRAG((kc < 9 ? pw : pw2) + (kc % 9) * 4608 + n * 1024) : LDS_FRAG((kc < 9 ? ph2 : ph3) + (kc % 9) * 4608);
        };
#pragma unroll
        for (int i = 0; i < WO_W; ++i) rdw(i);
#pragma unroll
        for (int n = 0; n < 5; ++n) v[n] = pin(v[n]);
        const auto rO = RSRC(o + (size_t)rt * 18432);
        const ProjOut po = proj_out(wl + L_XB, wl + L_STB, rt, cg, lane);
        asm volatile("" :: "s"(rO));
        WAIT_L(0);
        __builtin_amdgcn_s_setprio(REL_PRIO);
        BARX();
        STAMP(7);
        bf16x8 af[18];
        auto ldo = [&](int kc) { af[kc] = __builtin_bit_cast(bf16x8, __builtin_amdgcn_raw_buffer_load_b128(rO, lane * 16 + (kc & 3) * 1024, (kc >> 2) * 4096, 16)); };
#pragma unroll
        for (int kc = 0; kc < WO_PRE; ++kc) { ldo(kc); SCHED0(); if (kc >= PACE0 && kc + 1 < WO_PRE) PACE; SCHED0(); }
        static_for<90>([&](auto ic) {
            constexpr int i = ic.value, kc = i / 5, n = i % 5, j = WO_W + 2 * (i / 3) + i % 3;
            MFMA_T(wf[i], af[kc], v[n]);
            SCHED0();
            if (i % 3 < 2 && j < 90) rdw(j);
            if (i % WO_EV == 2 && i / WO_EV + WO_PRE < 18) ldo(i / WO_EV + WO_PRE);
            SCHED0();
        });
        asm volatile("s_nop 4" : "+v"(v[0]), "+v"(v[1]));
        STAMP(8);
        proj_store(v, po);
        __builtin_amdgcn_s_setprio(0);
    }
}

DEVI f32x2 gelu2(f32x2 v) {
    const f32x2 w = v * (v * v * -0.10294324f + -2.3022082f);
    const f32x2 d = (f32x2){__builtin_amdgcn_exp2f(w.x), __builtin_amdgcn_exp2f(w.y)} + 1.0f;
    return v * (f32x2){__builtin_amdgcn_rcpf(d.x), __builtin_amdgcn_rcpf(d.y)};
}
DEVI f32x4 gelu4(f32x4 v) { const f32x2 a = gelu2(v.xy), b = gelu2(v.zw); return (f32x4){a.x, a.y, b.x, b.y}; }
DEVI void rowsum16x8(const f32x4 a, const f32x4 b, float& o0, float& o1) {
    float t0, t1, t2, t3;
    asm volatile(
        "s_nop 1\n\t"
        "v_add_f32_dpp %2, %6, %6 row_ror:8 row_mask:0xf bank_mask:0x3\n\t"
        "v_add_f32_dpp %3, %7, %7 row_ror:8 row_mask:0xf bank_mask:0x3\n\t"
        "v_add_f32_dpp %4, %10, %10 row_ror:8 row_mask:0xf bank_mask:0x3\n\t"
        "v_add_f32_dpp %5, %11, %11 row_ror:8 row_mask:0xf bank_mask:0x3\n\t"
        "v_add_f32_dpp %2, %8, %8 row_ror:8 row_mask:0xf bank_mask:0xc\n\t"
        "v_add_f32_dpp %3, %9, %9 row_ror:8 row_mask:0xf bank_mask:0xc\n\t"
        "v_add_f32_dpp %4, %12, %12 row_ror:8 row_mask:0xf bank_mask:0xc\n\t"
        "v_add_f32_dpp %5, %13, %13 row_ror:8 row_mask:0xf bank_mask:0xc\n\t"
        "v_add_f32_dpp %0, %2, %2 row_half_mirror row_mask:0xf bank_mask:0x5\n\t"
        "s_nop 0\n\t"
        "v_add_f32_dpp %1, %4, %4 row_half_mirror row_mask:0xf bank_mask:0x5\n\t"
        "v_add_f32_dpp %0, %3, %3 row_half_mirror row_mask:0xf bank_mask:0xa\n\t"
        "v_add_f32_dpp %1, %5, %5 row_half_mirror row_mask:0xf bank_mask:0xa\n\t"
        "s_nop 0\n\t"
        "v_add_f32_dpp %0, %0, %0 quad_perm:[1,0,3,2] row_mask:0xf bank_mask:0xf\n\t"
        "v_add_f32_dpp %1, %1, %1 quad_perm:[1,0,3,2] row_mask:0xf bank_mask:0xf\n\t"
        "s_nop 0\n\t"
        "v_add_f32_dpp %0, %0, %0 quad_perm:[2,3,0,1] row_mask:0xf bank_mask:0xf\n\t"
        "v_add_f32_dpp %1, %1, %1 quad_perm:[2,3,0,1] row_mask:0xf bank_mask:0xf\n\t"
        : "=&v"(o0), "=&v"(o1), "=&v"(t0), "=&v"(t1), "=&v"(t2), "=&v"(t3)
        : "v"(a[0]), "v"(a[1]), "v"(a[2]), "v"(a[3]), "v"(b[0]), "v"(b[1]), "v"(b[2]), "v"(b[3]));
}

#define K5_PRE 9
#define RH_MAP 0
#define NET_LAYERS 2
constexpr int G_STAGE = 34816;
constexpr int K5_STAGE = 18432, K5_NS = 8;

constexpr int y_load_at(int slot) {
    int next = K5_PRE;
    for (int s = 0;; ++s) { const bool go = next < 18 + 2 * (s / 3); if (s == slot) return go ? next : -1; next += go; }
}

struct GTile { f32x4 a[4][4], e[2]; };
struct GFrag { bf16x8 a[2][4], b[2][5]; const LAS char *pa[2][2], *pb, *p8; int slot; };
DEVI void g_rd(GFrag& f, int kk, int i) {
    if (i < 4) f.a[kk][i] = LDS_FRAG(f.pa[kk][i >> 1] + (i & 1) * 2048);
    else if (i < 8) f.b[kk][i - 4] = LDS_FRAG(f.pb + (kk * 9 + i - 4) * 1024);
    else f.b[kk][4] = LDS_FRAG(f.p8 + kk * 9216);
}
template <int NS, int G, bool ROWS_IN_REGS>
DEVI void g_steps(GTile& c, GFrag& f, int n) {
    LOOP_ALIGN();
#pragma unroll 1
    do {
        int d;
#pragma unroll
        for (int kk = 0; kk < 2; ++kk) {
            WAIT_L(0);
            if (kk) BAR();
            SCHED0();
#pragma unroll
            for (int i = 0; i < 18; ++i) {
                const int nn = i >> 2, m = i & 3;
                f32x4& e = nn < 4 ? c.a[m][nn] : c.e[m & 1];
                e = ROWS_IN_REGS ? MFMA(f.a[kk][m], f.b[kk][nn], e) : MFMA(f.b[kk][nn], f.a[kk][m], e);
                SCHED0();
                if (i < 9) g_rd(f, kk ^ 1, i);
                if (kk == 0) {
                    if (i == 9) d = pin_s(f.slot == NS - 1 ? -(NS - 1) * G : G);
                    if (i >= 10 && i < 14) f.pa[(i - 10) >> 1][i & 1] = opaque(f.pa[(i - 10) >> 1][i & 1] + d);
                    if (i == 14) f.pb = opaque(f.pb + d);
                    if (i == 15) f.p8 = opaque(f.p8 + d);
                } else {
                    if (i == 10) f.slot = pin_s(f.slot == NS - 1 ? 0 : f.slot + 1);
                    if (i == 12) n = pin_s(n - 1);
                }
                SCHED0();
            }
        }
    } while (n);
}
DEVI void keep4(const f32x4 (&c)[4]) { asm volatile("" :: "v"(c[0]), "v"(c[1]), "v"(c[2]), "v"(c[3])); }
template <int D, int DB, bool FIRST, class HOOK>
DEVI void g_step_u(GTile& c, GFrag& f, const f32x4 (&c0)[4], HOOK&& hook) {
    static_for<36>([&](auto ic) {
        constexpr int kk = ic.value / 18, i = ic.value % 18, n = i >> 2, m = i & 3;
        if (i == 0) { WAIT_L(0); if (kk) BAR(); SCHED0(); }
        f32x4& e = n < 4 ? c.a[m][n] : c.e[m & 1];
        if (FIRST && kk == 0) asm volatile("v_mfma_f32_16x16x32_bf16 %0, %1, %2, %3" : "=&v"(e) : "v"(f.b[kk][n]), "v"(f.a[kk][m]), "v"(c0[m]));
        else MFMA_T(f.b[kk][n], f.a[kk][m], e);
        SCHED0();
        if (i < 9) g_rd(f, kk ^ 1, i);
        if (kk == 0) {
            if (i >= 10 && i < 14) f.pa[(i - 10) >> 1][i & 1] = opaque(f.pa[(i - 10) >> 1][i & 1] + D);
            if (i == 14) f.pb = opaque(f.pb + DB);
            if (i == 15) f.p8 = opaque(f.p8 + DB);
        }
        if (FIRST && ic.value == 20) keep4(c0);
        hook(ic);
        SCHED0();
    });
}
#define MF(d, a, b) "v_mfma_f32_16x16x32_bf16 " d ", " a ", " b ", " d "\n\t"
template <int D, int DB>
DEVI void g_step_gelu(GTile& c, GFrag& f, f32x4& z, float r, float c1) {
#pragma unroll
    for (int kk = 0; kk < 2; ++kk) {
        float va = z[2 * kk], vb = z[2 * kk + 1], ta, tb;
        WAIT_L(0); if (kk) BAR(); SCHED0();
#pragma unroll
        for (int i = 0; i < 9; ++i) {
            MFMA_T(f.b[kk][i >> 2], f.a[kk][i & 3], c.a[i & 3][i >> 2]);
            if (i == 5) asm volatile("v_mul_f32 %0, %0, %1" : "+v"(va) : "v"(r));
            if (i == 6) asm volatile("v_mul_f32 %0, %0, %1" : "+v"(vb) : "v"(r));
            if (i == 7) asm volatile("v_mul_f32 %0, %1, %1" : "=v"(ta) : "v"(va));
            if (i == 8) asm volatile("v_mul_f32 %0, %1, %1" : "=v"(tb) : "v"(vb));
            SCHED0();
            g_rd(f, kk ^ 1, i);
            SCHED0();
        }
        asm volatile(MF("%0", "%5", "%6") "v_fmaak_f32 %3, %3, %11, 0xc0135761\n\tv_fmaak_f32 %4, %4, %11, 0xc0135761\n\t"
                     MF("%1", "%5", "%7") "v_mul_f32 %3, %3, %9\n\tv_mul_f32 %4, %4, %10\n\t" MF("%2", "%5", "%8") "v_exp_f32 %3, %3"
                     : "+v"(c.a[1][2]), "+v"(c.a[2][2]), "+v"(c.a[3][2]), "+v"(ta), "+v"(tb)
                     : "v"(f.b[kk][2]), "v"(f.a[kk][1]), "v"(f.a[kk][2]), "v"(f.a[kk][3]), "v"(va), "v"(vb), "v"(c1));
        SCHED0(); f.pa[kk][0] = opaque(f.pa[kk][0] + D); SCHED0();
        asm volatile(MF("%0", "%6", "%7") "v_exp_f32 %5, %5\n\t" MF("%1", "%6", "%8") "v_add_f32 %4, 1.0, %4\n\tv_add_f32 %5, 1.0, %5\n\t"
                     MF("%2", "%6", "%9") "v_rcp_f32 %4, %4\n\t" MF("%3", "%6", "%10") "v_rcp_f32 %5, %5"
                     : "+v"(c.a[0][3]), "+v"(c.a[1][3]), "+v"(c.a[2][3]), "+v"(c.a[3][3]), "+v"(ta), "+v"(tb)
                     : "v"(f.b[kk][3]), "v"(f.a[kk][0]), "v"(f.a[kk][1]), "v"(f.a[kk][2]), "v"(f.a[kk][3]));
        SCHED0(); f.pa[kk][1] = opaque(f.pa[kk][1] + D); SCHED0();
        asm volatile(MF("%0", "%4", "%5") "v_mul_f32 %2, %2, %7\n\tv_mul_f32 %3, %3, %8\n\t" MF("%1", "%4", "%6")
                     : "+v"(c.e[0]), "+v"(c.e[1]), "+v"(va), "+v"(vb) : "v"(f.b[kk][4]), "v"(f.a[kk][0]), "v"(f.a[kk][1]), "v"(ta), "v"(tb));
        SCHED0();
        if (kk == 0) { f.pb = opaque(f.pb + DB); f.p8 = opaque(f.p8 + DB); }
        SCHED0();
        z[2 * kk] = va; z[2 * kk + 1] = vb;
    }
}
DEVI void g_fill(GTile& c, const float (&e)[4]) {
#pragma unroll
    for (int m = 0; m < 4; ++m) {
#pragma unroll
        for (int n = 0; n < 4; ++n) c.a[m][n] = pin((f32x4){e[m], e[m], e[m], e[m]});
        if (m < 2) c.e[m] = pin((f32x4){e[m], e[m], e[m], e[m]});
    }
}

#undef STAMP_SEC
#define STAMP_SEC 1
DEVI void gmlp_body(LAS char* lds, int wm, int wn, int pm, int cg, char* z2t, char* y, const u16* __restrict__ bsp STAMP_PARAM) {
    const int mlo = wm * 4 + wn * 2, mhi = wm * 4 + (1 - wn) * 2;
    const float zero4[4] = {0.f, 0.f, 0.f, 0.f};
    GTile z2, z1, acc;
    GFrag f;
    {
        const int lane = lane_id_fresh();
#pragma unroll
        for (int kk = 0; kk < 2; ++kk) { f.pa[kk][0] = lds + mlo * 2048 + kk * 1024 + lane * 16; f.pa[kk][1] = lds + mhi * 2048 + kk * 1024 + lane * 16; }
        f.pb = lds + 16384 + wn * 4096 + lane * 16; f.p8 = lds + 16384 + 8192 + lane * 16; f.slot = 0;
    }
    g_fill(z2, zero4); g_fill(z1, zero4);
#pragma unroll
    for (int i = 0; i < 4; ++i) f.pa[i >> 1][i & 1] = opaque(f.pa[i >> 1][i & 1]);
    f.pb = opaque(f.pb); f.p8 = opaque(f.p8);
    SCHED0();
    WAIT_V(0); BARX();
    BAR();
    BARX();
    STAMP(2);
#pragma unroll
    for (int i = 0; i < 9; ++i) g_rd(f, 0, i);
    g_steps<4, G_STAGE, true>(z2, f, 9);
    STAMP(3);
    {
        const int lane = lane_id_fresh(), r = lane & 15, q = lane >> 4;
        f32x4 ra[4];
#pragma unroll
        for (int m = 0; m < 4; ++m) ra[m] = *(const LAS f32x4*)(lds + G_TAB + ((m < 2 ? mlo : mhi) + (m & 1)) * 64 + q * 16);
        SCHED0();
#pragma unroll
        for (int m = 0; m < 4; ++m) {
            f32x4 s1, s2;
#pragma unroll
            for (int n = 0; n < 4; ++n) {
                const f32x4 g = gelu4(z2.a[m][n] * ra[m]);
                z2.a[m][n] = g;
                if (n == 0) { s1 = g; s2 = g * g; } else { s1 += g; s2 += g * g; }
            }
            if (m < 2) { const f32x4 g = gelu4(z2.e[m] * ra[m]); z2.e[m] = g; s1 += g; s2 += g * g; }
            float u0, u1;
            rowsum16x8(s1, s2, u0, u1);
            if ((r & 3) == 0) *(LAS f32x2*)(lds + G_PART + wn * 1024 + ((m < 2 ? mlo : mhi) + (m & 1)) * 128 + (q * 4 + (r >> 2)) * 8) = (f32x2){u0, u1};
            SCHED0();
        }
    }
    WAIT_L0(); BAR();
    STAMP(4);
    g_steps<4, G_STAGE, false>(z1, f, 4);
    unsigned b0, b1;
    f32x4 c0[4];
    g_step_u<G_STAGE, G_STAGE, false>(z1, f, c0, [&](auto ic) {
        if (ic.value != 18) return;
        STAMP(5);
        const int lane = lane_id_fresh(), q = lane >> 4;
        b0 = __builtin_amdgcn_raw_buffer_load_b32(RSRC(bsp), (lane & 15) * 4, pm * 256 + mlo * 32, 0);
        b1 = __builtin_amdgcn_raw_buffer_load_b32(RSRC(bsp), (lane & 15) * 4, pm * 256 + mhi * 32, 0);
#pragma unroll
        for (int g = 0; g < 2; ++g) {
            const auto rz2 = RSRC(z2t + (size_t)pm * 589824 + cg * 36864 + ((g ? mhi : mlo) >> 1) * 9216);
            const LAS char* tab = lds + G_TAB + (g ? mhi : mlo) * 64 + q * 16;
            LAS char* own = lds + (wm ? G_OWN : G_STAGE + 16384) + (g ^ wn) * 9216 + lane * 16;
            f32x4 ra[2], rb[2];
#pragma unroll
            for (int T = 0; T < 2; ++T) { ra[T] = *(const LAS f32x4*)(tab + 512 + T * 64); rb[T] = *(const LAS f32x4*)(tab + 1024 + T * 64); }
#pragma unroll
            for (int n = 0; n < (g ? 4 : 5); ++n) {
                const f32x4 v0 = (n < 4 ? z2.a[2 * g][n & 3] : z2.e[0]) * ra[0] + rb[0], v1 = (n < 4 ? z2.a[2 * g + 1][n & 3] : z2.e[1]) * ra[1] + rb[1];
                const u32x4 o = {pack2(v0[0], v1[0]), pack2(v0[1], v1[1]), pack2(v0[2], v1[2]), pack2(v0[3], v1[3])};
                ST16(rz2, lane * 16, n < 4 ? wn * 4096 + n * 1024 : 8192, o, 16);
                *(LAS u32x4*)(own + (n < 4 ? wn * 4096 + n * 1024 : 8192)) = o;
            }
        }
        STAMP(6);
    });
    static_for<3>([&](auto sc) {
        constexpr int D = sc.value == 0 ? G_STAGE : sc.value == 1 ? -3 * G_STAGE : 2 * G_STAGE;
        g_step_u<D, D, false>(z1, f, c0, [&](auto ic) { if (ic.value == 17 && sc.value == 1) WAIT_V(0); });
    });
    float ra[4], c1;
    {
        int lane, da[2];
        g_step_u<-G_STAGE, -G_STAGE, false>(z1, f, c0, [&](auto ic) {
            constexpr int i = ic.value;
            if (i == 9) lane = pin(lane_id_fresh());
            if (i == 14 || i == 15) da[i & 1] = pin((lane & 15) * 128 + (((4 * (i & 1) + (lane >> 4)) ^ (lane & 7)) * 16) - lane * 16 - (i & 1) * 1024);
            if (i == 16 || i == 17) f.pa[0][i & 1] = opaque(f.pa[0][i & 1] + da[0]);
            if (i == 27 || i == 28) f.pa[1][i & 1] = opaque(f.pa[1][i & 1] + da[1]);
            if (i >= 29 && i < 33) {
                constexpr int m = (i - 29) & 3;
                const float e = m & 1 ? bfhi(m < 2 ? b0 : b1) : bflo(m < 2 ? b0 : b1);
                c0[m] = pin((f32x4){e, e, e, e});
            }
            if (i == 33) {
#pragma unroll
                for (int m = 0; m < 4; ++m) ra[m] = *(const LAS float*)(lds + G_TAB + ((m < 2 ? mlo : mhi) + (m & 1)) * 64 + (lane & 15) * 4);
                c1 = pin(-0.10294324f);
            }
        });
    }
    STAMP(7);
    g_step_u<2 * G_STAGE, G_OWN - G_STAGE - 16384, true>(acc, f, c0, [](auto) {});
    static_for<18>([&](auto sc) {
        constexpr int st = sc.value, m = st < 16 ? st >> 2 : st & 1;
        constexpr int D = st == 0 ? -3 * G_STAGE : st % 3 == 1 ? 2 * G_STAGE : -G_STAGE;
        g_step_gelu<D, st == 0 ? 16384 - G_OWN : D>(acc, f, st < 16 ? z1.a[m][st & 3] : z1.e[m], ra[m], c1);
    });
    f.slot = 1;
    STAMP(9);
    g_steps<3, -G_STAGE, false>(acc, f, 13);
    STAMP(10);
    const int lane = lane_id_fresh();
#pragma unroll
    for (int g = 0; g < 2; ++g) {
        const auto ry = RSRC(y + (size_t)(pm * 8 + (g ? mhi : mlo)) * 73728 + cg * 4608);
#pragma unroll
        for (int T = 0; T < 2; ++T) {
            const int m = 2 * g + T;
#pragma unroll
            for (int p = 0; p < 2; ++p) {
                const f32x4 a = acc.a[m][2 * p] * z1.a[m][2 * p], c = acc.a[m][2 * p + 1] * z1.a[m][2 * p + 1];
                ST16(ry, lane * 16 + wn * 2048 + p * 1024, T * 73728, ((u32x4){pack2(a[0], a[1]), pack2(a[2], a[3]), pack2(c[0], c[1]), pack2(c[2], c[3])}), 16);
            }
            if (g == 0) {
                const f32x4 a = acc.e[T] * z1.e[T];
                ST8(ry, (lane >> 5) * 256 + (lane & 15) * 16 + ((lane >> 4) & 1) * 8, T * 73728 + 4096, ((u32x2){pack2(a[0], a[1]), pack2(a[2], a[3])}), 16);
            }
        }
    }
}

#define NET_IDS \
    int b = __builtin_amdgcn_readfirstlane(blockIdx.x), wid = wid8 & 3; \
    asm volatile("" : "+s"(b), "+s"(wid));     \
    const int xcd = b & 7, idx = b >> 3; \
    const int pa = (xcd >> 1) * 4 + (idx & 3), hp = (xcd & 1) * 8 + (idx >> 2); \
    const int pm = (xcd >> 2) * 8 + (idx & 7), cg = (xcd & 3) * 4 + (idx >> 3); \
    const int rh = RH_MAP ? xcd * 4 + (idx & 3) : (xcd >> 1) * 8 + (idx & 7), c5 = RH_MAP ? idx >> 2 : (xcd & 1) * 4 + (idx >> 3); \
    const int lead = (idx + (idx >> 3)) & 3; \
    auto kord = [&](int t, int n) { \
        const int k = t + ((int)(0x1F000000u << (((t - lead) & 3) * 4)) >> 28); \
        return (unsigned)k < (unsigned)n ? k : t; \
    }; \
    auto kord5 = [&](int t) { \
        const int k = t + ((int)(0x1F000000u << (((t - (RH_MAP ? c5 : lead)) & 3) * 4)) >> 28); \
        return (unsigned)k < 18u ? k : t; \
    };

typedef short s16x4 __attribute__((ext_vector_type(4)));
DEVI u32x4 tr_frag(const LAS char* p, int four_rows) {
    const u32x2 a = __builtin_bit_cast(u32x2, __builtin_amdgcn_ds_read_tr16_b64_v4i16((LAS s16x4*)p));
    const u32x2 b = __builtin_bit_cast(u32x2, __builtin_amdgcn_ds_read_tr16_b64_v4i16((LAS s16x4*)(p + four_rows)));
    return (u32x4){a.x, a.y, b.x, b.y};
}
DEVI float dot2(unsigned v, unsigned w, float acc) { return __builtin_amdgcn_fdot2_f32_bf16(__builtin_bit_cast(bf16x2_t, v), __builtin_bit_cast(bf16x2_t, w), acc, false); }
DEVI float sum2(unsigned v, float acc) { return dot2(v, 0x3f803f80u, acc); }
struct Sel { unsigned lo, hi, nlo, nhi; };
DEVI Sel make_sel() { return {(unsigned)pin_s(0x00003f80), (unsigned)pin_s(0x3f800000), (unsigned)pin_s(0x0000bf80), (unsigned)pin_s((int)0xbf800000u)}; }
DEVI unsigned centre2(unsigned v, f32x2& e, float mean, const Sel& k) {
    const float a = dot2(k.lo, v, e.x), b = dot2(k.hi, v, e.y);
    const unsigned r = pack2(a, b);
    e = (f32x2){dot2(k.nlo, r, a), dot2(k.nhi, r, b)} - mean;
    return r;
}
typedef u32x4 Retile[9];
template <int PITCH>
DEVI void retile_out_make(Retile& o, const LAS char* img, int lane) {
    const int c = lane & 15, q = lane >> 4, p = lane & 3;
    const LAS char* src = img + (q * 8 + (c >> 2)) * PITCH;
#pragma unroll
    for (int i = 0; i < 8; ++i) o[i] = tr_frag(src + p * 16 + (i >> 2) * 144 + ((i >> 1) & 1) * 64 + (i & 1) * 8, 4 * PITCH);
    o[8] = tr_frag(src + 128 + (p >> 1) * 144 + (p & 1) * 8, 4 * PITCH);
}
DEVI void retile_out_store(const Retile& o, char* dst, int nkc, int kc, int w, int lane) {
    const int c = lane & 15, q = lane >> 4;
    const auto rd = RSRC(dst + (size_t)(2 * w * nkc + kc) * 4608);
#pragma unroll
    for (int i = 0; i < 8; ++i) ST16(rd, lane * 16 + (i & 3) * 1024, (i >> 2) * nkc * 4608, o[i], 16);
    ST16(rd, (c >> 3) * nkc * 4608 + 4096 + q * 128 + (c & 7) * 16, 0, o[8], 16);
}

DEVI float dpp_ror(float v, int n) { return n == 8 ? __builtin_amdgcn_update_dpp(0.f, v, 0x128, 0xf, 0xf, true) : n == 4 ? __builtin_amdgcn_update_dpp(0.f, v, 0x124, 0xf, 0xf, true)
                                          : n == 2 ? __builtin_amdgcn_update_dpp(0.f, v, 0x122, 0xf, 0xf, true) : __builtin_amdgcn_update_dpp(0.f, v, 0x121, 0xf, 0xf, true); }
DEVI unsigned long long epoch_of(const char* ws) {
    SCHED0();
    const unsigned long long ep = *(const __attribute__((address_space(4))) unsigned long long*)(ws + pin_s((int)OFF_EP)) + 1ull;
    asm volatile("" :: "s"(ep));
    return ep;
}
DEVI void prologue_done(LAS char* lds, char* ws, unsigned long long ep, int b, int lane) {
    if (lds_arrive(lds + G_WORD + 8) == 7) ST16(RSRC(ws), lane ? 0x80000000u : (unsigned)OFF_FL + b * 16, 0, ((u32x4){(unsigned)ep, (unsigned)(ep >> 32), 0u, 0u}), 16);
}
DEVI void centre_frag(u32x4& f, f32x2 (&er)[4], float mean, const Sel& k) {
#pragma unroll
    for (int e = 0; e < 4; ++e) f[e] = centre2(f[e], er[e], mean, k);
}
DEVI unsigned long long prologue(LAS char* lds, char* ws, const char* x0, const char* wqkv, const u16* wo, const u16* w_in, int b, int pa, int wid8 PST_PARAM) {
    PST_DECL;
    const int lane = lane_id_fresh(), c = lane & 15, q = lane >> 4, w = wid8 & 3;
    const int jw = w - 1 + 3 * (((wid8 >> 2) ^ (b >> 3)) & 1);
    const bool wjob = b >= 16 && b < 112 && w, ijob = b >= 112 && w, ojob = b < 9;
    const unsigned l0 = (unsigned)(size_t)lds;
    LAS char* img = lds + 2 * K1_STAGE;
    LAS float* red = (LAS float*)(lds + G_PART);
    const int uw = b - 16, head = (uw & 7) * 4 + ((uw >> 3) & 3), part = uw >> 5, hp = head >> 1, odd = head & 1;
    const int c0 = part * 1152 + hp * 72 + odd * 40;
    const int ui = b - 112, st = (ui & ~15) + 2 * (ui & 7) + ((ui >> 3) & 1);
    if (wjob) {
        const auto rs = __builtin_amdgcn_make_buffer_rsrc((void*)wqkv, 0, 576 * 3456, 0x00020000);
        unsigned v[5];
#pragma unroll
        for (int i = 0; i < 5; ++i) v[i] = (((i * 64 + lane) * 21846) >> 16) * (3456 - 48) + lane * 16 + jw * 96 * 3456 + (c0 & ~15);
        dma4<false>(l0 + 2 * K1_STAGE + jw * 6144, rs, 0, v[0], v[1], v[2], v[3]);
        dma1(l0 + 2 * K1_STAGE + jw * 6144 + 4096, rs, 0, lane < 32 ? v[4] + 4096 : 0x80000000u);
    }
    if (ijob) {
        if (I_SLEEP) __builtin_amdgcn_s_sleep(I_SLEEP);
        const unsigned v = (lane >> 2) * 9216 + (lane & 3) * 16, d = 16 * 9216 - 1024;
        const auto rs = RSRC((const char*)(w_in + st * 32) + (size_t)jw * 96 * 9216);
        dma4<false>(l0 + 2 * K1_STAGE + jw * 6144, rs, 0, v, v + d, v + 2 * d, v + 3 * d);
        dma2<false>(l0 + 2 * K1_STAGE + jw * 6144 + 4096, rs, 64 * 9216, v, v + d);
    }
    if (ojob) {
        const auto rs = RSRC(wo + w * 144);
#pragma unroll
        for (int j = 0; j < 9; ++j) {
            const int i = lane + 64 * j, rl = (i * 3641) >> 16, kp = (b * 2 + (wid8 >> 2)) * 32 + rl;
            const int row = kp < 512 ? 18 * (kp >> 4) + (kp & 15) + 2 * ((kp >> 4) & 1) : 36 * ((kp - 512) >> 2) + 16 + (kp & 3);
            dma1(l0 + 2 * K1_STAGE + wid8 * 9216 + j * 1024, rs, 0, (unsigned)(row * 1152 + (i - rl * 18) * 16));
        }
    }
    if (wid8 == 4) { lds_word_set(lds + G_WORD + c * 4, 0u); WAIT_L0(); }
    BAR();
    if (W_ALONE && wid8 == 4 && b >= 16 && b < 112) lds_count_wait(lds + G_WORD + 12, W_ALONE);
    if (wid8 == 4) {
        AttnX ax = attn_x(x0, x0, 0, pa, 0, lane);
#pragma unroll
        for (int s = 0; s < 2; ++s)
#pragma unroll
            for (int i = 0; i < 4; ++i) { ax.oA = (pa * 8 + 2 * i) * 18432; attn_issueA(lds, ax, i, s, s); }
    }
    u32x4 xv[9];
    if (wid8 == 0) {
        const unsigned v = (lane >> 3) * 2304 + (lane & 7) * 16;
        const auto rx = RSRC(x0 + (size_t)slot2row(b * 8) * 1152);
#pragma unroll
        for (int i = 0; i < 9; ++i) asm volatile("s_nop 4\n\tbuffer_load_dwordx4 %0, %1, %2, 0 offen offset:%3" : "+v"(xv[i]) : "v"(v), "s"(rx), "i"(i * 128) : "memory");
        const auto rw = RSRC(ws);
        const u32x4 ones = {~0u, ~0u, ~0u, ~0u}, zero = {0u, 0u, 0u, 0u};
        const unsigned vo = lane * 16;
        ST16(rw, vo, (unsigned)OFF_ZST + b * 2048, ones, 16); ST16(rw, vo, (unsigned)OFF_ZST + b * 2048 + 1024, ones, 16);
        ST16(rw, vo, (unsigned)OFF_CNT + b * 2560, zero, 16); ST16(rw, vo, (unsigned)OFF_CNT + b * 2560 + 1024, zero, 16);
        ST16(rw, vo, lane < 32 ? (unsigned)OFF_CNT + b * 2560 + 2048 : (unsigned)(OFF_L0 + L_STB) + b * 512 - 512, zero, 16);
        ST16(rw, vo, (unsigned)(OFF_L0 + L_O) + (b >> 1) * 18432 + 16384 + (b & 1) * 1024, zero, 16);
        ST16(rw, b < 8 ? vo : 0x80000000u, (unsigned)OFF_FL2 + b * 1024, zero, 16);
    }
    PST(1);
    const unsigned long long ep = epoch_of(ws);
    const Sel k = make_sel();
    u32x4 f[6];
    unsigned vd, vd2;
    __amdgpu_buffer_rsrc_t rd, rd2;
    if (wjob || ijob) {
        const LAS char *s0, *s1;
        if (ijob) {
            int o[2];
#pragma unroll
            for (int h = 0; h < 2; ++h) {
                const int u = st * 2 + h, half = u >= 144, g = u - half * 144, cg = g / 9, j = g - cg * 9, p = lane & 3;
                o[h] = h * 32 + (j < 8 ? (p & 1) * 16 + (p >> 1) * 8 : p * 8);
                (h ? rd2 : rd) = RSRC(ws + OFF_TWIN + (size_t)cg * (18 * 18432) + (half * 18 + jw * 3) * 9216);
                (h ? vd2 : vd) = q * 256 + (j < 8 ? ((j & ~1) + (c >> 3)) * 1024 + ((j & 1) * 8 + (c & 7)) * 16 : 8192 + c * 16);
            }
            s0 = img + jw * 6144 + (q * 8 + (c >> 2)) * 64 + o[0]; s1 = s0 + (o[1] - o[0]);
        } else {
            s0 = img + jw * 6144 + (q * 8 + (c >> 2)) * 48 + (c0 & 15); s1 = s0 + 32 - odd * 8 + min(c & 3, 2) * 1536; s0 += (c & 3) * 8;
            rd = RSRC(ws + OFF_TWQKV + (size_t)(hp * 18 + jw * 3) * 6144 + (3 * odd + part) * 1024); vd = lane * 16;
            rd2 = RSRC(ws + OFF_TWT + (size_t)((hp * 3 + part) * 18 + jw * 3) * 256); vd2 = c < 12 && !odd ? (unsigned)((c >> 2) * 256 + q * 64 + (c & 3) * 16) : 0x80000000u;
        }
        s0 = opaque(s0); s1 = opaque(s1); vd = pin(vd); vd2 = pin(vd2);
        asm volatile("" :: "s"(rd), "s"(rd2));
        WAIT_V(0);
        PST(2);
        float sm[4] = {0.f, 0.f, 0.f, 0.f};
        if (ijob) {
#pragma unroll
            for (int i = 0; i < 6; ++i) f[i] = tr_frag((i < 3 ? s0 : s1) + (i % 3) * 2048, 256);
#pragma unroll
            for (int e = 0; e < 24; ++e) sm[(e >= 12) * 2 + (e & 1)] = sum2(f[e >> 2][e & 3], sm[(e >= 12) * 2 + (e & 1)]);
            sm[2] += sm[3];
        } else {
#pragma unroll
            for (int i = 0; i < 4; ++i) f[i] = tr_frag(i < 3 ? s0 + i * 1536 : s1, 192);
#pragma unroll
            for (int e = 0; e < 16; ++e) sm[(e >= 12) * 2 + (e & 1)] = sum2(f[e >> 2][e & 3], sm[(e >= 12) * 2 + (e & 1)]);
            sm[2] = c < 12 ? sm[2] + sm[3] : 0.f;
            sm[2] += dpp_ror(sm[2], 4); sm[2] += dpp_ror(sm[2], 8);
        }
        auto x = __builtin_amdgcn_permlane16_swap(__float_as_uint(sm[0] + sm[1]), __float_as_uint(sm[2]), false, false);
        red[jw * 64 + lane] = __uint_as_float(x[0]) + __uint_as_float(x[1]);
        if (lds_arrive(lds + G_WORD + 12) < 5) lds_count_wait(lds + G_WORD + 12, 6);
        PST(3);
        float t = 0.f;
#pragma unroll
        for (int i = 0; i < 6; ++i) t += red[i * 64 + lane];
        x = __builtin_amdgcn_permlane32_swap(__float_as_uint(t), __float_as_uint(t), false, false);
        t = (__uint_as_float(x[0]) + __uint_as_float(x[1])) * (1.0f / 576.0f);
        auto y = __builtin_amdgcn_permlane16_swap(__float_as_uint(t), __float_as_uint(t), false, false);
        const float m0 = __uint_as_float(y[0]), m1 = __uint_as_float(y[1]);
        f32x2 e0[4] = {{-m0, -m0}, {-m0, -m0}, {-m0, -m0}, {-m0, -m0}}, e1[4] = {{-m1, -m1}, {-m1, -m1}, {-m1, -m1}, {-m1, -m1}};
        const unsigned dk = ijob ? 9216 : 6144;
#pragma unroll
        for (int i = 0; i < 3; ++i) { centre_frag(f[i], e0, m0, k); ST16(rd, vd, i * dk, f[i], 16); }
#pragma unroll
        for (int i = 0; i < 3; ++i) if (i == 0 || ijob) { centre_frag(f[3 + i], e1, m1, k); ST16(rd2, vd2, i * 9216, f[3 + i], 16); }
        PST(4);
        if (wjob) {
            if (W_ALONE > 6) lds_arrive(lds + G_WORD + 12);
            const unsigned fo = pin((unsigned)OFF_FW + ((lane >> 3) * 16 + hp) * 1024 + (3 * odd + part) * 128 + (lane & 7) * 16);
            WAIT_V(0);
            PST(5);
            if (lds_arrive(lds + G_WORD + 4) == 5) {
                const u32x4 e4 = {(unsigned)ep, (unsigned)(ep >> 32), (unsigned)ep, (unsigned)(ep >> 32)};
                ST16(RSRC(ws), fo, 0, e4, 16); ST16(RSRC(ws), fo + 131072, 0, e4, 16);
            }
        }
    }
    if (wid8 == 0) {
        WAIT_V(7);
        asm volatile("" : "+v"(xv[0]), "+v"(xv[1]), "+v"(xv[2]), "+v"(xv[3]), "+v"(xv[4]), "+v"(xv[5]), "+v"(xv[6]), "+v"(xv[7]), "+v"(xv[8]));
        float s = 0.f, q2 = 0.f;
#pragma unroll
        for (int e = 0; e < 36; ++e) { s = sum2(xv[e >> 2][e & 3], s); q2 = dot2(xv[e >> 2][e & 3], xv[e >> 2][e & 3], q2); }
        ST8(RSRC(ws + OFF_L0 + L_STA + (size_t)b * 512), lane * 8, 0, ((u32x2){__float_as_uint(s), __float_as_uint(q2)}), 16);
    }
    if (ojob) {
        if (wid8) WAIT_V(0);
        Retile rt;
        retile_out_make<288>(rt, img + wid8 * 9216, lane); retile_out_store(rt, ws + OFF_TWO, 18, b * 2 + (wid8 >> 2), w, lane);
    }
    PST(7);
    PST_DUMP(wid8 == 0 || wid8 == 4 || wid8 == 5, wid8 ? wid8 : 2);
    return ep;
}

__global__ __launch_bounds__(512, 2) TRACE_ATTR void k_net(char* ws, const u16* wsp, const u16* bsp, char* out, const char* x0, const char* wqkv, const u16* wo,
                                                             const u16* w_in, const u16* w_out DBG_ARG) {
    extern __shared__ __attribute__((aligned(16))) char smem[];
    LAS char* lds = (LAS char*)smem;
    asm volatile("gm_code_begin:");
    if (blockIdx.x < 16) {
        unsigned long long pc; int d0, len;
        asm volatile("s_getpc_b64 %0\n1:\n\ts_mov_b32 %1, gm_code_begin-1b\n\ts_mov_b32 %2, gm_code_end1-gm_code_begin\n\ts_max_i32 %2, %2, gm_code_end2-gm_code_begin"
                     : "=s"(pc), "=s"(d0), "=s"(len) :: "scc");
        const auto rs = __builtin_amdgcn_make_buffer_rsrc((void*)((pc + d0) & ~15ull), 0, len, 0x00020000);
        dma1((unsigned)(size_t)lds + G_OWN, rs, 0, (threadIdx.x * 2 + (blockIdx.x >> 3)) * 128);
    }
    asm volatile("" :: "s"(ws), "s"(wsp), "s"(bsp), "s"(out), "s"(x0), "s"(wqkv), "s"(wo), "s"(w_in), "s"(w_out));
    ws = own_s(ws); wsp = own_s(wsp); bsp = own_s(bsp); out = own_s(out); x0 = own_s(x0); wqkv = own_s(wqkv); wo = own_s(wo); w_in = own_s(w_in); w_out = own_s(w_out);
    const char *twin = ws + OFF_TWIN, *twout = ws + OFF_TWOUT;
    STAMP_DECL;
    const int wid8 = __builtin_amdgcn_readfirstlane(threadIdx.x >> 6);
    unsigned long long ep;
    { NET_IDS ep = prologue(lds, ws, x0, wqkv, wo, w_in, b, pa, wid8 PST_FWD); }
    if (wid8 >= 4) {
        u32x4 wr[5][9];
#pragma unroll 1
      for (int layer = 0; layer < NET_LAYERS; ++layer) {
        NET_IDS
        TRACE_BEGIN;
        char* wl = ws + OFF_L0 + layer * SZ_L;
        const unsigned fl = layer * F_LAYER;
        const char *xin = wl + L_XB, *st = wl + L_STB, *z2t = wl + (layer ? L_Y : L_Z2T);
        unsigned long long* zst = (unsigned long long*)(ws + OFF_ZST + layer * SZ_ZST);
        const int lane = lane_id_fresh(), l3 = lane >> 3;
        const unsigned vo = lane * 16;
        const unsigned l0 = (unsigned)(size_t)lds, lA = l0 + wid * 4096, lB = l0 + 16384 + wid * 4608;
        const auto rX = RSRC(xin + (size_t)(pm * 8 + 2 * wid) * 18432), rI = RSRC(twin + (size_t)cg * (18 * 18432) + wid * 4608);
        auto issue1B = [&](int g, int slot) { const int k = kord(g < 9 ? g : (g < 13 ? g - 4 : g - 13), 9); dma5<true>(lB + slot * G_STAGE, rI, (g < 9 ? 9 + k : k) * 18432, vo); };
        auto issue1A = [&](int g, int slot) { dma4<true>(lA + slot * G_STAGE, rX, kord(g < 9 ? g : g - 13, 9) * 2048, vo, vo, vo + 16384, vo + 16384); };
        auto issue1 = [&](int g, int slot) { issue1B(g, slot); issue1A(g, slot); };
        const auto rA = RSRC((const char*)wsp + (size_t)(pm * 128 + wid * 32) * 4096), rB = RSRC(z2t + cg * 36864 + wid * 4608);
        const unsigned voA = (unsigned)(2 * l3 * 4096 + ((lane & 7) ^ l3) * 16);
        auto issueB = [&](int k, int slot) { dma5<true>(lB + slot * G_STAGE, rB, (k >> 1) * 589824 + (k & 1) * 18432, vo); };
        const auto rW = RSRC(twout + (size_t)c5 * (72 * 4608));
        auto issue5 = [&](int t) { dma5<true>(l0 + ((t + 6) & 7) * K5_STAGE + wid * 4608, rW, kord5(t) * K5_STAGE + wid * 4608, vo); };
        auto load5 = [&](int p, int i) {
            const int t0 = p ? 2 * p + 6 : 4, ow = kord5(t0 + i) * K5_STAGE + wid * 4096;
            asm volatile("s_nop 4\n\tbuffer_load_dwordx4 %0, %4, %5, %6 offen sc1\n\tbuffer_load_dwordx4 %1, %4, %5, %6 offen offset:1024 sc1\n\t"
                         "buffer_load_dwordx4 %2, %4, %5, %6 offen offset:2048 sc1\n\tbuffer_load_dwordx4 %3, %4, %5, %6 offen offset:3072 sc1"
                         : "+v"(wr[p][4 * i]), "+v"(wr[p][4 * i + 1]), "+v"(wr[p][4 * i + 2]), "+v"(wr[p][4 * i + 3]) : "v"(vo), "s"(rW), "s"(ow) : "memory");
            if (i) {
                const int oe = kord5(t0 + (wid >> 1)) * K5_STAGE + 16384 + (wid & 1) * 1024;
                asm volatile("s_nop 4\n\tbuffer_load_dwordx4 %0, %1, %2, %3 offen sc1" : "+v"(wr[p][8]) : "v"(vo), "s"(rW), "s"(oe) : "memory");
            }
        };
        auto put5 = [&](int w0, int w1) {
#pragma unroll
            for (int w = w0; w < w1; ++w) {
                const int p = w / 9, i = w % 9 == 4 ? -1 : w % 9 - (w % 9 > 4);
                LAS char* sl = lds + (((p ? 2 * p + 6 : 4) + 6) & 7) * K5_STAGE + lane * 16;
                if (i < 0) *(LAS u32x4*)(sl + (wid >> 1) * K5_STAGE + 16384 + (wid & 1) * 1024) = wr[p][8];
                else *(LAS u32x4*)(sl + (i >> 2) * K5_STAGE + wid * 4096 + (i & 3) * 1024) = wr[p][i];
            }
            WAIT_L0();
        };
        const int row = wid * 32 + (lane & 31);
        auto step1 = [&](int t) { BAR(); issue1(t + 4, t & 3); };
        const char* ps;
        unsigned fd, fd2;
        {
            unsigned fo, fi;
            const int ojob = layer || !wid ? 288 : b + (wid - 1) * 256, oimg = wid == 1 ? 2048 : G_STAGE;
            attn_producer(lds, ws, x0, wl, fl, ep, layer, pa, hp, wid, [&] { prologue_done(lds, ws, ep, b, lane); }, [&](int part) {
                if (part == 0 && ojob < 288) {
                    const auto rs = RSRC(w_out + (size_t)(ojob >> 2) * 32 * 576 + (ojob & 3) * 144);
#pragma unroll
                    for (int j = 0; j < 9; ++j) { const int i = lane + 64 * j; dma1(l0 + oimg + j * 1024, rs, 0, (unsigned)(((i * 3641) >> 16) * (1152 - 288) + i * 16)); }
                }
                if (part == 1) {
                    issue1B(0, 0); issue1B(1, 1);
                    fo = flag_out(fl + F_X1, 256, pa, hp, lane); fi = flag_in(fl + F_X1, 256, pm * 16 + cg, 16, lane);
                    ps = pin(st + (size_t)(pm * 128 + wid * 32) * 64 + lane * 16);
                    fd = pin((unsigned)OFF_FL2 + wid * 1024 + vo); fd2 = pin((unsigned)OFF_FL2 + 4096 + (lane & 31) * 16);
                }
            });
            if (WSP_PF && layer == 0 && wid >= 2 && ojob >= 288) {
                __builtin_amdgcn_s_sleep(OJOB_SLEEP);
#pragma unroll
                for (int i = 0; i < 4; ++i)
                    asm volatile("s_nop 4\n\tbuffer_load_dwordx4 %0, %4, %5, %6 offen\n\tbuffer_load_dwordx4 %1, %4, %5, %6 offen offset:1024\n\t"
                                 "buffer_load_dwordx4 %2, %4, %5, %6 offen offset:2048\n\tbuffer_load_dwordx4 %3, %4, %5, %6 offen offset:3072"
                                 : "+v"(wr[0][0]), "+v"(wr[0][1]), "+v"(wr[0][2]), "+v"(wr[0][3]) : "v"(vo), "s"(RSRC(wsp)), "s"(b * 32768 + (wid - 2) * 16384 + i * 4096) : "memory");
            }
            if (ojob < 288) {
                Retile& rt = wr[1];
                retile_out_make<288>(rt, lds + oimg, lane);
                __builtin_amdgcn_s_sleep(OJOB_SLEEP);
                retile_out_store(rt, ws + OFF_TWOUT, 72, ojob >> 2, ojob & 3, lane);
                WAIT_V(0);
                flag_raise(ws, lane ? 0x80000000u : (unsigned)OFF_FL2 + ojob * 16);
            }
            BARX();
            if (wid == 0) flag_raise(ws, fo);
            issue1B(2, 2); issue1B(3, 3);
            asm volatile("" :: "s"(rX));
            if (wid == 0) flags_wait<X1_FIRST>(ws, fi);
            BARX();
        }
        issue1A(0, 0); issue1A(1, 1); issue1A(2, 2);
        WAIT_V(8); BAR();
        issue1A(3, 3);
        {
            f32x4 sp[3];
            WAIT_V(8); step1(0);
            WAIT_V(13); step1(1);
            WAIT_V(18); step1(2);
            unsigned got[2];
            if (layer == 0) asm volatile("s_nop 4\n\tbuffer_load_dword %0, %2, %4, 0 offen sc1\n\tbuffer_load_dword %1, %3, %4, 0 offen sc1" : "+v"(got[0]), "+v"(got[1]) : "v"(fd), "v"(fd2), "s"(RSRC(ws)) : "memory");
            WAIT_IF(layer, 18, 20); step1(3);
            WAIT_IF(layer, 18, 20); step1(4);
            WAIT_V(18);
            if (layer == 0) {
                asm volatile("" : "+v"(got[0]), "+v"(got[1]));
                if (__any((got[0] & got[1]) == 0u)) { flags_wait<0>(ws, fd); flags_wait<0>(ws, fd2); }
            }
            BAR(); issue1B(9, 1); side_load(sp, ps, ps); if (layer == 0) load5(3, 0);
            WAIT_IF(layer, 17, 21); BAR(); issue1B(10, 2); if (layer == 0) load5(3, 1);
            WAIT_IF(layer, 5, 14); BARX();
            side_fence(sp);
#pragma unroll
            for (int i = 0; i < 2; ++i) lds_word_set(lds + G_TAB + (wid * 32 + i * 16 + (lane >> 2)) * 4, __float_as_uint(ln_rstd(sp[i])));
            issue1B(11, 3);
            WAIT_L0();
        }
        BAR(); issue1B(12, 0);
        if (layer == 1) {
            const auto rd = RSRC(out + (size_t)b * 9216 + wid * 2304);
            ST16(rd, vo, 0, ((u32x4){0u, 0u, 0u, 0u}), 16); ST16(rd, vo, 1024, ((u32x4){0u, 0u, 0u, 0u}), 16);
            if (lane < 16) ST16(rd, vo, 2048, ((u32x4){0u, 0u, 0u, 0u}), 16);
        }
        if (layer == 0) {
#pragma unroll
            for (int p = 0; p < 6; ++p) load5(p >> 1, p & 1);
        }
        WAIT_V(0);
        __builtin_amdgcn_s_setprio(3);
        const char* src = pin((const char*)zst + (size_t)(pm * 16 + (lane >> 4) * 4) * 1024 + wid * 256 + (lane & 15) * 16);
        const unsigned tw = pin(l0 + G_TAB + 512 + (lane >> 5) * 512 + (wid * 32 + (lane & 15) * 2 + ((lane >> 4) & 1)) * 4);
        const unsigned fz = flag_out(fl + F_Z2, 256, cg, pm, lane), fzi = pin(fl + F_Z2 + (cg * 16 + pm) * 256 + ((lane ^ pm) & 8) * 16 + (lane & 7) * 16);
        const unsigned vA[4] = {pin(voA), pin(voA + 16 * 4096 - 1024), pin(voA + 4096 - 2048), pin(voA + 17 * 4096 - 3072)};
        auto spA = [&](int so, unsigned la) { dma4<false>(la, rA, so, vA[0], vA[1], vA[2], vA[3]); };
        auto issueA = [&](int k, int slot) { spA(k * 128, lA + slot * G_STAGE); };
        {
            const LAS char* pp = opaque(lds + G_PART + row * 8);
            const auto rs = RSRC(zst + (size_t)(pm * 16 + cg) * 128);
            const unsigned vp = pin(lane < 32 ? (unsigned)row * 8u : 0x80000000u);
            asm volatile("" :: "s"(rs));
            BARX();
            const f32x2 a = *(const LAS f32x2*)pp, c = *(const LAS f32x2*)(pp + 1024);
            ST8(rs, vp, 0, ((u32x2){__float_as_uint(add1(a.x, c.x)), min(__float_as_uint(add1(a.y, c.y)), 0xfffffffeu)}), 16);
            SCHED0();
        }
        __builtin_amdgcn_s_setprio(0);
        {
            f32x4 g[4];
            float sm[4];
            auto gather = [&] {
                asm volatile("global_load_dwordx4 %0, %4, off sc1\n\tglobal_load_dwordx4 %1, %4, off offset:1024 sc1\n\t"
                             "global_load_dwordx4 %2, %4, off offset:2048 sc1\n\tglobal_load_dwordx4 %3, %4, off offset:3072 sc1"
                             : "=&v"(g[0]), "=&v"(g[1]), "=&v"(g[2]), "=&v"(g[3]) : "v"(src) : "memory");
            };
            auto total = [&] {
                asm volatile("" : "+v"(g[0]), "+v"(g[1]), "+v"(g[2]), "+v"(g[3]));
#pragma unroll
                for (int i = 0; i < 4; ++i) sm[i] = add1(add1(g[0][i], g[1][i]), add1(g[2][i], g[3][i]));
            };
            step1(9); step1(10);
            BARX(); gather(); issue1(15, 3);
            WAIT_V(22); BARX();
            __builtin_amdgcn_s_setprio(3);
            WAIT_V(9);
            total();
            if (__any(__builtin_isunordered(sm[1], sm[3]))) {
#pragma unroll 1
                for (unsigned spin = 0; spin < 400000u; ++spin) {
                    bool ok = true;
#pragma unroll
                    for (int i = 0; i < 4; ++i) ok &= __float_as_uint(g[i][1]) != 0xffffffffu && __float_as_uint(g[i][3]) != 0xffffffffu;
                    if (__all(ok)) break;
                    gather();
                    WAIT_V(0);
                    total();
                }
            }
            xq4_sum(sm);
            const bool odd = lane & 16;
            float mean;
            const float rs = rstd_of(odd ? sm[2] : sm[0], odd ? sm[3] : sm[1], 1.0f / 2304.0f, mean);
            asm volatile("ds_write_b32 %0, %1" :: "v"(tw), "v"(lane < 32 ? rs : -mean * rs) : "memory");
            __builtin_amdgcn_s_setprio(0);
            issue1B(16, 0);
            WAIT_L0();
        }
        BARX(); issue1A(16, 0); issueA(2 * pm, 1);
        auto kord2 = [&](int t) { const int j = kord(t - 2, 30); return ((j < 2 * (pm & 7) ? j : j + 2) + 2 * (pm & 8)) & 31; };
        unsigned seen;
        auto look = [&] { if (wid == 0) asm volatile("s_nop 4\n\tbuffer_load_dword %0, %1, %2, 0 offen sc1" : "+v"(seen) : "v"(fzi), "s"(RSRC(ws)) : "memory"); };
        auto need = [&](int n) {
            if (wid == 0) {
                asm volatile("" : "+v"(seen));
                if (__ballot(seen == 0u) & ((1ull << n) - 1ull)) flags_wait<0>(ws, lane < n ? fzi : fl + F_Z2 + (cg * 16 + pm) * 256 + pm * 16);
            }
        };
        auto issue = [&](int t, int sl) { const int k = kord2(t); issueB(k, sl); issueA(k, sl); };
        auto steps2 = [&](int t0, int t1) {
            int sl = (2 * t0 + 2) % 3 * G_STAGE, k = kord2(t0 + 3), ob = (k >> 1) * 589824 + (k & 1) * 18432;
            LOOP_ALIGN();
#pragma unroll 1
            for (int t = t0; t < t1; ++t) {
                WAIT_V(9); BARX();
                dma5<true>(lB + sl, rB, ob, vo);
                const int oa = pin_s(k * 128);
                const unsigned la = pin_s(lA + sl);
                k = pin_s(kord2(t + 4)); ob = pin_s((k >> 1) * 589824 + (k & 1) * 18432);
                sl = pin_s(sl == 0 ? 2 * G_STAGE : sl - G_STAGE);
                spA(oa, la);
            }
        };
        WAIT_V(13); BAR(); issue1(17, 2);
        WAIT_V(13); BARX();
        if (wid == 0) flag_raise(ws, fz);
        issueA(2 * pm + 1, 3); if (layer == 0) load5(4, 0);
        if (wid == 0) WAIT_IF(layer, 5, 9); else WAIT_IF(layer, 4, 8);
        BAR(); issueA(kord2(2), 0); if (layer == 0) load5(4, 1);
        BAR(); look(); issueA(kord2(3), 2);
        WAIT_V(4); need(3); BARX(); issueB(kord2(2), 0); issueB(kord2(3), 2); look();
        WAIT_IF(wid == 0, 6, 5); BAR(); issue(4, 1);
        WAIT_V(9); need(8);
        steps2(2, 11); look();
        WAIT_IF(wid == 0, 10, 9); BAR(); issue(14, 0);
        WAIT_V(9); need(16);
        steps2(12, 29);
        WAIT_V(9); BAR(); issue5(0); issue5(1); issue5(2);
        WAIT_V(15); BAR(); issue5(6); issue5(7);
        BAR(); issue5(3); put5(0, 9);
        unsigned vb;
        {
            vb = attn_vb(wid, lane);
            const unsigned fo = flag_out(fl + F_Y, 256, pm, cg, lane), fi = flag_in(fl + F_Y, 256, rh * 8 + c5, 16, lane);
            __builtin_amdgcn_s_setprio(3);
            WAIT_V(0); BARX();
            if (wid == 0) { flag_raise(ws, fo); flags_wait<Y_FIRST>(ws, fi); }
            __builtin_amdgcn_s_setprio(0);
            BARX();
        }
        put5(9, 13);
        BAR(); put5(13, 18);
        BAR(); put5(18, 24);
        BAR(); put5(24, 30);
        BAR(); put5(30, 36);
        BAR(); issue5(16); put5(36, 41);
        BAR(); issue5(17); put5(41, 45);
        WAIT_V(5); BAR();
        WAIT_V(0); BAR();
        if (layer == 0) { attn_issueB(lds, ws, hp, wid, vb, 0, 0); attn_issueB(lds, ws, hp, wid, vb, 1, 1); }
        BAR();
        if (layer == 0) {
            __builtin_amdgcn_s_setprio(3);
            const unsigned fo = flag_out(F_X2, 512, (rh >> 1) + wid, wid ? 16 + c5 : (rh & 1) * 8 + c5, wid == 0 || (wid == 1 && (rh & 1) && rh < 31) ? lane : 16);
            BARX();
            if (wid < 2) flag_raise(ws, fo);
        }
        TRACE_DUMP;
      }
        if (blockIdx.x == 0 && wid8 == 4) ST8(RSRC(ws), lane_id_fresh() ? 0x80000000u : (unsigned)OFF_EP, 0, ((u32x2){(unsigned)ep, (unsigned)(ep >> 32)}), 16);
        asm volatile("gm_code_end1:");
        return;
    }
#pragma unroll 1
  for (int layer = 0; layer < NET_LAYERS; ++layer) {
    NET_IDS
    TRACE_BEGIN;
    char* wl = ws + OFF_L0 + layer * SZ_L;
    const unsigned fl = layer * F_LAYER;
    const char* xin = wl + L_XB;
    char* y = wl + (layer ? L_Z2T : L_Y);
#undef STAMP_SEC
#define STAMP_SEC 0
    {
    STAMP_BEGIN;
    attn_consumer(lds, x0, wl, layer, pa, hp, wid, [&] { WAIT_V(0); prologue_done(lds, ws, ep, b, lane_id_fresh()); } STAMP_FWD);
    STAMP_END(1 + 5 * layer);
    }
#undef STAMP_SEC
#define STAMP_SEC 1
    STAMP_BEGIN;
    gmlp_body(lds, wid & 1, wid >> 1, pm, cg, wl + (layer ? L_Y : L_Z2T), y, bsp STAMP_FWD);
    {
        WAIT_V(0);
        BARX();
        STAMP(11);
        const int lane = lane_id_fresh(), r = lane & 15, q = lane >> 4;
        const int wk = wid >> 1, rt = rh * 4 + (wid & 1) * 2 + wk;
        const Res rx = proj_residual_load(xin, rt, c5, lane);
        const LAS char *pw[3], *ph[3];
#pragma unroll
        for (int i = 0; i < 3; ++i) {
            pw[i] = opaque(lds + 3 * i * K5_STAGE + wk * 4608 + lane * 16);
            ph[i] = opaque(lds + 3 * i * K5_STAGE + wk * 4608 + 4096 + q * 128 + (r & 7) * 16);
        }
        const auto rY = RSRC(y + (size_t)(rt & ~1) * 73728 + wk * 1024);
        const unsigned vy[2] = {pin((unsigned)(wk * 73728 + lane * 16)), pin((unsigned)((1 - wk) * 73728 + lane * 16))};
        int dk[4];
#pragma unroll
        for (int i = 0; i < 4; ++i) dk[i] = pin_s((kord5(4 + i) - 4 - i) * 4096);
        auto yoff = [&](int t) { return pin_s(t == 0 || t == 17 ? kord5(t) * 4096 : t * 4096 + dk[t & 3]); };
        bf16x8 af[5][4], wf[4][5];
        auto lda = [&](int so, int i) { return __builtin_bit_cast(bf16x8, __builtin_amdgcn_raw_buffer_load_b128(rY, vy[i & 1] + (i >> 1) * 2048, so, 16)); };
        auto rdw = [&](int t, int c, int n) {
            const int sl = (t + 6) & 7, o = (sl % 3) * K5_STAGE + c * 9216;
            wf[2 * (t & 1) + c][n] = n < 4 ? LDS_FRAG(pw[sl / 3] + o + n * 1024) : LDS_FRAG(ph[sl / 3] + o);
        };
#pragma unroll
        for (int n = 0; n < 10; ++n) rdw(0, n / 5, n % 5);
        int so[18];
#pragma unroll
        for (int i = 0; i < 5; ++i) so[i] = yoff(i);
        so[17] = yoff(17);
        f32x4 v[5], u[5];
        proj_residual(v, rx);
#pragma unroll
        for (int n = 0; n < 5; ++n) v[n] = pin(v[n]);
        LAS char* xo = (LAS char*)opaque(lds + wid * 4608 + lane * 16);
        LAS char* xo4 = (LAS char*)opaque(lds + wid * 4608 + 4096 + (lane & 31) * 16);
        const LAS char* xi = opaque(lds + (wid ^ 2) * 4608 + lane * 16);
        const LAS char* xi4 = opaque(lds + (wid ^ 2) * 4608 + 4096 + (lane & 31) * 16);
        const ProjOut po = layer ? proj_out_final(out, rt, c5, lane) : proj_out(wl + SZ_L + L_XA, wl + SZ_L + L_STA, rt, c5, lane);
        asm volatile("" :: "s"(rY));
        WAIT_L(0);
        BARX();
        STAMP(12);
#pragma unroll
        for (int i = 0; i < K5_PRE; ++i) { af[i >> 2][i & 3] = lda(so[i >> 2], i & 3); SCHED0(); if (i >= PACE0 && i + 1 < K5_PRE) PACE; SCHED0(); }
        static_for<18>([&](auto tc) {
            constexpr int t = tc.value;
            bf16x8 (&a)[4] = af[t % 5];
#pragma unroll
            for (int h = 0; h < 4; ++h) {
                const int c = t < 17 ? h >> 1 : h & 1, m = t < 17 ? h & 1 : 1 - (h >> 1);
                if (t < 17 ? m == 0 : h < 2) { if (t == 17 && h == 1) WAIT_L(0); else WAIT_L(5); }
#pragma unroll
                for (int n = 0; n < 5; ++n) {
                    f32x4& e = m ? u[n] : v[n];
                    if (t == 0 && h == 1) MFMA_0(wf[0][n], a[1], e); else MFMA_T(wf[2 * (t & 1) + c][n], a[2 * c + m], e);
                    SCHED0();
                    if (t < 17 && m == 0) rdw(t + 1, c, n);
                    if (t < 14 && m == 1 && !(n & 1)) { const int i = y_load_at(6 * t + 3 * c + n / 2); if (i >= 0) af[(i >> 2) % 5][i & 3] = lda(so[i >> 2], i & 3); }
                    if (t < 12 && h == 3 && n == 3) so[t + 5] = yoff(t + 5);
                    if (t == 17 && h * 5 + n >= 13 && h * 5 + n < 18) { const int i = h * 5 + n - 13; *(LAS f32x4*)(i < 4 ? xo + i * 1024 : xo4) = u[i]; }
                    SCHED0();
                }
            }
            if ((t & 1) && t < 17) BAR();
            SCHED0();
        });
        {
            WAIT_L0(); BARX();
#pragma unroll
            for (int n = 0; n < 5; ++n) u[n] = *(const LAS f32x4*)(n < 4 ? xi + n * 1024 : xi4);
            SCHED0();
            asm volatile("s_nop 1" : "+v"(v[0]), "+v"(v[1]), "+v"(v[2]), "+v"(v[3]), "+v"(v[4]));
#pragma unroll
            for (int n = 0; n < 5; ++n) v[n] += u[n];
        }
        STAMP(13);
        if (layer) proj_store<true>(v, po);
        else { proj_store(v, po); WAIT_V(0); BARX(); }
    }
    STAMP_END(3 + 5 * layer);
    TRACE_DUMP;
  }
  asm volatile("gm_code_end2:");
}

}

#define STOP_CHECK(n)

extern "C" void kernel_launch(void* const* d_in, const int* in_sizes, int n_in, void* d_out, int out_size, void* d_ws, size_t ws_size,
                              hipStream_t stream) {
    (void)in_sizes; (void)n_in; (void)out_size; (void)ws_size;
    const u16* x = (const u16*)d_in[0];
    const u16* wqkv = (const u16*)d_in[1];
    const u16* wo = (const u16*)d_in[2];
    const u16* w_in = (const u16*)d_in[4];
    const u16* w_sp = (const u16*)d_in[5];
    const u16* b_sp = (const u16*)d_in[6];
    const u16* w_out = (const u16*)d_in[7];
    char* ws = (char*)d_ws;
    (void)hipFuncSetAttribute((const void*)k_net, hipFuncAttributeMaxDynamicSharedMemorySize, NET_LDS);
    k_net<<<256, 512, NET_LDS, stream>>>(ws, w_sp, b_sp, (char*)d_out, (const char*)x, (const char*)wqkv, wo, w_in, w_out DBG_LAUNCH(0));
}
```

```cpp
#include <hip/hip_runtime.h>
#include <stdint.h>

namespace {

typedef unsigned short u16;
typedef short bf16x8 __attribute__((ext_vector_type(8)));
typedef float f32x4 __attribute__((ext_vector_type(4)));
typedef float f32x2 __attribute__((ext_vector_type(2)));
typedef unsigned u32x4 __attribute__((ext_vector_type(4)));
typedef unsigned u32x2 __attribute__((ext_vector_type(2)));
typedef __bf16 bf16x2_t __attribute__((ext_vector_type(2)));
#define LAS __attribute__((address_space(3)))
#define DEVI __device__ __forceinline__

constexpr int S_ = 2048, D_ = 576, FH = 2304;

constexpr size_t SZ_TWQKV = (size_t)16 * 18 * 6 * 1024;
constexpr size_t SZ_TWT = (size_t)48 * 18 * 256;
constexpr size_t SZ_TWO = (size_t)8 * 18 * 4608;
constexpr size_t SZ_TWIN = (size_t)16 * 18 * 18 * 1024;
constexpr size_t SZ_TWOUT = (size_t)8 * 72 * 4608;
constexpr size_t SZ_X = (size_t)S_ * D_ * 2;
constexpr size_t SZ_Z = (size_t)S_ * FH * 2;
constexpr size_t SZ_ST = (size_t)S_ * 64;
constexpr size_t SZ_ZST = (size_t)16 * 16 * 128 * 8;
constexpr size_t OFF_TWQKV = 0;
constexpr size_t OFF_TWT = OFF_TWQKV + SZ_TWQKV;
constexpr size_t OFF_TWO = OFF_TWT + SZ_TWT;
constexpr size_t OFF_TWIN = OFF_TWO + SZ_TWO;
constexpr size_t OFF_TWOUT = OFF_TWIN + SZ_TWIN;
constexpr size_t OFF_CNT = OFF_TWOUT + SZ_TWOUT;
constexpr size_t SZ_CNT = 10 * 65536;
constexpr size_t OFF_FW = OFF_CNT + SZ_CNT;
constexpr size_t OFF_FL = OFF_FW + 262144;
constexpr size_t OFF_EP = OFF_FL + 4096;
constexpr size_t OFF_FL2 = OFF_EP + 4096;
constexpr size_t OFF_ZST = OFF_FL2 + 8192;
constexpr size_t OFF_L0 = OFF_ZST + 2 * SZ_ZST;
constexpr size_t L_XA = 0, L_STA = L_XA + SZ_X, L_O = L_STA + SZ_ST, L_XB = L_O + SZ_X, L_STB = L_XB + SZ_X, L_Z2T = L_STB + SZ_ST, L_Y = L_Z2T + SZ_Z;
constexpr size_t SZ_L = 0;
static_assert(OFF_L0 + L_Y + SZ_Z <= ((size_t)256 << 20), "workspace");
constexpr unsigned F_O = OFF_CNT, F_X1 = F_O + 65536, F_Z2 = F_O + 131072, F_Y = F_O + 196608, F_LAYER = 262144, F_X2 = F_O + 2 * F_LAYER;

DEVI unsigned pack2(float a, float b) { f32x2 v = {a, b}; bf16x2_t r = __builtin_convertvector(v, bf16x2_t); return __builtin_bit_cast(unsigned, r); }
DEVI float bflo(unsigned u) { return __uint_as_float(u << 16); }
DEVI float bfhi(unsigned u) { return __uint_as_float(u & 0xffff0000u); }
DEVI int slot2row(int sg) { return (sg & ~31) + 2 * (sg & 15) + ((sg >> 4) & 1); }
DEVI float fmax2(float a, float b) { return __builtin_amdgcn_fmed3f(a, b, __builtin_inff()); }
#define XQ4(OP, MID) \
    asm volatile("s_nop 1\n\tv_permlane32_swap_b32 %0, %1\n\tv_permlane32_swap_b32 %2, %3\n\t" OP " %0, %0, %1\n\t" OP " %2, %2, %3\n\t" \
                 "s_nop 1\n\tv_permlane16_swap_b32 %0, %2\n\t" OP " %0, %0, %2\n\t" MID \
                 "v_mov_b32 %2, %0\n\ts_nop 1\n\tv_permlane16_swap_b32 %0, %2\n\tv_mov_b32 %1, %0\n\tv_mov_b32 %3, %2\n\ts_nop 0\n\t" \
                 "v_permlane32_swap_b32 %0, %1\n\tv_permlane32_swap_b32 %2, %3" : "+v"(x[0]), "+v"(x[1]), "+v"(x[2]), "+v"(x[3]))
DEVI void xq4_max(float (&x)[4]) { XQ4("v_max_f32", ""); }
DEVI void xq4_sum(float (&x)[4]) { XQ4("v_add_f32", ""); }
DEVI void xq4_sum_rcp(float (&x)[4]) { XQ4("v_add_f32", "v_rcp_f32 %0, %0\n\ts_nop 0\n\t"); }
template <class T> DEVI T pin(T v) { asm volatile("" : "+v"(v)); return v; }
template <int V> struct IC { static constexpr int value = V; };
template <int N, int I = 0, class F> DEVI void static_for(F&& f) { if constexpr (I < N) { f(IC<I>{}); static_for<N, I + 1>(f); } }
template <class T> DEVI T* own_s(T* p) { T* r; asm volatile("s_mov_b64 %0, %1" : "=&s"(r) : "s"(p)); return r; }
DEVI int pin_s(int v) { asm volatile("" : "+s"(v)); return v; }
DEVI float xq_sum(float v) {
    auto a = __builtin_amdgcn_permlane32_swap(__float_as_uint(v), __float_as_uint(v), false, false);
    v = __uint_as_float(a[0]) + __uint_as_float(a[1]);
    auto b = __builtin_amdgcn_permlane16_swap(__float_as_uint(v), __float_as_uint(v), false, false);
    return __uint_as_float(b[0]) + __uint_as_float(b[1]);
}
#define RSRC(p) __builtin_amdgcn_make_buffer_rsrc((void*)(p), 0, 0x7fffffff, 0x00020000)
#define ST16(rs, vo, so, v, aux) __builtin_amdgcn_raw_buffer_store_b128((v), (rs), (vo) + (so), 0, aux)
#define ST8(rs, vo, so, v, aux) __builtin_amdgcn_raw_buffer_store_b64((v), (rs), (vo), (so), aux)
#define WAIT_V(n) asm volatile("s_waitcnt vmcnt(%0)" ::"i"(n) : "memory")
#define WAIT_L0() asm volatile("s_waitcnt lgkmcnt(0)" ::: "memory")
#define BAR() do { asm volatile("" ::: "memory"); __builtin_amdgcn_s_barrier(); asm volatile("" ::: "memory"); } while (0)
#define BARX() do { __builtin_amdgcn_sched_barrier(0); BAR(); __builtin_amdgcn_sched_barrier(0); } while (0)
#define LOOP_ALIGN() asm volatile(".p2align 6")
#define SCHED0() __builtin_amdgcn_sched_barrier(0)
#define MFMA(a, b, c) __builtin_amdgcn_mfma_f32_16x16x32_bf16((a), (b), (c), 0, 0, 0)
#define MFMA_T(a, b, c) asm volatile("v_mfma_f32_16x16x32_bf16 %0, %1, %2, %0" : "+v"(c) : "v"(a), "v"(b))
#define MFMA_0(a, b, c) asm volatile("v_mfma_f32_16x16x32_bf16 %0, %1, %2, 0" : "=&v"(c) : "v"(a), "v"(b))
#define WAIT_L(n) __builtin_amdgcn_s_waitcnt(0xC07F | ((n) << 8))
#define LDS_FRAG(p) (*(const LAS bf16x8*)(p))
DEVI int lane_id_fresh() { int z = 0; asm volatile("" : "+v"(z)); return __builtin_amdgcn_mbcnt_hi(~0u, __builtin_amdgcn_mbcnt_lo(~0u, z)); }
DEVI const LAS char* opaque(const LAS char* p) { asm volatile("" : "+v"(p)); return p; }
#define DBG_ARG
#define DBG_LAUNCH(n)
#define STAMP(i)
#define STAMP_DECL
#define STAMP_BEGIN
#define STAMP_END(n)
#define STAMP_PARAM
#define STAMP_FWD

#define PST_PARAM
#define PST_FWD
#define PST_DECL
#define PST(i)
#define PST_DUMP(c, k)
#define TRACE_ATTR
#define TRACE_BEGIN
#define TRACE_DUMP

DEVI float add1(float a, float b) { float r; asm("v_add_f32 %0, %1, %2" : "=v"(r) : "v"(a), "v"(b)); return r; }
DEVI float rstd_of(float s, float q, float inv_n, float& mean) {
    float var;
    asm("v_mul_f32 %0, %2, %3\n\tv_mul_f32 %1, %2, %4\n\tv_fma_f32 %1, -%0, %0, %1" : "=&v"(mean), "=&v"(var) : "s"(inv_n), "v"(s), "v"(q));
    return __builtin_amdgcn_rsqf(fmax2(var, 0.f) + 1e-5f);
}
DEVI float ln_rstd(const f32x4 p) {
    float s, q, mean;
    asm("v_add_f32 %0, %2, %4\n\tv_add_f32 %1, %3, %5\n\ts_nop 0\n\t"
        "v_add_f32_dpp %0, %0, %0 quad_perm:[1,0,3,2] row_mask:0xf bank_mask:0xf\n\tv_add_f32_dpp %1, %1, %1 quad_perm:[1,0,3,2] row_mask:0xf bank_mask:0xf\n\ts_nop 0\n\t"
        "v_add_f32_dpp %0, %0, %0 quad_perm:[2,3,0,1] row_mask:0xf bank_mask:0xf\n\tv_add_f32_dpp %1, %1, %1 quad_perm:[2,3,0,1] row_mask:0xf bank_mask:0xf"
        : "=&v"(s), "=&v"(q) : "v"(p[0]), "v"(p[1]), "v"(p[2]), "v"(p[3]));
    return rstd_of(s, q, 1.0f / 576.0f, mean);
}

struct Res { u32x4 a, c; u32x2 e; };
DEVI Res proj_residual_load(const char* xin, int rt, int cg, int lane) {
    const auto rs = RSRC(xin + (size_t)rt * 18432 + cg * 2304);
    Res x;
    x.a = __builtin_amdgcn_raw_buffer_load_b128(rs, lane * 16, 0, 16); x.c = __builtin_amdgcn_raw_buffer_load_b128(rs, lane * 16 + 1024, 0, 16);
    x.e = __builtin_amdgcn_raw_buffer_load_b64(rs, 2048 + (lane & 15) * 16 + ((lane >> 4) & 1) * 8, 0, 16);
    return x;
}
DEVI void proj_residual(f32x4 (&v)[5], const Res& x) {
    v[0] = (f32x4){bflo(x.a.x), bfhi(x.a.x), bflo(x.a.y), bfhi(x.a.y)}; v[1] = (f32x4){bflo(x.a.z), bfhi(x.a.z), bflo(x.a.w), bfhi(x.a.w)};
    v[2] = (f32x4){bflo(x.c.x), bfhi(x.c.x), bflo(x.c.y), bfhi(x.c.y)}; v[3] = (f32x4){bflo(x.c.z), bfhi(x.c.z), bflo(x.c.w), bfhi(x.c.w)};
    v[4] = (f32x4){bflo(x.e.x), bfhi(x.e.x), bflo(x.e.y), bfhi(x.e.y)};
}
struct ProjOut { __amdgpu_buffer_rsrc_t x, st; unsigned v0, v1, v2, vs; };
DEVI ProjOut proj_out(char* xout, char* stout, int rt, int cg, int lane) {
    const int r = lane & 15, q = lane >> 4;
    ProjOut p = {RSRC(xout + (size_t)rt * 18432 + cg * 2304), RSRC(stout + (size_t)rt * 1024 + cg * 8),
                 pin((unsigned)lane * 16u), pin((unsigned)lane * 16u + 1024u), pin((unsigned)(2048 + r * 16 + (q & 1) * 8)), pin((unsigned)(r * 64 + (q >> 1) * 4))};
    asm volatile("" :: "s"(p.x), "s"(p.st));
    return p;
}
DEVI ProjOut proj_out_final(char* out, int rt, int cg, int lane) {
    const int q = lane >> 4;
    const unsigned o = (unsigned)(slot2row(rt * 16 + (lane & 15)) * 1152 + cg * 144);
    ProjOut p = {RSRC(out), RSRC(out), pin(o + q * 16), pin(o + 64 + q * 16), pin(o + 128 + (q & 1) * 8), 0u};
    return p;
}
template <bool FINAL = false>
DEVI void proj_store(f32x4 (&v)[5], const ProjOut& p) {
    ST16(p.x, p.v0, 0, ((u32x4){pack2(v[0][0], v[0][1]), pack2(v[0][2], v[0][3]), pack2(v[1][0], v[1][1]), pack2(v[1][2], v[1][3])}), FINAL ? 0 : 16);
    SCHED0();
    asm volatile("" : "+v"(v[2]), "+v"(v[3]), "+v"(v[4]));
    ST16(p.x, p.v1, 0, ((u32x4){pack2(v[2][0], v[2][1]), pack2(v[2][2], v[2][3]), pack2(v[3][0], v[3][1]), pack2(v[3][2], v[3][3])}), FINAL ? 0 : 16);
    SCHED0();
    ST8(p.x, p.v2, 0, ((u32x2){pack2(v[4][0], v[4][1]), pack2(v[4][2], v[4][3])}), FINAL ? 0 : 16);
    SCHED0();
    if (!FINAL) {
        const f32x4 s4 = ((v[0] + v[1]) + (v[2] + v[3])) + v[4] * 0.5f;
        const f32x4 q4 = ((v[0] * v[0] + v[1] * v[1]) + (v[2] * v[2] + v[3] * v[3])) + v[4] * v[4] * 0.5f;
        auto x = __builtin_amdgcn_permlane32_swap(__float_as_uint((s4[0] + s4[2]) + (s4[1] + s4[3])), __float_as_uint((q4[0] + q4[2]) + (q4[1] + q4[3])), false, false);
        const float t = __uint_as_float(x[0]) + __uint_as_float(x[1]);
        auto y = __builtin_amdgcn_permlane16_swap(__float_as_uint(t), __float_as_uint(t), false, false);
        __builtin_amdgcn_raw_buffer_store_b32(__float_as_uint(__uint_as_float(y[0]) + __uint_as_float(y[1])), p.st, p.vs, 0, 16);
        SCHED0();
    }
}

DEVI void side_load(f32x4 (&d)[3], const char* p, const char* p2) {
    asm volatile("global_load_dwordx4 %0, %3, off sc1\n\tglobal_load_dwordx4 %1, %3, off offset:1024 sc1\n\tglobal_load_dwordx4 %2, %4, off sc1"
                 : "=&v"(d[0]), "=&v"(d[1]), "=&v"(d[2]) : "v"(p), "v"(p2) : "memory");
}
DEVI void side_fence(f32x4 (&d)[3]) { asm volatile("" : "+v"(d[0]), "+v"(d[1]), "+v"(d[2])); }
DEVI void lds_word_set(LAS char* p, unsigned v) { asm volatile("ds_write_b32 %0, %1" :: "v"((unsigned)(size_t)p), "v"(v) : "memory"); }
#define DMA_I(v, off, sc) "buffer_load_dwordx4 " v ", %1, %2 offen offset:" off sc " lds\n\t"
#define DMA5(sc) asm volatile("s_mov_b32 m0, %0\n\ts_mov_b32 exec_hi, 0\n\ts_nop 2\n\t" DMA_I("%3", "0", sc) "s_mov_b32 exec_hi, -1\n\t" \
    DMA_I("%3", "512", sc) DMA_I("%3", "1536", sc) DMA_I("%3", "2560", sc) DMA_I("%3", "3584", sc) :: "s"(la), "s"(rs), "s"(so), "v"(vo) : "memory")
#define DMA4(sc) asm volatile("s_mov_b32 m0, %0\n\ts_nop 3\n\t" DMA_I("%3", "0", sc) DMA_I("%4", "1024", sc) DMA_I("%5", "2048", sc) DMA_I("%6", "3072", sc) \
    :: "s"(la), "s"(rs), "s"(so), "v"(v0), "v"(v1), "v"(v2), "v"(v3) : "memory")
template <bool SC1> DEVI void dma5(unsigned la, __amdgpu_buffer_rsrc_t rs, int so, unsigned vo) { if (SC1) DMA5(" sc1"); else DMA5(""); }
template <bool SC1> DEVI void dma4(unsigned la, __amdgpu_buffer_rsrc_t rs, int so, unsigned v0, unsigned v1, unsigned v2, unsigned v3) { if (SC1) DMA4(" sc1"); else DMA4(""); }
#define DMA2(sc) asm volatile("s_mov_b32 m0, %0\n\ts_nop 3\n\t" DMA_I("%3", "0", sc) DMA_I("%4", "1024", sc) :: "s"(la), "s"(rs), "s"(so), "v"(v0), "v"(v1) : "memory")
template <bool SC1> DEVI void dma2(unsigned la, __amdgpu_buffer_rsrc_t rs, int so, unsigned v0, unsigned v1) { if (SC1) DMA2(" sc1"); else DMA2(""); }
DEVI void dma1(unsigned la, __amdgpu_buffer_rsrc_t rs, int so, unsigned v0) { asm volatile("s_mov_b32 m0, %0\n\ts_nop 3\n\t" DMA_I("%3", "0", "") :: "s"(la), "s"(rs), "s"(so), "v"(v0) : "memory"); }
#define WAIT_IF(c, a, b) do { if (c) WAIT_V(a); else WAIT_V(b); } while (0)
template <int T0, int T1, int P, int S0 = 0, class ISSUE>
DEVI void ring_steps(ISSUE&& issue) {
    int slot = (T0 + S0) % 3;
    LOOP_ALIGN();
#pragma unroll 1
    for (int t = T0; t < T1; ++t) {
        WAIT_V(P); BAR();
        issue(t + 3, slot);
        slot = slot == 2 ? 0 : slot + 1;
    }
}

DEVI unsigned flag_out(unsigned use, int pitch, int group, int writer, int lane) { return pin(lane < 16 ? use + (group * 16 + lane) * pitch + writer * 16 : 0x80000000u); }
DEVI unsigned flag_in(unsigned use, int pitch, int reader, int n, int lane) { return pin(use + reader * pitch + (lane < n ? lane : 0) * 16); }
DEVI void flag_raise(const char* ws, unsigned vo) { ST16(RSRC(ws), vo, 0, ((u32x4){1u, 1u, 1u, 1u}), 16); SCHED0(); }
template <int FIRST>
DEVI void flags_wait(const char* ws, unsigned vo) {
    unsigned v, n = 400000u;
    asm volatile("s_nop 4\n\ts_sleep %4\n"
                 "1:\n\tbuffer_load_dword %0, %2, %3, 0 offen sc1\n\ts_waitcnt vmcnt(0)\n\tv_cmp_eq_u32 vcc, 0, %0\n\ts_cbranch_vccz 2f\n\t"
                 "s_sleep 1\n\ts_sub_u32 %1, %1, 1\n\ts_cbranch_scc0 1b\n2:"
                 : "=&v"(v), "+s"(n) : "v"(vo), "s"(RSRC(ws)), "i"(FIRST) : "vcc", "scc", "memory");
    SCHED0();
}
template <int FIRST>
DEVI void epoch_wait(const char* ws, unsigned vo, unsigned long long ep) {
    unsigned long long v; unsigned n = 400000u;
    asm volatile("s_nop 4\n\ts_sleep %5\n"
                 "1:\n\tbuffer_load_dwordx2 %0, %2, %3, 0 offen sc1\n\ts_waitcnt vmcnt(0)\n\tv_cmp_ne_u64 vcc, %4, %0\n\ts_cbranch_vccz 2f\n\t"
                 "s_sleep 1\n\ts_sub_u32 %1, %1, 1\n\ts_cbranch_scc0 1b\n2:"
                 : "=&v"(v), "+s"(n) : "v"(vo), "s"(RSRC(ws)), "s"(ep), "i"(FIRST) : "vcc", "scc", "memory");
    SCHED0();
}
DEVI unsigned lds_arrive(LAS char* p) {
    unsigned v;
    asm volatile("s_mov_b64 exec, 1\n\tds_add_rtn_u32 %0, %1, %2\n\ts_mov_b64 exec, -1\n\ts_waitcnt lgkmcnt(0)" : "=&v"(v) : "v"((unsigned)(size_t)p), "v"(1u) : "memory");
    return __builtin_amdgcn_readfirstlane(v);
}
DEVI void lds_count_wait(LAS char* p, unsigned n) {
#pragma unroll 1
    for (unsigned spin = 0; spin < 40000000u; ++spin) {
        unsigned v;
        asm volatile("ds_read_b32 %0, %1\n\ts_waitcnt lgkmcnt(0)" : "=v"(v) : "v"((unsigned)(size_t)p) : "memory");
        if (__builtin_amdgcn_readfirstlane(v) >= n) break;
    }
}

constexpr int K1_STAGE = 32768;
constexpr int WO_OFF = 69632;
constexpr int G_OWN = 139264;
constexpr int G_PART = G_OWN + 18432;
constexpr int G_TAB = G_PART + 2048;
constexpr int K1_SCR = G_TAB + 1536;
constexpr int G_WORD = K1_SCR + 576;
constexpr int NET_LDS = G_WORD + 64;

#define WO_PRE 9
#define WO_W 33
#define REL_PRIO 0
#define W_FIRST 2
#define W_ALONE 12
#define I_SLEEP 12
#define OJOB_SLEEP 20
#define PACE0 1
#define PACE __builtin_amdgcn_s_sleep(1)
#define POLLW 0
#define WO_EV 4
#define WSP_PF 0
#define W_PF 1
#define O_FIRST 10
#define X1_FIRST 6
#define X2_FIRST 6
#define Y_FIRST 10
#define Z2_FIRST 10
DEVI unsigned attn_vb(int wid, int lane) {
    const int c = lane & 15;
    return pin((unsigned)(wid < 3 ? lane * 16 : (c < 8 ? (c >> 1) & 1 : 2) * 4608 + (lane >> 4) * 64 + (c < 8 ? (c >> 2) * 2 + (c & 1) : c & 3) * 16));
}
DEVI void attn_issueB(LAS char* lds, const char* ws, int hp, int wid, unsigned vb, int s, int slot) {
    const unsigned la = (unsigned)(size_t)lds + slot * K1_STAGE + 18432 + wid * 4096;
    if (wid < 3) dma4<true>(la, RSRC(ws), (int)OFF_TWQKV + hp * (18 * 6144) + wid * 4096 + s * 12288, vb, vb, vb, vb);
    else asm volatile("s_mov_b32 m0, %0\n\ts_nop 3\n\tbuffer_load_dwordx4 %4, %1, %2 offen sc1 lds\n\tbuffer_load_dwordx4 %4, %1, %3 offen offset:1024 sc1 lds"
                      :: "s"(la), "s"(RSRC(ws)), "s"((int)OFF_TWT + hp * 13824 + s * 512), "s"((int)OFF_TWT + hp * 13824 + s * 512 + 256 - 1024), "v"(vb) : "memory");
}
struct AttnX { __amdgpu_buffer_rsrc_t rs; unsigned v[4], vh[2]; int oA, oh, step; };
DEVI AttnX attn_x(const char* x0, const char* xa, int layer, int pm, int wid, int lane) {
    const int l3 = lane >> 3, pmh = pm > 0 ? pm : 1;
    const unsigned v = layer ? lane * 16 : 2 * l3 * 1152 + ((lane & 7) ^ l3) * 16 + 4096, d1 = layer ? 0 : 16 * 1152 - 1024, d2 = layer ? 16384 : 1152 - 2048;
    const unsigned vh = layer ? (l3 & 1) * 18432 + (lane >> 4) * 256 + (8 + (lane & 7)) * 16 : v + 16 * 1152;
    return {RSRC(layer ? xa : x0 - 4096), {pin(v), pin(v + d1), pin(v + d2), pin(v + d1 + d2)}, {pin(vh), pin(vh + (layer ? 0 : 1152 - 1024))},
            (pm * 8 + 2 * wid) * 18432, (pmh * 8 - 2) * 18432, layer ? 2048 : 128};
}
DEVI void attn_issueA(LAS char* lds, const AttnX& a, int wid, int s, int slot) {
    const unsigned la = (unsigned)(size_t)lds + slot * K1_STAGE;
    if (wid == 3) dma2<true>(la, a.rs, a.oh + s * a.step, a.vh[0], a.vh[1]);
    dma4<true>(la + 2048 + wid * 4096, a.rs, a.oA + s * a.step, a.v[0], a.v[1], a.v[2], a.v[3]);
}
template <class DONE, class NEXT>
DEVI void attn_producer(LAS char* lds, const char* ws, const char* x0, const char* wl, unsigned fl, unsigned long long ep, int layer, int pm, int hp, int wid, DONE&& done, NEXT&& next) {
    const int lane = lane_id_fresh();
    const int pmh = pm > 0 ? pm : 1;
    const unsigned vo = lane * 16;
    const AttnX ax = attn_x(x0, wl + L_XA, layer, pm, wid, lane);
    const unsigned vb = attn_vb(wid, lane), fd = pin((unsigned)OFF_FL + wid * 1024 + vo);
    auto issueB = [&](int s, int slot) { attn_issueB(lds, ws, hp, wid, vb, s, slot); };
    auto issue = [&](int s, int slot) { attn_issueA(lds, ax, wid, s, slot); issueB(s, slot); };
    const int hr = lane >> 2;
    const char* ps = pin(wl + L_STA + (size_t)(pm * 128 + wid * 32) * 64 + lane * 16);
    const char* psh = pin(wl + L_STA + (size_t)((pmh * 8 - 2 + (hr >> 3)) * 16 + 8 + (hr & 7)) * 64 + (lane & 3) * 16);
    if (layer == 0) {
        const unsigned fi = pin((unsigned)OFF_FW + (pm * 16 + hp) * 1024 + (lane < 6 ? lane : 0) * 128);
        WAIT_V(0); done();
        if (wid == 0) epoch_wait<W_FIRST>(ws, fi, ep);
        BARX();
        issueB(0, 0); issueB(1, 1); issue(2, 2);
        WAIT_IF(wid == 3, 10, 12);
    } else {
        issueB(2, 2);
        const unsigned fi = flag_in(F_X2, 512, pm * 16 + hp, pm > 0 ? 24 : 16, lane);
        if (wid == 0) flags_wait<X2_FIRST>(ws, fi);
        BARX();
#pragma unroll
        for (int i = 0; i < 3; ++i) attn_issueA(lds, ax, wid, i, i);
        WAIT_IF(wid == 3, 12, 8);
    }
    __builtin_amdgcn_s_setprio(0);
    BAR();
    if (layer) WAIT_IF(wid == 3, 6, 4);
    ring_steps<0, 4, 8>(issue);
    {
        unsigned long long done;
        if (layer == 0) asm volatile("s_nop 4\n\tbuffer_load_dwordx2 %0, %1, %2, 0 offen sc1" : "+v"(done) : "v"(fd), "s"(RSRC(ws)) : "memory");
        WAIT_IF(layer, 8, 9); BAR(); issue(7, 1);
        WAIT_V(8);
        if (layer == 0) { asm volatile("" : "+v"(done)); if (__any(done != ep)) epoch_wait<0>(ws, fd, ep); }
        BAR(); issue(8, 2);
    }
    const auto rW = RSRC(ws + OFF_TWO + (size_t)(hp >> 1) * 82944 + wid * 4096);
    auto issueW = [&](int i) { dma4<true>((unsigned)(size_t)lds + WO_OFF + (i * 4 + wid) * 4096, rW, i * 16384, vo, vo, vo, vo); };
    {
        f32x4 sp[3];
        side_load(sp, ps, psh);
        WAIT_V(11); BAR(); issueW(2); issueW(3);
        WAIT_V(8); BARX();
        side_fence(sp);
#pragma unroll
        for (int i = 0; i < 3; ++i) lds_word_set(lds + K1_SCR + (i < 2 ? 16 + wid * 32 + i * 16 + hr : hr) * 4, __float_as_uint(ln_rstd(sp[i])));
        WAIT_L0();
    }
    BAR();
    next(0);
    issueW(0); issueW(1); issueW(4);
    if (wid == 0) dma2<true>((unsigned)(size_t)lds + WO_OFF + 80896, rW, 80896, vo, vo);
    next(1);
    const unsigned fo = flag_out(fl + F_O, 256, pm, hp, lane), fi = flag_in(fl + F_O, 256, pm * 16 + hp, 16, lane);
    WAIT_V(0);
    if (wid == POLLW) __builtin_amdgcn_s_setprio(3);
    BARX();
    if (wid == POLLW) { flag_raise(ws, fo); flags_wait<O_FIRST>(ws, fi); }
    __builtin_amdgcn_s_setprio(0);
    BARX();
}

#define STAMP_SEC 0
template <class DONE>
DEVI void attn_consumer(LAS char* lds, const char* x0, char* wl, int layer, int pm, int hp, int wid, DONE&& done STAMP_PARAM) {
    const int lane = lane_id_fresh(), r = lane & 15, q = lane >> 4;
    const char* xa = wl + L_XA;
    char* o = wl + L_O;
    const int T = wid & 1, h = wid >> 1;
    const LAS char *ph[2], *pa[2];
#pragma unroll
    for (int kk = 0; kk < 2; ++kk) {
        const int sw = ((4 * kk + q) ^ (r & 7)) * 16;
        ph[kk] = opaque(lds + (layer ? kk * 1024 + q * 256 + (r ^ (T ? 0 : 8)) * 16 : (r ^ (T ? 0 : 8)) * 128 + sw));
        pa[kk] = opaque(lds + T * 2048 + (layer ? kk * 1024 + lane * 16 : r * 128 + sw));
    }
    const LAS char* pb = lds + h * 3072 + lane * 16;
    const LAS char* pt = lds + lane * 16;
    f32x4 acc[5][4];
#pragma unroll
    for (int m = 0; m < 5; ++m)
#pragma unroll
        for (int n = 0; n < 4; ++n) if (m + n) acc[m][n] = pin((f32x4){0.f, 0.f, 0.f, 0.f});
    bf16x8 fa[2][5], fb[2][4];
    auto rd1 = [&](int kk, int i) {
        if (i < 3) fb[kk][i] = LDS_FRAG(pb + 18432 + kk * 6144 + i * 1024);
        else if (i == 3) fb[kk][3] = LDS_FRAG(pt + 30720 + kk * 1024);
        else if (i == 4) fa[kk][0] = LDS_FRAG(ph[kk]);
        else if (i < 9) fa[kk][i - 4] = LDS_FRAG(pa[kk] + 2048 + (i - 5) * 4096);
    };
    f32x4 mb, mb0;
#pragma unroll
    for (int j = 0; j < 4; ++j) {
        const bool ok = ((r - 4 * q - j) & 15) < 8;
        mb[j] = ok ? 0.f : -__builtin_inff();
        mb0[j] = (ok && (pm > 0 || 4 * q + j <= r)) ? 0.f : -__builtin_inff();
    }
    mb = pin(mb); mb0 = pin(mb0);
    const unsigned mp0 = pin((4 * q > r ? 0xffffu : 0u) | (4 * q + 1 > r ? 0xffff0000u : 0u)), mp1 = pin((4 * q + 2 > r ? 0xffffu : 0u) | (4 * q + 3 > r ? 0xffff0000u : 0u));
    const unsigned qtm = pin(q == h ? 0xffffffffu : 0u);
    const unsigned vom = pin((unsigned)(r * 16 + hp * 1024 + (2 * h + (q >> 1)) * 256 + (q & 1) * 8)), vot = pin((unsigned)(r * 16 + 16384 + (hp >> 1) * 256 + (hp & 1) * 8 + h * 4));
    const LAS char* tk0 = opaque(lds + K1_SCR + ((q * 4) ^ (T ? 0 : 8)) * 4);
    const LAS char* tk = opaque(lds + K1_SCR + ((1 + T) * 16 + q * 4) * 4);
    const LAS char* tq = opaque(lds + K1_SCR + ((1 + T) * 16 + r) * 4);
    pb = opaque(pb); pt = opaque(pt);
    STAMP(2);
    const int rt = pm * 8 + (hp & 1) * 4 + wid, cg = hp >> 1;
    const ProjOut pr = layer ? proj_out((char*)xa, (char*)xa, rt, cg, lane) : proj_out_final((char*)x0, rt, cg, lane);
    BAR();
    if (layer == 0) done();
    BARX();
    STAMP(3);
#pragma unroll
    for (int i = 0; i < 9; ++i) rd1(0, i);
    SCHED0();
    const Res rx = {__builtin_amdgcn_raw_buffer_load_b128(pr.x, pr.v0, 0, 16), __builtin_amdgcn_raw_buffer_load_b128(pr.x, pr.v1, 0, 16), __builtin_amdgcn_raw_buffer_load_b64(pr.x, pr.v2, 0, 16)};
    SCHED0();
    int slot = 0, left = 9;
    LOOP_ALIGN();
#pragma unroll 1
    do {
        int d;
#pragma unroll
        for (int kk = 0; kk < 2; ++kk) {
            WAIT_L(0);
            if (kk) BAR();
            SCHED0();
#pragma unroll
            for (int i = 1; i < 20; ++i) {
                const int m = i >> 2, n = i & 3;
                const bf16x8 &A = fa[kk][m], &B = fb[kk][n];
                acc[m][n] = n != 2 ? MFMA(B, A, acc[m][n]) : MFMA(A, B, acc[m][n]);
                SCHED0();
                if (i <= 9) rd1(kk ^ 1, i - 1);
                if (kk == 0) {
                    if (i == 10) d = pin_s(slot == 2 ? -2 * K1_STAGE : K1_STAGE);
                    if (i == 11 || i == 12) ph[i & 1] = opaque(ph[i & 1] + d);
                    if (i == 13 || i == 14) pa[i & 1] = opaque(pa[i & 1] + d);
                    if (i == 15) pb = opaque(pb + d);
                    if (i == 16) pt = opaque(pt + d);
                } else {
                    if (i == 11) slot = pin_s(slot == 2 ? 0 : slot + 1);
                    if (i == 13) left = pin_s(left - 1);
                }
                SCHED0();
            }
        }
    } while (left);
    STAMP(4);
    u32x2 ts[5], tf[5];
    {
        LAS char* xb = lds + wid * 320;
#pragma unroll
        for (int m = 0; m < 5; ++m) ts[m] = (u32x2){pack2(acc[m][3][0], acc[m][3][1]), pack2(acc[m][3][2], acc[m][3][3])};
        if (q == 2) {
#pragma unroll
            for (int m = 0; m < 5; ++m) {
                const unsigned w = h ? ts[m].y : ts[m].x;
                *(LAS u16*)(xb + m * 64 + r * 2) = (u16)w; *(LAS u16*)(xb + m * 64 + 32 + r * 2) = (u16)(w >> 16);
            }
        }
        asm volatile("" ::: "memory");
#pragma unroll
        for (int m = 0; m < 5; ++m) tf[m] = *(const LAS u32x2*)(xb + m * 64 + (r & 1) * 32 + q * 8);
    }
    {
        f32x4 rk[5]; float rq[4];
        rk[0] = *(const LAS f32x4*)(tk0);
#pragma unroll
        for (int g = 0; g < 4; ++g) { rk[g + 1] = *(const LAS f32x4*)(tk + g * 128); rq[g] = *(const LAS float*)(tq + g * 128); }
        const f32x4 zero = {0.f, 0.f, 0.f, 0.f};
        u32x4 kf[5];
#pragma unroll
        for (int m = 0; m < 5; ++m) kf[m] = (u32x4){pack2(acc[m][1][0], acc[m][1][1]), pack2(acc[m][1][2], acc[m][1][3]), ts[m].y, 0u};
        f32x4 sp[4], sc[4];
#pragma unroll
        for (int g = 0; g < 4; ++g) {
            const u32x4 qf = {pack2(acc[g + 1][0][0], acc[g + 1][0][1]), pack2(acc[g + 1][0][2], acc[g + 1][0][3]), ts[g + 1].x & qtm, 0u};
            sp[g] = MFMA(__builtin_bit_cast(bf16x8, kf[g]), __builtin_bit_cast(bf16x8, qf), zero);
            sc[g] = MFMA(__builtin_bit_cast(bf16x8, kf[g + 1]), __builtin_bit_cast(bf16x8, qf), zero);
        }
        u32x2 vf[5];
#pragma unroll
        for (int m = 0; m < 5; ++m) vf[m] = (u32x2){pack2(acc[m][2][0], acc[m][2][1]), pack2(acc[m][2][2], acc[m][2][3])};
        f32x4 rs[4], t[4]; float mx[4], sm[4];
#pragma unroll
        for (int g = 0; g < 4; ++g) {
#pragma unroll
            for (int j = 0; j < 4; ++j) { const bool pv = 4 * q + j > r; rs[g][j] = pv ? rk[g][j] : rk[g + 1][j]; t[g][j] = pv ? sp[g][j] : sc[g][j]; }
            t[g] = (t[g] * rs[g]) * (rq[g] * (0.23570226039551584f * 1.4426950408889634f)) + (g == 0 ? mb0 : mb);
            mx[g] = fmaxf(fmaxf(t[g][0], t[g][1]), fmaxf(t[g][2], t[g][3]));
        }
        xq4_max(mx);
#pragma unroll
        for (int g = 0; g < 4; ++g) {
            t[g] = t[g] - mx[g];
#pragma unroll
            for (int j = 0; j < 4; ++j) t[g][j] = __builtin_amdgcn_exp2f(t[g][j]);
            sm[g] = (t[g][0] + t[g][2]) + (t[g][1] + t[g][3]);
        }
        f32x4 om[4], ot[4];
#pragma unroll
        for (int g = 0; g < 4; ++g) {
            const f32x4 p = t[g] * rs[g];
            const unsigned p01 = pack2(p[0], p[1]), p23 = pack2(p[2], p[3]);
            const u32x4 pf = {p01 & mp0, p23 & mp1, p01 & ~mp0, p23 & ~mp1};
            om[g] = MFMA(__builtin_bit_cast(bf16x8, ((u32x4){vf[g].x, vf[g].y, vf[g + 1].x, vf[g + 1].y})), __builtin_bit_cast(bf16x8, pf), zero);
            ot[g] = MFMA(__builtin_bit_cast(bf16x8, ((u32x4){tf[g].x, tf[g].y, tf[g + 1].x, tf[g + 1].y})), __builtin_bit_cast(bf16x8, pf), zero);
        }
        xq4_sum_rcp(sm);
        const auto ro = RSRC(o + (size_t)(pm * 8 + T) * 18432);
        unsigned tw2[4];
#pragma unroll
        for (int g = 0; g < 4; ++g) {
            const f32x4 v = om[g] * sm[g];
            __builtin_amdgcn_raw_buffer_store_b64((u32x2){pack2(v[0], v[1]), pack2(v[2], v[3])}, ro, vom, g * 36864, 16);
            const f32x2 w = ot[g].xy * sm[g];
            tw2[g] = pack2(w.x, w.y);
        }
        if (q == 2) {
#pragma unroll
            for (int g = 0; g < 4; ++g) __builtin_amdgcn_raw_buffer_store_b32(tw2[g], ro, vot, g * 36864, 16);
        }
    }
    STAMP(5);
    {
        WAIT_V(0); BARX();
        STAMP(6);
        f32x4 v[5];
        proj_residual(v, rx);
        const LAS char* pw = opaque(lds + WO_OFF + lane * 16);
        const LAS char* pw2 = opaque(lds + WO_OFF + 41472 + lane * 16);
        const LAS char* ph2 = opaque(lds + WO_OFF + 4096 + q * 128 + (r & 7) * 16);
        const LAS char* ph3 = opaque(lds + WO_OFF + 41472 + 4096 + q * 128 + (r & 7) * 16);
        bf16x8 wf[90];
        auto rdw = [&](int i) {
            const int kc = i / 5, n = i % 5;
            wf[i] = n < 4 ? LDS_FRAG((kc < 9 ? pw : pw2) + (kc % 9) * 4608 + n * 1024) : LDS_FRAG((kc < 9 ? ph2 : ph3) + (kc % 9) * 4608);
        };
#pragma unroll
        for (int i = 0; i < WO_W; ++i) rdw(i);
#pragma unroll
        for (int n = 0; n < 5; ++n) v[n] = pin(v[n]);
        const auto rO = RSRC(o + (size_t)rt * 18432);
        const ProjOut po = proj_out(wl + L_XB, wl + L_STB, rt, cg, lane);
        asm volatile("" :: "s"(rO));
        WAIT_L(0);
        __builtin_amdgcn_s_setprio(REL_PRIO);
        BARX();
        STAMP(7);
        bf16x8 af[18];
        auto ldo = [&](int kc) { af[kc] = __builtin_bit_cast(bf16x8, __builtin_amdgcn_raw_buffer_load_b128(rO, lane * 16 + (kc & 3) * 1024, (kc >> 2) * 4096, 16)); };
#pragma unroll
        for (int kc = 0; kc < WO_PRE; ++kc) { ldo(kc); SCHED0(); if (kc >= PACE0 && kc + 1 < WO_PRE) PACE; SCHED0(); }
        static_for<90>([&](auto ic) {
            constexpr int i = ic.value, kc = i / 5, n = i % 5, j = WO_W + 2 * (i / 3) + i % 3;
            MFMA_T(wf[i], af[kc], v[n]);
            SCHED0();
            if (i % 3 < 2 && j < 90) rdw(j);
            if (i % WO_EV == 2 && i / WO_EV + WO_PRE < 18) ldo(i / WO_EV + WO_PRE);
            SCHED0();
        });
        asm volatile("s_nop 4" : "+v"(v[0]), "+v"(v[1]));
        STAMP(8);
        proj_store(v, po);
        __builtin_amdgcn_s_setprio(0);
    }
}

DEVI f32x2 gelu2(f32x2 v) {
    const f32x2 w = v * (v * v * -0.10294324f + -2.3022082f);
    const f32x2 d = (f32x2){__builtin_amdgcn_exp2f(w.x), __builtin_amdgcn_exp2f(w.y)} + 1.0f;
    return v * (f32x2){__builtin_amdgcn_rcpf(d.x), __builtin_amdgcn_rcpf(d.y)};
}
DEVI f32x4 gelu4(f32x4 v) { const f32x2 a = gelu2(v.xy), b = gelu2(v.zw); return (f32x4){a.x, a.y, b.x, b.y}; }
DEVI void rowsum16x8(const f32x4 a, const f32x4 b, float& o0, float& o1) {
    float t0, t1, t2, t3;
    asm volatile(
        "s_nop 1\n\t"
        "v_add_f32_dpp %2, %6, %6 row_ror:8 row_mask:0xf bank_mask:0x3\n\t"
        "v_add_f32_dpp %3, %7, %7 row_ror:8 row_mask:0xf bank_mask:0x3\n\t"
        "v_add_f32_dpp %4, %10, %10 row_ror:8 row_mask:0xf bank_mask:0x3\n\t"
        "v_add_f32_dpp %5, %11, %11 row_ror:8 row_mask:0xf bank_mask:0x3\n\t"
        "v_add_f32_dpp %2, %8, %8 row_ror:8 row_mask:0xf bank_mask:0xc\n\t"
        "v_add_f32_dpp %3, %9, %9 row_ror:8 row_mask:0xf bank_mask:0xc\n\t"
        "v_add_f32_dpp %4, %12, %12 row_ror:8 row_mask:0xf bank_mask:0xc\n\t"
        "v_add_f32_dpp %5, %13, %13 row_ror:8 row_mask:0xf bank_mask:0xc\n\t"
        "v_add_f32_dpp %0, %2, %2 row_half_mirror row_mask:0xf bank_mask:0x5\n\t"
        "s_nop 0\n\t"
        "v_add_f32_dpp %1, %4, %4 row_half_mirror row_mask:0xf bank_mask:0x5\n\t"
        "v_add_f32_dpp %0, %3, %3 row_half_mirror row_mask:0xf bank_mask:0xa\n\t"
        "v_add_f32_dpp %1, %5, %5 row_half_mirror row_mask:0xf bank_mask:0xa\n\t"
        "s_nop 0\n\t"
        "v_add_f32_dpp %0, %0, %0 quad_perm:[1,0,3,2] row_mask:0xf bank_mask:0xf\n\t"
        "v_add_f32_dpp %1, %1, %1 quad_perm:[1,0,3,2] row_mask:0xf bank_mask:0xf\n\t"
        "s_nop 0\n\t"
        "v_add_f32_dpp %0, %0, %0 quad_perm:[2,3,0,1] row_mask:0xf bank_mask:0xf\n\t"
        "v_add_f32_dpp %1, %1, %1 quad_perm:[2,3,0,1] row_mask:0xf bank_mask:0xf\n\t"
        : "=&v"(o0), "=&v"(o1), "=&v"(t0), "=&v"(t1), "=&v"(t2), "=&v"(t3)
        : "v"(a[0]), "v"(a[1]), "v"(a[2]), "v"(a[3]), "v"(b[0]), "v"(b[1]), "v"(b[2]), "v"(b[3]));
}

#define K5_PRE 9
#define RH_MAP 0
#define NET_LAYERS 2
constexpr int G_STAGE = 34816;
constexpr int K5_STAGE = 18432, K5_NS = 8;

constexpr int y_load_at(int slot) {
    int next = K5_PRE;
    for (int s = 0;; ++s) { const bool go = next < 18 + 2 * (s / 3); if (s == slot) return go ? next : -1; next += go; }
}

struct GTile { f32x4 a[4][4], e[2]; };
struct GFrag { bf16x8 a[2][4], b[2][5]; const LAS char *pa[2][2], *pb, *p8; int slot; };
DEVI void g_rd(GFrag& f, int kk, int i) {
    if (i < 4) f.a[kk][i] = LDS_FRAG(f.pa[kk][i >> 1] + (i & 1) * 2048);
    else if (i < 8) f.b[kk][i - 4] = LDS_FRAG(f.pb + (kk * 9 + i - 4) * 1024);
    else f.b[kk][4] = LDS_FRAG(f.p8 + kk * 9216);
}
template <int NS, int G, bool ROWS_IN_REGS>
DEVI void g_steps(GTile& c, GFrag& f, int n) {
    LOOP_ALIGN();
#pragma unroll 1
    do {
        int d;
#pragma unroll
        for (int kk = 0; kk < 2; ++kk) {
            WAIT_L(0);
            if (kk) BAR();
            SCHED0();
#pragma unroll
            for (int i = 0; i < 18; ++i) {
                const int nn = i >> 2, m = i & 3;
                f32x4& e = nn < 4 ? c.a[m][nn] : c.e[m & 1];
                e = ROWS_IN_REGS ? MFMA(f.a[kk][m], f.b[kk][nn], e) : MFMA(f.b[kk][nn], f.a[kk][m], e);
                SCHED0();
                if (i < 9) g_rd(f, kk ^ 1, i);
                if (kk == 0) {
                    if (i == 9) d = pin_s(f.slot == NS - 1 ? -(NS - 1) * G : G);
                    if (i >= 10 && i < 14) f.pa[(i - 10) >> 1][i & 1] = opaque(f.pa[(i - 10) >> 1][i & 1] + d);
                    if (i == 14) f.pb = opaque(f.pb + d);
                    if (i == 15) f.p8 = opaque(f.p8 + d);
                } else {
                    if (i == 10) f.slot = pin_s(f.slot == NS - 1 ? 0 : f.slot + 1);
                    if (i == 12) n = pin_s(n - 1);
                }
                SCHED0();
            }
        }
    } while (n);
}
DEVI void keep4(const f32x4 (&c)[4]) { asm volatile("" :: "v"(c[0]), "v"(c[1]), "v"(c[2]), "v"(c[3])); }
template <int D, int DB, bool FIRST, class HOOK>
DEVI void g_step_u(GTile& c, GFrag& f, const f32x4 (&c0)[4], HOOK&& hook) {
    static_for<36>([&](auto ic) {
        constexpr int kk = ic.value / 18, i = ic.value % 18, n = i >> 2, m = i & 3;
        if (i == 0) { WAIT_L(0); if (kk) BAR(); SCHED0(); }
        f32x4& e = n < 4 ? c.a[m][n] : c.e[m & 1];
        if (FIRST && kk == 0) asm volatile("v_mfma_f32_16x16x32_bf16 %0, %1, %2, %3" : "=&v"(e) : "v"(f.b[kk][n]), "v"(f.a[kk][m]), "v"(c0[m]));
        else MFMA_T(f.b[kk][n], f.a[kk][m], e);
        SCHED0();
        if (i < 9) g_rd(f, kk ^ 1, i);
        if (kk == 0) {
            if (i >= 10 && i < 14) f.pa[(i - 10) >> 1][i & 1] = opaque(f.pa[(i - 10) >> 1][i & 1] + D);
            if (i == 14) f.pb = opaque(f.pb + DB);
            if (i == 15) f.p8 = opaque(f.p8 + DB);
        }
        if (FIRST && ic.value == 20) keep4(c0);
        hook(ic);
        SCHED0();
    });
}
#define MF(d, a, b) "v_mfma_f32_16x16x32_bf16 " d ", " a ", " b ", " d "\n\t"
template <int D, int DB>
DEVI void g_step_gelu(GTile& c, GFrag& f, f32x4& z, float r, float c1) {
#pragma unroll
    for (int kk = 0; kk < 2; ++kk) {
        float va = z[2 * kk], vb = z[2 * kk + 1], ta, tb;
        WAIT_L(0); if (kk) BAR(); SCHED0();
#pragma unroll
        for (int i = 0; i < 9; ++i) {
            MFMA_T(f.b[kk][i >> 2], f.a[kk][i & 3], c.a[i & 3][i >> 2]);
            if (i == 5) asm volatile("v_mul_f32 %0, %0, %1" : "+v"(va) : "v"(r));
            if (i == 6) asm volatile("v_mul_f32 %0, %0, %1" : "+v"(vb) : "v"(r));
            if (i == 7) asm volatile("v_mul_f32 %0, %1, %1" : "=v"(ta) : "v"(va));
            if (i == 8) asm volatile("v_mul_f32 %0, %1, %1" : "=v"(tb) : "v"(vb));
            SCHED0();
            g_rd(f, kk ^ 1, i);
            SCHED0();
        }
        asm volatile(MF("%0", "%5", "%6") "v_fmaak_f32 %3, %3, %11, 0xc0135761\n\tv_fmaak_f32 %4, %4, %11, 0xc0135761\n\t"
                     MF("%1", "%5", "%7") "v_mul_f32 %3, %3, %9\n\tv_mul_f32 %4, %4, %10\n\t" MF("%2", "%5", "%8") "v_exp_f32 %3, %3"
                     : "+v"(c.a[1][2]), "+v"(c.a[2][2]), "+v"(c.a[3][2]), "+v"(ta), "+v"(tb)
                     : "v"(f.b[kk][2]), "v"(f.a[kk][1]), "v"(f.a[kk][2]), "v"(f.a[kk][3]), "v"(va), "v"(vb), "v"(c1));
        SCHED0(); f.pa[kk][0] = opaque(f.pa[kk][0] + D); SCHED0();
        asm volatile(MF("%0", "%6", "%7") "v_exp_f32 %5, %5\n\t" MF("%1", "%6", "%8") "v_add_f32 %4, 1.0, %4\n\tv_add_f32 %5, 1.0, %5\n\t"
                     MF("%2", "%6", "%9") "v_rcp_f32 %4, %4\n\t" MF("%3", "%6", "%10") "v_rcp_f32 %5, %5"
                     : "+v"(c.a[0][3]), "+v"(c.a[1][3]), "+v"(c.a[2][3]), "+v"(c.a[3][3]), "+v"(ta), "+v"(tb)
                     : "v"(f.b[kk][3]), "v"(f.a[kk][0]), "v"(f.a[kk][1]), "v"(f.a[kk][2]), "v"(f.a[kk][3]));
        SCHED0(); f.pa[kk][1] = opaque(f.pa[kk][1] + D); SCHED0();
        asm volatile(MF("%0", "%4", "%5") "v_mul_f32 %2, %2, %7\n\tv_mul_f32 %3, %3, %8\n\t" MF("%1", "%4", "%6")
                     : "+v"(c.e[0]), "+v"(c.e[1]), "+v"(va), "+v"(vb) : "v"(f.b[kk][4]), "v"(f.a[kk][0]), "v"(f.a[kk][1]), "v"(ta), "v"(tb));
        SCHED0();
        if (kk == 0) { f.pb = opaque(f.pb + DB); f.p8 = opaque(f.p8 + DB); }
        SCHED0();
        z[2 * kk] = va; z[2 * kk + 1] = vb;
    }
}
DEVI void g_fill(GTile& c, const float (&e)[4]) {
#pragma unroll
    for (int m = 0; m < 4; ++m) {
#pragma unroll
        for (int n = 0; n < 4; ++n) c.a[m][n] = pin((f32x4){e[m], e[m], e[m], e[m]});
        if (m < 2) c.e[m] = pin((f32x4){e[m], e[m], e[m], e[m]});
    }
}

#undef STAMP_SEC
#define STAMP_SEC 1
DEVI void gmlp_body(LAS char* lds, int wm, int wn, int pm, int cg, char* z2t, char* y, const u16* __restrict__ bsp STAMP_PARAM) {
    const int mlo = wm * 4 + wn * 2, mhi = wm * 4 + (1 - wn) * 2;
    const float zero4[4] = {0.f, 0.f, 0.f, 0.f};
    GTile z2, z1, acc;
    GFrag f;
    {
        const int lane = lane_id_fresh();
#pragma unroll
        for (int kk = 0; kk < 2; ++kk) { f.pa[kk][0] = lds + mlo * 2048 + kk * 1024 + lane * 16; f.pa[kk][1] = lds + mhi * 2048 + kk * 1024 + lane * 16; }
        f.pb = lds + 16384 + wn * 4096 + lane * 16; f.p8 = lds + 16384 + 8192 + lane * 16; f.slot = 0;
    }
    g_fill(z2, zero4); g_fill(z1, zero4);
#pragma unroll
    for (int i = 0; i < 4; ++i) f.pa[i >> 1][i & 1] = opaque(f.pa[i >> 1][i & 1]);
    f.pb = opaque(f.pb); f.p8 = opaque(f.p8);
    SCHED0();
    WAIT_V(0); BARX();
    BAR();
    BARX();
    STAMP(2);
#pragma unroll
    for (int i = 0; i < 9; ++i) g_rd(f, 0, i);
    g_steps<4, G_STAGE, true>(z2, f, 9);
    STAMP(3);
    {
        const int lane = lane_id_fresh(), r = lane & 15, q = lane >> 4;
        f32x4 ra[4];
#pragma unroll
        for (int m = 0; m < 4; ++m) ra[m] = *(const LAS f32x4*)(lds + G_TAB + ((m < 2 ? mlo : mhi) + (m & 1)) * 64 + q * 16);
        SCHED0();
#pragma unroll
        for (int m = 0; m < 4; ++m) {
            f32x4 s1, s2;
#pragma unroll
            for (int n = 0; n < 4; ++n) {
                const f32x4 g = gelu4(z2.a[m][n] * ra[m]);
                z2.a[m][n] = g;
                if (n == 0) { s1 = g; s2 = g * g; } else { s1 += g; s2 += g * g; }
            }
            if (m < 2) { const f32x4 g = gelu4(z2.e[m] * ra[m]); z2.e[m] = g; s1 += g; s2 += g * g; }
            float u0, u1;
            rowsum16x8(s1, s2, u0, u1);
            if ((r & 3) == 0) *(LAS f32x2*)(lds + G_PART + wn * 1024 + ((m < 2 ? mlo : mhi) + (m & 1)) * 128 + (q * 4 + (r >> 2)) * 8) = (f32x2){u0, u1};
            SCHED0();
        }
    }
    WAIT_L0(); BAR();
    STAMP(4);
    g_steps<4, G_STAGE, false>(z1, f, 4);
    unsigned b0, b1;
    f32x4 c0[4];
    g_step_u<G_STAGE, G_STAGE, false>(z1, f, c0, [&](auto ic) {
        if (ic.value != 18) return;
        STAMP(5);
        const int lane = lane_id_fresh(), q = lane >> 4;
        b0 = __builtin_amdgcn_raw_buffer_load_b32(RSRC(bsp), (lane & 15) * 4, pm * 256 + mlo * 32, 0);
        b1 = __builtin_amdgcn_raw_buffer_load_b32(RSRC(bsp), (lane & 15) * 4, pm * 256 + mhi * 32, 0);
#pragma unroll
        for (int g = 0; g < 2; ++g) {
            const auto rz2 = RSRC(z2t + (size_t)pm * 589824 + cg * 36864 + ((g ? mhi : mlo) >> 1) * 9216);
            const LAS char* tab = lds + G_TAB + (g ? mhi : mlo) * 64 + q * 16;
            LAS char* own = lds + (wm ? G_OWN : G_STAGE + 16384) + (g ^ wn) * 9216 + lane * 16;
            f32x4 ra[2], rb[2];
#pragma unroll
            for (int T = 0; T < 2; ++T) { ra[T] = *(const LAS f32x4*)(tab + 512 + T * 64); rb[T] = *(const LAS f32x4*)(tab + 1024 + T * 64); }
#pragma unroll
            for (int n = 0; n < (g ? 4 : 5); ++n) {
                const f32x4 v0 = (n < 4 ? z2.a[2 * g][n & 3] : z2.e[0]) * ra[0] + rb[0], v1 = (n < 4 ? z2.a[2 * g + 1][n & 3] : z2.e[1]) * ra[1] + rb[1];
                const u32x4 o = {pack2(v0[0], v1[0]), pack2(v0[1], v1[1]), pack2(v0[2], v1[2]), pack2(v0[3], v1[3])};
                ST16(rz2, lane * 16, n < 4 ? wn * 4096 + n * 1024 : 8192, o, 16);
                *(LAS u32x4*)(own + (n < 4 ? wn * 4096 + n * 1024 : 8192)) = o;
            }
        }
        STAMP(6);
    });
    static_for<3>([&](auto sc) {
        constexpr int D = sc.value == 0 ? G_STAGE : sc.value == 1 ? -3 * G_STAGE : 2 * G_STAGE;
        g_step_u<D, D, false>(z1, f, c0, [&](auto ic) { if (ic.value == 17 && sc.value == 1) WAIT_V(0); });
    });
    float ra[4], c1;
    {
        int lane, da[2];
        g_step_u<-G_STAGE, -G_STAGE, false>(z1, f, c0, [&](auto ic) {
            constexpr int i = ic.value;
            if (i == 9) lane = pin(lane_id_fresh());
            if (i == 14 || i == 15) da[i & 1] = pin((lane & 15) * 128 + (((4 * (i & 1) + (lane >> 4)) ^ (lane & 7)) * 16) - lane * 16 - (i & 1) * 1024);
            if (i == 16 || i == 17) f.pa[0][i & 1] = opaque(f.pa[0][i & 1] + da[0]);
            if (i == 27 || i == 28) f.pa[1][i & 1] = opaque(f.pa[1][i & 1] + da[1]);
            if (i >= 29 && i < 33) {
                constexpr int m = (i - 29) & 3;
                const float e = m & 1 ? bfhi(m < 2 ? b0 : b1) : bflo(m < 2 ? b0 : b1);
                c0[m] = pin((f32x4){e, e, e, e});
            }
            if (i == 33) {
#pragma unroll
                for (int m = 0; m < 4; ++m) ra[m] = *(const LAS float*)(lds + G_TAB + ((m < 2 ? mlo : mhi) + (m & 1)) * 64 + (lane & 15) * 4);
                c1 = pin(-0.10294324f);
            }
        });
    }
    STAMP(7);
    g_step_u<2 * G_STAGE, G_OWN - G_STAGE - 16384, true>(acc, f, c0, [](auto) {});
    static_for<18>([&](auto sc) {
        constexpr int st = sc.value, m = st < 16 ? st >> 2 : st & 1;
        constexpr int D = st == 0 ? -3 * G_STAGE : st % 3 == 1 ? 2 * G_STAGE : -G_STAGE;
        g_step_gelu<D, st == 0 ? 16384 - G_OWN : D>(acc, f, st < 16 ? z1.a[m][st & 3] : z1.e[m], ra[m], c1);
    });
    f.slot = 1;
    STAMP(9);
    g_steps<3, -G_STAGE, false>(acc, f, 13);
    STAMP(10);
    const int lane = lane_id_fresh();
#pragma unroll
    for (int g = 0; g < 2; ++g) {
        const auto ry = RSRC(y + (size_t)(pm * 8 + (g ? mhi : mlo)) * 73728 + cg * 4608);
#pragma unroll
        for (int T = 0; T < 2; ++T) {
            const int m = 2 * g + T;
#pragma unroll
            for (int p = 0; p < 2; ++p) {
                const f32x4 a = acc.a[m][2 * p] * z1.a[m][2 * p], c = acc.a[m][2 * p + 1] * z1.a[m][2 * p + 1];
                ST16(ry, lane * 16 + wn * 2048 + p * 1024, T * 73728, ((u32x4){pack2(a[0], a[1]), pack2(a[2], a[3]), pack2(c[0], c[1]), pack2(c[2], c[3])}), 16);
            }
            if (g == 0) {
                const f32x4 a = acc.e[T] * z1.e[T];
                ST8(ry, (lane >> 5) * 256 + (lane & 15) * 16 + ((lane >> 4) & 1) * 8, T * 73728 + 4096, ((u32x2){pack2(a[0], a[1]), pack2(a[2], a[3])}), 16);
            }
        }
    }
}

#define NET_IDS \
    int b = __builtin_amdgcn_readfirstlane(blockIdx.x), wid = wid8 & 3; \
    asm volatile("" : "+s"(b), "+s"(wid));     \
    const int xcd = b & 7, idx = b >> 3; \
    const int pa = (xcd >> 1) * 4 + (idx & 3), hp = (xcd & 1) * 8 + (idx >> 2); \
    const int pm = (xcd >> 2) * 8 + (idx & 7), cg = (xcd & 3) * 4 + (idx >> 3); \
    const int rh = RH_MAP ? xcd * 4 + (idx & 3) : (xcd >> 1) * 8 + (idx & 7), c5 = RH_MAP ? idx >> 2 : (xcd & 1) * 4 + (idx >> 3); \
    const int lead = (idx + (idx >> 3)) & 3; \
    auto kord = [&](int t, int n) { \
        const int k = t + ((int)(0x1F000000u << (((t - lead) & 3) * 4)) >> 28); \
        return (unsigned)k < (unsigned)n ? k : t; \
    }; \
    auto kord5 = [&](int t) { \
        const int k = t + ((int)(0x1F000000u << (((t - (RH_MAP ? c5 : lead)) & 3) * 4)) >> 28); \
        return (unsigned)k < 18u ? k : t; \
    };

typedef short s16x4 __attribute__((ext_vector_type(4)));
DEVI u32x4 tr_frag(const LAS char* p, int four_rows) {
    const u32x2 a = __builtin_bit_cast(u32x2, __builtin_amdgcn_ds_read_tr16_b64_v4i16((LAS s16x4*)p));
    const u32x2 b = __builtin_bit_cast(u32x2, __builtin_amdgcn_ds_read_tr16_b64_v4i16((LAS s16x4*)(p + four_rows)));
    return (u32x4){a.x, a.y, b.x, b.y};
}
DEVI float dot2(unsigned v, unsigned w, float acc) { return __builtin_amdgcn_fdot2_f32_bf16(__builtin_bit_cast(bf16x2_t, v), __builtin_bit_cast(bf16x2_t, w), acc, false); }
DEVI float sum2(unsigned v, float acc) { return dot2(v, 0x3f803f80u, acc); }
struct Sel { unsigned lo, hi, nlo, nhi; };
DEVI Sel make_sel() { return {(unsigned)pin_s(0x00003f80), (unsigned)pin_s(0x3f800000), (unsigned)pin_s(0x0000bf80), (unsigned)pin_s((int)0xbf800000u)}; }
DEVI unsigned centre2(unsigned v, f32x2& e, float mean, const Sel& k) {
    const float a = dot2(k.lo, v, e.x), b = dot2(k.hi, v, e.y);
    const unsigned r = pack2(a, b);
    e = (f32x2){dot2(k.nlo, r, a), dot2(k.nhi, r, b)} - mean;
    return r;
}
typedef u32x4 Retile[9];
template <int PITCH>
DEVI void retile_out_make(Retile& o, const LAS char* img, int lane) {
    const int c = lane & 15, q = lane >> 4, p = lane & 3;
    const LAS char* src = img + (q * 8 + (c >> 2)) * PITCH;
#pragma unroll
    for (int i = 0; i < 8; ++i) o[i] = tr_frag(src + p * 16 + (i >> 2) * 144 + ((i >> 1) & 1) * 64 + (i & 1) * 8, 4 * PITCH);
    o[8] = tr_frag(src + 128 + (p >> 1) * 144 + (p & 1) * 8, 4 * PITCH);
}
DEVI void retile_out_store(const Retile& o, char* dst, int nkc, int kc, int w, int lane) {
    const int c = lane & 15, q = lane >> 4;
    const auto rd = RSRC(dst + (size_t)(2 * w * nkc + kc) * 4608);
#pragma unroll
    for (int i = 0; i < 8; ++i) ST16(rd, lane * 16 + (i & 3) * 1024, (i >> 2) * nkc * 4608, o[i], 16);
    ST16(rd, (c >> 3) * nkc * 4608 + 4096 + q * 128 + (c & 7) * 16, 0, o[8], 16);
}

DEVI float dpp_ror(float v, int n) { return n == 8 ? __builtin_amdgcn_update_dpp(0.f, v, 0x128, 0xf, 0xf, true) : n == 4 ? __builtin_amdgcn_update_dpp(0.f, v, 0x124, 0xf, 0xf, true)
                                          : n == 2 ? __builtin_amdgcn_update_dpp(0.f, v, 0x122, 0xf, 0xf, true) : __builtin_amdgcn_update_dpp(0.f, v, 0x121, 0xf, 0xf, true); }
DEVI unsigned long long epoch_of(const char* ws) {
    SCHED0();
    const unsigned long long ep = *(const __attribute__((address_space(4))) unsigned long long*)(ws + pin_s((int)OFF_EP)) + 1ull;
    asm volatile("" :: "s"(ep));
    return ep;
}
DEVI void prologue_done(LAS char* lds, char* ws, unsigned long long ep, int b, int lane) {
    if (lds_arrive(lds + G_WORD + 8) == 7) ST16(RSRC(ws), lane ? 0x80000000u : (unsigned)OFF_FL + b * 16, 0, ((u32x4){(unsigned)ep, (unsigned)(ep >> 32), 0u, 0u}), 16);
}
DEVI void centre_frag(u32x4& f, f32x2 (&er)[4], float mean, const Sel& k) {
#pragma unroll
    for (int e = 0; e < 4; ++e) f[e] = centre2(f[e], er[e], mean, k);
}
template <class ARGS>
DEVI unsigned long long prologue(LAS char* lds, char*& ws, const char*& x0, const char*& wqkv, const u16*& wo, const u16*& w_in, int b, int pa, int wid8, ARGS&& args PST_PARAM) {
    PST_DECL;
    const int lane = lane_id_fresh(), c = lane & 15, q = lane >> 4, w = wid8 & 3;
    const int jw = w - 1 + 3 * (((wid8 >> 2) ^ (b >> 3)) & 1);
    const bool wjob = b >= 16 && b < 112 && w, ijob = b >= 112 && w;
    const int uo = (wid8 - 1 - (wid8 >> 2)) * 16 + b;
    const bool ojob = b < 16 && w && uo < 72;
    const unsigned l0 = (unsigned)(size_t)lds;
    LAS char* img = lds + 2 * K1_STAGE;
    LAS float* red = (LAS float*)(lds + G_PART);
    const int uw = b - 16, head = (uw & 7) * 4 + ((uw >> 3) & 3), part = uw >> 5, hp = head >> 1, odd = head & 1;
    const int c0 = part * 1152 + hp * 72 + odd * 40;
    const int ui = b - 112, st = (ui & ~15) + 2 * (ui & 7) + ((ui >> 3) & 1);
    {
        const int pitch = pin_s(b < 112 ? 3456 : 9216), so = pin_s(jw * 96 * pitch + (b < 112 ? c0 & ~15 : st * 64));
        const unsigned v = (lane & 3) < (b >= 112 ? 4 : (head & 3) == 3 ? 2 : 3) ? (lane >> 2) * pitch + (lane & 3) * 16 : 0x80000000u, d = 16 * pitch - 1024;
        const unsigned v0 = pin(v), v1 = pin(v + d), v2 = pin(v + 2 * d), v3 = pin(v + 3 * d);
        const int e = (ui >> 3) * 192 + wid8 * 64 + lane, row = (e * 10923) >> 16, r6 = e - row * 6;
        const unsigned vp = pin((unsigned)(row * 3456 + (r6 >> 1) * 1152 + (r6 & 1) * 128));
        const unsigned la = pin_s(l0 + 2 * K1_STAGE + jw * 6144);
        asm volatile("" :: "s"(wqkv), "s"(w_in));
        if (W_PF && b >= 112 && wid8 < 3) dma1(l0 + G_OWN, RSRC(wqkv), (b & 7) * 128, vp);
        if (w && b >= 16) {
            if (b >= 112) __builtin_amdgcn_s_sleep(I_SLEEP);
            const auto rs = RSRC(b < 112 ? wqkv : (const char*)w_in);
            dma4<false>(la, rs, so, v0, v1, v2, v3);
            dma2<false>(la + 4096, rs, so + 64 * pitch, v0, v1);
        }
        args();
    }
    if (ojob) {
        const auto rs = RSRC(wo + (uo & 3) * 144);
#pragma unroll
        for (int j = 0; j < 9; ++j) {
            const int i = lane + 64 * j, rl = (i * 3641) >> 16, kp = (uo >> 2) * 32 + rl;
            const int row = kp < 512 ? 18 * (kp >> 4) + (kp & 15) + 2 * ((kp >> 4) & 1) : 36 * ((kp - 512) >> 2) + 16 + (kp & 3);
            dma1(l0 + 2 * K1_STAGE + wid8 * 9216 + j * 1024, rs, 0, (unsigned)(row * 1152 + (i - rl * 18) * 16));
        }
    }
    if (wid8 == 4) { lds_word_set(lds + G_WORD + c * 4, 0u); WAIT_L0(); }
    BAR();
    unsigned long long ep;
    if (wid8 == 4) ep = epoch_of(ws);
    if (W_ALONE && wid8 == 4 && b >= 16 && b < 112) lds_count_wait(lds + G_WORD + 12, W_ALONE);
    if (wid8 == 4) {
        AttnX ax = attn_x(x0, x0, 0, pa, 0, lane);
#pragma unroll
        for (int s = 0; s < 2; ++s)
#pragma unroll
            for (int i = 0; i < 4; ++i) { ax.oA = (pa * 8 + 2 * i) * 18432; attn_issueA(lds, ax, i, s, s); }
    }
    u32x4 xv[9];
    if (wid8 == 0) {
        const unsigned v = (lane >> 3) * 2304 + (lane & 7) * 16;
        const auto rx = RSRC(x0 + (size_t)slot2row(b * 8) * 1152);
#pragma unroll
        for (int i = 0; i < 9; ++i) asm volatile("s_nop 4\n\tbuffer_load_dwordx4 %0, %1, %2, 0 offen offset:%3" : "+v"(xv[i]) : "v"(v), "s"(rx), "i"(i * 128) : "memory");
        const auto rw = RSRC(ws);
        const u32x4 ones = {~0u, ~0u, ~0u, ~0u}, zero = {0u, 0u, 0u, 0u};
        const unsigned vo = lane * 16;
        ST16(rw, vo, (unsigned)OFF_ZST + b * 2048, ones, 16); ST16(rw, vo, (unsigned)OFF_ZST + b * 2048 + 1024, ones, 16);
        ST16(rw, vo, (unsigned)OFF_CNT + b * 2560, zero, 16); ST16(rw, vo, (unsigned)OFF_CNT + b * 2560 + 1024, zero, 16);
        ST16(rw, vo, lane < 32 ? (unsigned)OFF_CNT + b * 2560 + 2048 : (unsigned)(OFF_L0 + L_STB) + b * 512 - 512, zero, 16);
        ST16(rw, vo, (unsigned)(OFF_L0 + L_O) + (b >> 1) * 18432 + 16384 + (b & 1) * 1024, zero, 16);
        ST16(rw, b < 8 ? vo : 0x80000000u, (unsigned)OFF_FL2 + b * 1024, zero, 16);
    }
    PST(1);
    const Sel k = make_sel();
    u32x4 f[6];
    unsigned vd, vd2;
    __amdgpu_buffer_rsrc_t rd, rd2;
    if (wjob || ijob) {
        const LAS char *s0, *s1;
        if (ijob) {
            int o[2];
#pragma unroll
            for (int h = 0; h < 2; ++h) {
                const int u = st * 2 + h, half = u >= 144, g = u - half * 144, cg = g / 9, j = g - cg * 9, p = lane & 3;
                o[h] = h * 32 + (j < 8 ? (p & 1) * 16 + (p >> 1) * 8 : p * 8);
                (h ? rd2 : rd) = RSRC(ws + OFF_TWIN + (size_t)cg * (18 * 18432) + (half * 18 + jw * 3) * 9216);
                (h ? vd2 : vd) = q * 256 + (j < 8 ? ((j & ~1) + (c >> 3)) * 1024 + ((j & 1) * 8 + (c & 7)) * 16 : 8192 + c * 16);
            }
            s0 = img + jw * 6144 + (q * 8 + (c >> 2)) * 64 + o[0]; s1 = s0 + (o[1] - o[0]);
        } else {
            s0 = img + jw * 6144 + (q * 8 + (c >> 2)) * 64 + (c0 & 15); s1 = s0 + 32 + min(c & 3, 2) * 2048; s0 += (c & 3) * 8;
            rd = RSRC(ws + OFF_TWQKV + (size_t)(hp * 18 + jw * 3) * 6144 + (3 * odd + part) * 1024); vd = lane * 16;
            rd2 = RSRC(ws + OFF_TWT + (size_t)((hp * 3 + part) * 18 + jw * 3) * 256); vd2 = c < 12 && !odd ? (unsigned)((c >> 2) * 256 + q * 64 + (c & 3) * 16) : 0x80000000u;
        }
        s0 = opaque(s0); s1 = opaque(s1); vd = pin(vd); vd2 = pin(vd2);
        asm volatile("" :: "s"(rd), "s"(rd2));
        WAIT_V(0);
        PST(2);
        float sm[4] = {0.f, 0.f, 0.f, 0.f};
#pragma unroll
        for (int i = 0; i < 4; ++i) f[i] = tr_frag((i < 3 ? s0 : s1) + (i % 3) * 2048, 256);
        if (ijob) { f[4] = tr_frag(s1 + 2048, 256); f[5] = tr_frag(s1 + 4096, 256); }
#pragma unroll
        for (int e = 0; e < 16; ++e) sm[(e >= 12) * 2 + (e & 1)] = sum2(f[e >> 2][e & 3], sm[(e >= 12) * 2 + (e & 1)]);
        if (ijob) {
#pragma unroll
            for (int e = 16; e < 24; ++e) sm[2 + (e & 1)] = sum2(f[e >> 2][e & 3], sm[2 + (e & 1)]);
            sm[2] += sm[3];
        } else {
            sm[2] = c < 12 ? sm[2] + sm[3] : 0.f;
            sm[2] += dpp_ror(sm[2], 4); sm[2] += dpp_ror(sm[2], 8);
        }
        auto x = __builtin_amdgcn_permlane16_swap(__float_as_uint(sm[0] + sm[1]), __float_as_uint(sm[2]), false, false);
        red[jw * 64 + lane] = __uint_as_float(x[0]) + __uint_as_float(x[1]);
        if (lds_arrive(lds + G_WORD + 12) < 5) lds_count_wait(lds + G_WORD + 12, 6);
        PST(3);
        float t = 0.f;
#pragma unroll
        for (int i = 0; i < 6; ++i) t += red[i * 64 + lane];
        x = __builtin_amdgcn_permlane32_swap(__float_as_uint(t), __float_as_uint(t), false, false);
        t = (__uint_as_float(x[0]) + __uint_as_float(x[1])) * (1.0f / 576.0f);
        auto y = __builtin_amdgcn_permlane16_swap(__float_as_uint(t), __float_as_uint(t), false, false);
        const float m0 = __uint_as_float(y[0]), m1 = __uint_as_float(y[1]);
        f32x2 e0[4] = {{-m0, -m0}, {-m0, -m0}, {-m0, -m0}, {-m0, -m0}}, e1[4] = {{-m1, -m1}, {-m1, -m1}, {-m1, -m1}, {-m1, -m1}};
        const unsigned dk = ijob ? 9216 : 6144;
#pragma unroll
        for (int i = 0; i < 3; ++i) { centre_frag(f[i], e0, m0, k); ST16(rd, vd, i * dk, f[i], 16); }
#pragma unroll
        for (int i = 0; i < 3; ++i) if (i == 0 || ijob) { centre_frag(f[3 + i], e1, m1, k); ST16(rd2, vd2, i * 9216, f[3 + i], 16); }
        PST(4);
        if (wjob && W_ALONE > 6) lds_arrive(lds + G_WORD + 12);
    }
    if (wid8 != 4) ep = epoch_of(ws);
    {
        if (wjob) {
            const unsigned fo = pin((unsigned)OFF_FW + ((lane >> 3) * 16 + hp) * 1024 + (3 * odd + part) * 128 + (lane & 7) * 16);
            WAIT_V(0);
            PST(5);
            if (lds_arrive(lds + G_WORD + 4) == 5) {
                const u32x4 e4 = {(unsigned)ep, (unsigned)(ep >> 32), (unsigned)ep, (unsigned)(ep >> 32)};
                ST16(RSRC(ws), fo, 0, e4, 16); ST16(RSRC(ws), fo + 131072, 0, e4, 16);
            }
        }
    }
    if (wid8 == 0) {
        WAIT_V(7);
        asm volatile("" : "+v"(xv[0]), "+v"(xv[1]), "+v"(xv[2]), "+v"(xv[3]), "+v"(xv[4]), "+v"(xv[5]), "+v"(xv[6]), "+v"(xv[7]), "+v"(xv[8]));
        float s = 0.f, q2 = 0.f;
#pragma unroll
        for (int e = 0; e < 36; ++e) { s = sum2(xv[e >> 2][e & 3], s); q2 = dot2(xv[e >> 2][e & 3], xv[e >> 2][e & 3], q2); }
        ST8(RSRC(ws + OFF_L0 + L_STA + (size_t)b * 512), lane * 8, 0, ((u32x2){__float_as_uint(s), __float_as_uint(q2)}), 16);
    }
    if (ojob) {
        WAIT_V(0);
        Retile rt;
        retile_out_make<288>(rt, img + wid8 * 9216, lane); retile_out_store(rt, ws + OFF_TWO, 18, uo >> 2, uo & 3, lane);
    }
    PST(7);
    PST_DUMP(wid8 == 0 || wid8 == 4 || wid8 == 5, wid8 ? wid8 : 2);
    return ep;
}

__global__ __launch_bounds__(512, 2) TRACE_ATTR void k_net(char* ws, const u16* wsp, const u16* bsp, char* out, const char* x0, const char* wqkv, const u16* wo,
                                                             const u16* w_in, const u16* w_out DBG_ARG) {
    extern __shared__ __attribute__((aligned(16))) char smem[];
    LAS char* lds = (LAS char*)smem;
    asm volatile("gm_code_begin:");
    if (blockIdx.x < 16) {
        unsigned long long pc; int d0, len;
        asm volatile("s_getpc_b64 %0\n1:\n\ts_mov_b32 %1, gm_code_begin-1b\n\ts_mov_b32 %2, gm_code_end1-gm_code_begin\n\ts_max_i32 %2, %2, gm_code_end2-gm_code_begin"
                     : "=s"(pc), "=s"(d0), "=s"(len) :: "scc");
        const auto rs = __builtin_amdgcn_make_buffer_rsrc((void*)((pc + d0) & ~15ull), 0, len, 0x00020000);
        dma1((unsigned)(size_t)lds + G_OWN, rs, 0, (threadIdx.x * 2 + (blockIdx.x >> 3)) * 128);
    }
    STAMP_DECL;
    const int wid8 = __builtin_amdgcn_readfirstlane(threadIdx.x >> 6);
    unsigned long long ep;
    {
        NET_IDS
        ep = prologue(lds, ws, x0, wqkv, wo, w_in, b, pa, wid8, [&] {
            ws = own_s(ws); wsp = own_s(wsp); bsp = own_s(bsp); out = own_s(out); x0 = own_s(x0); wo = own_s(wo); w_out = own_s(w_out);
        } PST_FWD);
    }
    const char *twin = ws + OFF_TWIN, *twout = ws + OFF_TWOUT;
    if (wid8 >= 4) {
        u32x4 wr[5][9];
#pragma unroll 1
      for (int layer = 0; layer < NET_LAYERS; ++layer) {
        NET_IDS
        TRACE_BEGIN;
        char* wl = ws + OFF_L0 + layer * SZ_L;
        const unsigned fl = layer * F_LAYER;
        const char *xin = wl + L_XB, *st = wl + L_STB, *z2t = wl + (layer ? L_Y : L_Z2T);
        unsigned long long* zst = (unsigned long long*)(ws + OFF_ZST + layer * SZ_ZST);
        const int lane = lane_id_fresh(), l3 = lane >> 3;
        const unsigned vo = lane * 16;
        const unsigned l0 = (unsigned)(size_t)lds, lA = l0 + wid * 4096, lB = l0 + 16384 + wid * 4608;
        const auto rX = RSRC(xin + (size_t)(pm * 8 + 2 * wid) * 18432), rI = RSRC(twin + (size_t)cg * (18 * 18432) + wid * 4608);
        auto issue1B = [&](int g, int slot) { const int k = kord(g < 9 ? g : (g < 13 ? g - 4 : g - 13), 9); dma5<true>(lB + slot * G_STAGE, rI, (g < 9 ? 9 + k : k) * 18432, vo); };
        auto issue1A = [&](int g, int slot) { dma4<true>(lA + slot * G_STAGE, rX, kord(g < 9 ? g : g - 13, 9) * 2048, vo, vo, vo + 16384, vo + 16384); };
        auto issue1 = [&](int g, int slot) { issue1B(g, slot); issue1A(g, slot); };
        const auto rA = RSRC((const char*)wsp + (size_t)(pm * 128 + wid * 32) * 4096), rB = RSRC(z2t + cg * 36864 + wid * 4608);
        const unsigned voA = (unsigned)(2 * l3 * 4096 + ((lane & 7) ^ l3) * 16);
        auto issueB = [&](int k, int slot) { dma5<true>(lB + slot * G_STAGE, rB, (k >> 1) * 589824 + (k & 1) * 18432, vo); };
        const auto rW = RSRC(twout + (size_t)c5 * (72 * 4608));
        auto issue5 = [&](int t) { dma5<true>(l0 + ((t + 6) & 7) * K5_STAGE + wid * 4608, rW, kord5(t) * K5_STAGE + wid * 4608, vo); };
        auto load5 = [&](int p, int i) {
            const int t0 = p ? 2 * p + 6 : 4, ow = kord5(t0 + i) * K5_STAGE + wid * 4096;
            asm volatile("s_nop 4\n\tbuffer_load_dwordx4 %0, %4, %5, %6 offen sc1\n\tbuffer_load_dwordx4 %1, %4, %5, %6 offen offset:1024 sc1\n\t"
                         "buffer_load_dwordx4 %2, %4, %5, %6 offen offset:2048 sc1\n\tbuffer_load_dwordx4 %3, %4, %5, %6 offen offset:3072 sc1"
                         : "+v"(wr[p][4 * i]), "+v"(wr[p][4 * i + 1]), "+v"(wr[p][4 * i + 2]), "+v"(wr[p][4 * i + 3]) : "v"(vo), "s"(rW), "s"(ow) : "memory");
            if (i) {
                const int oe = kord5(t0 + (wid >> 1)) * K5_STAGE + 16384 + (wid & 1) * 1024;
                asm volatile("s_nop 4\n\tbuffer_load_dwordx4 %0, %1, %2, %3 offen sc1" : "+v"(wr[p][8]) : "v"(vo), "s"(rW), "s"(oe) : "memory");
            }
        };
        auto put5 = [&](int w0, int w1) {
#pragma unroll
            for (int w = w0; w < w1; ++w) {
                const int p = w / 9, i = w % 9 == 4 ? -1 : w % 9 - (w % 9 > 4);
                LAS char* sl = lds + (((p ? 2 * p + 6 : 4) + 6) & 7) * K5_STAGE + lane * 16;
                if (i < 0) *(LAS u32x4*)(sl + (wid >> 1) * K5_STAGE + 16384 + (wid & 1) * 1024) = wr[p][8];
                else *(LAS u32x4*)(sl + (i >> 2) * K5_STAGE + wid * 4096 + (i & 3) * 1024) = wr[p][i];
            }
            WAIT_L0();
        };
        const int row = wid * 32 + (lane & 31);
        auto step1 = [&](int t) { BAR(); issue1(t + 4, t & 3); };
        const char* ps;
        unsigned fd, fd2;
        {
            unsigned fo, fi;
            const int ojob = layer || !wid ? 288 : b + (wid - 1) * 256, oimg = wid == 1 ? 2048 : G_STAGE;
            attn_producer(lds, ws, x0, wl, fl, ep, layer, pa, hp, wid, [&] { prologue_done(lds, ws, ep, b, lane); }, [&](int part) {
                if (part == 0 && ojob < 288) {
                    const auto rs = RSRC(w_out + (size_t)(ojob >> 2) * 32 * 576 + (ojob & 3) * 144);
#pragma unroll
                    for (int j = 0; j < 9; ++j) { const int i = lane + 64 * j; dma1(l0 + oimg + j * 1024, rs, 0, (unsigned)(((i * 3641) >> 16) * (1152 - 288) + i * 16)); }
                }
                if (part == 1) {
                    issue1B(0, 0); issue1B(1, 1);
                    fo = flag_out(fl + F_X1, 256, pa, hp, lane); fi = flag_in(fl + F_X1, 256, pm * 16 + cg, 16, lane);
                    ps = pin(st + (size_t)(pm * 128 + wid * 32) * 64 + lane * 16);
                    fd = pin((unsigned)OFF_FL2 + wid * 1024 + vo); fd2 = pin((unsigned)OFF_FL2 + 4096 + (lane & 31) * 16);
                }
            });
            if (WSP_PF && layer == 0 && wid >= 2 && ojob >= 288) {
                __builtin_amdgcn_s_sleep(OJOB_SLEEP);
#pragma unroll
                for (int i = 0; i < 4; ++i)
                    asm volatile("s_nop 4\n\tbuffer_load_dwordx4 %0, %4, %5, %6 offen\n\tbuffer_load_dwordx4 %1, %4, %5, %6 offen offset:1024\n\t"
                                 "buffer_load_dwordx4 %2, %4, %5, %6 offen offset:2048\n\tbuffer_load_dwordx4 %3, %4, %5, %6 offen offset:3072"
                                 : "+v"(wr[0][0]), "+v"(wr[0][1]), "+v"(wr[0][2]), "+v"(wr[0][3]) : "v"(vo), "s"(RSRC(wsp)), "s"(b * 32768 + (wid - 2) * 16384 + i * 4096) : "memory");
            }
            if (ojob < 288) {
                Retile& rt = wr[1];
                retile_out_make<288>(rt, lds + oimg, lane);
                __builtin_amdgcn_s_sleep(OJOB_SLEEP);
                retile_out_store(rt, ws + OFF_TWOUT, 72, ojob >> 2, ojob & 3, lane);
                WAIT_V(0);
                flag_raise(ws, lane ? 0x80000000u : (unsigned)OFF_FL2 + ojob * 16);
            }
            BARX();
            if (wid == 0) flag_raise(ws, fo);
            issue1B(2, 2); issue1B(3, 3);
            asm volatile("" :: "s"(rX));
            if (wid == 0) flags_wait<X1_FIRST>(ws, fi);
            BARX();
        }
        issue1A(0, 0); issue1A(1, 1); issue1A(2, 2);
        WAIT_V(8); BAR();
        issue1A(3, 3);
        {
            f32x4 sp[3];
            WAIT_V(8); step1(0);
            WAIT_V(13); step1(1);
            WAIT_V(18); step1(2);
            unsigned got[2];
            if (layer == 0) asm volatile("s_nop 4\n\tbuffer_load_dword %0, %2, %4, 0 offen sc1\n\tbuffer_load_dword %1, %3, %4, 0 offen sc1" : "+v"(got[0]), "+v"(got[1]) : "v"(fd), "v"(fd2), "s"(RSRC(ws)) : "memory");
            WAIT_IF(layer, 18, 20); step1(3);
            WAIT_IF(layer, 18, 20); step1(4);
            WAIT_V(18);
            if (layer == 0) {
                asm volatile("" : "+v"(got[0]), "+v"(got[1]));
                if (__any((got[0] & got[1]) == 0u)) { flags_wait<0>(ws, fd); flags_wait<0>(ws, fd2); }
            }
            BAR(); issue1B(9, 1); side_load(sp, ps, ps); if (layer == 0) load5(3, 0);
            WAIT_IF(layer, 17, 21); BAR(); issue1B(10, 2); if (layer == 0) load5(3, 1);
            WAIT_IF(layer, 5, 14); BARX();
            side_fence(sp);
#pragma unroll
            for (int i = 0; i < 2; ++i) lds_word_set(lds + G_TAB + (wid * 32 + i * 16 + (lane >> 2)) * 4, __float_as_uint(ln_rstd(sp[i])));
            issue1B(11, 3);
            WAIT_L0();
        }
        BAR(); issue1B(12, 0);
        if (layer == 1) {
            const auto rd = RSRC(out + (size_t)b * 9216 + wid * 2304);
            ST16(rd, vo, 0, ((u32x4){0u, 0u, 0u, 0u}), 16); ST16(rd, vo, 1024, ((u32x4){0u, 0u, 0u, 0u}), 16);
            if (lane < 16) ST16(rd, vo, 2048, ((u32x4){0u, 0u, 0u, 0u}), 16);
        }
        if (layer == 0) {
#pragma unroll
            for (int p = 0; p < 6; ++p) load5(p >> 1, p & 1);
        }
        WAIT_V(0);
        __builtin_amdgcn_s_setprio(3);
        const char* src = pin((const char*)zst + (size_t)(pm * 16 + (lane >> 4) * 4) * 1024 + wid * 256 + (lane & 15) * 16);
        const unsigned tw = pin(l0 + G_TAB + 512 + (lane >> 5) * 512 + (wid * 32 + (lane & 15) * 2 + ((lane >> 4) & 1)) * 4);
        const unsigned fz = flag_out(fl + F_Z2, 256, cg, pm, lane), fzi = pin(fl + F_Z2 + (cg * 16 + pm) * 256 + ((lane ^ pm) & 8) * 16 + (lane & 7) * 16);
        const unsigned vA[4] = {pin(voA), pin(voA + 16 * 4096 - 1024), pin(voA + 4096 - 2048), pin(voA + 17 * 4096 - 3072)};
        auto spA = [&](int so, unsigned la) { dma4<false>(la, rA, so, vA[0], vA[1], vA[2], vA[3]); };
        auto issueA = [&](int k, int slot) { spA(k * 128, lA + slot * G_STAGE); };
        {
            const LAS char* pp = opaque(lds + G_PART + row * 8);
            const auto rs = RSRC(zst + (size_t)(pm * 16 + cg) * 128);
            const unsigned vp = pin(lane < 32 ? (unsigned)row * 8u : 0x80000000u);
            asm volatile("" :: "s"(rs));
            BARX();
            const f32x2 a = *(const LAS f32x2*)pp, c = *(const LAS f32x2*)(pp + 1024);
            ST8(rs, vp, 0, ((u32x2){__float_as_uint(add1(a.x, c.x)), min(__float_as_uint(add1(a.y, c.y)), 0xfffffffeu)}), 16);
            SCHED0();
        }
        __builtin_amdgcn_s_setprio(0);
        {
            f32x4 g[4];
            float sm[4];
            auto gather = [&] {
                asm volatile("global_load_dwordx4 %0, %4, off sc1\n\tglobal_load_dwordx4 %1, %4, off offset:1024 sc1\n\t"
                             "global_load_dwordx4 %2, %4, off offset:2048 sc1\n\tglobal_load_dwordx4 %3, %4, off offset:3072 sc1"
                             : "=&v"(g[0]), "=&v"(g[1]), "=&v"(g[2]), "=&v"(g[3]) : "v"(src) : "memory");
            };
            auto total = [&] {
                asm volatile("" : "+v"(g[0]), "+v"(g[1]), "+v"(g[2]), "+v"(g[3]));
#pragma unroll
                for (int i = 0; i < 4; ++i) sm[i] = add1(add1(g[0][i], g[1][i]), add1(g[2][i], g[3][i]));
            };
            step1(9); step1(10);
            BARX(); gather(); issue1(15, 3);
            WAIT_V(22); BARX();
            __builtin_amdgcn_s_setprio(3);
            WAIT_V(9);
            total();
            if (__any(__builtin_isunordered(sm[1], sm[3]))) {
#pragma unroll 1
                for (unsigned spin = 0; spin < 400000u; ++spin) {
                    bool ok = true;
#pragma unroll
                    for (int i = 0; i < 4; ++i) ok &= __float_as_uint(g[i][1]) != 0xffffffffu && __float_as_uint(g[i][3]) != 0xffffffffu;
                    if (__all(ok)) break;
                    gather();
                    WAIT_V(0);
                    total();
                }
            }
            xq4_sum(sm);
            const bool odd = lane & 16;
            float mean;
            const float rs = rstd_of(odd ? sm[2] : sm[0], odd ? sm[3] : sm[1], 1.0f / 2304.0f, mean);
            asm volatile("ds_write_b32 %0, %1" :: "v"(tw), "v"(lane < 32 ? rs : -mean * rs) : "memory");
            __builtin_amdgcn_s_setprio(0);
            issue1B(16, 0);
            WAIT_L0();
        }
        BARX(); issue1A(16, 0); issueA(2 * pm, 1);
        auto kord2 = [&](int t) { const int j = kord(t - 2, 30); return ((j < 2 * (pm & 7) ? j : j + 2) + 2 * (pm & 8)) & 31; };
        unsigned seen;
        auto look = [&] { if (wid == 0) asm volatile("s_nop 4\n\tbuffer_load_dword %0, %1, %2, 0 offen sc1" : "+v"(seen) : "v"(fzi), "s"(RSRC(ws)) : "memory"); };
        auto need = [&](int n) {
            if (wid == 0) {
                asm volatile("" : "+v"(seen));
                if (__ballot(seen == 0u) & ((1ull << n) - 1ull)) flags_wait<0>(ws, lane < n ? fzi : fl + F_Z2 + (cg * 16 + pm) * 256 + pm * 16);
            }
        };
        auto issue = [&](int t, int sl) { const int k = kord2(t); issueB(k, sl); issueA(k, sl); };
        auto steps2 = [&](int t0, int t1) {
            int sl = (2 * t0 + 2) % 3 * G_STAGE, k = kord2(t0 + 3), ob = (k >> 1) * 589824 + (k & 1) * 18432;
            LOOP_ALIGN();
#pragma unroll 1
            for (int t = t0; t < t1; ++t) {
                WAIT_V(9); BARX();
                dma5<true>(lB + sl, rB, ob, vo);
                const int oa = pin_s(k * 128);
                const unsigned la = pin_s(lA + sl);
                k = pin_s(kord2(t + 4)); ob = pin_s((k >> 1) * 589824 + (k & 1) * 18432);
                sl = pin_s(sl == 0 ? 2 * G_STAGE : sl - G_STAGE);
                spA(oa, la);
            }
        };
        WAIT_V(13); BAR(); issue1(17, 2);
        WAIT_V(13); BARX();
        if (wid == 0) flag_raise(ws, fz);
        issueA(2 * pm + 1, 3); if (layer == 0) load5(4, 0);
        if (wid == 0) WAIT_IF(layer, 5, 9); else WAIT_IF(layer, 4, 8);
        BAR(); issueA(kord2(2), 0); if (layer == 0) load5(4, 1);
        BAR(); look(); issueA(kord2(3), 2);
        WAIT_V(4); need(3); BARX(); issueB(kord2(2), 0); issueB(kord2(3), 2); look();
        WAIT_IF(wid == 0, 6, 5); BAR(); issue(4, 1);
        WAIT_V(9); need(8);
        steps2(2, 11); look();
        WAIT_IF(wid == 0, 10, 9); BAR(); issue(14, 0);
        WAIT_V(9); need(16);
        steps2(12, 29);
        WAIT_V(9); BAR(); issue5(0); issue5(1); issue5(2);
        WAIT_V(15); BAR(); issue5(6); issue5(7);
        BAR(); issue5(3); put5(0, 9);
        unsigned vb;
        {
            vb = attn_vb(wid, lane);
            const unsigned fo = flag_out(fl + F_Y, 256, pm, cg, lane), fi = flag_in(fl + F_Y, 256, rh * 8 + c5, 16, lane);
            __builtin_amdgcn_s_setprio(3);
            WAIT_V(0); BARX();
            if (wid == 0) { flag_raise(ws, fo); flags_wait<Y_FIRST>(ws, fi); }
            __builtin_amdgcn_s_setprio(0);
            BARX();
        }
        put5(9, 13);
        BAR(); put5(13, 18);
        BAR(); put5(18, 24);
        BAR(); put5(24, 30);
        BAR(); put5(30, 36);
        BAR(); issue5(16); put5(36, 41);
        BAR(); issue5(17); put5(41, 45);
        WAIT_V(5); BAR();
        WAIT_V(0); BAR();
        if (layer == 0) { attn_issueB(lds, ws, hp, wid, vb, 0, 0); attn_issueB(lds, ws, hp, wid, vb, 1, 1); }
        BAR();
        if (layer == 0) {
            __builtin_amdgcn_s_setprio(3);
            const unsigned fo = flag_out(F_X2, 512, (rh >> 1) + wid, wid ? 16 + c5 : (rh & 1) * 8 + c5, wid == 0 || (wid == 1 && (rh & 1) && rh < 31) ? lane : 16);
            BARX();
            if (wid < 2) flag_raise(ws, fo);
        }
        TRACE_DUMP;
      }
        if (blockIdx.x == 0 && wid8 == 4) ST8(RSRC(ws), lane_id_fresh() ? 0x80000000u : (unsigned)OFF_EP, 0, ((u32x2){(unsigned)ep, (unsigned)(ep >> 32)}), 16);
        asm volatile("gm_code_end1:");
        return;
    }
#pragma unroll 1
  for (int layer = 0; layer < NET_LAYERS; ++layer) {
    NET_IDS
    TRACE_BEGIN;
    char* wl = ws + OFF_L0 + layer * SZ_L;
    const unsigned fl = layer * F_LAYER;
    const char* xin = wl + L_XB;
    char* y = wl + (layer ? L_Z2T : L_Y);
#undef STAMP_SEC
#define STAMP_SEC 0
    {
    STAMP_BEGIN;
    attn_consumer(lds, x0, wl, layer, pa, hp, wid, [&] { WAIT_V(0); prologue_done(lds, ws, ep, b, lane_id_fresh()); } STAMP_FWD);
    STAMP_END(1 + 5 * layer);
    }
#undef STAMP_SEC
#define STAMP_SEC 1
    STAMP_BEGIN;
    gmlp_body(lds, wid & 1, wid >> 1, pm, cg, wl + (layer ? L_Y : L_Z2T), y, bsp STAMP_FWD);
    {
        WAIT_V(0);
        BARX();
        STAMP(11);
        const int lane = lane_id_fresh(), r = lane & 15, q = lane >> 4;
        const int wk = wid >> 1, rt = rh * 4 + (wid & 1) * 2 + wk;
        const Res rx = proj_residual_load(xin, rt, c5, lane);
        const LAS char *pw[3], *ph[3];
#pragma unroll
        for (int i = 0; i < 3; ++i) {
            pw[i] = opaque(lds + 3 * i * K5_STAGE + wk * 4608 + lane * 16);
            ph[i] = opaque(lds + 3 * i * K5_STAGE + wk * 4608 + 4096 + q * 128 + (r & 7) * 16);
        }
        const auto rY = RSRC(y + (size_t)(rt & ~1) * 73728 + wk * 1024);
        const unsigned vy[2] = {pin((unsigned)(wk * 73728 + lane * 16)), pin((unsigned)((1 - wk) * 73728 + lane * 16))};
        int dk[4];
#pragma unroll
        for (int i = 0; i < 4; ++i) dk[i] = pin_s((kord5(4 + i) - 4 - i) * 4096);
        auto yoff = [&](int t) { return pin_s(t == 0 || t == 17 ? kord5(t) * 4096 : t * 4096 + dk[t & 3]); };
        bf16x8 af[5][4], wf[4][5];
        auto lda = [&](int so, int i) { return __builtin_bit_cast(bf16x8, __builtin_amdgcn_raw_buffer_load_b128(rY, vy[i & 1] + (i >> 1) * 2048, so, 16)); };
        auto rdw = [&](int t, int c, int n) {
            const int sl = (t + 6) & 7, o = (sl % 3) * K5_STAGE + c * 9216;
            wf[2 * (t & 1) + c][n] = n < 4 ? LDS_FRAG(pw[sl / 3] + o + n * 1024) : LDS_FRAG(ph[sl / 3] + o);
        };
#pragma unroll
        for (int n = 0; n < 10; ++n) rdw(0, n / 5, n % 5);
        int so[18];
#pragma unroll
        for (int i = 0; i < 5; ++i) so[i] = yoff(i);
        so[17] = yoff(17);
        f32x4 v[5], u[5];
        proj_residual(v, rx);
#pragma unroll
        for (int n = 0; n < 5; ++n) v[n] = pin(v[n]);
        LAS char* xo = (LAS char*)opaque(lds + wid * 4608 + lane * 16);
        LAS char* xo4 = (LAS char*)opaque(lds + wid * 4608 + 4096 + (lane & 31) * 16);
        const LAS char* xi = opaque(lds + (wid ^ 2) * 4608 + lane * 16);
        const LAS char* xi4 = opaque(lds + (wid ^ 2) * 4608 + 4096 + (lane & 31) * 16);
        const ProjOut po = layer ? proj_out_final(out, rt, c5, lane) : proj_out(wl + SZ_L + L_XA, wl + SZ_L + L_STA, rt, c5, lane);
        asm volatile("" :: "s"(rY));
        WAIT_L(0);
        BARX();
        STAMP(12);
#pragma unroll
        for (int i = 0; i < K5_PRE; ++i) { af[i >> 2][i & 3] = lda(so[i >> 2], i & 3); SCHED0(); if (i >= PACE0 && i + 1 < K5_PRE) PACE; SCHED0(); }
        static_for<18>([&](auto tc) {
            constexpr int t = tc.value;
            bf16x8 (&a)[4] = af[t % 5];
#pragma unroll
            for (int h = 0; h < 4; ++h) {
                const int c = t < 17 ? h >> 1 : h & 1, m = t < 17 ? h & 1 : 1 - (h >> 1);
                if (t < 17 ? m == 0 : h < 2) { if (t == 17 && h == 1) WAIT_L(0); else WAIT_L(5); }
#pragma unroll
                for (int n = 0; n < 5; ++n) {
                    f32x4& e = m ? u[n] : v[n];
                    if (t == 0 && h == 1) MFMA_0(wf[0][n], a[1], e); else MFMA_T(wf[2 * (t & 1) + c][n], a[2 * c + m], e);
                    SCHED0();
                    if (t < 17 && m == 0) rdw(t + 1, c, n);
                    if (t < 14 && m == 1 && !(n & 1)) { const int i = y_load_at(6 * t + 3 * c + n / 2); if (i >= 0) af[(i >> 2) % 5][i & 3] = lda(so[i >> 2], i & 3); }
                    if (t < 12 && h == 3 && n == 3) so[t + 5] = yoff(t + 5);
                    if (t == 17 && h * 5 + n >= 13 && h * 5 + n < 18) { const int i = h * 5 + n - 13; *(LAS f32x4*)(i < 4 ? xo + i * 1024 : xo4) = u[i]; }
                    SCHED0();
                }
            }
            if ((t & 1) && t < 17) BAR();
            SCHED0();
        });
        {
            WAIT_L0(); BARX();
#pragma unroll
            for (int n = 0; n < 5; ++n) u[n] = *(const LAS f32x4*)(n < 4 ? xi + n * 1024 : xi4);
            SCHED0();
            asm volatile("s_nop 1" : "+v"(v[0]), "+v"(v[1]), "+v"(v[2]), "+v"(v[3]), "+v"(v[4]));
#pragma unroll
            for (int n = 0; n < 5; ++n) v[n] += u[n];
        }
        STAMP(13);
        if (layer) proj_store<true>(v, po);
        else { proj_store(v, po); WAIT_V(0); BARX(); }
    }
    STAMP_END(3 + 5 * layer);
    TRACE_DUMP;
  }
  asm volatile("gm_code_end2:");
}

}

#define STOP_CHECK(n)

extern "C" void kernel_launch(void* const* d_in, const int* in_sizes, int n_in, void* d_out, int out_size, void* d_ws, size_t ws_size,
                              hipStream_t stream) {
    (void)in_sizes; (void)n_in; (void)out_size; (void)ws_size;
    const u16* x = (const u16*)d_in[0];
    const u16* wqkv = (const u16*)d_in[1];
    const u16* wo = (const u16*)d_in[2];
    const u16* w_in = (const u16*)d_in[4];
    const u16* w_sp = (const u16*)d_in[5];
    const u16* b_sp = (const u16*)d_in[6];
    const u16* w_out = (const u16*)d_in[7];
    char* ws = (char*)d_ws;
    (void)hipFuncSetAttribute((const void*)k_net, hipFuncAttributeMaxDynamicSharedMemorySize, NET_LDS);
    k_net<<<256, 512, NET_LDS, stream>>>(ws, w_sp, b_sp, (char*)d_out, (const char*)x, (const char*)wqkv, wo, w_in, w_out DBG_LAUNCH(0));
}
```

```cpp
#include <hip/hip_runtime.h>
#include <stdint.h>

namespace {

typedef unsigned short u16;
typedef short bf16x8 __attribute__((ext_vector_type(8)));
typedef float f32x4 __attribute__((ext_vector_type(4)));
typedef float f32x2 __attribute__((ext_vector_type(2)));
typedef unsigned u32x4 __attribute__((ext_vector_type(4)));
typedef unsigned u32x2 __attribute__((ext_vector_type(2)));
typedef __bf16 bf16x2_t __attribute__((ext_vector_type(2)));
#define LAS __attribute__((address_space(3)))
#define DEVI __device__ __forceinline__

constexpr int S_ = 2048, D_ = 576, FH = 2304;

constexpr size_t SZ_TWQKV = (size_t)16 * 18 * 6 * 1024;
constexpr size_t SZ_TWT = (size_t)48 * 18 * 256;
constexpr size_t SZ_TWO = (size_t)8 * 18 * 4608;
constexpr size_t SZ_TWIN = (size_t)16 * 18 * 18 * 1024;
constexpr size_t SZ_TWOUT = (size_t)8 * 72 * 4608;
constexpr size_t SZ_X = (size_t)S_ * D_ * 2;
constexpr size_t SZ_Z = (size_t)S_ * FH * 2;
constexpr size_t SZ_ST = (size_t)S_ * 64;
constexpr size_t SZ_ZST = (size_t)16 * 16 * 128 * 8;
constexpr size_t OFF_TWQKV = 0;
constexpr size_t OFF_TWT = OFF_TWQKV + SZ_TWQKV;
constexpr size_t OFF_TWO = OFF_TWT + SZ_TWT;
constexpr size_t OFF_TWIN = OFF_TWO + SZ_TWO;
constexpr size_t OFF_TWOUT = OFF_TWIN + SZ_TWIN;
constexpr size_t OFF_CNT = OFF_TWOUT + SZ_TWOUT;
constexpr size_t SZ_CNT = 10 * 65536;
constexpr size_t OFF_FW = OFF_CNT + SZ_CNT;
constexpr size_t OFF_FL = OFF_FW + 262144;
constexpr size_t OFF_EP = OFF_FL + 4096;
constexpr size_t OFF_FL2 = OFF_EP + 4096;
constexpr size_t OFF_ZST = OFF_FL2 + 8192;
constexpr size_t OFF_L0 = OFF_ZST + 2 * SZ_ZST;
constexpr size_t L_XA = 0, L_STA = L_XA + SZ_X, L_O = L_STA + SZ_ST, L_XB = L_O + SZ_X, L_STB = L_XB + SZ_X, L_Z2T = L_STB + SZ_ST, L_Y = L_Z2T + SZ_Z;
constexpr size_t SZ_L = 0;
static_assert(OFF_L0 + L_Y + SZ_Z <= ((size_t)256 << 20), "workspace");
constexpr unsigned F_O = OFF_CNT, F_X1 = F_O + 65536, F_Z2 = F_O + 131072, F_Y = F_O + 196608, F_LAYER = 262144, F_X2 = F_O + 2 * F_LAYER;

DEVI unsigned pack2(float a, float b) { f32x2 v = {a, b}; bf16x2_t r = __builtin_convertvector(v, bf16x2_t); return __builtin_bit_cast(unsigned, r); }
DEVI float bflo(unsigned u) { return __uint_as_float(u << 16); }
DEVI float bfhi(unsigned u) { return __uint_as_float(u & 0xffff0000u); }
DEVI int slot2row(int sg) { return (sg & ~31) + 2 * (sg & 15) + ((sg >> 4) & 1); }
DEVI float fmax2(float a, float b) { return __builtin_amdgcn_fmed3f(a, b, __builtin_inff()); }
#define XQ4(OP, MID) \
    asm volatile("s_nop 1\n\tv_permlane32_swap_b32 %0, %1\n\tv_permlane32_swap_b32 %2, %3\n\t" OP " %0, %0, %1\n\t" OP " %2, %2, %3\n\t" \
                 "s_nop 1\n\tv_permlane16_swap_b32 %0, %2\n\t" OP " %0, %0, %2\n\t" MID \
                 "v_mov_b32 %2, %0\n\ts_nop 1\n\tv_permlane16_swap_b32 %0, %2\n\tv_mov_b32 %1, %0\n\tv_mov_b32 %3, %2\n\ts_nop 0\n\t" \
                 "v_permlane32_swap_b32 %0, %1\n\tv_permlane32_swap_b32 %2, %3" : "+v"(x[0]), "+v"(x[1]), "+v"(x[2]), "+v"(x[3]))
DEVI void xq4_max(float (&x)[4]) { XQ4("v_max_f32", ""); }
DEVI void xq4_sum(float (&x)[4]) { XQ4("v_add_f32", ""); }
DEVI void xq4_sum_rcp(float (&x)[4]) { XQ4("v_add_f32", "v_rcp_f32 %0, %0\n\ts_nop 0\n\t"); }
template <class T> DEVI T pin(T v) { asm volatile("" : "+v"(v)); return v; }
template <int V> struct IC { static constexpr int value = V; };
template <int N, int I = 0, class F> DEVI void static_for(F&& f) { if constexpr (I < N) { f(IC<I>{}); static_for<N, I + 1>(f); } }
template <class T> DEVI T* own_s(T* p) { T* r; asm volatile("s_mov_b64 %0, %1" : "=&s"(r) : "s"(p)); return r; }
DEVI int pin_s(int v) { asm volatile("" : "+s"(v)); return v; }
DEVI float xq_sum(float v) {
    auto a = __builtin_amdgcn_permlane32_swap(__float_as_uint(v), __float_as_uint(v), false, false);
    v = __uint_as_float(a[0]) + __uint_as_float(a[1]);
    auto b = __builtin_amdgcn_permlane16_swap(__float_as_uint(v), __float_as_uint(v), false, false);
    return __uint_as_float(b[0]) + __uint_as_float(b[1]);
}
#define RSRC(p) __builtin_amdgcn_make_buffer_rsrc((void*)(p), 0, 0x7fffffff, 0x00020000)
#define ST16(rs, vo, so, v, aux) __builtin_amdgcn_raw_buffer_store_b128((v), (rs), (vo) + (so), 0, aux)
#define ST8(rs, vo, so, v, aux) __builtin_amdgcn_raw_buffer_store_b64((v), (rs), (vo), (so), aux)
#define WAIT_V(n) asm volatile("s_waitcnt vmcnt(%0)" ::"i"(n) : "memory")
#define WAIT_L0() asm volatile("s_waitcnt lgkmcnt(0)" ::: "memory")
#define BAR() do { asm volatile("" ::: "memory"); __builtin_amdgcn_s_barrier(); asm volatile("" ::: "memory"); } while (0)
#define BARX() do { __builtin_amdgcn_sched_barrier(0); BAR(); __builtin_amdgcn_sched_barrier(0); } while (0)
#define LOOP_ALIGN() asm volatile(".p2align 6")
#define SCHED0() __builtin_amdgcn_sched_barrier(0)
#define MFMA(a, b, c) __builtin_amdgcn_mfma_f32_16x16x32_bf16((a), (b), (c), 0, 0, 0)
#define MFMA_T(a, b, c) asm volatile("v_mfma_f32_16x16x32_bf16 %0, %1, %2, %0" : "+v"(c) : "v"(a), "v"(b))
#define MFMA_0(a, b, c) asm volatile("v_mfma_f32_16x16x32_bf16 %0, %1, %2, 0" : "=&v"(c) : "v"(a), "v"(b))
#define WAIT_L(n) __builtin_amdgcn_s_waitcnt(0xC07F | ((n) << 8))
#define LDS_FRAG(p) (*(const LAS bf16x8*)(p))
DEVI int lane_id_fresh() { int z = 0; asm volatile("" : "+v"(z)); return __builtin_amdgcn_mbcnt_hi(~0u, __builtin_amdgcn_mbcnt_lo(~0u, z)); }
DEVI const LAS char* opaque(const LAS char* p) { asm volatile("" : "+v"(p)); return p; }
#define DBG_ARG
#define DBG_LAUNCH(n)
#define STAMP(i)
#define STAMP_DECL
#define STAMP_BEGIN
#define STAMP_END(n)
#define STAMP_PARAM
#define STAMP_FWD

#define PST_PARAM
#define PST_FWD
#define PST_DECL
#define PST(i)
#define PST_DUMP(c, k)
#define TRACE_ATTR
#define TRACE_BEGIN
#define TRACE_DUMP

DEVI float add1(float a, float b) { float r; asm("v_add_f32 %0, %1, %2" : "=v"(r) : "v"(a), "v"(b)); return r; }
DEVI float rstd_of(float s, float q, float inv_n, float& mean) {
    float var;
    asm("v_mul_f32 %0, %2, %3\n\tv_mul_f32 %1, %2, %4\n\tv_fma_f32 %1, -%0, %0, %1" : "=&v"(mean), "=&v"(var) : "s"(inv_n), "v"(s), "v"(q));
    return __builtin_amdgcn_rsqf(fmax2(var, 0.f) + 1e-5f);
}
DEVI float ln_rstd(const f32x4 p) {
    float s, q, mean;
    asm("v_add_f32 %0, %2, %4\n\tv_add_f32 %1, %3, %5\n\ts_nop 0\n\t"
        "v_add_f32_dpp %0, %0, %0 quad_perm:[1,0,3,2] row_mask:0xf bank_mask:0xf\n\tv_add_f32_dpp %1, %1, %1 quad_perm:[1,0,3,2] row_mask:0xf bank_mask:0xf\n\ts_nop 0\n\t"
        "v_add_f32_dpp %0, %0, %0 quad_perm:[2,3,0,1] row_mask:0xf bank_mask:0xf\n\tv_add_f32_dpp %1, %1, %1 quad_perm:[2,3,0,1] row_mask:0xf bank_mask:0xf"
        : "=&v"(s), "=&v"(q) : "v"(p[0]), "v"(p[1]), "v"(p[2]), "v"(p[3]));
    return rstd_of(s, q, 1.0f / 576.0f, mean);
}

struct Res { u32x4 a, c; u32x2 e; };
DEVI Res proj_residual_load(const char* xin, int rt, int cg, int lane) {
    const auto rs = RSRC(xin + (size_t)rt * 18432 + cg * 2304);
    Res x;
    x.a = __builtin_amdgcn_raw_buffer_load_b128(rs, lane * 16, 0, 16); x.c = __builtin_amdgcn_raw_buffer_load_b128(rs, lane * 16 + 1024, 0, 16);
    x.e = __builtin_amdgcn_raw_buffer_load_b64(rs, 2048 + (lane & 15) * 16 + ((lane >> 4) & 1) * 8, 0, 16);
    return x;
}
DEVI void proj_residual(f32x4 (&v)[5], const Res& x) {
    v[0] = (f32x4){bflo(x.a.x), bfhi(x.a.x), bflo(x.a.y), bfhi(x.a.y)}; v[1] = (f32x4){bflo(x.a.z), bfhi(x.a.z), bflo(x.a.w), bfhi(x.a.w)};
    v[2] = (f32x4){bflo(x.c.x), bfhi(x.c.x), bflo(x.c.y), bfhi(x.c.y)}; v[3] = (f32x4){bflo(x.c.z), bfhi(x.c.z), bflo(x.c.w), bfhi(x.c.w)};
    v[4] = (f32x4){bflo(x.e.x), bfhi(x.e.x), bflo(x.e.y), bfhi(x.e.y)};
}
struct ProjOut { __amdgpu_buffer_rsrc_t x, st; unsigned v0, v1, v2, vs; };
DEVI ProjOut proj_out(char* xout, char* stout, int rt, int cg, int lane) {
    const int r = lane & 15, q = lane >> 4;
    ProjOut p = {RSRC(xout + (size_t)rt * 18432 + cg * 2304), RSRC(stout + (size_t)rt * 1024 + cg * 8),
                 pin((unsigned)lane * 16u), pin((unsigned)lane * 16u + 1024u), pin((unsigned)(2048 + r * 16 + (q & 1) * 8)), pin((unsigned)(r * 64 + (q >> 1) * 4))};
    asm volatile("" :: "s"(p.x), "s"(p.st));
    return p;
}
DEVI ProjOut proj_out_final(char* out, int rt, int cg, int lane) {
    const int q = lane >> 4;
    const unsigned o = (unsigned)(slot2row(rt * 16 + (lane & 15)) * 1152 + cg * 144);
    ProjOut p = {RSRC(out), RSRC(out), pin(o + q * 16), pin(o + 64 + q * 16), pin(o + 128 + (q & 1) * 8), 0u};
    return p;
}
template <bool FINAL = false>
DEVI void proj_store(f32x4 (&v)[5], const ProjOut& p) {
    ST16(p.x, p.v0, 0, ((u32x4){pack2(v[0][0], v[0][1]), pack2(v[0][2], v[0][3]), pack2(v[1][0], v[1][1]), pack2(v[1][2], v[1][3])}), FINAL ? 0 : 16);
    SCHED0();
    asm volatile("" : "+v"(v[2]), "+v"(v[3]), "+v"(v[4]));
    ST16(p.x, p.v1, 0, ((u32x4){pack2(v[2][0], v[2][1]), pack2(v[2][2], v[2][3]), pack2(v[3][0], v[3][1]), pack2(v[3][2], v[3][3])}), FINAL ? 0 : 16);
    SCHED0();
    ST8(p.x, p.v2, 0, ((u32x2){pack2(v[4][0], v[4][1]), pack2(v[4][2], v[4][3])}), FINAL ? 0 : 16);
    SCHED0();
    if (!FINAL) {
        const f32x4 s4 = ((v[0] + v[1]) + (v[2] + v[3])) + v[4] * 0.5f;
        const f32x4 q4 = ((v[0] * v[0] + v[1] * v[1]) + (v[2] * v[2] + v[3] * v[3])) + v[4] * v[4] * 0.5f;
        auto x = __builtin_amdgcn_permlane32_swap(__float_as_uint((s4[0] + s4[2]) + (s4[1] + s4[3])), __float_as_uint((q4[0] + q4[2]) + (q4[1] + q4[3])), false, false);
        const float t = __uint_as_float(x[0]) + __uint_as_float(x[1]);
        auto y = __builtin_amdgcn_permlane16_swap(__float_as_uint(t), __float_as_uint(t), false, false);
        __builtin_amdgcn_raw_buffer_store_b32(__float_as_uint(__uint_as_float(y[0]) + __uint_as_float(y[1])), p.st, p.vs, 0, 16);
        SCHED0();
    }
}

DEVI void side_load(f32x4 (&d)[3], const char* p, const char* p2) {
    asm volatile("global_load_dwordx4 %0, %3, off sc1\n\tglobal_load_dwordx4 %1, %3, off offset:1024 sc1\n\tglobal_load_dwordx4 %2, %4, off sc1"
                 : "=&v"(d[0]), "=&v"(d[1]), "=&v"(d[2]) : "v"(p), "v"(p2) : "memory");
}
DEVI void side_fence(f32x4 (&d)[3]) { asm volatile("" : "+v"(d[0]), "+v"(d[1]), "+v"(d[2])); }
DEVI void lds_word_set(LAS char* p, unsigned v) { asm volatile("ds_write_b32 %0, %1" :: "v"((unsigned)(size_t)p), "v"(v) : "memory"); }
#define DMA_I(v, off, sc) "buffer_load_dwordx4 " v ", %1, %2 offen offset:" off sc " lds\n\t"
#define DMA5(sc) asm volatile("s_mov_b32 m0, %0\n\ts_mov_b32 exec_hi, 0\n\ts_nop 2\n\t" DMA_I("%3", "0", sc) "s_mov_b32 exec_hi, -1\n\t" \
    DMA_I("%3", "512", sc) DMA_I("%3", "1536", sc) DMA_I("%3", "2560", sc) DMA_I("%3", "3584", sc) :: "s"(la), "s"(rs), "s"(so), "v"(vo) : "memory")
#define DMA4(sc) asm volatile("s_mov_b32 m0, %0\n\ts_nop 3\n\t" DMA_I("%3", "0", sc) DMA_I("%4", "1024", sc) DMA_I("%5", "2048", sc) DMA_I("%6", "3072", sc) \
    :: "s"(la), "s"(rs), "s"(so), "v"(v0), "v"(v1), "v"(v2), "v"(v3) : "memory")
template <bool SC1> DEVI void dma5(unsigned la, __amdgpu_buffer_rsrc_t rs, int so, unsigned vo) { if (SC1) DMA5(" sc1"); else DMA5(""); }
template <bool SC1> DEVI void dma4(unsigned la, __amdgpu_buffer_rsrc_t rs, int so, unsigned v0, unsigned v1, unsigned v2, unsigned v3) { if (SC1) DMA4(" sc1"); else DMA4(""); }
#define DMA2(sc) asm volatile("s_mov_b32 m0, %0\n\ts_nop 3\n\t" DMA_I("%3", "0", sc) DMA_I("%4", "1024", sc) :: "s"(la), "s"(rs), "s"(so), "v"(v0), "v"(v1) : "memory")
template <bool SC1> DEVI void dma2(unsigned la, __amdgpu_buffer_rsrc_t rs, int so, unsigned v0, unsigned v1) { if (SC1) DMA2(" sc1"); else DMA2(""); }
DEVI void dma1(unsigned la, __amdgpu_buffer_rsrc_t rs, int so, unsigned v0) { asm volatile("s_mov_b32 m0, %0\n\ts_nop 3\n\t" DMA_I("%3", "0", "") :: "s"(la), "s"(rs), "s"(so), "v"(v0) : "memory"); }
#define WAIT_IF(c, a, b) do { if (c) WAIT_V(a); else WAIT_V(b); } while (0)
template <int T0, int T1, int P, int S0 = 0, class ISSUE>
DEVI void ring_steps(ISSUE&& issue) {
    int slot = (T0 + S0) % 3;
    LOOP_ALIGN();
#pragma unroll 1
    for (int t = T0; t < T1; ++t) {
        WAIT_V(P); BAR();
        issue(t + 3, slot);
        slot = slot == 2 ? 0 : slot + 1;
    }
}

DEVI unsigned flag_out(unsigned use, int pitch, int group, int writer, int lane) { return pin(lane < 16 ? use + (group * 16 + lane) * pitch + writer * 16 : 0x80000000u); }
DEVI unsigned flag_in(unsigned use, int pitch, int reader, int n, int lane) { return pin(use + reader * pitch + (lane < n ? lane : 0) * 16); }
DEVI void flag_raise(const char* ws, unsigned vo) { ST16(RSRC(ws), vo, 0, ((u32x4){1u, 1u, 1u, 1u}), 16); SCHED0(); }
template <int FIRST>
DEVI void flags_wait(const char* ws, unsigned vo) {
    unsigned v, n = 400000u;
    asm volatile("s_nop 4\n\ts_sleep %4\n"
                 "1:\n\tbuffer_load_dword %0, %2, %3, 0 offen sc1\n\ts_waitcnt vmcnt(0)\n\tv_cmp_eq_u32 vcc, 0, %0\n\ts_cbranch_vccz 2f\n\t"
                 "s_sleep 1\n\ts_sub_u32 %1, %1, 1\n\ts_cbranch_scc0 1b\n2:"
                 : "=&v"(v), "+s"(n) : "v"(vo), "s"(RSRC(ws)), "i"(FIRST) : "vcc", "scc", "memory");
    SCHED0();
}
template <int FIRST>
DEVI void epoch_wait(const char* ws, unsigned vo, unsigned long long ep) {
    unsigned long long v; unsigned n = 400000u;
    asm volatile("s_nop 4\n\ts_sleep %5\n"
                 "1:\n\tbuffer_load_dwordx2 %0, %2, %3, 0 offen sc1\n\ts_waitcnt vmcnt(0)\n\tv_cmp_ne_u64 vcc, %4, %0\n\ts_cbranch_vccz 2f\n\t"
                 "s_sleep 1\n\ts_sub_u32 %1, %1, 1\n\ts_cbranch_scc0 1b\n2:"
                 : "=&v"(v), "+s"(n) : "v"(vo), "s"(RSRC(ws)), "s"(ep), "i"(FIRST) : "vcc", "scc", "memory");
    SCHED0();
}
DEVI unsigned lds_arrive(LAS char* p) {
    unsigned v;
    asm volatile("s_mov_b64 exec, 1\n\tds_add_rtn_u32 %0, %1, %2\n\ts_mov_b64 exec, -1\n\ts_waitcnt lgkmcnt(0)" : "=&v"(v) : "v"((unsigned)(size_t)p), "v"(1u) : "memory");
    return __builtin_amdgcn_readfirstlane(v);
}
DEVI void lds_count_wait(LAS char* p, unsigned n) {
#pragma unroll 1
    for (unsigned spin = 0; spin < 40000000u; ++spin) {
        unsigned v;
        asm volatile("ds_read_b32 %0, %1\n\ts_waitcnt lgkmcnt(0)" : "=v"(v) : "v"((unsigned)(size_t)p) : "memory");
        if (__builtin_amdgcn_readfirstlane(v) >= n) break;
    }
}

constexpr int K1_STAGE = 32768;
constexpr int WO_OFF = 69632;
constexpr int G_OWN = 139264;
constexpr int G_PART = G_OWN + 18432;
constexpr int G_TAB = G_PART + 2048;
constexpr int K1_SCR = G_TAB + 1536;
constexpr int G_WORD = K1_SCR + 576;
constexpr int NET_LDS = G_WORD + 64;

#define WO_PRE 9
#define WO_W 33
#define REL_PRIO 0
#define W_FIRST 2
#define W_ALONE 12
#define I_SLEEP 12
#define OJOB_SLEEP 20
#define PACE0 1
#define PACE __builtin_amdgcn_s_sleep(1)
#define POLLW 0
#define WO_EV 4
#define WSP_PF 0
#define W_PF 1
#define O_FIRST 10
#define X1_FIRST 6
#define X2_FIRST 6
#define Y_FIRST 10
#define Z2_FIRST 10
DEVI unsigned attn_vb(int wid, int lane) {
    const int c = lane & 15;
    return pin((unsigned)(wid < 3 ? lane * 16 : (c < 8 ? (c >> 1) & 1 : 2) * 4608 + (lane >> 4) * 64 + (c < 8 ? (c >> 2) * 2 + (c & 1) : c & 3) * 16));
}
DEVI void attn_issueB(LAS char* lds, const char* ws, int hp, int wid, unsigned vb, int s, int slot) {
    const unsigned la = (unsigned)(size_t)lds + slot * K1_STAGE + 18432 + wid * 4096;
    if (wid < 3) dma4<true>(la, RSRC(ws), (int)OFF_TWQKV + hp * (18 * 6144) + wid * 4096 + s * 12288, vb, vb, vb, vb);
    else asm volatile("s_mov_b32 m0, %0\n\ts_nop 3\n\tbuffer_load_dwordx4 %4, %1, %2 offen sc1 lds\n\tbuffer_load_dwordx4 %4, %1, %3 offen offset:1024 sc1 lds"
                      :: "s"(la), "s"(RSRC(ws)), "s"((int)OFF_TWT + hp * 13824 + s * 512), "s"((int)OFF_TWT + hp * 13824 + s * 512 + 256 - 1024), "v"(vb) : "memory");
}
struct AttnX { __amdgpu_buffer_rsrc_t rs; unsigned v[4], vh[2]; int oA, oh, step; };
DEVI AttnX attn_x(const char* x0, const char* xa, int layer, int pm, int wid, int lane) {
    const int l3 = lane >> 3, pmh = pm > 0 ? pm : 1;
    const unsigned v = layer ? lane * 16 : 2 * l3 * 1152 + ((lane & 7) ^ l3) * 16 + 4096, d1 = layer ? 0 : 16 * 1152 - 1024, d2 = layer ? 16384 : 1152 - 2048;
    const unsigned vh = layer ? (l3 & 1) * 18432 + (lane >> 4) * 256 + (8 + (lane & 7)) * 16 : v + 16 * 1152;
    return {RSRC(layer ? xa : x0 - 4096), {pin(v), pin(v + d1), pin(v + d2), pin(v + d1 + d2)}, {pin(vh), pin(vh + (layer ? 0 : 1152 - 1024))},
            (pm * 8 + 2 * wid) * 18432, (pmh * 8 - 2) * 18432, layer ? 2048 : 128};
}
DEVI void attn_issueA(LAS char* lds, const AttnX& a, int wid, int s, int slot) {
    const unsigned la = (unsigned)(size_t)lds + slot * K1_STAGE;
    if (wid == 3) dma2<true>(la, a.rs, a.oh + s * a.step, a.vh[0], a.vh[1]);
    dma4<true>(la + 2048 + wid * 4096, a.rs, a.oA + s * a.step, a.v[0], a.v[1], a.v[2], a.v[3]);
}
template <class DONE, class NEXT>
DEVI void attn_producer(LAS char* lds, const char* ws, const char* x0, const char* wl, unsigned fl, unsigned long long ep, int layer, int pm, int hp, int wid, DONE&& done, NEXT&& next) {
    const int lane = lane_id_fresh();
    const int pmh = pm > 0 ? pm : 1;
    const unsigned vo = lane * 16;
    const AttnX ax = attn_x(x0, wl + L_XA, layer, pm, wid, lane);
    const unsigned vb = attn_vb(wid, lane), fd = pin((unsigned)OFF_FL + wid * 1024 + vo);
    auto issueB = [&](int s, int slot) { attn_issueB(lds, ws, hp, wid, vb, s, slot); };
    auto issue = [&](int s, int slot) { attn_issueA(lds, ax, wid, s, slot); issueB(s, slot); };
    const int hr = lane >> 2;
    const char* ps = pin(wl + L_STA + (size_t)(pm * 128 + wid * 32) * 64 + lane * 16);
    const char* psh = pin(wl + L_STA + (size_t)((pmh * 8 - 2 + (hr >> 3)) * 16 + 8 + (hr & 7)) * 64 + (lane & 3) * 16);
    if (layer == 0) {
        const unsigned fi = pin((unsigned)OFF_FW + (pm * 16 + hp) * 1024 + (lane < 6 ? lane : 0) * 128);
        WAIT_V(0); done();
        if (wid == 0) epoch_wait<W_FIRST>(ws, fi, ep);
        BARX();
        issueB(0, 0); issueB(1, 1); issue(2, 2);
        WAIT_IF(wid == 3, 10, 12);
    } else {
        issueB(2, 2);
        const unsigned fi = flag_in(F_X2, 512, pm * 16 + hp, pm > 0 ? 24 : 16, lane);
        if (wid == 0) flags_wait<X2_FIRST>(ws, fi);
        BARX();
#pragma unroll
        for (int i = 0; i < 3; ++i) attn_issueA(lds, ax, wid, i, i);
        WAIT_IF(wid == 3, 12, 8);
    }
    __builtin_amdgcn_s_setprio(0);
    BAR();
    if (layer) WAIT_IF(wid == 3, 6, 4);
    ring_steps<0, 4, 8>(issue);
    {
        unsigned long long done;
        if (layer == 0) asm volatile("s_nop 4\n\tbuffer_load_dwordx2 %0, %1, %2, 0 offen sc1" : "+v"(done) : "v"(fd), "s"(RSRC(ws)) : "memory");
        WAIT_IF(layer, 8, 9); BAR(); issue(7, 1);
        WAIT_V(8);
        if (layer == 0) { asm volatile("" : "+v"(done)); if (__any(done != ep)) epoch_wait<0>(ws, fd, ep); }
        BAR(); issue(8, 2);
    }
    const auto rW = RSRC(ws + OFF_TWO + (size_t)(hp >> 1) * 82944 + wid * 4096);
    auto issueW = [&](int i) { dma4<true>((unsigned)(size_t)lds + WO_OFF + (i * 4 + wid) * 4096, rW, i * 16384, vo, vo, vo, vo); };
    {
        f32x4 sp[3];
        side_load(sp, ps, psh);
        WAIT_V(11); BAR(); issueW(2); issueW(3);
        WAIT_V(8); BARX();
        side_fence(sp);
#pragma unroll
        for (int i = 0; i < 3; ++i) lds_word_set(lds + K1_SCR + (i < 2 ? 16 + wid * 32 + i * 16 + hr : hr) * 4, __float_as_uint(ln_rstd(sp[i])));
        WAIT_L0();
    }
    BAR();
    next(0);
    issueW(0); issueW(1); issueW(4);
    if (wid == 0) dma2<true>((unsigned)(size_t)lds + WO_OFF + 80896, rW, 80896, vo, vo);
    next(1);
    const unsigned fo = flag_out(fl + F_O, 256, pm, hp, lane), fi = flag_in(fl + F_O, 256, pm * 16 + hp, 16, lane);
    WAIT_V(0);
    if (wid == POLLW) __builtin_amdgcn_s_setprio(3);
    BARX();
    if (wid == POLLW) { flag_raise(ws, fo); flags_wait<O_FIRST>(ws, fi); }
    __builtin_amdgcn_s_setprio(0);
    BARX();
}

#define STAMP_SEC 0
template <class DONE>
DEVI void attn_consumer(LAS char* lds, const char* x0, char* wl, int layer, int pm, int hp, int wid, DONE&& done STAMP_PARAM) {
    const int lane = lane_id_fresh(), r = lane & 15, q = lane >> 4;
    const char* xa = wl + L_XA;
    char* o = wl + L_O;
    const int T = wid & 1, h = wid >> 1;
    const LAS char *ph[2], *pa[2];
#pragma unroll
    for (int kk = 0; kk < 2; ++kk) {
        const int sw = ((4 * kk + q) ^ (r & 7)) * 16;
        ph[kk] = opaque(lds + (layer ? kk * 1024 + q * 256 + (r ^ (T ? 0 : 8)) * 16 : (r ^ (T ? 0 : 8)) * 128 + sw));
        pa[kk] = opaque(lds + T * 2048 + (layer ? kk * 1024 + lane * 16 : r * 128 + sw));
    }
    const LAS char* pb = lds + h * 3072 + lane * 16;
    const LAS char* pt = lds + lane * 16;
    f32x4 acc[5][4];
#pragma unroll
    for (int m = 0; m < 5; ++m)
#pragma unroll
        for (int n = 0; n < 4; ++n) if (m + n) acc[m][n] = pin((f32x4){0.f, 0.f, 0.f, 0.f});
    bf16x8 fa[2][5], fb[2][4];
    auto rd1 = [&](int kk, int i) {
        if (i < 3) fb[kk][i] = LDS_FRAG(pb + 18432 + kk * 6144 + i * 1024);
        else if (i == 3) fb[kk][3] = LDS_FRAG(pt + 30720 + kk * 1024);
        else if (i == 4) fa[kk][0] = LDS_FRAG(ph[kk]);
        else if (i < 9) fa[kk][i - 4] = LDS_FRAG(pa[kk] + 2048 + (i - 5) * 4096);
    };
    f32x4 mb, mb0;
#pragma unroll
    for (int j = 0; j < 4; ++j) {
        const bool ok = ((r - 4 * q - j) & 15) < 8;
        mb[j] = ok ? 0.f : -__builtin_inff();
        mb0[j] = (ok && (pm > 0 || 4 * q + j <= r)) ? 0.f : -__builtin_inff();
    }
    mb = pin(mb); mb0 = pin(mb0);
    const unsigned mp0 = pin((4 * q > r ? 0xffffu : 0u) | (4 * q + 1 > r ? 0xffff0000u : 0u)), mp1 = pin((4 * q + 2 > r ? 0xffffu : 0u) | (4 * q + 3 > r ? 0xffff0000u : 0u));
    const unsigned qtm = pin(q == h ? 0xffffffffu : 0u);
    const unsigned vom = pin((unsigned)(r * 16 + hp * 1024 + (2 * h + (q >> 1)) * 256 + (q & 1) * 8)), vot = pin((unsigned)(r * 16 + 16384 + (hp >> 1) * 256 + (hp & 1) * 8 + h * 4));
    const LAS char* tk0 = opaque(lds + K1_SCR + ((q * 4) ^ (T ? 0 : 8)) * 4);
    const LAS char* tk = opaque(lds + K1_SCR + ((1 + T) * 16 + q * 4) * 4);
    const LAS char* tq = opaque(lds + K1_SCR + ((1 + T) * 16 + r) * 4);
    pb = opaque(pb); pt = opaque(pt);
    STAMP(2);
    const int rt = pm * 8 + (hp & 1) * 4 + wid, cg = hp >> 1;
    const ProjOut pr = layer ? proj_out((char*)xa, (char*)xa, rt, cg, lane) : proj_out_final((char*)x0, rt, cg, lane);
    BAR();
    if (layer == 0) done();
    BARX();
    STAMP(3);
#pragma unroll
    for (int i = 0; i < 9; ++i) rd1(0, i);
    SCHED0();
    const Res rx = {__builtin_amdgcn_raw_buffer_load_b128(pr.x, pr.v0, 0, 16), __builtin_amdgcn_raw_buffer_load_b128(pr.x, pr.v1, 0, 16), __builtin_amdgcn_raw_buffer_load_b64(pr.x, pr.v2, 0, 16)};
    SCHED0();
    int slot = 0, left = 9;
    LOOP_ALIGN();
#pragma unroll 1
    do {
        int d;
#pragma unroll
        for (int kk = 0; kk < 2; ++kk) {
            WAIT_L(0);
            if (kk) BAR();
            SCHED0();
#pragma unroll
            for (int i = 1; i < 20; ++i) {
                const int m = i >> 2, n = i & 3;
                const bf16x8 &A = fa[kk][m], &B = fb[kk][n];
                acc[m][n] = n != 2 ? MFMA(B, A, acc[m][n]) : MFMA(A, B, acc[m][n]);
                SCHED0();
                if (i <= 9) rd1(kk ^ 1, i - 1);
                if (kk == 0) {
                    if (i == 10) d = pin_s(slot == 2 ? -2 * K1_STAGE : K1_STAGE);
                    if (i == 11 || i == 12) ph[i & 1] = opaque(ph[i & 1] + d);
                    if (i == 13 || i == 14) pa[i & 1] = opaque(pa[i & 1] + d);
                    if (i == 15) pb = opaque(pb + d);
                    if (i == 16) pt = opaque(pt + d);
                } else {
                    if (i == 11) slot = pin_s(slot == 2 ? 0 : slot + 1);
                    if (i == 13) left = pin_s(left - 1);
                }
                SCHED0();
            }
        }
    } while (left);
    STAMP(4);
    u32x2 ts[5], tf[5];
    {
        LAS char* xb = lds + wid * 320;
#pragma unroll
        for (int m = 0; m < 5; ++m) ts[m] = (u32x2){pack2(acc[m][3][0], acc[m][3][1]), pack2(acc[m][3][2], acc[m][3][3])};
        if (q == 2) {
#pragma unroll
            for (int m = 0; m < 5; ++m) {
                const unsigned w = h ? ts[m].y : ts[m].x;
                *(LAS u16*)(xb + m * 64 + r * 2) = (u16)w; *(LAS u16*)(xb + m * 64 + 32 + r * 2) = (u16)(w >> 16);
            }
        }
        asm volatile("" ::: "memory");
#pragma unroll
        for (int m = 0; m < 5; ++m) tf[m] = *(const LAS u32x2*)(xb + m * 64 + (r & 1) * 32 + q * 8);
    }
    {
        f32x4 rk[5]; float rq[4];
        rk[0] = *(const LAS f32x4*)(tk0);
#pragma unroll
        for (int g = 0; g < 4; ++g) { rk[g + 1] = *(const LAS f32x4*)(tk + g * 128); rq[g] = *(const LAS float*)(tq + g * 128); }
        const f32x4 zero = {0.f, 0.f, 0.f, 0.f};
        u32x4 kf[5];
#pragma unroll
        for (int m = 0; m < 5; ++m) kf[m] = (u32x4){pack2(acc[m][1][0], acc[m][1][1]), pack2(acc[m][1][2], acc[m][1][3]), ts[m].y, 0u};
        f32x4 sp[4], sc[4];
#pragma unroll
        for (int g = 0; g < 4; ++g) {
            const u32x4 qf = {pack2(acc[g + 1][0][0], acc[g + 1][0][1]), pack2(acc[g + 1][0][2], acc[g + 1][0][3]), ts[g + 1].x & qtm, 0u};
            sp[g] = MFMA(__builtin_bit_cast(bf16x8, kf[g]), __builtin_bit_cast(bf16x8, qf), zero);
            sc[g] = MFMA(__builtin_bit_cast(bf16x8, kf[g + 1]), __builtin_bit_cast(bf16x8, qf), zero);
        }
        u32x2 vf[5];
#pragma unroll
        for (int m = 0; m < 5; ++m) vf[m] = (u32x2){pack2(acc[m][2][0], acc[m][2][1]), pack2(acc[m][2][2], acc[m][2][3])};
        f32x4 rs[4], t[4]; float mx[4], sm[4];
#pragma unroll
        for (int g = 0; g < 4; ++g) {
#pragma unroll
            for (int j = 0; j < 4; ++j) { const bool pv = 4 * q + j > r; rs[g][j] = pv ? rk[g][j] : rk[g + 1][j]; t[g][j] = pv ? sp[g][j] : sc[g][j]; }
            t[g] = (t[g] * rs[g]) * (rq[g] * (0.23570226039551584f * 1.4426950408889634f)) + (g == 0 ? mb0 : mb);
            mx[g] = fmaxf(fmaxf(t[g][0], t[g][1]), fmaxf(t[g][2], t[g][3]));
        }
        xq4_max(mx);
#pragma unroll
        for (int g = 0; g < 4; ++g) {
            t[g] = t[g] - mx[g];
#pragma unroll
            for (int j = 0; j < 4; ++j) t[g][j] = __builtin_amdgcn_exp2f(t[g][j]);
            sm[g] = (t[g][0] + t[g][2]) + (t[g][1] + t[g][3]);
        }
        f32x4 om[4], ot[4];
#pragma unroll
        for (int g = 0; g < 4; ++g) {
            const f32x4 p = t[g] * rs[g];
            const unsigned p01 = pack2(p[0], p[1]), p23 = pack2(p[2], p[3]);
            const u32x4 pf = {p01 & mp0, p23 & mp1, p01 & ~mp0, p23 & ~mp1};
            om[g] = MFMA(__builtin_bit_cast(bf16x8, ((u32x4){vf[g].x, vf[g].y, vf[g + 1].x, vf[g + 1].y})), __builtin_bit_cast(bf16x8, pf), zero);
            ot[g] = MFMA(__builtin_bit_cast(bf16x8, ((u32x4){tf[g].x, tf[g].y, tf[g + 1].x, tf[g + 1].y})), __builtin_bit_cast(bf16x8, pf), zero);
        }
        xq4_sum_rcp(sm);
        const auto ro = RSRC(o + (size_t)(pm * 8 + T) * 18432);
        unsigned tw2[4];
#pragma unroll
        for (int g = 0; g < 4; ++g) {
            const f32x4 v = om[g] * sm[g];
            __builtin_amdgcn_raw_buffer_store_b64((u32x2){pack2(v[0], v[1]), pack2(v[2], v[3])}, ro, vom, g * 36864, 16);
            const f32x2 w = ot[g].xy * sm[g];
            tw2[g] = pack2(w.x, w.y);
        }
        if (q == 2) {
#pragma unroll
            for (int g = 0; g < 4; ++g) __builtin_amdgcn_raw_buffer_store_b32(tw2[g], ro, vot, g * 36864, 16);
        }
    }
    STAMP(5);
    {
        WAIT_V(0); BARX();
        STAMP(6);
        f32x4 v[5];
        proj_residual(v, rx);
        const LAS char* pw = opaque(lds + WO_OFF + lane * 16);
        const LAS char* pw2 = opaque(lds + WO_OFF + 41472 + lane * 16);
        const LAS char* ph2 = opaque(lds + WO_OFF + 4096 + q * 128 + (r & 7) * 16);
        const LAS char* ph3 = opaque(lds + WO_OFF + 41472 + 4096 + q * 128 + (r & 7) * 16);
        bf16x8 wf[90];
        auto rdw = [&](int i) {
            const int kc = i / 5, n = i % 5;
            wf[i] = n < 4 ? LDS_FRAG((kc < 9 ? pw : pw2) + (kc % 9) * 4608 + n * 1024) : LDS_FRAG((kc < 9 ? ph2 : ph3) + (kc % 9) * 4608);
        };
#pragma unroll
        for (int i = 0; i < WO_W; ++i) rdw(i);
#pragma unroll
        for (int n = 0; n < 5; ++n) v[n] = pin(v[n]);
        const auto rO = RSRC(o + (size_t)rt * 18432);
        const ProjOut po = proj_out(wl + L_XB, wl + L_STB, rt, cg, lane);
        asm volatile("" :: "s"(rO));
        WAIT_L(0);
        __builtin_amdgcn_s_setprio(REL_PRIO);
        BARX();
        STAMP(7);
        bf16x8 af[18];
        auto ldo = [&](int kc) { af[kc] = __builtin_bit_cast(bf16x8, __builtin_amdgcn_raw_buffer_load_b128(rO, lane * 16 + (kc & 3) * 1024, (kc >> 2) * 4096, 16)); };
#pragma unroll
        for (int kc = 0; kc < WO_PRE; ++kc) { ldo(kc); SCHED0(); if (kc >= PACE0 && kc + 1 < WO_PRE) PACE; SCHED0(); }
        static_for<90>([&](auto ic) {
            constexpr int i = ic.value, kc = i / 5, n = i % 5, j = WO_W + 2 * (i / 3) + i % 3;
            MFMA_T(wf[i], af[kc], v[n]);
            SCHED0();
            if (i % 3 < 2 && j < 90) rdw(j);
            if (i % WO_EV == 2 && i / WO_EV + WO_PRE < 18) ldo(i / WO_EV + WO_PRE);
            SCHED0();
        });
        asm volatile("s_nop 4" : "+v"(v[0]), "+v"(v[1]));
        STAMP(8);
        proj_store(v, po);
        __builtin_amdgcn_s_setprio(0);
    }
}

DEVI f32x2 gelu2(f32x2 v) {
    const f32x2 w = v * (v * v * -0.10294324f + -2.3022082f);
    const f32x2 d = (f32x2){__builtin_amdgcn_exp2f(w.x), __builtin_amdgcn_exp2f(w.y)} + 1.0f;
    return v * (f32x2){__builtin_amdgcn_rcpf(d.x), __builtin_amdgcn_rcpf(d.y)};
}
DEVI f32x4 gelu4(f32x4 v) { const f32x2 a = gelu2(v.xy), b = gelu2(v.zw); return (f32x4){a.x, a.y, b.x, b.y}; }
DEVI void rowsum16x8(const f32x4 a, const f32x4 b, float& o0, float& o1) {
    float t0, t1, t2, t3;
    asm volatile(
        "s_nop 1\n\t"
        "v_add_f32_dpp %2, %6, %6 row_ror:8 row_mask:0xf bank_mask:0x3\n\t"
        "v_add_f32_dpp %3, %7, %7 row_ror:8 row_mask:0xf bank_mask:0x3\n\t"
        "v_add_f32_dpp %4, %10, %10 row_ror:8 row_mask:0xf bank_mask:0x3\n\t"
        "v_add_f32_dpp %5, %11, %11 row_ror:8 row_mask:0xf bank_mask:0x3\n\t"
        "v_add_f32_dpp %2, %8, %8 row_ror:8 row_mask:0xf bank_mask:0xc\n\t"
        "v_add_f32_dpp %3, %9, %9 row_ror:8 row_mask:0xf bank_mask:0xc\n\t"
        "v_add_f32_dpp %4, %12, %12 row_ror:8 row_mask:0xf bank_mask:0xc\n\t"
        "v_add_f32_dpp %5, %13, %13 row_ror:8 row_mask:0xf bank_mask:0xc\n\t"
        "v_add_f32_dpp %0, %2, %2 row_half_mirror row_mask:0xf bank_mask:0x5\n\t"
        "s_nop 0\n\t"
        "v_add_f32_dpp %1, %4, %4 row_half_mirror row_mask:0xf bank_mask:0x5\n\t"
        "v_add_f32_dpp %0, %3, %3 row_half_mirror row_mask:0xf bank_mask:0xa\n\t"
        "v_add_f32_dpp %1, %5, %5 row_half_mirror row_mask:0xf bank_mask:0xa\n\t"
        "s_nop 0\n\t"
        "v_add_f32_dpp %0, %0, %0 quad_perm:[1,0,3,2] row_mask:0xf bank_mask:0xf\n\t"
        "v_add_f32_dpp %1, %1, %1 quad_perm:[1,0,3,2] row_mask:0xf bank_mask:0xf\n\t"
        "s_nop 0\n\t"
        "v_add_f32_dpp %0, %0, %0 quad_perm:[2,3,0,1] row_mask:0xf bank_mask:0xf\n\t"
        "v_add_f32_dpp %1, %1, %1 quad_perm:[2,3,0,1] row_mask:0xf bank_mask:0xf\n\t"
        : "=&v"(o0), "=&v"(o1), "=&v"(t0), "=&v"(t1), "=&v"(t2), "=&v"(t3)
        : "v"(a[0]), "v"(a[1]), "v"(a[2]), "v"(a[3]), "v"(b[0]), "v"(b[1]), "v"(b[2]), "v"(b[3]));
}

#define K5_PRE 9
#define RH_MAP 0
#define NET_LAYERS 2
constexpr int G_STAGE = 34816;
constexpr int K5_STAGE = 18432, K5_NS = 8;

constexpr int y_load_at(int slot) {
    int next = K5_PRE;
    for (int s = 0;; ++s) { const bool go = next < 18 + 2 * (s / 3); if (s == slot) return go ? next : -1; next += go; }
}

struct GTile { f32x4 a[4][4], e[2]; };
struct GFrag { bf16x8 a[2][4], b[2][5]; const LAS char *pa[2][2], *pb, *p8; int slot; };
DEVI void g_rd(GFrag& f, int kk, int i) {
    if (i < 4) f.a[kk][i] = LDS_FRAG(f.pa[kk][i >> 1] + (i & 1) * 2048);
    else if (i < 8) f.b[kk][i - 4] = LDS_FRAG(f.pb + (kk * 9 + i - 4) * 1024);
    else f.b[kk][4] = LDS_FRAG(f.p8 + kk * 9216);
}
template <int NS, int G, bool ROWS_IN_REGS>
DEVI void g_steps(GTile& c, GFrag& f, int n) {
    LOOP_ALIGN();
#pragma unroll 1
    do {
        int d;
#pragma unroll
        for (int kk = 0; kk < 2; ++kk) {
            WAIT_L(0);
            if (kk) BAR();
            SCHED0();
#pragma unroll
            for (int i = 0; i < 18; ++i) {
                const int nn = i >> 2, m = i & 3;
                f32x4& e = nn < 4 ? c.a[m][nn] : c.e[m & 1];
                e = ROWS_IN_REGS ? MFMA(f.a[kk][m], f.b[kk][nn], e) : MFMA(f.b[kk][nn], f.a[kk][m], e);
                SCHED0();
                if (i < 9) g_rd(f, kk ^ 1, i);
                if (kk == 0) {
                    if (i == 9) d = pin_s(f.slot == NS - 1 ? -(NS - 1) * G : G);
                    if (i >= 10 && i < 14) f.pa[(i - 10) >> 1][i & 1] = opaque(f.pa[(i - 10) >> 1][i & 1] + d);
                    if (i == 14) f.pb = opaque(f.pb + d);
                    if (i == 15) f.p8 = opaque(f.p8 + d);
                } else {
                    if (i == 10) f.slot = pin_s(f.slot == NS - 1 ? 0 : f.slot + 1);
                    if (i == 12) n = pin_s(n - 1);
                }
                SCHED0();
            }
        }
    } while (n);
}
DEVI void keep4(const f32x4 (&c)[4]) { asm volatile("" :: "v"(c[0]), "v"(c[1]), "v"(c[2]), "v"(c[3])); }
template <int D, int DB, bool FIRST, class HOOK>
DEVI void g_step_u(GTile& c, GFrag& f, const f32x4 (&c0)[4], HOOK&& hook) {
    static_for<36>([&](auto ic) {
        constexpr int kk = ic.value / 18, i = ic.value % 18, n = i >> 2, m = i & 3;
        if (i == 0) { WAIT_L(0); if (kk) BAR(); SCHED0(); }
        f32x4& e = n < 4 ? c.a[m][n] : c.e[m & 1];
        if (FIRST && kk == 0) asm volatile("v_mfma_f32_16x16x32_bf16 %0, %1, %2, %3" : "=&v"(e) : "v"(f.b[kk][n]), "v"(f.a[kk][m]), "v"(c0[m]));
        else MFMA_T(f.b[kk][n], f.a[kk][m], e);
        SCHED0();
        if (i < 9) g_rd(f, kk ^ 1, i);
        if (kk == 0) {
            if (i >= 10 && i < 14) f.pa[(i - 10) >> 1][i & 1] = opaque(f.pa[(i - 10) >> 1][i & 1] + D);
            if (i == 14) f.pb = opaque(f.pb + DB);
            if (i == 15) f.p8 = opaque(f.p8 + DB);
        }
        if (FIRST && ic.value == 20) keep4(c0);
        hook(ic);
        SCHED0();
    });
}
#define MF(d, a, b) "v_mfma_f32_16x16x32_bf16 " d ", " a ", " b ", " d "\n\t"
template <int D, int DB>
DEVI void g_step_gelu(GTile& c, GFrag& f, f32x4& z, float r, float c1) {
#pragma unroll
    for (int kk = 0; kk < 2; ++kk) {
        float va = z[2 * kk], vb = z[2 * kk + 1], ta, tb;
        WAIT_L(0); if (kk) BAR(); SCHED0();
#pragma unroll
        for (int i = 0; i < 9; ++i) {
            MFMA_T(f.b[kk][i >> 2], f.a[kk][i & 3], c.a[i & 3][i >> 2]);
            if (i == 5) asm volatile("v_mul_f32 %0, %0, %1" : "+v"(va) : "v"(r));
            if (i == 6) asm volatile("v_mul_f32 %0, %0, %1" : "+v"(vb) : "v"(r));
            if (i == 7) asm volatile("v_mul_f32 %0, %1, %1" : "=v"(ta) : "v"(va));
            if (i == 8) asm volatile("v_mul_f32 %0, %1, %1" : "=v"(tb) : "v"(vb));
            SCHED0();
            g_rd(f, kk ^ 1, i);
            SCHED0();
        }
        asm volatile(MF("%0", "%5", "%6") "v_fmaak_f32 %3, %3, %11, 0xc0135761\n\tv_fmaak_f32 %4, %4, %11, 0xc0135761\n\t"
                     MF("%1", "%5", "%7") "v_mul_f32 %3, %3, %9\n\tv_mul_f32 %4, %4, %10\n\t" MF("%2", "%5", "%8") "v_exp_f32 %3, %3"
                     : "+v"(c.a[1][2]), "+v"(c.a[2][2]), "+v"(c.a[3][2]), "+v"(ta), "+v"(tb)
                     : "v"(f.b[kk][2]), "v"(f.a[kk][1]), "v"(f.a[kk][2]), "v"(f.a[kk][3]), "v"(va), "v"(vb), "v"(c1));
        SCHED0(); f.pa[kk][0] = opaque(f.pa[kk][0] + D); SCHED0();
        asm volatile(MF("%0", "%6", "%7") "v_exp_f32 %5, %5\n\t" MF("%1", "%6", "%8") "v_add_f32 %4, 1.0, %4\n\tv_add_f32 %5, 1.0, %5\n\t"
                     MF("%2", "%6", "%9") "v_rcp_f32 %4, %4\n\t" MF("%3", "%6", "%10") "v_rcp_f32 %5, %5"
                     : "+v"(c.a[0][3]), "+v"(c.a[1][3]), "+v"(c.a[2][3]), "+v"(c.a[3][3]), "+v"(ta), "+v"(tb)
                     : "v"(f.b[kk][3]), "v"(f.a[kk][0]), "v"(f.a[kk][1]), "v"(f.a[kk][2]), "v"(f.a[kk][3]));
        SCHED0(); f.pa[kk][1] = opaque(f.pa[kk][1] + D); SCHED0();
        asm volatile(MF("%0", "%4", "%5") "v_mul_f32 %2, %2, %7\n\tv_mul_f32 %3, %3, %8\n\t" MF("%1", "%4", "%6")
                     : "+v"(c.e[0]), "+v"(c.e[1]), "+v"(va), "+v"(vb) : "v"(f.b[kk][4]), "v"(f.a[kk][0]), "v"(f.a[kk][1]), "v"(ta), "v"(tb));
        SCHED0();
        if (kk == 0) { f.pb = opaque(f.pb + DB); f.p8 = opaque(f.p8 + DB); }
        SCHED0();
        z[2 * kk] = va; z[2 * kk + 1] = vb;
    }
}
DEVI void g_fill(GTile& c, const float (&e)[4]) {
#pragma unroll
    for (int m = 0; m < 4; ++m) {
#pragma unroll
        for (int n = 0; n < 4; ++n) c.a[m][n] = pin((f32x4){e[m], e[m], e[m], e[m]});
        if (m < 2) c.e[m] = pin((f32x4){e[m], e[m], e[m], e[m]});
    }
}

#undef STAMP_SEC
#define STAMP_SEC 1
DEVI void gmlp_body(LAS char* lds, int wm, int wn, int pm, int cg, char* z2t, char* y, const u16* __restrict__ bsp STAMP_PARAM) {
    const int mlo = wm * 4 + wn * 2, mhi = wm * 4 + (1 - wn) * 2;
    const float zero4[4] = {0.f, 0.f, 0.f, 0.f};
    GTile z2, z1, acc;
    GFrag f;
    {
        const int lane = lane_id_fresh();
#pragma unroll
        for (int kk = 0; kk < 2; ++kk) { f.pa[kk][0] = lds + mlo * 2048 + kk * 1024 + lane * 16; f.pa[kk][1] = lds + mhi * 2048 + kk * 1024 + lane * 16; }
        f.pb = lds + 16384 + wn * 4096 + lane * 16; f.p8 = lds + 16384 + 8192 + lane * 16; f.slot = 0;
    }
    g_fill(z2, zero4); g_fill(z1, zero4);
#pragma unroll
    for (int i = 0; i < 4; ++i) f.pa[i >> 1][i & 1] = opaque(f.pa[i >> 1][i & 1]);
    f.pb = opaque(f.pb); f.p8 = opaque(f.p8);
    SCHED0();
    WAIT_V(0); BARX();
    BAR();
    BARX();
    STAMP(2);
#pragma unroll
    for (int i = 0; i < 9; ++i) g_rd(f, 0, i);
    g_steps<4, G_STAGE, true>(z2, f, 9);
    STAMP(3);
    {
        const int lane = lane_id_fresh(), r = lane & 15, q = lane >> 4;
        f32x4 ra[4];
#pragma unroll
        for (int m = 0; m < 4; ++m) ra[m] = *(const LAS f32x4*)(lds + G_TAB + ((m < 2 ? mlo : mhi) + (m & 1)) * 64 + q * 16);
        SCHED0();
#pragma unroll
        for (int m = 0; m < 4; ++m) {
            f32x4 s1, s2;
#pragma unroll
            for (int n = 0; n < 4; ++n) {
                const f32x4 g = gelu4(z2.a[m][n] * ra[m]);
                z2.a[m][n] = g;
                if (n == 0) { s1 = g; s2 = g * g; } else { s1 += g; s2 += g * g; }
            }
            if (m < 2) { const f32x4 g = gelu4(z2.e[m] * ra[m]); z2.e[m] = g; s1 += g; s2 += g * g; }
            float u0, u1;
            rowsum16x8(s1, s2, u0, u1);
            if ((r & 3) == 0) *(LAS f32x2*)(lds + G_PART + wn * 1024 + ((m < 2 ? mlo : mhi) + (m & 1)) * 128 + (q * 4 + (r >> 2)) * 8) = (f32x2){u0, u1};
            SCHED0();
        }
    }
    WAIT_L0(); BAR();
    STAMP(4);
    g_steps<4, G_STAGE, false>(z1, f, 4);
    unsigned b0, b1;
    f32x4 c0[4];
    g_step_u<G_STAGE, G_STAGE, false>(z1, f, c0, [&](auto ic) {
        if (ic.value != 18) return;
        STAMP(5);
        const int lane = lane_id_fresh(), q = lane >> 4;
        b0 = __builtin_amdgcn_raw_buffer_load_b32(RSRC(bsp), (lane & 15) * 4, pm * 256 + mlo * 32, 0);
        b1 = __builtin_amdgcn_raw_buffer_load_b32(RSRC(bsp), (lane & 15) * 4, pm * 256 + mhi * 32, 0);
#pragma unroll
        for (int g = 0; g < 2; ++g) {
            const auto rz2 = RSRC(z2t + (size_t)pm * 589824 + cg * 36864 + ((g ? mhi : mlo) >> 1) * 9216);
            const LAS char* tab = lds + G_TAB + (g ? mhi : mlo) * 64 + q * 16;
            LAS char* own = lds + (wm ? G_OWN : G_STAGE + 16384) + (g ^ wn) * 9216 + lane * 16;
            f32x4 ra[2], rb[2];
#pragma unroll
            for (int T = 0; T < 2; ++T) { ra[T] = *(const LAS f32x4*)(tab + 512 + T * 64); rb[T] = *(const LAS f32x4*)(tab + 1024 + T * 64); }
#pragma unroll
            for (int n = 0; n < (g ? 4 : 5); ++n) {
                const f32x4 v0 = (n < 4 ? z2.a[2 * g][n & 3] : z2.e[0]) * ra[0] + rb[0], v1 = (n < 4 ? z2.a[2 * g + 1][n & 3] : z2.e[1]) * ra[1] + rb[1];
                const u32x4 o = {pack2(v0[0], v1[0]), pack2(v0[1], v1[1]), pack2(v0[2], v1[2]), pack2(v0[3], v1[3])};
                ST16(rz2, lane * 16, n < 4 ? wn * 4096 + n * 1024 : 8192, o, 16);
                *(LAS u32x4*)(own + (n < 4 ? wn * 4096 + n * 1024 : 8192)) = o;
            }
        }
        STAMP(6);
    });
    static_for<3>([&](auto sc) {
        constexpr int D = sc.value == 0 ? G_STAGE : sc.value == 1 ? -3 * G_STAGE : 2 * G_STAGE;
        g_step_u<D, D, false>(z1, f, c0, [&](auto ic) { if (ic.value == 17 && sc.value == 1) WAIT_V(0); });
    });
    float ra[4], c1;
    {
        int lane, da[2];
        g_step_u<-G_STAGE, -G_STAGE, false>(z1, f, c0, [&](auto ic) {
            constexpr int i = ic.value;
            if (i == 9) lane = pin(lane_id_fresh());
            if (i == 14 || i == 15) da[i & 1] = pin((lane & 15) * 128 + (((4 * (i & 1) + (lane >> 4)) ^ (lane & 7)) * 16) - lane * 16 - (i & 1) * 1024);
            if (i == 16 || i == 17) f.pa[0][i & 1] = opaque(f.pa[0][i & 1] + da[0]);
            if (i == 27 || i == 28) f.pa[1][i & 1] = opaque(f.pa[1][i & 1] + da[1]);
            if (i >= 29 && i < 33) {
                constexpr int m = (i - 29) & 3;
                const float e = m & 1 ? bfhi(m < 2 ? b0 : b1) : bflo(m < 2 ? b0 : b1);
                c0[m] = pin((f32x4){e, e, e, e});
            }
            if (i == 33) {
#pragma unroll
                for (int m = 0; m < 4; ++m) ra[m] = *(const LAS float*)(lds + G_TAB + ((m < 2 ? mlo : mhi) + (m & 1)) * 64 + (lane & 15) * 4);
                c1 = pin(-0.10294324f);
            }
        });
    }
    STAMP(7);
    g_step_u<2 * G_STAGE, G_OWN - G_STAGE - 16384, true>(acc, f, c0, [](auto) {});
    static_for<18>([&](auto sc) {
        constexpr int st = sc.value, m = st < 16 ? st >> 2 : st & 1;
        constexpr int D = st == 0 ? -3 * G_STAGE : st % 3 == 1 ? 2 * G_STAGE : -G_STAGE;
        g_step_gelu<D, st == 0 ? 16384 - G_OWN : D>(acc, f, st < 16 ? z1.a[m][st & 3] : z1.e[m], ra[m], c1);
    });
    f.slot = 1;
    STAMP(9);
    g_steps<3, -G_STAGE, false>(acc, f, 13);
    STAMP(10);
    const int lane = lane_id_fresh();
#pragma unroll
    for (int g = 0; g < 2; ++g) {
        const auto ry = RSRC(y + (size_t)(pm * 8 + (g ? mhi : mlo)) * 73728 + cg * 4608);
#pragma unroll
        for (int T = 0; T < 2; ++T) {
            const int m = 2 * g + T;
#pragma unroll
            for (int p = 0; p < 2; ++p) {
                const f32x4 a = acc.a[m][2 * p] * z1.a[m][2 * p], c = acc.a[m][2 * p + 1] * z1.a[m][2 * p + 1];
                ST16(ry, lane * 16 + wn * 2048 + p * 1024, T * 73728, ((u32x4){pack2(a[0], a[1]), pack2(a[2], a[3]), pack2(c[0], c[1]), pack2(c[2], c[3])}), 16);
            }
            if (g == 0) {
                const f32x4 a = acc.e[T] * z1.e[T];
                ST8(ry, (lane >> 5) * 256 + (lane & 15) * 16 + ((lane >> 4) & 1) * 8, T * 73728 + 4096, ((u32x2){pack2(a[0], a[1]), pack2(a[2], a[3])}), 16);
            }
        }
    }
}

#define NET_IDS \
    int b = __builtin_amdgcn_readfirstlane(blockIdx.x), wid = wid8 & 3; \
    asm volatile("" : "+s"(b), "+s"(wid));     \
    const int xcd = b & 7, idx = b >> 3; \
    const int pa = (xcd >> 1) * 4 + (idx & 3), hp = (xcd & 1) * 8 + (idx >> 2); \
    const int pm = (xcd >> 2) * 8 + (idx & 7), cg = (xcd & 3) * 4 + (idx >> 3); \
    const int rh = RH_MAP ? xcd * 4 + (idx & 3) : (xcd >> 1) * 8 + (idx & 7), c5 = RH_MAP ? idx >> 2 : (xcd & 1) * 4 + (idx >> 3); \
    const int lead = (idx + (idx >> 3)) & 3; \
    auto kord = [&](int t, int n) { \
        const int k = t + ((int)(0x1F000000u << (((t - lead) & 3) * 4)) >> 28); \
        return (unsigned)k < (unsigned)n ? k : t; \
    }; \
    auto kord5 = [&](int t) { \
        const int k = t + ((int)(0x1F000000u << (((t - (RH_MAP ? c5 : lead)) & 3) * 4)) >> 28); \
        return (unsigned)k < 18u ? k : t; \
    };

typedef short s16x4 __attribute__((ext_vector_type(4)));
DEVI u32x4 tr_frag(const LAS char* p, int four_rows) {
    const u32x2 a = __builtin_bit_cast(u32x2, __builtin_amdgcn_ds_read_tr16_b64_v4i16((LAS s16x4*)p));
    const u32x2 b = __builtin_bit_cast(u32x2, __builtin_amdgcn_ds_read_tr16_b64_v4i16((LAS s16x4*)(p + four_rows)));
    return (u32x4){a.x, a.y, b.x, b.y};
}
DEVI float dot2(unsigned v, unsigned w, float acc) { return __builtin_amdgcn_fdot2_f32_bf16(__builtin_bit_cast(bf16x2_t, v), __builtin_bit_cast(bf16x2_t, w), acc, false); }
DEVI float sum2(unsigned v, float acc) { return dot2(v, 0x3f803f80u, acc); }
struct Sel { unsigned lo, hi, nlo, nhi; };
DEVI Sel make_sel() { return {(unsigned)pin_s(0x00003f80), (unsigned)pin_s(0x3f800000), (unsigned)pin_s(0x0000bf80), (unsigned)pin_s((int)0xbf800000u)}; }
DEVI unsigned centre2(unsigned v, f32x2& e, float mean, const Sel& k) {
    const float a = dot2(k.lo, v, e.x), b = dot2(k.hi, v, e.y);
    const unsigned r = pack2(a, b);
    e = (f32x2){dot2(k.nlo, r, a), dot2(k.nhi, r, b)} - mean;
    return r;
}
typedef u32x4 Retile[9];
template <int PITCH>
DEVI void retile_out_make(Retile& o, const LAS char* img, int lane) {
    const int c = lane & 15, q = lane >> 4, p = lane & 3;
    const LAS char* src = img + (q * 8 + (c >> 2)) * PITCH;
#pragma unroll
    for (int i = 0; i < 8; ++i) o[i] = tr_frag(src + p * 16 + (i >> 2) * 144 + ((i >> 1) & 1) * 64 + (i & 1) * 8, 4 * PITCH);
    o[8] = tr_frag(src + 128 + (p >> 1) * 144 + (p & 1) * 8, 4 * PITCH);
}
DEVI void retile_out_store(const Retile& o, char* dst, int nkc, int kc, int w, int lane) {
    const int c = lane & 15, q = lane >> 4;
    const auto rd = RSRC(dst + (size_t)(2 * w * nkc + kc) * 4608);
#pragma unroll
    for (int i = 0; i < 8; ++i) ST16(rd, lane * 16 + (i & 3) * 1024, (i >> 2) * nkc * 4608, o[i], 16);
    ST16(rd, (c >> 3) * nkc * 4608 + 4096 + q * 128 + (c & 7) * 16, 0, o[8], 16);
}

DEVI float dpp_ror(float v, int n) { return n == 8 ? __builtin_amdgcn_update_dpp(0.f, v, 0x128, 0xf, 0xf, true) : n == 4 ? __builtin_amdgcn_update_dpp(0.f, v, 0x124, 0xf, 0xf, true)
                                          : n == 2 ? __builtin_amdgcn_update_dpp(0.f, v, 0x122, 0xf, 0xf, true) : __builtin_amdgcn_update_dpp(0.f, v, 0x121, 0xf, 0xf, true); }
DEVI unsigned long long epoch_of(const char* ws) {
    SCHED0();
    const unsigned long long ep = *(const __attribute__((address_space(4))) unsigned long long*)(ws + pin_s((int)OFF_EP)) + 1ull;
    asm volatile("" :: "s"(ep));
    return ep;
}
DEVI void prologue_done(LAS char* lds, char* ws, unsigned long long ep, int b, int lane) {
    if (lds_arrive(lds + G_WORD + 8) == 7) ST16(RSRC(ws), lane ? 0x80000000u : (unsigned)OFF_FL + b * 16, 0, ((u32x4){(unsigned)ep, (unsigned)(ep >> 32), 0u, 0u}), 16);
}
DEVI void centre_frag(u32x4& f, f32x2 (&er)[4], float mean, const Sel& k) {
#pragma unroll
    for (int e = 0; e < 4; ++e) f[e] = centre2(f[e], er[e], mean, k);
}
template <class ARGS>
DEVI unsigned long long prologue(LAS char* lds, char*& ws, const char*& x0, const char*& wqkv, const u16*& wo, const u16*& w_in, int b, int pa, int wid8, ARGS&& args PST_PARAM) {
    PST_DECL;
    const int lane = lane_id_fresh(), c = lane & 15, q = lane >> 4, w = wid8 & 3;
    const int jw = w - 1 + 3 * (((wid8 >> 2) ^ (b >> 3)) & 1);
    const bool wjob = b >= 16 && b < 112 && w, ijob = b >= 112 && w;
    const int uo = (wid8 - 1 - (wid8 >> 2)) * 16 + b;
    const bool ojob = b < 16 && w && uo < 72;
    const unsigned l0 = (unsigned)(size_t)lds;
    LAS char* img = lds + 2 * K1_STAGE;
    LAS float* red = (LAS float*)(lds + G_PART);
    const int uw = b - 16, head = (uw & 7) * 4 + ((uw >> 3) & 3), part = uw >> 5, hp = head >> 1, odd = head & 1;
    const int c0 = part * 1152 + hp * 72 + odd * 40;
    const int ui = b - 112, st = (ui & ~15) + 2 * (ui & 7) + ((ui >> 3) & 1);
    {
        const int pitch = pin_s(b < 112 ? 3456 : 9216), so = pin_s(jw * 96 * pitch + (b < 112 ? c0 & ~15 : st * 64));
        const unsigned v = (lane & 3) < (b >= 112 ? 4 : (head & 3) == 3 ? 2 : 3) ? (lane >> 2) * pitch + (lane & 3) * 16 : 0x80000000u, d = 16 * pitch - 1024;
        const unsigned v0 = pin(v), v1 = pin(v + d), v2 = pin(v + 2 * d), v3 = pin(v + 3 * d);
        const int e = (ui >> 3) * 192 + wid8 * 64 + lane, row = (e * 10923) >> 16, r6 = e - row * 6;
        const unsigned vp = pin((unsigned)(row * 3456 + (r6 >> 1) * 1152 + (r6 & 1) * 128));
        const unsigned la = pin_s(l0 + 2 * K1_STAGE + jw * 6144);
        args(0);
        if (W_PF && b >= 112 && wid8 < 3) dma1(l0 + G_OWN, RSRC(wqkv), (b & 7) * 128, vp);
        if (w && b >= 16) {
            if (b >= 112) __builtin_amdgcn_s_sleep(I_SLEEP);
            const auto rs = RSRC(b < 112 ? wqkv : (const char*)w_in);
            dma4<false>(la, rs, so, v0, v1, v2, v3);
            dma2<false>(la + 4096, rs, so + 64 * pitch, v0, v1);
        }
        args(1);
    }
    if (ojob) {
        const auto rs = RSRC(wo + (uo & 3) * 144);
#pragma unroll
        for (int j = 0; j < 9; ++j) {
            const int i = lane + 64 * j, rl = (i * 3641) >> 16, kp = (uo >> 2) * 32 + rl;
            const int row = kp < 512 ? 18 * (kp >> 4) + (kp & 15) + 2 * ((kp >> 4) & 1) : 36 * ((kp - 512) >> 2) + 16 + (kp & 3);
            dma1(l0 + 2 * K1_STAGE + wid8 * 9216 + j * 1024, rs, 0, (unsigned)(row * 1152 + (i - rl * 18) * 16));
        }
    }
    if (wid8 == 4) { lds_word_set(lds + G_WORD + c * 4, 0u); WAIT_L0(); }
    BAR();
    unsigned long long ep;
    if (wid8 == 4) ep = epoch_of(ws);
    if (W_ALONE && wid8 == 4 && b >= 16 && b < 112) lds_count_wait(lds + G_WORD + 12, W_ALONE);
    if (wid8 == 4) {
        AttnX ax = attn_x(x0, x0, 0, pa, 0, lane);
#pragma unroll
        for (int s = 0; s < 2; ++s)
#pragma unroll
            for (int i = 0; i < 4; ++i) { ax.oA = (pa * 8 + 2 * i) * 18432; attn_issueA(lds, ax, i, s, s); }
    }
    u32x4 xv[9];
    if (wid8 == 0) {
        const unsigned v = (lane >> 3) * 2304 + (lane & 7) * 16;
        const auto rx = RSRC(x0 + (size_t)slot2row(b * 8) * 1152);
#pragma unroll
        for (int i = 0; i < 9; ++i) asm volatile("s_nop 4\n\tbuffer_load_dwordx4 %0, %1, %2, 0 offen offset:%3" : "+v"(xv[i]) : "v"(v), "s"(rx), "i"(i * 128) : "memory");
        const auto rw = RSRC(ws);
        const u32x4 ones = {~0u, ~0u, ~0u, ~0u}, zero = {0u, 0u, 0u, 0u};
        const unsigned vo = lane * 16;
        ST16(rw, vo, (unsigned)OFF_ZST + b * 2048, ones, 16); ST16(rw, vo, (unsigned)OFF_ZST + b * 2048 + 1024, ones, 16);
        ST16(rw, vo, (unsigned)OFF_CNT + b * 2560, zero, 16); ST16(rw, vo, (unsigned)OFF_CNT + b * 2560 + 1024, zero, 16);
        ST16(rw, vo, lane < 32 ? (unsigned)OFF_CNT + b * 2560 + 2048 : (unsigned)(OFF_L0 + L_STB) + b * 512 - 512, zero, 16);
        ST16(rw, vo, (unsigned)(OFF_L0 + L_O) + (b >> 1) * 18432 + 16384 + (b & 1) * 1024, zero, 16);
        ST16(rw, b < 8 ? vo : 0x80000000u, (unsigned)OFF_FL2 + b * 1024, zero, 16);
    }
    PST(1);
    const Sel k = make_sel();
    u32x4 f[6];
    unsigned vd, vd2;
    __amdgpu_buffer_rsrc_t rd, rd2;
    if (wjob || ijob) {
        const LAS char *s0, *s1;
        if (ijob) {
            int o[2];
#pragma unroll
            for (int h = 0; h < 2; ++h) {
                const int u = st * 2 + h, half = u >= 144, g = u - half * 144, cg = g / 9, j = g - cg * 9, p = lane & 3;
                o[h] = h * 32 + (j < 8 ? (p & 1) * 16 + (p >> 1) * 8 : p * 8);
                (h ? rd2 : rd) = RSRC(ws + OFF_TWIN + (size_t)cg * (18 * 18432) + (half * 18 + jw * 3) * 9216);
                (h ? vd2 : vd) = q * 256 + (j < 8 ? ((j & ~1) + (c >> 3)) * 1024 + ((j & 1) * 8 + (c & 7)) * 16 : 8192 + c * 16);
            }
            s0 = img + jw * 6144 + (q * 8 + (c >> 2)) * 64 + o[0]; s1 = s0 + (o[1] - o[0]);
        } else {
            s0 = img + jw * 6144 + (q * 8 + (c >> 2)) * 64 + (c0 & 15); s1 = s0 + 32 + min(c & 3, 2) * 2048; s0 += (c & 3) * 8;
            rd = RSRC(ws + OFF_TWQKV + (size_t)(hp * 18 + jw * 3) * 6144 + (3 * odd + part) * 1024); vd = lane * 16;
            rd2 = RSRC(ws + OFF_TWT + (size_t)((hp * 3 + part) * 18 + jw * 3) * 256); vd2 = c < 12 && !odd ? (unsigned)((c >> 2) * 256 + q * 64 + (c & 3) * 16) : 0x80000000u;
        }
        s0 = opaque(s0); s1 = opaque(s1); vd = pin(vd); vd2 = pin(vd2);
        asm volatile("" :: "s"(rd), "s"(rd2));
        WAIT_V(0);
        PST(2);
        float sm[4] = {0.f, 0.f, 0.f, 0.f};
#pragma unroll
        for (int i = 0; i < 4; ++i) f[i] = tr_frag((i < 3 ? s0 : s1) + (i % 3) * 2048, 256);
        if (ijob) { f[4] = tr_frag(s1 + 2048, 256); f[5] = tr_frag(s1 + 4096, 256); }
#pragma unroll
        for (int e = 0; e < 16; ++e) sm[(e >= 12) * 2 + (e & 1)] = sum2(f[e >> 2][e & 3], sm[(e >= 12) * 2 + (e & 1)]);
        if (ijob) {
#pragma unroll
            for (int e = 16; e < 24; ++e) sm[2 + (e & 1)] = sum2(f[e >> 2][e & 3], sm[2 + (e & 1)]);
            sm[2] += sm[3];
        } else {
            sm[2] = c < 12 ? sm[2] + sm[3] : 0.f;
            sm[2] += dpp_ror(sm[2], 4); sm[2] += dpp_ror(sm[2], 8);
        }
        auto x = __builtin_amdgcn_permlane16_swap(__float_as_uint(sm[0] + sm[1]), __float_as_uint(sm[2]), false, false);
        red[jw * 64 + lane] = __uint_as_float(x[0]) + __uint_as_float(x[1]);
        if (lds_arrive(lds + G_WORD + 12) < 5) lds_count_wait(lds + G_WORD + 12, 6);
        PST(3);
        float t = 0.f;
#pragma unroll
        for (int i = 0; i < 6; ++i) t += red[i * 64 + lane];
        x = __builtin_amdgcn_permlane32_swap(__float_as_uint(t), __float_as_uint(t), false, false);
        t = (__uint_as_float(x[0]) + __uint_as_float(x[1])) * (1.0f / 576.0f);
        auto y = __builtin_amdgcn_permlane16_swap(__float_as_uint(t), __float_as_uint(t), false, false);
        const float m0 = __uint_as_float(y[0]), m1 = __uint_as_float(y[1]);
        f32x2 e0[4] = {{-m0, -m0}, {-m0, -m0}, {-m0, -m0}, {-m0, -m0}}, e1[4] = {{-m1, -m1}, {-m1, -m1}, {-m1, -m1}, {-m1, -m1}};
        const unsigned dk = ijob ? 9216 : 6144;
#pragma unroll
        for (int i = 0; i < 3; ++i) { centre_frag(f[i], e0, m0, k); ST16(rd, vd, i * dk, f[i], 16); }
#pragma unroll
        for (int i = 0; i < 3; ++i) if (i == 0 || ijob) { centre_frag(f[3 + i], e1, m1, k); ST16(rd2, vd2, i * 9216, f[3 + i], 16); }
        PST(4);
        if (wjob && W_ALONE > 6) lds_arrive(lds + G_WORD + 12);
    }
    if (wid8 != 4) ep = epoch_of(ws);
    {
        if (wjob) {
            const unsigned fo = pin((unsigned)OFF_FW + ((lane >> 3) * 16 + hp) * 1024 + (3 * odd + part) * 128 + (lane & 7) * 16);
            WAIT_V(0);
            PST(5);
            if (lds_arrive(lds + G_WORD + 4) == 5) {
                const u32x4 e4 = {(unsigned)ep, (unsigned)(ep >> 32), (unsigned)ep, (unsigned)(ep >> 32)};
                ST16(RSRC(ws), fo, 0, e4, 16); ST16(RSRC(ws), fo + 131072, 0, e4, 16);
            }
        }
    }
    if (wid8 == 0) {
        WAIT_V(7);
        asm volatile("" : "+v"(xv[0]), "+v"(xv[1]), "+v"(xv[2]), "+v"(xv[3]), "+v"(xv[4]), "+v"(xv[5]), "+v"(xv[6]), "+v"(xv[7]), "+v"(xv[8]));
        float s = 0.f, q2 = 0.f;
#pragma unroll
        for (int e = 0; e < 36; ++e) { s = sum2(xv[e >> 2][e & 3], s); q2 = dot2(xv[e >> 2][e & 3], xv[e >> 2][e & 3], q2); }
        ST8(RSRC(ws + OFF_L0 + L_STA + (size_t)b * 512), lane * 8, 0, ((u32x2){__float_as_uint(s), __float_as_uint(q2)}), 16);
    }
    if (ojob) {
        WAIT_V(0);
        Retile rt;
        retile_out_make<288>(rt, img + wid8 * 9216, lane); retile_out_store(rt, ws + OFF_TWO, 18, uo >> 2, uo & 3, lane);
    }
    PST(7);
    PST_DUMP(wid8 == 0 || wid8 == 4 || wid8 == 5, wid8 ? wid8 : 2);
    return ep;
}

__global__ __launch_bounds__(512, 2) TRACE_ATTR void k_net(char* ws, const u16* wsp, const u16* bsp, char* out, const char* x0, const char* wqkv, const u16* wo,
                                                             const u16* w_in, const u16* w_out DBG_ARG) {
    extern __shared__ __attribute__((aligned(16))) char smem[];
    LAS char* lds = (LAS char*)smem;
    asm volatile("gm_code_begin:");
    if (blockIdx.x < 16) {
        unsigned long long pc; int d0, len;
        asm volatile("s_getpc_b64 %0\n1:\n\ts_mov_b32 %1, gm_code_begin-1b\n\ts_mov_b32 %2, gm_code_end1-gm_code_begin\n\ts_max_i32 %2, %2, gm_code_end2-gm_code_begin"
                     : "=s"(pc), "=s"(d0), "=s"(len) :: "scc");
        const auto rs = __builtin_amdgcn_make_buffer_rsrc((void*)((pc + d0) & ~15ull), 0, len, 0x00020000);
        dma1((unsigned)(size_t)lds + G_OWN, rs, 0, (threadIdx.x * 2 + (blockIdx.x >> 3)) * 128);
    }
    STAMP_DECL;
    const int wid8 = __builtin_amdgcn_readfirstlane(threadIdx.x >> 6);
    unsigned long long ep;
    {
        NET_IDS
        ep = prologue(lds, ws, x0, wqkv, wo, w_in, b, pa, wid8, [&](int part) {
            if (part == 0) { asm volatile("" :: "s"(wqkv), "s"(w_in), "s"(w_out)); return; }
            ws = own_s(ws); wsp = own_s(wsp); bsp = own_s(bsp); out = own_s(out); x0 = own_s(x0); wo = own_s(wo); w_out = own_s(w_out);
        } PST_FWD);
    }
    const char *twin = ws + OFF_TWIN, *twout = ws + OFF_TWOUT;
    if (wid8 >= 4) {
        u32x4 wr[5][9];
#pragma unroll 1
      for (int layer = 0; layer < NET_LAYERS; ++layer) {
        NET_IDS
        TRACE_BEGIN;
        char* wl = ws + OFF_L0 + layer * SZ_L;
        const unsigned fl = layer * F_LAYER;
        const char *xin = wl + L_XB, *st = wl + L_STB, *z2t = wl + (layer ? L_Y : L_Z2T);
        unsigned long long* zst = (unsigned long long*)(ws + OFF_ZST + layer * SZ_ZST);
        const int lane = lane_id_fresh(), l3 = lane >> 3;
        const unsigned vo = lane * 16;
        const unsigned l0 = (unsigned)(size_t)lds, lA = l0 + wid * 4096, lB = l0 + 16384 + wid * 4608;
        const auto rX = RSRC(xin + (size_t)(pm * 8 + 2 * wid) * 18432), rI = RSRC(twin + (size_t)cg * (18 * 18432) + wid * 4608);
        auto issue1B = [&](int g, int slot) { const int k = kord(g < 9 ? g : (g < 13 ? g - 4 : g - 13), 9); dma5<true>(lB + slot * G_STAGE, rI, (g < 9 ? 9 + k : k) * 18432, vo); };
        auto issue1A = [&](int g, int slot) { dma4<true>(lA + slot * G_STAGE, rX, kord(g < 9 ? g : g - 13, 9) * 2048, vo, vo, vo + 16384, vo + 16384); };
        auto issue1 = [&](int g, int slot) { issue1B(g, slot); issue1A(g, slot); };
        const auto rA = RSRC((const char*)wsp + (size_t)(pm * 128 + wid * 32) * 4096), rB = RSRC(z2t + cg * 36864 + wid * 4608);
        const unsigned voA = (unsigned)(2 * l3 * 4096 + ((lane & 7) ^ l3) * 16);
        auto issueB = [&](int k, int slot) { dma5<true>(lB + slot * G_STAGE, rB, (k >> 1) * 589824 + (k & 1) * 18432, vo); };
        const auto rW = RSRC(twout + (size_t)c5 * (72 * 4608));
        auto issue5 = [&](int t) { dma5<true>(l0 + ((t + 6) & 7) * K5_STAGE + wid * 4608, rW, kord5(t) * K5_STAGE + wid * 4608, vo); };
        auto load5 = [&](int p, int i) {
            const int t0 = p ? 2 * p + 6 : 4, ow = kord5(t0 + i) * K5_STAGE + wid * 4096;
            asm volatile("s_nop 4\n\tbuffer_load_dwordx4 %0, %4, %5, %6 offen sc1\n\tbuffer_load_dwordx4 %1, %4, %5, %6 offen offset:1024 sc1\n\t"
                         "buffer_load_dwordx4 %2, %4, %5, %6 offen offset:2048 sc1\n\tbuffer_load_dwordx4 %3, %4, %5, %6 offen offset:3072 sc1"
                         : "+v"(wr[p][4 * i]), "+v"(wr[p][4 * i + 1]), "+v"(wr[p][4 * i + 2]), "+v"(wr[p][4 * i + 3]) : "v"(vo), "s"(rW), "s"(ow) : "memory");
            if (i) {
                const int oe = kord5(t0 + (wid >> 1)) * K5_STAGE + 16384 + (wid & 1) * 1024;
                asm volatile("s_nop 4\n\tbuffer_load_dwordx4 %0, %1, %2, %3 offen sc1" : "+v"(wr[p][8]) : "v"(vo), "s"(rW), "s"(oe) : "memory");
            }
        };
        auto put5 = [&](int w0, int w1) {
#pragma unroll
            for (int w = w0; w < w1; ++w) {
                const int p = w / 9, i = w % 9 == 4 ? -1 : w % 9 - (w % 9 > 4);
                LAS char* sl = lds + (((p ? 2 * p + 6 : 4) + 6) & 7) * K5_STAGE + lane * 16;
                if (i < 0) *(LAS u32x4*)(sl + (wid >> 1) * K5_STAGE + 16384 + (wid & 1) * 1024) = wr[p][8];
                else *(LAS u32x4*)(sl + (i >> 2) * K5_STAGE + wid * 4096 + (i & 3) * 1024) = wr[p][i];
            }
            WAIT_L0();
        };
        const int row = wid * 32 + (lane & 31);
        auto step1 = [&](int t) { BAR(); issue1(t + 4, t & 3); };
        const char* ps;
        unsigned fd, fd2;
        {
            unsigned fo, fi;
            const int ojob = layer || !wid ? 288 : b + (wid - 1) * 256, oimg = wid == 1 ? 2048 : G_STAGE;
            attn_producer(lds, ws, x0, wl, fl, ep, layer, pa, hp, wid, [&] { prologue_done(lds, ws, ep, b, lane); }, [&](int part) {
                if (part == 0 && ojob < 288) {
                    const auto rs = RSRC(w_out + (size_t)(ojob >> 2) * 32 * 576 + (ojob & 3) * 144);
#pragma unroll
                    for (int j = 0; j < 9; ++j) { const int i = lane + 64 * j; dma1(l0 + oimg + j * 1024, rs, 0, (unsigned)(((i * 3641) >> 16) * (1152 - 288) + i * 16)); }
                }
                if (part == 1) {
                    issue1B(0, 0); issue1B(1, 1);
                    fo = flag_out(fl + F_X1, 256, pa, hp, lane); fi = flag_in(fl + F_X1, 256, pm * 16 + cg, 16, lane);
                    ps = pin(st + (size_t)(pm * 128 + wid * 32) * 64 + lane * 16);
                    fd = pin((unsigned)OFF_FL2 + wid * 1024 + vo); fd2 = pin((unsigned)OFF_FL2 + 4096 + (lane & 31) * 16);
                }
            });
            if (WSP_PF && layer == 0 && wid >= 2 && ojob >= 288) {
                __builtin_amdgcn_s_sleep(OJOB_SLEEP);
#pragma unroll
                for (int i = 0; i < 4; ++i)
                    asm volatile("s_nop 4\n\tbuffer_load_dwordx4 %0, %4, %5, %6 offen\n\tbuffer_load_dwordx4 %1, %4, %5, %6 offen offset:1024\n\t"
                                 "buffer_load_dwordx4 %2, %4, %5, %6 offen offset:2048\n\tbuffer_load_dwordx4 %3, %4, %5, %6 offen offset:3072"
                                 : "+v"(wr[0][0]), "+v"(wr[0][1]), "+v"(wr[0][2]), "+v"(wr[0][3]) : "v"(vo), "s"(RSRC(wsp)), "s"(b * 32768 + (wid - 2) * 16384 + i * 4096) : "memory");
            }
            if (ojob < 288) {
                Retile& rt = wr[1];
                retile_out_make<288>(rt, lds + oimg, lane);
                __builtin_amdgcn_s_sleep(OJOB_SLEEP);
                retile_out_store(rt, ws + OFF_TWOUT, 72, ojob >> 2, ojob & 3, lane);
                WAIT_V(0);
                flag_raise(ws, lane ? 0x80000000u : (unsigned)OFF_FL2 + ojob * 16);
            }
            BARX();
            if (wid == 0) flag_raise(ws, fo);
            issue1B(2, 2); issue1B(3, 3);
            asm volatile("" :: "s"(rX));
            if (wid == 0) flags_wait<X1_FIRST>(ws, fi);
            BARX();
        }
        issue1A(0, 0); issue1A(1, 1); issue1A(2, 2);
        WAIT_V(8); BAR();
        issue1A(3, 3);
        {
            f32x4 sp[3];
            WAIT_V(8); step1(0);
            WAIT_V(13); step1(1);
            WAIT_V(18); step1(2);
            unsigned got[2];
            if (layer == 0) asm volatile("s_nop 4\n\tbuffer_load_dword %0, %2, %4, 0 offen sc1\n\tbuffer_load_dword %1, %3, %4, 0 offen sc1" : "+v"(got[0]), "+v"(got[1]) : "v"(fd), "v"(fd2), "s"(RSRC(ws)) : "memory");
            WAIT_IF(layer, 18, 20); step1(3);
            WAIT_IF(layer, 18, 20); step1(4);
            WAIT_V(18);
            if (layer == 0) {
                asm volatile("" : "+v"(got[0]), "+v"(got[1]));
                if (__any((got[0] & got[1]) == 0u)) { flags_wait<0>(ws, fd); flags_wait<0>(ws, fd2); }
            }
            BAR(); issue1B(9, 1); side_load(sp, ps, ps); if (layer == 0) load5(3, 0);
            WAIT_IF(layer, 17, 21); BAR(); issue1B(10, 2); if (layer == 0) load5(3, 1);
            WAIT_IF(layer, 5, 14); BARX();
            side_fence(sp);
#pragma unroll
            for (int i = 0; i < 2; ++i) lds_word_set(lds + G_TAB + (wid * 32 + i * 16 + (lane >> 2)) * 4, __float_as_uint(ln_rstd(sp[i])));
            issue1B(11, 3);
            WAIT_L0();
        }
        BAR(); issue1B(12, 0);
        if (layer == 1) {
            const auto rd = RSRC(out + (size_t)b * 9216 + wid * 2304);
            ST16(rd, vo, 0, ((u32x4){0u, 0u, 0u, 0u}), 16); ST16(rd, vo, 1024, ((u32x4){0u, 0u, 0u, 0u}), 16);
            if (lane < 16) ST16(rd, vo, 2048, ((u32x4){0u, 0u, 0u, 0u}), 16);
        }
        if (layer == 0) {
#pragma unroll
            for (int p = 0; p < 6; ++p) load5(p >> 1, p & 1);
        }
        WAIT_V(0);
        __builtin_amdgcn_s_setprio(3);
        const char* src = pin((const char*)zst + (size_t)(pm * 16 + (lane >> 4) * 4) * 1024 + wid * 256 + (lane & 15) * 16);
        const unsigned tw = pin(l0 + G_TAB + 512 + (lane >> 5) * 512 + (wid * 32 + (lane & 15) * 2 + ((lane >> 4) & 1)) * 4);
        const unsigned fz = flag_out(fl + F_Z2, 256, cg, pm, lane), fzi = pin(fl + F_Z2 + (cg * 16 + pm) * 256 + ((lane ^ pm) & 8) * 16 + (lane & 7) * 16);
        const unsigned vA[4] = {pin(voA), pin(voA + 16 * 4096 - 1024), pin(voA + 4096 - 2048), pin(voA + 17 * 4096 - 3072)};
        auto spA = [&](int so, unsigned la) { dma4<false>(la, rA, so, vA[0], vA[1], vA[2], vA[3]); };
        auto issueA = [&](int k, int slot) { spA(k * 128, lA + slot * G_STAGE); };
        {
            const LAS char* pp = opaque(lds + G_PART + row * 8);
            const auto rs = RSRC(zst + (size_t)(pm * 16 + cg) * 128);
            const unsigned vp = pin(lane < 32 ? (unsigned)row * 8u : 0x80000000u);
            asm volatile("" :: "s"(rs));
            BARX();
            const f32x2 a = *(const LAS f32x2*)pp, c = *(const LAS f32x2*)(pp + 1024);
            ST8(rs, vp, 0, ((u32x2){__float_as_uint(add1(a.x, c.x)), min(__float_as_uint(add1(a.y, c.y)), 0xfffffffeu)}), 16);
            SCHED0();
        }
        __builtin_amdgcn_s_setprio(0);
        {
            f32x4 g[4];
            float sm[4];
            auto gather = [&] {
                asm volatile("global_load_dwordx4 %0, %4, off sc1\n\tglobal_load_dwordx4 %1, %4, off offset:1024 sc1\n\t"
                             "global_load_dwordx4 %2, %4, off offset:2048 sc1\n\tglobal_load_dwordx4 %3, %4, off offset:3072 sc1"
                             : "=&v"(g[0]), "=&v"(g[1]), "=&v"(g[2]), "=&v"(g[3]) : "v"(src) : "memory");
            };
            auto total = [&] {
                asm volatile("" : "+v"(g[0]), "+v"(g[1]), "+v"(g[2]), "+v"(g[3]));
#pragma unroll
                for (int i = 0; i < 4; ++i) sm[i] = add1(add1(g[0][i], g[1][i]), add1(g[2][i], g[3][i]));
            };
            step1(9); step1(10);
            BARX(); gather(); issue1(15, 3);
            WAIT_V(22); BARX();
            __builtin_amdgcn_s_setprio(3);
            WAIT_V(9);
            total();
            if (__any(__builtin_isunordered(sm[1], sm[3]))) {
#pragma unroll 1
                for (unsigned spin = 0; spin < 400000u; ++spin) {
                    bool ok = true;
#pragma unroll
                    for (int i = 0; i < 4; ++i) ok &= __float_as_uint(g[i][1]) != 0xffffffffu && __float_as_uint(g[i][3]) != 0xffffffffu;
                    if (__all(ok)) break;
                    gather();
                    WAIT_V(0);
                    total();
                }
            }
            xq4_sum(sm);
            const bool odd = lane & 16;
            float mean;
            const float rs = rstd_of(odd ? sm[2] : sm[0], odd ? sm[3] : sm[1], 1.0f / 2304.0f, mean);
            asm volatile("ds_write_b32 %0, %1" :: "v"(tw), "v"(lane < 32 ? rs : -mean * rs) : "memory");
            __builtin_amdgcn_s_setprio(0);
            issue1B(16, 0);
            WAIT_L0();
        }
        BARX(); issue1A(16, 0); issueA(2 * pm, 1);
        auto kord2 = [&](int t) { const int j = kord(t - 2, 30); return ((j < 2 * (pm & 7) ? j : j + 2) + 2 * (pm & 8)) & 31; };
        unsigned seen;
        auto look = [&] { if (wid == 0) asm volatile("s_nop 4\n\tbuffer_load_dword %0, %1, %2, 0 offen sc1" : "+v"(seen) : "v"(fzi), "s"(RSRC(ws)) : "memory"); };
        auto need = [&](int n) {
            if (wid == 0) {
                asm volatile("" : "+v"(seen));
                if (__ballot(seen == 0u) & ((1ull << n) - 1ull)) flags_wait<0>(ws, lane < n ? fzi : fl + F_Z2 + (cg * 16 + pm) * 256 + pm * 16);
            }
        };
        auto issue = [&](int t, int sl) { const int k = kord2(t); issueB(k, sl); issueA(k, sl); };
        auto steps2 = [&](int t0, int t1) {
            int sl = (2 * t0 + 2) % 3 * G_STAGE, k = kord2(t0 + 3), ob = (k >> 1) * 589824 + (k & 1) * 18432;
            LOOP_ALIGN();
#pragma unroll 1
            for (int t = t0; t < t1; ++t) {
                WAIT_V(9); BARX();
                dma5<true>(lB + sl, rB, ob, vo);
                const int oa = pin_s(k * 128);
                const unsigned la = pin_s(lA + sl);
                k = pin_s(kord2(t + 4)); ob = pin_s((k >> 1) * 589824 + (k & 1) * 18432);
                sl = pin_s(sl == 0 ? 2 * G_STAGE : sl - G_STAGE);
                spA(oa, la);
            }
        };
        WAIT_V(13); BAR(); issue1(17, 2);
        WAIT_V(13); BARX();
        if (wid == 0) flag_raise(ws, fz);
        issueA(2 * pm + 1, 3); if (layer == 0) load5(4, 0);
        if (wid == 0) WAIT_IF(layer, 5, 9); else WAIT_IF(layer, 4, 8);
        BAR(); issueA(kord2(2), 0); if (layer == 0) load5(4, 1);
        BAR(); look(); issueA(kord2(3), 2);
        WAIT_V(4); need(3); BARX(); issueB(kord2(2), 0); issueB(kord2(3), 2); look();
        WAIT_IF(wid == 0, 6, 5); BAR(); issue(4, 1);
        WAIT_V(9); need(8);
        steps2(2, 11); look();
        WAIT_IF(wid == 0, 10, 9); BAR(); issue(14, 0);
        WAIT_V(9); need(16);
        steps2(12, 29);
        WAIT_V(9); BAR(); issue5(0); issue5(1); issue5(2);
        WAIT_V(15); BAR(); issue5(6); issue5(7);
        BAR(); issue5(3); put5(0, 9);
        unsigned vb;
        {
            vb = attn_vb(wid, lane);
            const unsigned fo = flag_out(fl + F_Y, 256, pm, cg, lane), fi = flag_in(fl + F_Y, 256, rh * 8 + c5, 16, lane);
            __builtin_amdgcn_s_setprio(3);
            WAIT_V(0); BARX();
            if (wid == 0) { flag_raise(ws, fo); flags_wait<Y_FIRST>(ws, fi); }
            __builtin_amdgcn_s_setprio(0);
            BARX();
        }
        put5(9, 13);
        BAR(); put5(13, 18);
        BAR(); put5(18, 24);
        BAR(); put5(24, 30);
        BAR(); put5(30, 36);
        BAR(); issue5(16); put5(36, 41);
        BAR(); issue5(17); put5(41, 45);
        WAIT_V(5); BAR();
        WAIT_V(0); BAR();
        if (layer == 0) { attn_issueB(lds, ws, hp, wid, vb, 0, 0); attn_issueB(lds, ws, hp, wid, vb, 1, 1); }
        BAR();
        if (layer == 0) {
            __builtin_amdgcn_s_setprio(3);
            const unsigned fo = flag_out(F_X2, 512, (rh >> 1) + wid, wid ? 16 + c5 : (rh & 1) * 8 + c5, wid == 0 || (wid == 1 && (rh & 1) && rh < 31) ? lane : 16);
            BARX();
            if (wid < 2) flag_raise(ws, fo);
        }
        TRACE_DUMP;
      }
        if (blockIdx.x == 0 && wid8 == 4) ST8(RSRC(ws), lane_id_fresh() ? 0x80000000u : (unsigned)OFF_EP, 0, ((u32x2){(unsigned)ep, (unsigned)(ep >> 32)}), 16);
        asm volatile("gm_code_end1:");
        return;
    }
#pragma unroll 1
  for (int layer = 0; layer < NET_LAYERS; ++layer) {
    NET_IDS
    TRACE_BEGIN;
    char* wl = ws + OFF_L0 + layer * SZ_L;
    const unsigned fl = layer * F_LAYER;
    const char* xin = wl + L_XB;
    char* y = wl + (layer ? L_Z2T : L_Y);
#undef STAMP_SEC
#define STAMP_SEC 0
    {
    STAMP_BEGIN;
    attn_consumer(lds, x0, wl, layer, pa, hp, wid, [&] { WAIT_V(0); prologue_done(lds, ws, ep, b, lane_id_fresh()); } STAMP_FWD);
    STAMP_END(1 + 5 * layer);
    }
#undef STAMP_SEC
#define STAMP_SEC 1
    STAMP_BEGIN;
    gmlp_body(lds, wid & 1, wid >> 1, pm, cg, wl + (layer ? L_Y : L_Z2T), y, bsp STAMP_FWD);
    {
        WAIT_V(0);
        BARX();
        STAMP(11);
        const int lane = lane_id_fresh(), r = lane & 15, q = lane >> 4;
        const int wk = wid >> 1, rt = rh * 4 + (wid & 1) * 2 + wk;
        const Res rx = proj_residual_load(xin, rt, c5, lane);
        const LAS char *pw[3], *ph[3];
#pragma unroll
        for (int i = 0; i < 3; ++i) {
            pw[i] = opaque(lds + 3 * i * K5_STAGE + wk * 4608 + lane * 16);
            ph[i] = opaque(lds + 3 * i * K5_STAGE + wk * 4608 + 4096 + q * 128 + (r & 7) * 16);
        }
        const auto rY = RSRC(y + (size_t)(rt & ~1) * 73728 + wk * 1024);
        const unsigned vy[2] = {pin((unsigned)(wk * 73728 + lane * 16)), pin((unsigned)((1 - wk) * 73728 + lane * 16))};
        int dk[4];
#pragma unroll
        for (int i = 0; i < 4; ++i) dk[i] = pin_s((kord5(4 + i) - 4 - i) * 4096);
        auto yoff = [&](int t) { return pin_s(t == 0 || t == 17 ? kord5(t) * 4096 : t * 4096 + dk[t & 3]); };
        bf16x8 af[5][4], wf[4][5];
        auto lda = [&](int so, int i) { return __builtin_bit_cast(bf16x8, __builtin_amdgcn_raw_buffer_load_b128(rY, vy[i & 1] + (i >> 1) * 2048, so, 16)); };
        auto rdw = [&](int t, int c, int n) {
            const int sl = (t + 6) & 7, o = (sl % 3) * K5_STAGE + c * 9216;
            wf[2 * (t & 1) + c][n] = n < 4 ? LDS_FRAG(pw[sl / 3] + o + n * 1024) : LDS_FRAG(ph[sl / 3] + o);
        };
#pragma unroll
        for (int n = 0; n < 10; ++n) rdw(0, n / 5, n % 5);
        int so[18];
#pragma unroll
        for (int i = 0; i < 5; ++i) so[i] = yoff(i);
        so[17] = yoff(17);
        f32x4 v[5], u[5];
        proj_residual(v, rx);
#pragma unroll
        for (int n = 0; n < 5; ++n) v[n] = pin(v[n]);
        LAS char* xo = (LAS char*)opaque(lds + wid * 4608 + lane * 16);
        LAS char* xo4 = (LAS char*)opaque(lds + wid * 4608 + 4096 + (lane & 31) * 16);
        const LAS char* xi = opaque(lds + (wid ^ 2) * 4608 + lane * 16);
        const LAS char* xi4 = opaque(lds + (wid ^ 2) * 4608 + 4096 + (lane & 31) * 16);
        const ProjOut po = layer ? proj_out_final(out, rt, c5, lane) : proj_out(wl + SZ_L + L_XA, wl + SZ_L + L_STA, rt, c5, lane);
        asm volatile("" :: "s"(rY));
        WAIT_L(0);
        BARX();
        STAMP(12);
#pragma unroll
        for (int i = 0; i < K5_PRE; ++i) { af[i >> 2][i & 3] = lda(so[i >> 2], i & 3); SCHED0(); if (i >= PACE0 && i + 1 < K5_PRE) PACE; SCHED0(); }
        static_for<18>([&](auto tc) {
            constexpr int t = tc.value;
            bf16x8 (&a)[4] = af[t % 5];
#pragma unroll
            for (int h = 0; h < 4; ++h) {
                const int c = t < 17 ? h >> 1 : h & 1, m = t < 17 ? h & 1 : 1 - (h >> 1);
                if (t < 17 ? m == 0 : h < 2) { if (t == 17 && h == 1) WAIT_L(0); else WAIT_L(5); }
#pragma unroll
                for (int n = 0; n < 5; ++n) {
                    f32x4& e = m ? u[n] : v[n];
                    if (t == 0 && h == 1) MFMA_0(wf[0][n], a[1], e); else MFMA_T(wf[2 * (t & 1) + c][n], a[2 * c + m], e);
                    SCHED0();
                    if (t < 17 && m == 0) rdw(t + 1, c, n);
                    if (t < 14 && m == 1 && !(n & 1)) { const int i = y_load_at(6 * t + 3 * c + n / 2); if (i >= 0) af[(i >> 2) % 5][i & 3] = lda(so[i >> 2], i & 3); }
                    if (t < 12 && h == 3 && n == 3) so[t + 5] = yoff(t + 5);
                    if (t == 17 && h * 5 + n >= 13 && h * 5 + n < 18) { const int i = h * 5 + n - 13; *(LAS f32x4*)(i < 4 ? xo + i * 1024 : xo4) = u[i]; }
                    SCHED0();
                }
            }
            if ((t & 1) && t < 17) BAR();
            SCHED0();
        });
        {
            WAIT_L0(); BARX();
#pragma unroll
            for (int n = 0; n < 5; ++n) u[n] = *(const LAS f32x4*)(n < 4 ? xi + n * 1024 : xi4);
            SCHED0();
            asm volatile("s_nop 1" : "+v"(v[0]), "+v"(v[1]), "+v"(v[2]), "+v"(v[3]), "+v"(v[4]));
#pragma unroll
            for (int n = 0; n < 5; ++n) v[n] += u[n];
        }
        STAMP(13);
        if (layer) proj_store<true>(v, po);
        else { proj_store(v, po); WAIT_V(0); BARX(); }
    }
    STAMP_END(3 + 5 * layer);
    TRACE_DUMP;
  }
  asm volatile("gm_code_end2:");
}

}

#define STOP_CHECK(n)

extern "C" void kernel_launch(void* const* d_in, const int* in_sizes, int n_in, void* d_out, int out_size, void* d_ws, size_t ws_size,
                              hipStream_t stream) {
    (void)in_sizes; (void)n_in; (void)out_size; (void)ws_size;
    const u16* x = (const u16*)d_in[0];
    const u16* wqkv = (const u16*)d_in[1];
    const u16* wo = (const u16*)d_in[2];
    const u16* w_in = (const u16*)d_in[4];
    const u16* w_sp = (const u16*)d_in[5];
    const u16* b_sp = (const u16*)d_in[6];
    const u16* w_out = (const u16*)d_in[7];
    char* ws = (char*)d_ws;
    (void)hipFuncSetAttribute((const void*)k_net, hipFuncAttributeMaxDynamicSharedMemorySize, NET_LDS);
    k_net<<<256, 512, NET_LDS, stream>>>(ws, w_sp, b_sp, (char*)d_out, (const char*)x, (const char*)wqkv, wo, w_in, w_out DBG_LAUNCH(0));
}
```

```cpp
#include <hip/hip_runtime.h>
#include <stdint.h>

namespace {

typedef unsigned short u16;
typedef short bf16x8 __attribute__((ext_vector_type(8)));
typedef float f32x4 __attribute__((ext_vector_type(4)));
typedef float f32x2 __attribute__((ext_vector_type(2)));
typedef unsigned u32x4 __attribute__((ext_vector_type(4)));
typedef unsigned u32x2 __attribute__((ext_vector_type(2)));
typedef __bf16 bf16x2_t __attribute__((ext_vector_type(2)));
#define LAS __attribute__((address_space(3)))
#define DEVI __device__ __forceinline__

constexpr int S_ = 2048, D_ = 576, FH = 2304;

constexpr size_t SZ_TWQKV = (size_t)16 * 18 * 6 * 1024;
constexpr size_t SZ_TWT = (size_t)48 * 18 * 256;
constexpr size_t SZ_TWO = (size_t)8 * 18 * 4608;
constexpr size_t SZ_TWIN = (size_t)16 * 18 * 18 * 1024;
constexpr size_t SZ_TWOUT = (size_t)8 * 72 * 4608;
constexpr size_t SZ_X = (size_t)S_ * D_ * 2;
constexpr size_t SZ_Z = (size_t)S_ * FH * 2;
constexpr size_t SZ_ST = (size_t)S_ * 64;
constexpr size_t SZ_ZST = (size_t)16 * 16 * 128 * 8;
constexpr size_t OFF_TWQKV = 0;
constexpr size_t OFF_TWT = OFF_TWQKV + SZ_TWQKV;
constexpr size_t OFF_TWO = OFF_TWT + SZ_TWT;
constexpr size_t OFF_TWIN = OFF_TWO + SZ_TWO;
constexpr size_t OFF_TWOUT = OFF_TWIN + SZ_TWIN;
constexpr size_t OFF_CNT = OFF_TWOUT + SZ_TWOUT;
constexpr size_t SZ_CNT = 10 * 65536;
constexpr size_t OFF_FW = OFF_CNT + SZ_CNT;
constexpr size_t OFF_FL = OFF_FW + 262144;
constexpr size_t OFF_EP = OFF_FL + 4096;
constexpr size_t OFF_FL2 = OFF_EP + 4096;
constexpr size_t OFF_ZST = OFF_FL2 + 12288;
constexpr size_t OFF_L0 = OFF_ZST + 2 * SZ_ZST;
constexpr size_t L_XA = 0, L_STA = L_XA + SZ_X, L_O = L_STA + SZ_ST, L_XB = L_O + SZ_X, L_STB = L_XB + SZ_X, L_Z2T = L_STB + SZ_ST, L_Y = L_Z2T + SZ_Z;
constexpr size_t SZ_L = 0;
static_assert(OFF_L0 + L_Y + SZ_Z <= ((size_t)256 << 20), "workspace");
constexpr unsigned F_O = OFF_CNT, F_X1 = F_O + 65536, F_Z2 = F_O + 131072, F_Y = F_O + 196608, F_LAYER = 262144, F_X2 = F_O + 2 * F_LAYER;

DEVI unsigned pack2(float a, float b) { f32x2 v = {a, b}; bf16x2_t r = __builtin_convertvector(v, bf16x2_t); return __builtin_bit_cast(unsigned, r); }
DEVI float bflo(unsigned u) { return __uint_as_float(u << 16); }
DEVI float bfhi(unsigned u) { return __uint_as_float(u & 0xffff0000u); }
DEVI int slot2row(int sg) { return (sg & ~31) + 2 * (sg & 15) + ((sg >> 4) & 1); }
DEVI float fmax2(float a, float b) { return __builtin_amdgcn_fmed3f(a, b, __builtin_inff()); }
#define XQ4(OP, MID) \
    asm volatile("s_nop 1\n\tv_permlane32_swap_b32 %0, %1\n\tv_permlane32_swap_b32 %2, %3\n\t" OP " %0, %0, %1\n\t" OP " %2, %2, %3\n\t" \
                 "s_nop 1\n\tv_permlane16_swap_b32 %0, %2\n\t" OP " %0, %0, %2\n\t" MID \
                 "v_mov_b32 %2, %0\n\ts_nop 1\n\tv_permlane16_swap_b32 %0, %2\n\tv_mov_b32 %1, %0\n\tv_mov_b32 %3, %2\n\ts_nop 0\n\t" \
                 "v_permlane32_swap_b32 %0, %1\n\tv_permlane32_swap_b32 %2, %3" : "+v"(x[0]), "+v"(x[1]), "+v"(x[2]), "+v"(x[3]))
DEVI void xq4_max(float (&x)[4]) { XQ4("v_max_f32", ""); }
DEVI void xq4_sum(float (&x)[4]) { XQ4("v_add_f32", ""); }
DEVI void xq4_sum_rcp(float (&x)[4]) { XQ4("v_add_f32", "v_rcp_f32 %0, %0\n\ts_nop 0\n\t"); }
template <class T> DEVI T pin(T v) { asm volatile("" : "+v"(v)); return v; }
template <int V> struct IC { static constexpr int value = V; };
template <int N, int I = 0, class F> DEVI void static_for(F&& f) { if constexpr (I < N) { f(IC<I>{}); static_for<N, I + 1>(f); } }
template <class T> DEVI T* own_s(T* p) { T* r; asm volatile("s_mov_b64 %0, %1" : "=&s"(r) : "s"(p)); return r; }
DEVI int pin_s(int v) { asm volatile("" : "+s"(v)); return v; }
DEVI float xq_sum(float v) {
    auto a = __builtin_amdgcn_permlane32_swap(__float_as_uint(v), __float_as_uint(v), false, false);
    v = __uint_as_float(a[0]) + __uint_as_float(a[1]);
    auto b = __builtin_amdgcn_permlane16_swap(__float_as_uint(v), __float_as_uint(v), false, false);
    return __uint_as_float(b[0]) + __uint_as_float(b[1]);
}
#define RSRC(p) __builtin_amdgcn_make_buffer_rsrc((void*)(p), 0, 0x7fffffff, 0x00020000)
#define ST16(rs, vo, so, v, aux) __builtin_amdgcn_raw_buffer_store_b128((v), (rs), (vo) + (so), 0, aux)
#define ST8(rs, vo, so, v, aux) __builtin_amdgcn_raw_buffer_store_b64((v), (rs), (vo), (so), aux)
#define WAIT_V(n) asm volatile("s_waitcnt vmcnt(%0)" ::"i"(n) : "memory")
#define WAIT_L0() asm volatile("s_waitcnt lgkmcnt(0)" ::: "memory")
#define BAR() do { asm volatile("" ::: "memory"); __builtin_amdgcn_s_barrier(); asm volatile("" ::: "memory"); } while (0)
#define BARX() do { __builtin_amdgcn_sched_barrier(0); BAR(); __builtin_amdgcn_sched_barrier(0); } while (0)
#define LOOP_ALIGN() asm volatile(".p2align 6")
#define SCHED0() __builtin_amdgcn_sched_barrier(0)
#define MFMA(a, b, c) __builtin_amdgcn_mfma_f32_16x16x32_bf16((a), (b), (c), 0, 0, 0)
#define MFMA_T(a, b, c) asm volatile("v_mfma_f32_16x16x32_bf16 %0, %1, %2, %0" : "+v"(c) : "v"(a), "v"(b))
#define MFMA_0(a, b, c) asm volatile("v_mfma_f32_16x16x32_bf16 %0, %1, %2, 0" : "=&v"(c) : "v"(a), "v"(b))
#define WAIT_L(n) __builtin_amdgcn_s_waitcnt(0xC07F | ((n) << 8))
#define LDS_FRAG(p) (*(const LAS bf16x8*)(p))
DEVI int lane_id_fresh() { int z = 0; asm volatile("" : "+v"(z)); return __builtin_amdgcn_mbcnt_hi(~0u, __builtin_amdgcn_mbcnt_lo(~0u, z)); }
DEVI const LAS char* opaque(const LAS char* p) { asm volatile("" : "+v"(p)); return p; }
#define DBG_ARG
#define DBG_LAUNCH(n)
#define STAMP(i)
#define STAMP_DECL
#define STAMP_BEGIN
#define STAMP_END(n)
#define STAMP_PARAM
#define STAMP_FWD

#define PST_PARAM
#define PST_FWD
#define PST_DECL
#define PST(i)
#define PST_DUMP(c, k)
#define TRACE_ATTR
#define TRACE_BEGIN
#define TRACE_DUMP

DEVI float add1(float a, float b) { float r; asm("v_add_f32 %0, %1, %2" : "=v"(r) : "v"(a), "v"(b)); return r; }
DEVI float rstd_of(float s, float q, float inv_n, float& mean) {
    float var;
    asm("v_mul_f32 %0, %2, %3\n\tv_mul_f32 %1, %2, %4\n\tv_fma_f32 %1, -%0, %0, %1" : "=&v"(mean), "=&v"(var) : "s"(inv_n), "v"(s), "v"(q));
    return __builtin_amdgcn_rsqf(fmax2(var, 0.f) + 1e-5f);
}
DEVI float ln_rstd(const f32x4 p) {
    float s, q, mean;
    asm("v_add_f32 %0, %2, %4\n\tv_add_f32 %1, %3, %5\n\ts_nop 0\n\t"
        "v_add_f32_dpp %0, %0, %0 quad_perm:[1,0,3,2] row_mask:0xf bank_mask:0xf\n\tv_add_f32_dpp %1, %1, %1 quad_perm:[1,0,3,2] row_mask:0xf bank_mask:0xf\n\ts_nop 0\n\t"
        "v_add_f32_dpp %0, %0, %0 quad_perm:[2,3,0,1] row_mask:0xf bank_mask:0xf\n\tv_add_f32_dpp %1, %1, %1 quad_perm:[2,3,0,1] row_mask:0xf bank_mask:0xf"
        : "=&v"(s), "=&v"(q) : "v"(p[0]), "v"(p[1]), "v"(p[2]), "v"(p[3]));
    return rstd_of(s, q, 1.0f / 576.0f, mean);
}

struct Res { u32x4 a, c; u32x2 e; };
DEVI Res proj_residual_load(const char* xin, int rt, int cg, int lane) {
    const auto rs = RSRC(xin + (size_t)rt * 18432 + cg * 2304);
    Res x;
    x.a = __builtin_amdgcn_raw_buffer_load_b128(rs, lane * 16, 0, 16); x.c = __builtin_amdgcn_raw_buffer_load_b128(rs, lane * 16 + 1024, 0, 16);
    x.e = __builtin_amdgcn_raw_buffer_load_b64(rs, 2048 + (lane & 15) * 16 + ((lane >> 4) & 1) * 8, 0, 16);
    return x;
}
DEVI void proj_residual(f32x4 (&v)[5], const Res& x) {
    v[0] = (f32x4){bflo(x.a.x), bfhi(x.a.x), bflo(x.a.y), bfhi(x.a.y)}; v[1] = (f32x4){bflo(x.a.z), bfhi(x.a.z), bflo(x.a.w), bfhi(x.a.w)};
    v[2] = (f32x4){bflo(x.c.x), bfhi(x.c.x), bflo(x.c.y), bfhi(x.c.y)}; v[3] = (f32x4){bflo(x.c.z), bfhi(x.c.z), bflo(x.c.w), bfhi(x.c.w)};
    v[4] = (f32x4){bflo(x.e.x), bfhi(x.e.x), bflo(x.e.y), bfhi(x.e.y)};
}
struct ProjOut { __amdgpu_buffer_rsrc_t x, st; unsigned v0, v1, v2, vs; };
DEVI ProjOut proj_out(char* xout, char* stout, int rt, int cg, int lane) {
    const int r = lane & 15, q = lane >> 4;
    ProjOut p = {RSRC(xout + (size_t)rt * 18432 + cg * 2304), RSRC(stout + (size_t)rt * 1024 + cg * 8),
                 pin((unsigned)lane * 16u), pin((unsigned)lane * 16u + 1024u), pin((unsigned)(2048 + r * 16 + (q & 1) * 8)), pin((unsigned)(r * 64 + (q >> 1) * 4))};
    asm volatile("" :: "s"(p.x), "s"(p.st));
    return p;
}
DEVI ProjOut proj_out_final(char* out, int rt, int cg, int lane) {
    const int q = lane >> 4;
    const unsigned o = (unsigned)(slot2row(rt * 16 + (lane & 15)) * 1152 + cg * 144);
    ProjOut p = {RSRC(out), RSRC(out), pin(o + q * 16), pin(o + 64 + q * 16), pin(o + 128 + (q & 1) * 8), 0u};
    return p;
}
template <bool FINAL = false>
DEVI void proj_store(f32x4 (&v)[5], const ProjOut& p) {
    ST16(p.x, p.v0, 0, ((u32x4){pack2(v[0][0], v[0][1]), pack2(v[0][2], v[0][3]), pack2(v[1][0], v[1][1]), pack2(v[1][2], v[1][3])}), FINAL ? 0 : 16);
    SCHED0();
    asm volatile("" : "+v"(v[2]), "+v"(v[3]), "+v"(v[4]));
    ST16(p.x, p.v1, 0, ((u32x4){pack2(v[2][0], v[2][1]), pack2(v[2][2], v[2][3]), pack2(v[3][0], v[3][1]), pack2(v[3][2], v[3][3])}), FINAL ? 0 : 16);
    SCHED0();
    ST8(p.x, p.v2, 0, ((u32x2){pack2(v[4][0], v[4][1]), pack2(v[4][2], v[4][3])}), FINAL ? 0 : 16);
    SCHED0();
    if (!FINAL) {
        const f32x4 s4 = ((v[0] + v[1]) + (v[2] + v[3])) + v[4] * 0.5f;
        const f32x4 q4 = ((v[0] * v[0] + v[1] * v[1]) + (v[2] * v[2] + v[3] * v[3])) + v[4] * v[4] * 0.5f;
        auto x = __builtin_amdgcn_permlane32_swap(__float_as_uint((s4[0] + s4[2]) + (s4[1] + s4[3])), __float_as_uint((q4[0] + q4[2]) + (q4[1] + q4[3])), false, false);
        const float t = __uint_as_float(x[0]) + __uint_as_float(x[1]);
        auto y = __builtin_amdgcn_permlane16_swap(__float_as_uint(t), __float_as_uint(t), false, false);
        __builtin_amdgcn_raw_buffer_store_b32(__float_as_uint(__uint_as_float(y[0]) + __uint_as_float(y[1])), p.st, p.vs, 0, 16);
        SCHED0();
    }
}

DEVI void side_load(f32x4 (&d)[3], const char* p, const char* p2) {
    asm volatile("global_load_dwordx4 %0, %3, off sc1\n\tglobal_load_dwordx4 %1, %3, off offset:1024 sc1\n\tglobal_load_dwordx4 %2, %4, off sc1"
                 : "=&v"(d[0]), "=&v"(d[1]), "=&v"(d[2]) : "v"(p), "v"(p2) : "memory");
}
DEVI void side_fence(f32x4 (&d)[3]) { asm volatile("" : "+v"(d[0]), "+v"(d[1]), "+v"(d[2])); }
DEVI void lds_word_set(LAS char* p, unsigned v) { asm volatile("ds_write_b32 %0, %1" :: "v"((unsigned)(size_t)p), "v"(v) : "memory"); }
#define DMA_I(v, off, sc) "buffer_load_dwordx4 " v ", %1, %2 offen offset:" off sc " lds\n\t"
#define DMA5(sc) asm volatile("s_mov_b32 m0, %0\n\ts_mov_b32 exec_hi, 0\n\ts_nop 2\n\t" DMA_I("%3", "0", sc) "s_mov_b32 exec_hi, -1\n\t" \
    DMA_I("%3", "512", sc) DMA_I("%3", "1536", sc) DMA_I("%3", "2560", sc) DMA_I("%3", "3584", sc) :: "s"(la), "s"(rs), "s"(so), "v"(vo) : "memory")
#define DMA4(sc) asm volatile("s_mov_b32 m0, %0\n\ts_nop 3\n\t" DMA_I("%3", "0", sc) DMA_I("%4", "1024", sc) DMA_I("%5", "2048", sc) DMA_I("%6", "3072", sc) \
    :: "s"(la), "s"(rs), "s"(so), "v"(v0), "v"(v1), "v"(v2), "v"(v3) : "memory")
template <bool SC1> DEVI void dma5(unsigned la, __amdgpu_buffer_rsrc_t rs, int so, unsigned vo) { if (SC1) DMA5(" sc1"); else DMA5(""); }
template <bool SC1> DEVI void dma4(unsigned la, __amdgpu_buffer_rsrc_t rs, int so, unsigned v0, unsigned v1, unsigned v2, unsigned v3) { if (SC1) DMA4(" sc1"); else DMA4(""); }
#define DMA2(sc) asm volatile("s_mov_b32 m0, %0\n\ts_nop 3\n\t" DMA_I("%3", "0", sc) DMA_I("%4", "1024", sc) :: "s"(la), "s"(rs), "s"(so), "v"(v0), "v"(v1) : "memory")
template <bool SC1> DEVI void dma2(unsigned la, __amdgpu_buffer_rsrc_t rs, int so, unsigned v0, unsigned v1) { if (SC1) DMA2(" sc1"); else DMA2(""); }
DEVI void dma1(unsigned la, __amdgpu_buffer_rsrc_t rs, int so, unsigned v0) { asm volatile("s_mov_b32 m0, %0\n\ts_nop 3\n\t" DMA_I("%3", "0", "") :: "s"(la), "s"(rs), "s"(so), "v"(v0) : "memory"); }
#define WAIT_IF(c, a, b) do { if (c) WAIT_V(a); else WAIT_V(b); } while (0)
template <int T0, int T1, int P, int S0 = 0, class ISSUE>
DEVI void ring_steps(ISSUE&& issue) {
    int slot = (T0 + S0) % 3;
    LOOP_ALIGN();
#pragma unroll 1
    for (int t = T0; t < T1; ++t) {
        WAIT_V(P); BAR();
        issue(t + 3, slot);
        slot = slot == 2 ? 0 : slot + 1;
    }
}

DEVI unsigned flag_out(unsigned use, int pitch, int group, int writer, int lane) { return pin(lane < 16 ? use + (group * 16 + lane) * pitch + writer * 16 : 0x80000000u); }
DEVI unsigned flag_in(unsigned use, int pitch, int reader, int n, int lane) { return pin(use + reader * pitch + (lane < n ? lane : 0) * 16); }
DEVI void flag_raise(const char* ws, unsigned vo) { ST16(RSRC(ws), vo, 0, ((u32x4){1u, 1u, 1u, 1u}), 16); SCHED0(); }
template <int FIRST, int AGAIN = 1>
DEVI void flags_wait(const char* ws, unsigned vo) {
    unsigned v, n = 400000u;
    asm volatile("s_nop 4\n\ts_sleep %4\n"
                 "1:\n\tbuffer_load_dword %0, %2, %3, 0 offen sc1\n\ts_waitcnt vmcnt(0)\n\tv_cmp_eq_u32 vcc, 0, %0\n\ts_cbranch_vccz 2f\n\t"
                 "s_sleep %5\n\ts_sub_u32 %1, %1, 1\n\ts_cbranch_scc0 1b\n2:"
                 : "=&v"(v), "+s"(n) : "v"(vo), "s"(RSRC(ws)), "i"(FIRST), "i"(AGAIN) : "vcc", "scc", "memory");
    SCHED0();
}
template <int FIRST>
DEVI void epoch_wait(const char* ws, unsigned vo, unsigned long long ep) {
    unsigned long long v; unsigned n = 400000u;
    asm volatile("s_nop 4\n\ts_sleep %5\n"
                 "1:\n\tbuffer_load_dwordx2 %0, %2, %3, 0 offen sc1\n\ts_waitcnt vmcnt(0)\n\tv_cmp_ne_u64 vcc, %4, %0\n\ts_cbranch_vccz 2f\n\t"
                 "s_sleep 1\n\ts_sub_u32 %1, %1, 1\n\ts_cbranch_scc0 1b\n2:"
                 : "=&v"(v), "+s"(n) : "v"(vo), "s"(RSRC(ws)), "s"(ep), "i"(FIRST) : "vcc", "scc", "memory");
    SCHED0();
}
DEVI unsigned lds_arrive(LAS char* p) {
    unsigned v;
    asm volatile("s_mov_b64 exec, 1\n\tds_add_rtn_u32 %0, %1, %2\n\ts_mov_b64 exec, -1\n\ts_waitcnt lgkmcnt(0)" : "=&v"(v) : "v"((unsigned)(size_t)p), "v"(1u) : "memory");
    return __builtin_amdgcn_readfirstlane(v);
}
DEVI void lds_count_wait(LAS char* p, unsigned n) {
#pragma unroll 1
    for (unsigned spin = 0; spin < 40000000u; ++spin) {
        unsigned v;
        asm volatile("ds_read_b32 %0, %1\n\ts_waitcnt lgkmcnt(0)" : "=v"(v) : "v"((unsigned)(size_t)p) : "memory");
        if (__builtin_amdgcn_readfirstlane(v) >= n) break;
    }
}

constexpr int K1_STAGE = 32768;
constexpr int WO_OFF = 69632;
constexpr int G_OWN = 139264;
constexpr int G_PART = G_OWN + 18432;
constexpr int G_TAB = G_PART + 2048;
constexpr int K1_SCR = G_TAB + 1536;
constexpr int G_WORD = K1_SCR + 576;
constexpr int NET_LDS = G_WORD + 64;

#define WO_PRE 9
#define WO_W 33
#define REL_PRIO 0
#define W_FIRST 2
#define I_SLEEP 30
#define OJOB_SLEEP 20
#define PACE0 1
#define PACE __builtin_amdgcn_s_sleep(1)
#define POLLW 0
#define WO_EV 4
#define WIN_EARLY 0
#define WIN_LOOK 40
#define O_FIRST 10
#define X1_FIRST 6
#define X2_FIRST 6
#define Y_FIRST 10
#define Z2_FIRST 10
DEVI int win_strip(int b) { return ((b - 112) & 7) * 18 + ((b - 112) >> 3); }
DEVI int win_third(int b, int w) { return b & 8 ? 3 - w : w - 1; }
DEVI unsigned attn_vb(int wid, int lane) {
    const int c = lane & 15;
    return pin((unsigned)(wid < 3 ? lane * 16 : (c < 8 ? (c >> 1) & 1 : 2) * 4608 + (lane >> 4) * 64 + (c < 8 ? (c >> 2) * 2 + (c & 1) : c & 3) * 16));
}
DEVI void attn_issueB(LAS char* lds, const char* ws, int hp, int wid, unsigned vb, int s, int slot) {
    const unsigned la = (unsigned)(size_t)lds + slot * K1_STAGE + 18432 + wid * 4096;
    if (wid < 3) dma4<true>(la, RSRC(ws), (int)OFF_TWQKV + hp * (18 * 6144) + wid * 4096 + s * 12288, vb, vb, vb, vb);
    else asm volatile("s_mov_b32 m0, %0\n\ts_nop 3\n\tbuffer_load_dwordx4 %4, %1, %2 offen sc1 lds\n\tbuffer_load_dwordx4 %4, %1, %3 offen offset:1024 sc1 lds"
                      :: "s"(la), "s"(RSRC(ws)), "s"((int)OFF_TWT + hp * 13824 + s * 512), "s"((int)OFF_TWT + hp * 13824 + s * 512 + 256 - 1024), "v"(vb) : "memory");
}
struct AttnX { __amdgpu_buffer_rsrc_t rs; unsigned v[4], vh[2]; int oA, oh, step; };
DEVI AttnX attn_x(const char* x0, const char* xa, int layer, int pm, int wid, int lane) {
    const int l3 = lane >> 3, pmh = pm > 0 ? pm : 1;
    const unsigned v = layer ? lane * 16 : 2 * l3 * 1152 + ((lane & 7) ^ l3) * 16 + 4096, d1 = layer ? 0 : 16 * 1152 - 1024, d2 = layer ? 16384 : 1152 - 2048;
    const unsigned vh = layer ? (l3 & 1) * 18432 + (lane >> 4) * 256 + (8 + (lane & 7)) * 16 : v + 16 * 1152;
    return {RSRC(layer ? xa : x0 - 4096), {pin(v), pin(v + d1), pin(v + d2), pin(v + d1 + d2)}, {pin(vh), pin(vh + (layer ? 0 : 1152 - 1024))},
            (pm * 8 + 2 * wid) * 18432, (pmh * 8 - 2) * 18432, layer ? 2048 : 128};
}
DEVI void attn_issueA(LAS char* lds, const AttnX& a, int wid, int s, int slot) {
    const unsigned la = (unsigned)(size_t)lds + slot * K1_STAGE;
    if (wid == 3) dma2<true>(la, a.rs, a.oh + s * a.step, a.vh[0], a.vh[1]);
    dma4<true>(la + 2048 + wid * 4096, a.rs, a.oA + s * a.step, a.v[0], a.v[1], a.v[2], a.v[3]);
}
template <class DONE, class NEXT>
DEVI void attn_producer(LAS char* lds, const char* ws, const char* x0, const char* wl, unsigned fl, unsigned long long ep, int layer, int pm, int hp, int wid, DONE&& done, NEXT&& next) {
    const int lane = lane_id_fresh();
    const int pmh = pm > 0 ? pm : 1;
    const unsigned vo = lane * 16;
    const AttnX ax = attn_x(x0, wl + L_XA, layer, pm, wid, lane);
    const unsigned vb = attn_vb(wid, lane), fd = pin((unsigned)OFF_FL + wid * 1024 + vo);
    auto issueB = [&](int s, int slot) { attn_issueB(lds, ws, hp, wid, vb, s, slot); };
    auto issue = [&](int s, int slot) { attn_issueA(lds, ax, wid, s, slot); issueB(s, slot); };
    const int hr = lane >> 2;
    const char* ps = pin(wl + L_STA + (size_t)(pm * 128 + wid * 32) * 64 + lane * 16);
    const char* psh = pin(wl + L_STA + (size_t)((pmh * 8 - 2 + (hr >> 3)) * 16 + 8 + (hr & 7)) * 64 + (lane & 3) * 16);
    if (layer == 0) {
        const unsigned fi = pin((unsigned)OFF_FW + (pm * 16 + hp) * 1024 + (lane < 6 ? lane : 0) * 128);
        done();
        if (wid == 0) epoch_wait<W_FIRST>(ws, fi, ep);
        BARX();
        issueB(0, 0); issueB(1, 1); issue(2, 2);
        WAIT_IF(wid == 3, 10, 12);
    } else {
        issueB(2, 2);
        const unsigned fi = flag_in(F_X2, 512, pm * 16 + hp, pm > 0 ? 24 : 16, lane);
        if (wid == 0) flags_wait<X2_FIRST>(ws, fi);
        BARX();
#pragma unroll
        for (int i = 0; i < 3; ++i) attn_issueA(lds, ax, wid, i, i);
        WAIT_IF(wid == 3, 12, 8);
    }
    __builtin_amdgcn_s_setprio(0);
    BAR();
    if (layer) WAIT_IF(wid == 3, 6, 4);
    next(-1);
    ring_steps<0, 4, 8>(issue);
    {
        unsigned long long done;
        if (layer == 0) asm volatile("s_nop 4\n\tbuffer_load_dwordx2 %0, %1, %2, 0 offen sc1" : "+v"(done) : "v"(fd), "s"(RSRC(ws)) : "memory");
        WAIT_IF(layer, 8, 9); BAR(); issue(7, 1);
        WAIT_V(8);
        if (layer == 0) { asm volatile("" : "+v"(done)); if (__any(done != ep)) epoch_wait<0>(ws, fd, ep); }
        BAR(); issue(8, 2);
    }
    const auto rW = RSRC(ws + OFF_TWO + (size_t)(hp >> 1) * 82944 + wid * 4096);
    auto issueW = [&](int i) { dma4<true>((unsigned)(size_t)lds + WO_OFF + (i * 4 + wid) * 4096, rW, i * 16384, vo, vo, vo, vo); };
    {
        f32x4 sp[3];
        side_load(sp, ps, psh);
        WAIT_V(11); BAR(); issueW(2); issueW(3);
        WAIT_V(8); BARX();
        side_fence(sp);
#pragma unroll
        for (int i = 0; i < 3; ++i) lds_word_set(lds + K1_SCR + (i < 2 ? 16 + wid * 32 + i * 16 + hr : hr) * 4, __float_as_uint(ln_rstd(sp[i])));
        WAIT_L0();
    }
    BAR();
    next(0);
    issueW(0); issueW(1); issueW(4);
    if (wid == 0) dma2<true>((unsigned)(size_t)lds + WO_OFF + 80896, rW, 80896, vo, vo);
    next(1);
    const unsigned fo = flag_out(fl + F_O, 256, pm, hp, lane), fi = flag_in(fl + F_O, 256, pm * 16 + hp, 16, lane);
    WAIT_V(0);
    if (wid == POLLW) __builtin_amdgcn_s_setprio(3);
    BARX();
    next(2);
    if (wid == POLLW) { flag_raise(ws, fo); flags_wait<O_FIRST>(ws, fi); }
    __builtin_amdgcn_s_setprio(0);
    BARX();
}

#define STAMP_SEC 0
template <class DONE>
DEVI void attn_consumer(LAS char* lds, const char* x0, char* wl, int layer, int pm, int hp, int wid, DONE&& done STAMP_PARAM) {
    const int lane = lane_id_fresh(), r = lane & 15, q = lane >> 4;
    const char* xa = wl + L_XA;
    char* o = wl + L_O;
    const int T = wid & 1, h = wid >> 1;
    const LAS char *ph[2], *pa[2];
#pragma unroll
    for (int kk = 0; kk < 2; ++kk) {
        const int sw = ((4 * kk + q) ^ (r & 7)) * 16;
        ph[kk] = opaque(lds + (layer ? kk * 1024 + q * 256 + (r ^ (T ? 0 : 8)) * 16 : (r ^ (T ? 0 : 8)) * 128 + sw));
        pa[kk] = opaque(lds + T * 2048 + (layer ? kk * 1024 + lane * 16 : r * 128 + sw));
    }
    const LAS char* pb = lds + h * 3072 + lane * 16;
    const LAS char* pt = lds + lane * 16;
    f32x4 acc[5][4];
#pragma unroll
    for (int m = 0; m < 5; ++m)
#pragma unroll
        for (int n = 0; n < 4; ++n) if (m + n) acc[m][n] = pin((f32x4){0.f, 0.f, 0.f, 0.f});
    bf16x8 fa[2][5], fb[2][4];
    auto rd1 = [&](int kk, int i) {
        if (i < 3) fb[kk][i] = LDS_FRAG(pb + 18432 + kk * 6144 + i * 1024);
        else if (i == 3) fb[kk][3] = LDS_FRAG(pt + 30720 + kk * 1024);
        else if (i == 4) fa[kk][0] = LDS_FRAG(ph[kk]);
        else if (i < 9) fa[kk][i - 4] = LDS_FRAG(pa[kk] + 2048 + (i - 5) * 4096);
    };
    f32x4 mb, mb0;
#pragma unroll
    for (int j = 0; j < 4; ++j) {
        const bool ok = ((r - 4 * q - j) & 15) < 8;
        mb[j] = ok ? 0.f : -__builtin_inff();
        mb0[j] = (ok && (pm > 0 || 4 * q + j <= r)) ? 0.f : -__builtin_inff();
    }
    mb = pin(mb); mb0 = pin(mb0);
    const unsigned mp0 = pin((4 * q > r ? 0xffffu : 0u) | (4 * q + 1 > r ? 0xffff0000u : 0u)), mp1 = pin((4 * q + 2 > r ? 0xffffu : 0u) | (4 * q + 3 > r ? 0xffff0000u : 0u));
    const unsigned qtm = pin(q == h ? 0xffffffffu : 0u);
    const unsigned vom = pin((unsigned)(r * 16 + hp * 1024 + (2 * h + (q >> 1)) * 256 + (q & 1) * 8)), vot = pin((unsigned)(r * 16 + 16384 + (hp >> 1) * 256 + (hp & 1) * 8 + h * 4));
    const LAS char* tk0 = opaque(lds + K1_SCR + ((q * 4) ^ (T ? 0 : 8)) * 4);
    const LAS char* tk = opaque(lds + K1_SCR + ((1 + T) * 16 + q * 4) * 4);
    const LAS char* tq = opaque(lds + K1_SCR + ((1 + T) * 16 + r) * 4);
    pb = opaque(pb); pt = opaque(pt);
    STAMP(2);
    const int rt = pm * 8 + (hp & 1) * 4 + wid, cg = hp >> 1;
    const ProjOut pr = layer ? proj_out((char*)xa, (char*)xa, rt, cg, lane) : proj_out_final((char*)x0, rt, cg, lane);
    BAR();
    if (layer == 0) done();
    BARX();
    STAMP(3);
#pragma unroll
    for (int i = 0; i < 9; ++i) rd1(0, i);
    SCHED0();
    const Res rx = {__builtin_amdgcn_raw_buffer_load_b128(pr.x, pr.v0, 0, 16), __builtin_amdgcn_raw_buffer_load_b128(pr.x, pr.v1, 0, 16), __builtin_amdgcn_raw_buffer_load_b64(pr.x, pr.v2, 0, 16)};
    SCHED0();
    int slot = 0, left = 9;
    LOOP_ALIGN();
#pragma unroll 1
    do {
        int d;
#pragma unroll
        for (int kk = 0; kk < 2; ++kk) {
            WAIT_L(0);
            if (kk) BAR();
            SCHED0();
#pragma unroll
            for (int i = 1; i < 20; ++i) {
                const int m = i >> 2, n = i & 3;
                const bf16x8 &A = fa[kk][m], &B = fb[kk][n];
                acc[m][n] = n != 2 ? MFMA(B, A, acc[m][n]) : MFMA(A, B, acc[m][n]);
                SCHED0();
                if (i <= 9) rd1(kk ^ 1, i - 1);
                if (kk == 0) {
                    if (i == 10) d = pin_s(slot == 2 ? -2 * K1_STAGE : K1_STAGE);
                    if (i == 11 || i == 12) ph[i & 1] = opaque(ph[i & 1] + d);
                    if (i == 13 || i == 14) pa[i & 1] = opaque(pa[i & 1] + d);
                    if (i == 15) pb = opaque(pb + d);
                    if (i == 16) pt = opaque(pt + d);
                } else {
                    if (i == 11) slot = pin_s(slot == 2 ? 0 : slot + 1);
                    if (i == 13) left = pin_s(left - 1);
                }
                SCHED0();
            }
        }
    } while (left);
    STAMP(4);
    u32x2 ts[5], tf[5];
    {
        LAS char* xb = lds + wid * 320;
#pragma unroll
        for (int m = 0; m < 5; ++m) ts[m] = (u32x2){pack2(acc[m][3][0], acc[m][3][1]), pack2(acc[m][3][2], acc[m][3][3])};
        if (q == 2) {
#pragma unroll
            for (int m = 0; m < 5; ++m) {
                const unsigned w = h ? ts[m].y : ts[m].x;
                *(LAS u16*)(xb + m * 64 + r * 2) = (u16)w; *(LAS u16*)(xb + m * 64 + 32 + r * 2) = (u16)(w >> 16);
            }
        }
        asm volatile("" ::: "memory");
#pragma unroll
        for (int m = 0; m < 5; ++m) tf[m] = *(const LAS u32x2*)(xb + m * 64 + (r & 1) * 32 + q * 8);
    }
    {
        f32x4 rk[5]; float rq[4];
        rk[0] = *(const LAS f32x4*)(tk0);
#pragma unroll
        for (int g = 0; g < 4; ++g) { rk[g + 1] = *(const LAS f32x4*)(tk + g * 128); rq[g] = *(const LAS float*)(tq + g * 128); }
        const f32x4 zero = {0.f, 0.f, 0.f, 0.f};
        u32x4 kf[5];
#pragma unroll
        for (int m = 0; m < 5; ++m) kf[m] = (u32x4){pack2(acc[m][1][0], acc[m][1][1]), pack2(acc[m][1][2], acc[m][1][3]), ts[m].y, 0u};
        f32x4 sp[4], sc[4];
#pragma unroll
        for (int g = 0; g < 4; ++g) {
            const u32x4 qf = {pack2(acc[g + 1][0][0], acc[g + 1][0][1]), pack2(acc[g + 1][0][2], acc[g + 1][0][3]), ts[g + 1].x & qtm, 0u};
            sp[g] = MFMA(__builtin_bit_cast(bf16x8, kf[g]), __builtin_bit_cast(bf16x8, qf), zero);
            sc[g] = MFMA(__builtin_bit_cast(bf16x8, kf[g + 1]), __builtin_bit_cast(bf16x8, qf), zero);
        }
        u32x2 vf[5];
#pragma unroll
        for (int m = 0; m < 5; ++m) vf[m] = (u32x2){pack2(acc[m][2][0], acc[m][2][1]), pack2(acc[m][2][2], acc[m][2][3])};
        f32x4 rs[4], t[4]; float mx[4], sm[4];
#pragma unroll
        for (int g = 0; g < 4; ++g) {
#pragma unroll
            for (int j = 0; j < 4; ++j) { const bool pv = 4 * q + j > r; rs[g][j] = pv ? rk[g][j] : rk[g + 1][j]; t[g][j] = pv ? sp[g][j] : sc[g][j]; }
            t[g] = (t[g] * rs[g]) * (rq[g] * (0.23570226039551584f * 1.4426950408889634f)) + (g == 0 ? mb0 : mb);
            mx[g] = fmaxf(fmaxf(t[g][0], t[g][1]), fmaxf(t[g][2], t[g][3]));
        }
        xq4_max(mx);
#pragma unroll
        for (int g = 0; g < 4; ++g) {
            t[g] = t[g] - mx[g];
#pragma unroll
            for (int j = 0; j < 4; ++j) t[g][j] = __builtin_amdgcn_exp2f(t[g][j]);
            sm[g] = (t[g][0] + t[g][2]) + (t[g][1] + t[g][3]);
        }
        f32x4 om[4], ot[4];
#pragma unroll
        for (int g = 0; g < 4; ++g) {
            const f32x4 p = t[g] * rs[g];
            const unsigned p01 = pack2(p[0], p[1]), p23 = pack2(p[2], p[3]);
            const u32x4 pf = {p01 & mp0, p23 & mp1, p01 & ~mp0, p23 & ~mp1};
            om[g] = MFMA(__builtin_bit_cast(bf16x8, ((u32x4){vf[g].x, vf[g].y, vf[g + 1].x, vf[g + 1].y})), __builtin_bit_cast(bf16x8, pf), zero);
            ot[g] = MFMA(__builtin_bit_cast(bf16x8, ((u32x4){tf[g].x, tf[g].y, tf[g + 1].x, tf[g + 1].y})), __builtin_bit_cast(bf16x8, pf), zero);
        }
        xq4_sum_rcp(sm);
        const auto ro = RSRC(o + (size_t)(pm * 8 + T) * 18432);
        unsigned tw2[4];
#pragma unroll
        for (int g = 0; g < 4; ++g) {
            const f32x4 v = om[g] * sm[g];
            __builtin_amdgcn_raw_buffer_store_b64((u32x2){pack2(v[0], v[1]), pack2(v[2], v[3])}, ro, vom, g * 36864, 16);
            const f32x2 w = ot[g].xy * sm[g];
            tw2[g] = pack2(w.x, w.y);
        }
        if (q == 2) {
#pragma unroll
            for (int g = 0; g < 4; ++g) __builtin_amdgcn_raw_buffer_store_b32(tw2[g], ro, vot, g * 36864, 16);
        }
    }
    STAMP(5);
    {
        WAIT_V(0); BARX();
        STAMP(6);
        f32x4 v[5];
        proj_residual(v, rx);
        const LAS char* pw = opaque(lds + WO_OFF + lane * 16);
        const LAS char* pw2 = opaque(lds + WO_OFF + 41472 + lane * 16);
        const LAS char* ph2 = opaque(lds + WO_OFF + 4096 + q * 128 + (r & 7) * 16);
        const LAS char* ph3 = opaque(lds + WO_OFF + 41472 + 4096 + q * 128 + (r & 7) * 16);
        bf16x8 wf[90];
        auto rdw = [&](int i) {
            const int kc = i / 5, n = i % 5;
            wf[i] = n < 4 ? LDS_FRAG((kc < 9 ? pw : pw2) + (kc % 9) * 4608 + n * 1024) : LDS_FRAG((kc < 9 ? ph2 : ph3) + (kc % 9) * 4608);
        };
#pragma unroll
        for (int i = 0; i < WO_W; ++i) rdw(i);
#pragma unroll
        for (int n = 0; n < 5; ++n) v[n] = pin(v[n]);
        const auto rO = RSRC(o + (size_t)rt * 18432);
        const ProjOut po = proj_out(wl + L_XB, wl + L_STB, rt, cg, lane);
        asm volatile("" :: "s"(rO));
        WAIT_L(0);
        __builtin_amdgcn_s_setprio(REL_PRIO);
        BARX();
        STAMP(7);
        bf16x8 af[18];
        auto ldo = [&](int kc) { af[kc] = __builtin_bit_cast(bf16x8, __builtin_amdgcn_raw_buffer_load_b128(rO, lane * 16 + (kc & 3) * 1024, (kc >> 2) * 4096, 16)); };
#pragma unroll
        for (int kc = 0; kc < WO_PRE; ++kc) { ldo(kc); SCHED0(); if (kc >= PACE0 && kc + 1 < WO_PRE) PACE; SCHED0(); }
        static_for<90>([&](auto ic) {
            constexpr int i = ic.value, kc = i / 5, n = i % 5, j = WO_W + 2 * (i / 3) + i % 3;
            MFMA_T(wf[i], af[kc], v[n]);
            SCHED0();
            if (i % 3 < 2 && j < 90) rdw(j);
            if (i % WO_EV == 2 && i / WO_EV + WO_PRE < 18) ldo(i / WO_EV + WO_PRE);
            SCHED0();
        });
        asm volatile("s_nop 4" : "+v"(v[0]), "+v"(v[1]));
        STAMP(8);
        proj_store(v, po);
        __builtin_amdgcn_s_setprio(0);
    }
}

DEVI f32x2 gelu2(f32x2 v) {
    const f32x2 w = v * (v * v * -0.10294324f + -2.3022082f);
    const f32x2 d = (f32x2){__builtin_amdgcn_exp2f(w.x), __builtin_amdgcn_exp2f(w.y)} + 1.0f;
    return v * (f32x2){__builtin_amdgcn_rcpf(d.x), __builtin_amdgcn_rcpf(d.y)};
}
DEVI f32x4 gelu4(f32x4 v) { const f32x2 a = gelu2(v.xy), b = gelu2(v.zw); return (f32x4){a.x, a.y, b.x, b.y}; }
DEVI void rowsum16x8(const f32x4 a, const f32x4 b, float& o0, float& o1) {
    float t0, t1, t2, t3;
    asm volatile(
        "s_nop 1\n\t"
        "v_add_f32_dpp %2, %6, %6 row_ror:8 row_mask:0xf bank_mask:0x3\n\t"
        "v_add_f32_dpp %3, %7, %7 row_ror:8 row_mask:0xf bank_mask:0x3\n\t"
        "v_add_f32_dpp %4, %10, %10 row_ror:8 row_mask:0xf bank_mask:0x3\n\t"
        "v_add_f32_dpp %5, %11, %11 row_ror:8 row_mask:0xf bank_mask:0x3\n\t"
        "v_add_f32_dpp %2, %8, %8 row_ror:8 row_mask:0xf bank_mask:0xc\n\t"
        "v_add_f32_dpp %3, %9, %9 row_ror:8 row_mask:0xf bank_mask:0xc\n\t"
        "v_add_f32_dpp %4, %12, %12 row_ror:8 row_mask:0xf bank_mask:0xc\n\t"
        "v_add_f32_dpp %5, %13, %13 row_ror:8 row_mask:0xf bank_mask:0xc\n\t"
        "v_add_f32_dpp %0, %2, %2 row_half_mirror row_mask:0xf bank_mask:0x5\n\t"
        "s_nop 0\n\t"
        "v_add_f32_dpp %1, %4, %4 row_half_mirror row_mask:0xf bank_mask:0x5\n\t"
        "v_add_f32_dpp %0, %3, %3 row_half_mirror row_mask:0xf bank_mask:0xa\n\t"
        "v_add_f32_dpp %1, %5, %5 row_half_mirror row_mask:0xf bank_mask:0xa\n\t"
        "s_nop 0\n\t"
        "v_add_f32_dpp %0, %0, %0 quad_perm:[1,0,3,2] row_mask:0xf bank_mask:0xf\n\t"
        "v_add_f32_dpp %1, %1, %1 quad_perm:[1,0,3,2] row_mask:0xf bank_mask:0xf\n\t"
        "s_nop 0\n\t"
        "v_add_f32_dpp %0, %0, %0 quad_perm:[2,3,0,1] row_mask:0xf bank_mask:0xf\n\t"
        "v_add_f32_dpp %1, %1, %1 quad_perm:[2,3,0,1] row_mask:0xf bank_mask:0xf\n\t"
        : "=&v"(o0), "=&v"(o1), "=&v"(t0), "=&v"(t1), "=&v"(t2), "=&v"(t3)
        : "v"(a[0]), "v"(a[1]), "v"(a[2]), "v"(a[3]), "v"(b[0]), "v"(b[1]), "v"(b[2]), "v"(b[3]));
}

#define K5_PRE 9
#define RH_MAP 0
#define NET_LAYERS 2
constexpr int G_STAGE = 34816;
constexpr int K5_STAGE = 18432, K5_NS = 8;

constexpr int y_load_at(int slot) {
    int next = K5_PRE;
    for (int s = 0;; ++s) { const bool go = next < 18 + 2 * (s / 3); if (s == slot) return go ? next : -1; next += go; }
}

struct GTile { f32x4 a[4][4], e[2]; };
struct GFrag { bf16x8 a[2][4], b[2][5]; const LAS char *pa[2][2], *pb, *p8; int slot; };
DEVI void g_rd(GFrag& f, int kk, int i) {
    if (i < 4) f.a[kk][i] = LDS_FRAG(f.pa[kk][i >> 1] + (i & 1) * 2048);
    else if (i < 8) f.b[kk][i - 4] = LDS_FRAG(f.pb + (kk * 9 + i - 4) * 1024);
    else f.b[kk][4] = LDS_FRAG(f.p8 + kk * 9216);
}
template <int NS, int G, bool ROWS_IN_REGS>
DEVI void g_steps(GTile& c, GFrag& f, int n) {
    LOOP_ALIGN();
#pragma unroll 1
    do {
        int d;
#pragma unroll
        for (int kk = 0; kk < 2; ++kk) {
            WAIT_L(0);
            if (kk) BAR();
            SCHED0();
#pragma unroll
            for (int i = 0; i < 18; ++i) {
                const int nn = i >> 2, m = i & 3;
                f32x4& e = nn < 4 ? c.a[m][nn] : c.e[m & 1];
                e = ROWS_IN_REGS ? MFMA(f.a[kk][m], f.b[kk][nn], e) : MFMA(f.b[kk][nn], f.a[kk][m], e);
                SCHED0();
                if (i < 9) g_rd(f, kk ^ 1, i);
                if (kk == 0) {
                    if (i == 9) d = pin_s(f.slot == NS - 1 ? -(NS - 1) * G : G);
                    if (i >= 10 && i < 14) f.pa[(i - 10) >> 1][i & 1] = opaque(f.pa[(i - 10) >> 1][i & 1] + d);
                    if (i == 14) f.pb = opaque(f.pb + d);
                    if (i == 15) f.p8 = opaque(f.p8 + d);
                } else {
                    if (i == 10) f.slot = pin_s(f.slot == NS - 1 ? 0 : f.slot + 1);
                    if (i == 12) n = pin_s(n - 1);
                }
                SCHED0();
            }
        }
    } while (n);
}
DEVI void keep4(const f32x4 (&c)[4]) { asm volatile("" :: "v"(c[0]), "v"(c[1]), "v"(c[2]), "v"(c[3])); }
template <int D, int DB, bool FIRST, class HOOK>
DEVI void g_step_u(GTile& c, GFrag& f, const f32x4 (&c0)[4], HOOK&& hook) {
    static_for<36>([&](auto ic) {
        constexpr int kk = ic.value / 18, i = ic.value % 18, n = i >> 2, m = i & 3;
        if (i == 0) { WAIT_L(0); if (kk) BAR(); SCHED0(); }
        f32x4& e = n < 4 ? c.a[m][n] : c.e[m & 1];
        if (FIRST && kk == 0) asm volatile("v_mfma_f32_16x16x32_bf16 %0, %1, %2, %3" : "=&v"(e) : "v"(f.b[kk][n]), "v"(f.a[kk][m]), "v"(c0[m]));
        else MFMA_T(f.b[kk][n], f.a[kk][m], e);
        SCHED0();
        if (i < 9) g_rd(f, kk ^ 1, i);
        if (kk == 0) {
            if (i >= 10 && i < 14) f.pa[(i - 10) >> 1][i & 1] = opaque(f.pa[(i - 10) >> 1][i & 1] + D);
            if (i == 14) f.pb = opaque(f.pb + DB);
            if (i == 15) f.p8 = opaque(f.p8 + DB);
        }
        if (FIRST && ic.value == 20) keep4(c0);
        hook(ic);
        SCHED0();
    });
}
#define MF(d, a, b) "v_mfma_f32_16x16x32_bf16 " d ", " a ", " b ", " d "\n\t"
template <int D, int DB>
DEVI void g_step_gelu(GTile& c, GFrag& f, f32x4& z, float r, float c1) {
#pragma unroll
    for (int kk = 0; kk < 2; ++kk) {
        float va = z[2 * kk], vb = z[2 * kk + 1], ta, tb;
        WAIT_L(0); if (kk) BAR(); SCHED0();
#pragma unroll
        for (int i = 0; i < 9; ++i) {
            MFMA_T(f.b[kk][i >> 2], f.a[kk][i & 3], c.a[i & 3][i >> 2]);
            if (i == 5) asm volatile("v_mul_f32 %0, %0, %1" : "+v"(va) : "v"(r));
            if (i == 6) asm volatile("v_mul_f32 %0, %0, %1" : "+v"(vb) : "v"(r));
            if (i == 7) asm volatile("v_mul_f32 %0, %1, %1" : "=v"(ta) : "v"(va));
            if (i == 8) asm volatile("v_mul_f32 %0, %1, %1" : "=v"(tb) : "v"(vb));
            SCHED0();
            g_rd(f, kk ^ 1, i);
            SCHED0();
        }
        asm volatile(MF("%0", "%5", "%6") "v_fmaak_f32 %3, %3, %11, 0xc0135761\n\tv_fmaak_f32 %4, %4, %11, 0xc0135761\n\t"
                     MF("%1", "%5", "%7") "v_mul_f32 %3, %3, %9\n\tv_mul_f32 %4, %4, %10\n\t" MF("%2", "%5", "%8") "v_exp_f32 %3, %3"
                     : "+v"(c.a[1][2]), "+v"(c.a[2][2]), "+v"(c.a[3][2]), "+v"(ta), "+v"(tb)
                     : "v"(f.b[kk][2]), "v"(f.a[kk][1]), "v"(f.a[kk][2]), "v"(f.a[kk][3]), "v"(va), "v"(vb), "v"(c1));
        SCHED0(); f.pa[kk][0] = opaque(f.pa[kk][0] + D); SCHED0();
        asm volatile(MF("%0", "%6", "%7") "v_exp_f32 %5, %5\n\t" MF("%1", "%6", "%8") "v_add_f32 %4, 1.0, %4\n\tv_add_f32 %5, 1.0, %5\n\t"
                     MF("%2", "%6", "%9") "v_rcp_f32 %4, %4\n\t" MF("%3", "%6", "%10") "v_rcp_f32 %5, %5"
                     : "+v"(c.a[0][3]), "+v"(c.a[1][3]), "+v"(c.a[2][3]), "+v"(c.a[3][3]), "+v"(ta), "+v"(tb)
                     : "v"(f.b[kk][3]), "v"(f.a[kk][0]), "v"(f.a[kk][1]), "v"(f.a[kk][2]), "v"(f.a[kk][3]));
        SCHED0(); f.pa[kk][1] = opaque(f.pa[kk][1] + D); SCHED0();
        asm volatile(MF("%0", "%4", "%5") "v_mul_f32 %2, %2, %7\n\tv_mul_f32 %3, %3, %8\n\t" MF("%1", "%4", "%6")
                     : "+v"(c.e[0]), "+v"(c.e[1]), "+v"(va), "+v"(vb) : "v"(f.b[kk][4]), "v"(f.a[kk][0]), "v"(f.a[kk][1]), "v"(ta), "v"(tb));
        SCHED0();
        if (kk == 0) { f.pb = opaque(f.pb + DB); f.p8 = opaque(f.p8 + DB); }
        SCHED0();
        z[2 * kk] = va; z[2 * kk + 1] = vb;
    }
}
DEVI void g_fill(GTile& c, const float (&e)[4]) {
#pragma unroll
    for (int m = 0; m < 4; ++m) {
#pragma unroll
        for (int n = 0; n < 4; ++n) c.a[m][n] = pin((f32x4){e[m], e[m], e[m], e[m]});
        if (m < 2) c.e[m] = pin((f32x4){e[m], e[m], e[m], e[m]});
    }
}

#undef STAMP_SEC
#define STAMP_SEC 1
DEVI void gmlp_body(LAS char* lds, int wm, int wn, int pm, int cg, char* z2t, char* y, const u16* __restrict__ bsp STAMP_PARAM) {
    const int mlo = wm * 4 + wn * 2, mhi = wm * 4 + (1 - wn) * 2;
    const float zero4[4] = {0.f, 0.f, 0.f, 0.f};
    GTile z2, z1, acc;
    GFrag f;
    {
        const int lane = lane_id_fresh();
#pragma unroll
        for (int kk = 0; kk < 2; ++kk) { f.pa[kk][0] = lds + mlo * 2048 + kk * 1024 + lane * 16; f.pa[kk][1] = lds + mhi * 2048 + kk * 1024 + lane * 16; }
        f.pb = lds + 16384 + wn * 4096 + lane * 16; f.p8 = lds + 16384 + 8192 + lane * 16; f.slot = 0;
    }
    g_fill(z2, zero4); g_fill(z1, zero4);
#pragma unroll
    for (int i = 0; i < 4; ++i) f.pa[i >> 1][i & 1] = opaque(f.pa[i >> 1][i & 1]);
    f.pb = opaque(f.pb); f.p8 = opaque(f.p8);
    SCHED0();
    WAIT_V(0); BARX();
    BAR();
    BARX();
    STAMP(2);
#pragma unroll
    for (int i = 0; i < 9; ++i) g_rd(f, 0, i);
    g_steps<4, G_STAGE, true>(z2, f, 9);
    STAMP(3);
    {
        const int lane = lane_id_fresh(), r = lane & 15, q = lane >> 4;
        f32x4 ra[4];
#pragma unroll
        for (int m = 0; m < 4; ++m) ra[m] = *(const LAS f32x4*)(lds + G_TAB + ((m < 2 ? mlo : mhi) + (m & 1)) * 64 + q * 16);
        SCHED0();
#pragma unroll
        for (int m = 0; m < 4; ++m) {
            f32x4 s1, s2;
#pragma unroll
            for (int n = 0; n < 4; ++n) {
                const f32x4 g = gelu4(z2.a[m][n] * ra[m]);
                z2.a[m][n] = g;
                if (n == 0) { s1 = g; s2 = g * g; } else { s1 += g; s2 += g * g; }
            }
            if (m < 2) { const f32x4 g = gelu4(z2.e[m] * ra[m]); z2.e[m] = g; s1 += g; s2 += g * g; }
            float u0, u1;
            rowsum16x8(s1, s2, u0, u1);
            if ((r & 3) == 0) *(LAS f32x2*)(lds + G_PART + wn * 1024 + ((m < 2 ? mlo : mhi) + (m & 1)) * 128 + (q * 4 + (r >> 2)) * 8) = (f32x2){u0, u1};
            SCHED0();
        }
    }
    WAIT_L0(); BAR();
    STAMP(4);
    g_steps<4, G_STAGE, false>(z1, f, 4);
    unsigned b0, b1;
    f32x4 c0[4];
    g_step_u<G_STAGE, G_STAGE, false>(z1, f, c0, [&](auto ic) {
        if (ic.value != 18) return;
        STAMP(5);
        const int lane = lane_id_fresh(), q = lane >> 4;
        b0 = __builtin_amdgcn_raw_buffer_load_b32(RSRC(bsp), (lane & 15) * 4, pm * 256 + mlo * 32, 0);
        b1 = __builtin_amdgcn_raw_buffer_load_b32(RSRC(bsp), (lane & 15) * 4, pm * 256 + mhi * 32, 0);
#pragma unroll
        for (int g = 0; g < 2; ++g) {
            const auto rz2 = RSRC(z2t + (size_t)pm * 589824 + cg * 36864 + ((g ? mhi : mlo) >> 1) * 9216);
            const LAS char* tab = lds + G_TAB + (g ? mhi : mlo) * 64 + q * 16;
            LAS char* own = lds + (wm ? G_OWN : G_STAGE + 16384) + (g ^ wn) * 9216 + lane * 16;
            f32x4 ra[2], rb[2];
#pragma unroll
            for (int T = 0; T < 2; ++T) { ra[T] = *(const LAS f32x4*)(tab + 512 + T * 64); rb[T] = *(const LAS f32x4*)(tab + 1024 + T * 64); }
#pragma unroll
            for (int n = 0; n < (g ? 4 : 5); ++n) {
                const f32x4 v0 = (n < 4 ? z2.a[2 * g][n & 3] : z2.e[0]) * ra[0] + rb[0], v1 = (n < 4 ? z2.a[2 * g + 1][n & 3] : z2.e[1]) * ra[1] + rb[1];
                const u32x4 o = {pack2(v0[0], v1[0]), pack2(v0[1], v1[1]), pack2(v0[2], v1[2]), pack2(v0[3], v1[3])};
                ST16(rz2, lane * 16, n < 4 ? wn * 4096 + n * 1024 : 8192, o, 16);
                *(LAS u32x4*)(own + (n < 4 ? wn * 4096 + n * 1024 : 8192)) = o;
            }
        }
        STAMP(6);
    });
    static_for<3>([&](auto sc) {
        constexpr int D = sc.value == 0 ? G_STAGE : sc.value == 1 ? -3 * G_STAGE : 2 * G_STAGE;
        g_step_u<D, D, false>(z1, f, c0, [&](auto ic) { if (ic.value == 17 && sc.value == 1) WAIT_V(0); });
    });
    float ra[4], c1;
    {
        int lane, da[2];
        g_step_u<-G_STAGE, -G_STAGE, false>(z1, f, c0, [&](auto ic) {
            constexpr int i = ic.value;
            if (i == 9) lane = pin(lane_id_fresh());
            if (i == 14 || i == 15) da[i & 1] = pin((lane & 15) * 128 + (((4 * (i & 1) + (lane >> 4)) ^ (lane & 7)) * 16) - lane * 16 - (i & 1) * 1024);
            if (i == 16 || i == 17) f.pa[0][i & 1] = opaque(f.pa[0][i & 1] + da[0]);
            if (i == 27 || i == 28) f.pa[1][i & 1] = opaque(f.pa[1][i & 1] + da[1]);
            if (i >= 29 && i < 33) {
                constexpr int m = (i - 29) & 3;
                const float e = m & 1 ? bfhi(m < 2 ? b0 : b1) : bflo(m < 2 ? b0 : b1);
                c0[m] = pin((f32x4){e, e, e, e});
            }
            if (i == 33) {
#pragma unroll
                for (int m = 0; m < 4; ++m) ra[m] = *(const LAS float*)(lds + G_TAB + ((m < 2 ? mlo : mhi) + (m & 1)) * 64 + (lane & 15) * 4);
                c1 = pin(-0.10294324f);
            }
        });
    }
    STAMP(7);
    g_step_u<2 * G_STAGE, G_OWN - G_STAGE - 16384, true>(acc, f, c0, [](auto) {});
    static_for<18>([&](auto sc) {
        constexpr int st = sc.value, m = st < 16 ? st >> 2 : st & 1;
        constexpr int D = st == 0 ? -3 * G_STAGE : st % 3 == 1 ? 2 * G_STAGE : -G_STAGE;
        g_step_gelu<D, st == 0 ? 16384 - G_OWN : D>(acc, f, st < 16 ? z1.a[m][st & 3] : z1.e[m], ra[m], c1);
    });
    f.slot = 1;
    STAMP(9);
    g_steps<3, -G_STAGE, false>(acc, f, 13);
    STAMP(10);
    const int lane = lane_id_fresh();
#pragma unroll
    for (int g = 0; g < 2; ++g) {
        const auto ry = RSRC(y + (size_t)(pm * 8 + (g ? mhi : mlo)) * 73728 + cg * 4608);
#pragma unroll
        for (int T = 0; T < 2; ++T) {
            const int m = 2 * g + T;
#pragma unroll
            for (int p = 0; p < 2; ++p) {
                const f32x4 a = acc.a[m][2 * p] * z1.a[m][2 * p], c = acc.a[m][2 * p + 1] * z1.a[m][2 * p + 1];
                ST16(ry, lane * 16 + wn * 2048 + p * 1024, T * 73728, ((u32x4){pack2(a[0], a[1]), pack2(a[2], a[3]), pack2(c[0], c[1]), pack2(c[2], c[3])}), 16);
            }
            if (g == 0) {
                const f32x4 a = acc.e[T] * z1.e[T];
                ST8(ry, (lane >> 5) * 256 + (lane & 15) * 16 + ((lane >> 4) & 1) * 8, T * 73728 + 4096, ((u32x2){pack2(a[0], a[1]), pack2(a[2], a[3])}), 16);
            }
        }
    }
}

#define NET_IDS \
    int b = __builtin_amdgcn_readfirstlane(blockIdx.x), wid = wid8 & 3; \
    asm volatile("" : "+s"(b), "+s"(wid));     \
    const int xcd = b & 7, idx = b >> 3; \
    const int pa = (xcd >> 1) * 4 + (idx & 3), hp = (xcd & 1) * 8 + (idx >> 2); \
    const int pm = (xcd >> 2) * 8 + (idx & 7), cg = (xcd & 3) * 4 + (idx >> 3); \
    const int rh = RH_MAP ? xcd * 4 + (idx & 3) : (xcd >> 1) * 8 + (idx & 7), c5 = RH_MAP ? idx >> 2 : (xcd & 1) * 4 + (idx >> 3); \
    const int lead = (idx + (idx >> 3)) & 3; \
    auto kord = [&](int t, int n) { \
        const int k = t + ((int)(0x1F000000u << (((t - lead) & 3) * 4)) >> 28); \
        return (unsigned)k < (unsigned)n ? k : t; \
    }; \
    auto kord5 = [&](int t) { \
        const int k = t + ((int)(0x1F000000u << (((t - (RH_MAP ? c5 : lead)) & 3) * 4)) >> 28); \
        return (unsigned)k < 18u ? k : t; \
    };

typedef short s16x4 __attribute__((ext_vector_type(4)));
DEVI u32x4 tr_frag(const LAS char* p, int four_rows) {
    const u32x2 a = __builtin_bit_cast(u32x2, __builtin_amdgcn_ds_read_tr16_b64_v4i16((LAS s16x4*)p));
    const u32x2 b = __builtin_bit_cast(u32x2, __builtin_amdgcn_ds_read_tr16_b64_v4i16((LAS s16x4*)(p + four_rows)));
    return (u32x4){a.x, a.y, b.x, b.y};
}
DEVI float dot2(unsigned v, unsigned w, float acc) { return __builtin_amdgcn_fdot2_f32_bf16(__builtin_bit_cast(bf16x2_t, v), __builtin_bit_cast(bf16x2_t, w), acc, false); }
DEVI float sum2(unsigned v, float acc) { return dot2(v, 0x3f803f80u, acc); }
struct Sel { unsigned lo, hi, nlo, nhi; };
DEVI Sel make_sel() { return {(unsigned)pin_s(0x00003f80), (unsigned)pin_s(0x3f800000), (unsigned)pin_s(0x0000bf80), (unsigned)pin_s((int)0xbf800000u)}; }
DEVI unsigned centre2(unsigned v, f32x2& e, float mean, const Sel& k) {
    const float a = dot2(k.lo, v, e.x), b = dot2(k.hi, v, e.y);
    const unsigned r = pack2(a, b);
    e = (f32x2){dot2(k.nlo, r, a), dot2(k.nhi, r, b)} - mean;
    return r;
}
typedef u32x4 Retile[9];
template <int PITCH>
DEVI void retile_out_make(Retile& o, const LAS char* img, int lane) {
    const int c = lane & 15, q = lane >> 4, p = lane & 3;
    const LAS char* src = img + (q * 8 + (c >> 2)) * PITCH;
#pragma unroll
    for (int i = 0; i < 8; ++i) o[i] = tr_frag(src + p * 16 + (i >> 2) * 144 + ((i >> 1) & 1) * 64 + (i & 1) * 8, 4 * PITCH);
    o[8] = tr_frag(src + 128 + (p >> 1) * 144 + (p & 1) * 8, 4 * PITCH);
}
DEVI void retile_out_store(const Retile& o, char* dst, int nkc, int kc, int w, int lane) {
    const int c = lane & 15, q = lane >> 4;
    const auto rd = RSRC(dst + (size_t)(2 * w * nkc + kc) * 4608);
#pragma unroll
    for (int i = 0; i < 8; ++i) ST16(rd, lane * 16 + (i & 3) * 1024, (i >> 2) * nkc * 4608, o[i], 16);
    ST16(rd, (c >> 3) * nkc * 4608 + 4096 + q * 128 + (c & 7) * 16, 0, o[8], 16);
}

DEVI float dpp_ror(float v, int n) { return n == 8 ? __builtin_amdgcn_update_dpp(0.f, v, 0x128, 0xf, 0xf, true) : n == 4 ? __builtin_amdgcn_update_dpp(0.f, v, 0x124, 0xf, 0xf, true)
                                          : n == 2 ? __builtin_amdgcn_update_dpp(0.f, v, 0x122, 0xf, 0xf, true) : __builtin_amdgcn_update_dpp(0.f, v, 0x121, 0xf, 0xf, true); }
DEVI unsigned long long epoch_of(const char* ws) {
    SCHED0();
    const unsigned long long ep = *(const __attribute__((address_space(4))) unsigned long long*)(ws + pin_s((int)OFF_EP)) + 1ull;
    asm volatile("" :: "s"(ep));
    return ep;
}
DEVI void prologue_done(LAS char* lds, char* ws, unsigned long long ep, int b, int lane) {
    if (lds_arrive(lds + G_WORD + 8) == 7) ST16(RSRC(ws), lane ? 0x80000000u : (unsigned)OFF_FL + b * 16, 0, ((u32x4){(unsigned)ep, (unsigned)(ep >> 32), 0u, 0u}), 16);
}
DEVI void centre_frag(u32x4& f, f32x2 (&er)[4], float mean, const Sel& k) {
#pragma unroll
    for (int e = 0; e < 4; ++e) f[e] = centre2(f[e], er[e], mean, k);
}
template <int N>
DEVI void column_means(LAS char* lds, LAS char* cnt, int me, int lane, float a, float b, float& ma, float& mb) {
    LAS float* red = (LAS float*)(lds + G_PART);
    auto x = __builtin_amdgcn_permlane16_swap(__float_as_uint(a), __float_as_uint(b), false, false);
    red[me * 64 + lane] = __uint_as_float(x[0]) + __uint_as_float(x[1]);
    if (lds_arrive(cnt) < N - 1) lds_count_wait(cnt, N);
    float t = 0.f;
#pragma unroll
    for (int i = 0; i < N; ++i) t += red[i * 64 + lane];
    x = __builtin_amdgcn_permlane32_swap(__float_as_uint(t), __float_as_uint(t), false, false);
    t = (__uint_as_float(x[0]) + __uint_as_float(x[1])) * (1.0f / 576.0f);
    x = __builtin_amdgcn_permlane16_swap(__float_as_uint(t), __float_as_uint(t), false, false);
    ma = __uint_as_float(x[0]); mb = __uint_as_float(x[1]);
}
template <class ARGS>
DEVI unsigned long long prologue(LAS char* lds, char*& ws, const char*& x0, const char*& wqkv, const u16*& wo, const u16*& w_in, int b, int pa, int wid8, ARGS&& args PST_PARAM) {
    PST_DECL;
    const int lane = lane_id_fresh(), c = lane & 15, q = lane >> 4, w = wid8 & 3;
    const int jw = w - 1 + 3 * (((wid8 >> 2) ^ (b >> 3)) & 1);
    const bool wjob = b >= 16 && b < 112 && w;
    const int uo = (wid8 - 1 - (wid8 >> 2)) * 16 + b;
    const bool ojob = b < 16 && w && uo < 72;
    const unsigned l0 = (unsigned)(size_t)lds;
    LAS char* img = lds + 2 * K1_STAGE;
    const int uw = b - 16, head = (uw & 7) * 4 + ((uw >> 3) & 3), part = uw >> 5, hp = head >> 1, odd = head & 1;
    const int c0 = part * 1152 + hp * 72 + odd * 40;
    {
        const int so = pin_s(jw * 96 * 3456 + (c0 & ~15));
        const unsigned v = (lane & 3) < ((head & 3) == 3 ? 2 : 3) ? (lane >> 2) * 3456 + (lane & 3) * 16 : 0x80000000u, d = 16 * 3456 - 1024;
        const unsigned v0 = pin(v), v1 = pin(v + d), v2 = pin(v + 2 * d), v3 = pin(v + 3 * d);
        const int e = ((b - 112) >> 3) * 192 + wid8 * 64 + lane, row = (e * 10923) >> 16, r = e - row * 6;
        const unsigned vp = pin((unsigned)(row * 3456 + (r >> 1) * 1152 + (r & 1) * 128));
        const unsigned la = pin_s(l0 + 2 * K1_STAGE + jw * 6144);
        args(0);
        if (b >= 112 && wid8 < 3) dma1(l0 + G_OWN, RSRC(wqkv), (b & 7) * 128, vp);
        if (wjob) { dma4<false>(la, RSRC(wqkv), so, v0, v1, v2, v3); dma2<false>(la + 4096, RSRC(wqkv), so + 64 * 3456, v0, v1); }
        args(1);
    }
    if (ojob) {
        const auto rs = RSRC(wo + (uo & 3) * 144);
#pragma unroll
        for (int j = 0; j < 9; ++j) {
            const int i = lane + 64 * j, rl = (i * 3641) >> 16, kp = (uo >> 2) * 32 + rl;
            const int row = kp < 512 ? 18 * (kp >> 4) + (kp & 15) + 2 * ((kp >> 4) & 1) : 36 * ((kp - 512) >> 2) + 16 + (kp & 3);
            dma1(l0 + 2 * K1_STAGE + wid8 * 9216 + j * 1024, rs, 0, (unsigned)(row * 1152 + (i - rl * 18) * 16));
        }
    }
    if (wid8 == 4) { lds_word_set(lds + G_WORD + c * 4, 0u); WAIT_L0(); }
    BAR();
    unsigned long long ep;
    if (wid8 == 4) ep = epoch_of(ws);
    if (wid8 == 4 && b >= 16 && b < 112) lds_count_wait(lds + G_WORD + 12, 12);
    if (wid8 > 4 && b >= 112) {
        __builtin_amdgcn_s_sleep(I_SLEEP);
        const unsigned v = (lane >> 2) * 9216 + (lane & 3) * 16, d = 16 * 9216 - 1024;
#pragma unroll
        for (int i = 0; i < 3; ++i) dma4<false>(l0 + 3 * K1_STAGE + (w - 1) * 12288 + i * 4096, RSRC(w_in + win_strip(b) * 32), (win_third(b, w) * 192 + i * 64) * 9216, v, v + d, v + 2 * d, v + 3 * d);
    }
    if (wid8 == 4) {
        AttnX ax = attn_x(x0, x0, 0, pa, 0, lane);
#pragma unroll
        for (int s = 0; s < 2; ++s)
#pragma unroll
            for (int i = 0; i < 4; ++i) { ax.oA = (pa * 8 + 2 * i) * 18432; attn_issueA(lds, ax, i, s, s); }
    }
    u32x4 xv[9];
    if (wid8 == 0) {
        const unsigned v = (lane >> 3) * 2304 + (lane & 7) * 16;
        const auto rx = RSRC(x0 + (size_t)slot2row(b * 8) * 1152);
#pragma unroll
        for (int i = 0; i < 9; ++i) asm volatile("s_nop 4\n\tbuffer_load_dwordx4 %0, %1, %2, 0 offen offset:%3" : "+v"(xv[i]) : "v"(v), "s"(rx), "i"(i * 128) : "memory");
        const auto rw = RSRC(ws);
        const u32x4 ones = {~0u, ~0u, ~0u, ~0u}, zero = {0u, 0u, 0u, 0u};
        const unsigned vo = lane * 16;
        ST16(rw, vo, (unsigned)OFF_ZST + b * 2048, ones, 16); ST16(rw, vo, (unsigned)OFF_ZST + b * 2048 + 1024, ones, 16);
        ST16(rw, vo, (unsigned)OFF_CNT + b * 2560, zero, 16); ST16(rw, vo, (unsigned)OFF_CNT + b * 2560 + 1024, zero, 16);
        ST16(rw, vo, lane < 32 ? (unsigned)OFF_CNT + b * 2560 + 2048 : (unsigned)(OFF_L0 + L_STB) + b * 512 - 512, zero, 16);
        ST16(rw, vo, (unsigned)(OFF_L0 + L_O) + (b >> 1) * 18432 + 16384 + (b & 1) * 1024, zero, 16);
        ST16(rw, b < 12 ? vo : 0x80000000u, (unsigned)OFF_FL2 + b * 1024, zero, 16);
    }
    PST(1);
    if (wjob) {
        const Sel k = make_sel();
        const LAS char* s0 = opaque(img + jw * 6144 + (q * 8 + (c >> 2)) * 64 + (c0 & 15) + (c & 3) * 8);
        const LAS char* s1 = opaque(img + jw * 6144 + (q * 8 + (c >> 2)) * 64 + (c0 & 15) + 32 + min(c & 3, 2) * 2048);
        const auto rd = RSRC(ws + OFF_TWQKV + (size_t)(hp * 18 + jw * 3) * 6144 + (3 * odd + part) * 1024), rd2 = RSRC(ws + OFF_TWT + (size_t)((hp * 3 + part) * 18 + jw * 3) * 256);
        const unsigned vd2 = pin(c < 12 && !odd ? (unsigned)((c >> 2) * 256 + q * 64 + (c & 3) * 16) : 0x80000000u);
        asm volatile("" :: "s"(rd), "s"(rd2));
        WAIT_V(0);
        PST(2);
        u32x4 f[4];
        float sm[4] = {0.f, 0.f, 0.f, 0.f}, m0, m1;
#pragma unroll
        for (int i = 0; i < 4; ++i) f[i] = tr_frag(i < 3 ? s0 + i * 2048 : s1, 256);
#pragma unroll
        for (int e = 0; e < 16; ++e) sm[(e >= 12) * 2 + (e & 1)] = sum2(f[e >> 2][e & 3], sm[(e >= 12) * 2 + (e & 1)]);
        sm[2] = c < 12 ? sm[2] + sm[3] : 0.f;
        sm[2] += dpp_ror(sm[2], 4); sm[2] += dpp_ror(sm[2], 8);
        column_means<6>(lds, lds + G_WORD + 12, jw, lane, sm[0] + sm[1], sm[2], m0, m1);
        PST(3);
        f32x2 e0[4] = {{-m0, -m0}, {-m0, -m0}, {-m0, -m0}, {-m0, -m0}}, e1[4] = {{-m1, -m1}, {-m1, -m1}, {-m1, -m1}, {-m1, -m1}};
#pragma unroll
        for (int i = 0; i < 3; ++i) { centre_frag(f[i], e0, m0, k); ST16(rd, lane * 16, i * 6144, f[i], 16); }
        centre_frag(f[3], e1, m1, k); ST16(rd2, vd2, 0, f[3], 16);
        PST(4);
        lds_arrive(lds + G_WORD + 12);
    }
    if (wid8 != 4) ep = epoch_of(ws);
    {
        if (wjob) {
            const unsigned fo = pin((unsigned)OFF_FW + ((lane >> 3) * 16 + hp) * 1024 + (3 * odd + part) * 128 + (lane & 7) * 16);
            WAIT_V(0);
            PST(5);
            if (lds_arrive(lds + G_WORD + 4) == 5) {
                const u32x4 e4 = {(unsigned)ep, (unsigned)(ep >> 32), (unsigned)ep, (unsigned)(ep >> 32)};
                ST16(RSRC(ws), fo, 0, e4, 16); ST16(RSRC(ws), fo + 131072, 0, e4, 16);
            }
        }
    }
    if (wid8 == 0) {
        WAIT_V(7);
        asm volatile("" : "+v"(xv[0]), "+v"(xv[1]), "+v"(xv[2]), "+v"(xv[3]), "+v"(xv[4]), "+v"(xv[5]), "+v"(xv[6]), "+v"(xv[7]), "+v"(xv[8]));
        float s = 0.f, q2 = 0.f;
#pragma unroll
        for (int e = 0; e < 36; ++e) { s = sum2(xv[e >> 2][e & 3], s); q2 = dot2(xv[e >> 2][e & 3], xv[e >> 2][e & 3], q2); }
        ST8(RSRC(ws + OFF_L0 + L_STA + (size_t)b * 512), lane * 8, 0, ((u32x2){__float_as_uint(s), __float_as_uint(q2)}), 16);
    }
    if (ojob) {
        WAIT_V(0);
        Retile rt;
        retile_out_make<288>(rt, img + wid8 * 9216, lane); retile_out_store(rt, ws + OFF_TWO, 18, uo >> 2, uo & 3, lane);
    }
    PST(7);
    PST_DUMP(wid8 == 0 || wid8 == 4 || wid8 == 5, wid8 ? wid8 : 2);
    return ep;
}

__global__ __launch_bounds__(512, 2) TRACE_ATTR void k_net(char* ws, const u16* wsp, const u16* bsp, char* out, const char* x0, const char* wqkv, const u16* wo,
                                                             const u16* w_in, const u16* w_out DBG_ARG) {
    extern __shared__ __attribute__((aligned(16))) char smem[];
    LAS char* lds = (LAS char*)smem;
    asm volatile("gm_code_begin:");
    if (blockIdx.x < 16) {
        unsigned long long pc; int d0, len;
        asm volatile("s_getpc_b64 %0\n1:\n\ts_mov_b32 %1, gm_code_begin-1b\n\ts_mov_b32 %2, gm_code_end1-gm_code_begin\n\ts_max_i32 %2, %2, gm_code_end2-gm_code_begin"
                     : "=s"(pc), "=s"(d0), "=s"(len) :: "scc");
        const auto rs = __builtin_amdgcn_make_buffer_rsrc((void*)((pc + d0) & ~15ull), 0, len, 0x00020000);
        dma1((unsigned)(size_t)lds + G_OWN, rs, 0, (threadIdx.x * 2 + (blockIdx.x >> 3)) * 128);
    }
    STAMP_DECL;
    const int wid8 = __builtin_amdgcn_readfirstlane(threadIdx.x >> 6);
    unsigned long long ep;
    {
        NET_IDS
        ep = prologue(lds, ws, x0, wqkv, wo, w_in, b, pa, wid8, [&](int part) {
            if (part == 0) { asm volatile("" :: "s"(wqkv), "s"(w_in), "s"(w_out)); return; }
            ws = own_s(ws); wsp = own_s(wsp); bsp = own_s(bsp); out = own_s(out); x0 = own_s(x0); wo = own_s(wo); w_out = own_s(w_out);
        } PST_FWD);
    }
    const char *twin = ws + OFF_TWIN, *twout = ws + OFF_TWOUT;
    if (wid8 >= 4) {
        u32x4 wr[5][9];
#pragma unroll 1
      for (int layer = 0; layer < NET_LAYERS; ++layer) {
        NET_IDS
        TRACE_BEGIN;
        char* wl = ws + OFF_L0 + layer * SZ_L;
        const unsigned fl = layer * F_LAYER;
        const char *xin = wl + L_XB, *st = wl + L_STB, *z2t = wl + (layer ? L_Y : L_Z2T);
        unsigned long long* zst = (unsigned long long*)(ws + OFF_ZST + layer * SZ_ZST);
        const int lane = lane_id_fresh(), l3 = lane >> 3;
        const unsigned vo = lane * 16;
        const unsigned l0 = (unsigned)(size_t)lds, lA = l0 + wid * 4096, lB = l0 + 16384 + wid * 4608;
        const auto rX = RSRC(xin + (size_t)(pm * 8 + 2 * wid) * 18432), rI = RSRC(twin + (size_t)cg * (18 * 18432) + wid * 4608);
        auto issue1B = [&](int g, int slot) { const int k = kord(g < 9 ? g : (g < 13 ? g - 4 : g - 13), 9); dma5<true>(lB + slot * G_STAGE, rI, (g < 9 ? 9 + k : k) * 18432, vo); };
        auto issue1A = [&](int g, int slot) { dma4<true>(lA + slot * G_STAGE, rX, kord(g < 9 ? g : g - 13, 9) * 2048, vo, vo, vo + 16384, vo + 16384); };
        auto issue1 = [&](int g, int slot) { issue1B(g, slot); issue1A(g, slot); };
        const auto rA = RSRC((const char*)wsp + (size_t)(pm * 128 + wid * 32) * 4096), rB = RSRC(z2t + cg * 36864 + wid * 4608);
        const unsigned voA = (unsigned)(2 * l3 * 4096 + ((lane & 7) ^ l3) * 16);
        auto issueB = [&](int k, int slot) { dma5<true>(lB + slot * G_STAGE, rB, (k >> 1) * 589824 + (k & 1) * 18432, vo); };
        const auto rW = RSRC(twout + (size_t)c5 * (72 * 4608));
        auto issue5 = [&](int t) { dma5<true>(l0 + ((t + 6) & 7) * K5_STAGE + wid * 4608, rW, kord5(t) * K5_STAGE + wid * 4608, vo); };
        auto load5 = [&](int p, int i) {
            const int t0 = p ? 2 * p + 6 : 4, ow = kord5(t0 + i) * K5_STAGE + wid * 4096;
            asm volatile("s_nop 4\n\tbuffer_load_dwordx4 %0, %4, %5, %6 offen sc1\n\tbuffer_load_dwordx4 %1, %4, %5, %6 offen offset:1024 sc1\n\t"
                         "buffer_load_dwordx4 %2, %4, %5, %6 offen offset:2048 sc1\n\tbuffer_load_dwordx4 %3, %4, %5, %6 offen offset:3072 sc1"
                         : "+v"(wr[p][4 * i]), "+v"(wr[p][4 * i + 1]), "+v"(wr[p][4 * i + 2]), "+v"(wr[p][4 * i + 3]) : "v"(vo), "s"(rW), "s"(ow) : "memory");
            if (i) {
                const int oe = kord5(t0 + (wid >> 1)) * K5_STAGE + 16384 + (wid & 1) * 1024;
                asm volatile("s_nop 4\n\tbuffer_load_dwordx4 %0, %1, %2, %3 offen sc1" : "+v"(wr[p][8]) : "v"(vo), "s"(rW), "s"(oe) : "memory");
            }
        };
        auto put5 = [&](int w0, int w1) {
#pragma unroll
            for (int w = w0; w < w1; ++w) {
                const int p = w / 9, i = w % 9 == 4 ? -1 : w % 9 - (w % 9 > 4);
                LAS char* sl = lds + (((p ? 2 * p + 6 : 4) + 6) & 7) * K5_STAGE + lane * 16;
                if (i < 0) *(LAS u32x4*)(sl + (wid >> 1) * K5_STAGE + 16384 + (wid & 1) * 1024) = wr[p][8];
                else *(LAS u32x4*)(sl + (i >> 2) * K5_STAGE + wid * 4096 + (i & 3) * 1024) = wr[p][i];
            }
            WAIT_L0();
        };
        const int row = wid * 32 + (lane & 31);
        auto step1 = [&](int t) { BAR(); issue1(t + 4, t & 3); };
        const char* ps;
        unsigned fd, fd2;
        {
            unsigned fo, fi;
            const bool oj = !layer && wid && b < 96, ij = !layer && wid && b >= 112, early = win_strip(b) >= WIN_EARLY;
            const int ojob = b * 3 + wid - 1, oimg = 2048 + (wid - 1) * 9216;
            float m0, m1;
            unsigned fw;
            auto win_store = [&](int h) {
                const int u = win_strip(b) * 2 + h, half = u >= 144, g = u - half * 144, ug = g / 9, j = g - ug * 9, c = lane & 15;
                const auto rd = RSRC(ws + OFF_TWIN + (size_t)ug * (18 * 18432) + (half * 18 + win_third(b, wid) * 6) * 9216);
                const unsigned vd = (lane >> 4) * 256 + (j < 8 ? ((j & ~1) + (c >> 3)) * 1024 + ((j & 1) * 8 + (c & 7)) * 16 : 8192 + c * 16);
#pragma unroll
                for (int i = 0; i < 6; ++i) ST16(rd, vd, i * 9216, wr[1 + h][i], 16);
            };
            auto win_half = [&](int h) {
                const Sel k = make_sel();
                const float m = h ? m1 : m0;
                f32x2 er[4] = {{-m, -m}, {-m, -m}, {-m, -m}, {-m, -m}};
#pragma unroll
                for (int i = 0; i < 6; ++i) centre_frag(wr[1 + h][i], er, m, k);
            };
            attn_producer(lds, ws, x0, wl, fl, ep, layer, pa, hp, wid, [&] { if (b < 16) WAIT_V(0); prologue_done(lds, ws, ep, b, lane); }, [&](int part) {
                if (part == -1 && ij) {
#pragma unroll
                    for (int h = 0; h < 2; ++h) {
                        const LAS char* src = lds + 3 * K1_STAGE + (wid - 1) * 12288 + ((lane >> 4) * 8 + ((lane & 15) >> 2)) * 64 + h * 32
                                              + ((win_strip(b) * 2 + h) % 9 < 8 ? (lane & 1) * 16 + (lane & 2) * 4 : (lane & 3) * 8);
#pragma unroll
                        for (int i = 0; i < 6; ++i) wr[1 + h][i] = tr_frag(src + i * 2048, 256);
                    }
                    WAIT_L(0);
                }
                if (part == 0 && oj) {
                    const auto rs = RSRC(w_out + (size_t)(ojob >> 2) * 32 * 576 + (ojob & 3) * 144);
#pragma unroll
                    for (int j = 0; j < 9; ++j) { const int i = lane + 64 * j; dma1(l0 + oimg + j * 1024, rs, 0, (unsigned)(((i * 3641) >> 16) * (1152 - 288) + i * 16)); }
                }
                if (part == 1) {
                    if (layer) { issue1B(0, 0); issue1B(1, 1); }
                    fo = flag_out(fl + F_X1, 256, pa, hp, lane); fi = flag_in(fl + F_X1, 256, pm * 16 + cg, 16, lane);
                    ps = pin(st + (size_t)(pm * 128 + wid * 32) * 64 + lane * 16);
                    fd = pin((unsigned)OFF_FL2 + wid * 1024 + vo);
                    fd2 = pin((unsigned)OFF_FL2 + (lane < 32 ? 256 + lane : 288 + ((cg * 9) >> 1) * 3 + min(lane & 15, 14)) * 16);
                    fw = pin((unsigned)OFF_FL2 + (288 + (72 + ((cg * 9) >> 1)) * 3 + min(lane & 15, 14)) * 16);
                    if (ij) {
                        float sm[4] = {0.f, 0.f, 0.f, 0.f};
#pragma unroll
                        for (int e = 0; e < 48; ++e) sm[(e >= 24) * 2 + (e & 1)] = sum2(wr[1 + (e >= 24)][(e % 24) >> 2][e & 3], sm[(e >= 24) * 2 + (e & 1)]);
                        column_means<3>(lds, lds + G_WORD, wid - 1, lane, sm[0] + sm[1], sm[2] + sm[3], m0, m1);
                        win_half(0);
                    }
                }
                if (part == 2 && ij) { if (early) win_store(0); win_half(1); if (early) win_store(1); }
                if (part == 2 && oj) { retile_out_make<288>(wr[1], lds + oimg, lane); WAIT_L(0); }
            });
            if (oj || ij) {
                if (oj || !early) {
                    __builtin_amdgcn_s_sleep(OJOB_SLEEP);
                    if (oj) retile_out_store(wr[1], ws + OFF_TWOUT, 72, ojob >> 2, ojob & 3, lane); else { win_store(0); win_store(1); }
                }
                WAIT_V(0);
                flag_raise(ws, lane ? 0x80000000u : (unsigned)OFF_FL2 + (oj ? ojob : 288 + win_strip(b) * 3 + wid - 1) * 16);
            }
            if (layer == 0) {
                if (ij) __builtin_amdgcn_s_sleep(WIN_LOOK - 12); else if (!oj) __builtin_amdgcn_s_sleep(WIN_LOOK);
                flags_wait<0, 8>(ws, fw);
            }
            BARX();
            if (wid == 0) flag_raise(ws, fo);
            if (layer == 0) { issue1B(0, 0); issue1B(1, 1); }
            issue1B(2, 2); issue1B(3, 3);
            asm volatile("" :: "s"(rX));
            if (wid == 0) { if (layer) __builtin_amdgcn_s_sleep(X1_FIRST); flags_wait<0>(ws, fi); }
            BARX();
        }
        issue1A(0, 0); issue1A(1, 1); issue1A(2, 2);
        WAIT_V(8); BAR();
        issue1A(3, 3);
        {
            f32x4 sp[3];
            WAIT_V(8); step1(0);
            WAIT_V(13); step1(1);
            WAIT_V(18); step1(2);
            unsigned got[2];
            if (layer == 0) asm volatile("s_nop 4\n\tbuffer_load_dword %0, %2, %4, 0 offen sc1\n\tbuffer_load_dword %1, %3, %4, 0 offen sc1" : "+v"(got[0]), "+v"(got[1]) : "v"(fd), "v"(fd2), "s"(RSRC(ws)) : "memory");
            WAIT_IF(layer, 18, 20); step1(3);
            WAIT_IF(layer, 18, 20); step1(4);
            WAIT_V(18);
            if (layer == 0) {
                asm volatile("" : "+v"(got[0]), "+v"(got[1]));
                if (__any((got[0] & got[1]) == 0u)) { flags_wait<0>(ws, fd); flags_wait<0>(ws, fd2); }
            }
            BAR(); issue1B(9, 1); side_load(sp, ps, ps); if (layer == 0) load5(3, 0);
            WAIT_IF(layer, 17, 21); BAR(); issue1B(10, 2); if (layer == 0) load5(3, 1);
            WAIT_IF(layer, 5, 14); BARX();
            side_fence(sp);
#pragma unroll
            for (int i = 0; i < 2; ++i) lds_word_set(lds + G_TAB + (wid * 32 + i * 16 + (lane >> 2)) * 4, __float_as_uint(ln_rstd(sp[i])));
            issue1B(11, 3);
            WAIT_L0();
        }
        BAR(); issue1B(12, 0);
        if (layer == 1) {
            const auto rd = RSRC(out + (size_t)b * 9216 + wid * 2304);
            ST16(rd, vo, 0, ((u32x4){0u, 0u, 0u, 0u}), 16); ST16(rd, vo, 1024, ((u32x4){0u, 0u, 0u, 0u}), 16);
            if (lane < 16) ST16(rd, vo, 2048, ((u32x4){0u, 0u, 0u, 0u}), 16);
        }
        if (layer == 0) {
#pragma unroll
            for (int p = 0; p < 6; ++p) load5(p >> 1, p & 1);
        }
        WAIT_V(0);
        __builtin_amdgcn_s_setprio(3);
        const char* src = pin((const char*)zst + (size_t)(pm * 16 + (lane >> 4) * 4) * 1024 + wid * 256 + (lane & 15) * 16);
        const unsigned tw = pin(l0 + G_TAB + 512 + (lane >> 5) * 512 + (wid * 32 + (lane & 15) * 2 + ((lane >> 4) & 1)) * 4);
        const unsigned fz = flag_out(fl + F_Z2, 256, cg, pm, lane), fzi = pin(fl + F_Z2 + (cg * 16 + pm) * 256 + ((lane ^ pm) & 8) * 16 + (lane & 7) * 16);
        const unsigned vA[4] = {pin(voA), pin(voA + 16 * 4096 - 1024), pin(voA + 4096 - 2048), pin(voA + 17 * 4096 - 3072)};
        auto spA = [&](int so, unsigned la) { dma4<false>(la, rA, so, vA[0], vA[1], vA[2], vA[3]); };
        auto issueA = [&](int k, int slot) { spA(k * 128, lA + slot * G_STAGE); };
        {
            const LAS char* pp = opaque(lds + G_PART + row * 8);
            const auto rs = RSRC(zst + (size_t)(pm * 16 + cg) * 128);
            const unsigned vp = pin(lane < 32 ? (unsigned)row * 8u : 0x80000000u);
            asm volatile("" :: "s"(rs));
            BARX();
            const f32x2 a = *(const LAS f32x2*)pp, c = *(const LAS f32x2*)(pp + 1024);
            ST8(rs, vp, 0, ((u32x2){__float_as_uint(add1(a.x, c.x)), min(__float_as_uint(add1(a.y, c.y)), 0xfffffffeu)}), 16);
            SCHED0();
        }
        __builtin_amdgcn_s_setprio(0);
        {
            f32x4 g[4];
            float sm[4];
            auto gather = [&] {
                asm volatile("global_load_dwordx4 %0, %4, off sc1\n\tglobal_load_dwordx4 %1, %4, off offset:1024 sc1\n\t"
                             "global_load_dwordx4 %2, %4, off offset:2048 sc1\n\tglobal_load_dwordx4 %3, %4, off offset:3072 sc1"
                             : "=&v"(g[0]), "=&v"(g[1]), "=&v"(g[2]), "=&v"(g[3]) : "v"(src) : "memory");
            };
            auto total = [&] {
                asm volatile("" : "+v"(g[0]), "+v"(g[1]), "+v"(g[2]), "+v"(g[3]));
#pragma unroll
                for (int i = 0; i < 4; ++i) sm[i] = add1(add1(g[0][i], g[1][i]), add1(g[2][i], g[3][i]));
            };
            step1(9); step1(10);
            BARX(); gather(); issue1(15, 3);
            WAIT_V(22); BARX();
            __builtin_amdgcn_s_setprio(3);
            WAIT_V(9);
            total();
            if (__any(__builtin_isunordered(sm[1], sm[3]))) {
#pragma unroll 1
                for (unsigned spin = 0; spin < 400000u; ++spin) {
                    bool ok = true;
#pragma unroll
                    for (int i = 0; i < 4; ++i) ok &= __float_as_uint(g[i][1]) != 0xffffffffu && __float_as_uint(g[i][3]) != 0xffffffffu;
                    if (__all(ok)) break;
                    gather();
                    WAIT_V(0);
                    total();
                }
            }
            xq4_sum(sm);
            const bool odd = lane & 16;
            float mean;
            const float rs = rstd_of(odd ? sm[2] : sm[0], odd ? sm[3] : sm[1], 1.0f / 2304.0f, mean);
            asm volatile("ds_write_b32 %0, %1" :: "v"(tw), "v"(lane < 32 ? rs : -mean * rs) : "memory");
            __builtin_amdgcn_s_setprio(0);
            issue1B(16, 0);
            WAIT_L0();
        }
        BARX(); issue1A(16, 0); issueA(2 * pm, 1);
        auto kord2 = [&](int t) { const int j = kord(t - 2, 30); return ((j < 2 * (pm & 7) ? j : j + 2) + 2 * (pm & 8)) & 31; };
        unsigned seen;
        auto look = [&] { if (wid == 0) asm volatile("s_nop 4\n\tbuffer_load_dword %0, %1, %2, 0 offen sc1" : "+v"(seen) : "v"(fzi), "s"(RSRC(ws)) : "memory"); };
        auto need = [&](int n) {
            if (wid == 0) {
                asm volatile("" : "+v"(seen));
                if (__ballot(seen == 0u) & ((1ull << n) - 1ull)) flags_wait<0>(ws, lane < n ? fzi : fl + F_Z2 + (cg * 16 + pm) * 256 + pm * 16);
            }
        };
        auto issue = [&](int t, int sl) { const int k = kord2(t); issueB(k, sl); issueA(k, sl); };
        auto steps2 = [&](int t0, int t1) {
            int sl = (2 * t0 + 2) % 3 * G_STAGE, k = kord2(t0 + 3), ob = (k >> 1) * 589824 + (k & 1) * 18432;
            LOOP_ALIGN();
#pragma unroll 1
            for (int t = t0; t < t1; ++t) {
                WAIT_V(9); BARX();
                dma5<true>(lB + sl, rB, ob, vo);
                const int oa = pin_s(k * 128);
                const unsigned la = pin_s(lA + sl);
                k = pin_s(kord2(t + 4)); ob = pin_s((k >> 1) * 589824 + (k & 1) * 18432);
                sl = pin_s(sl == 0 ? 2 * G_STAGE : sl - G_STAGE);
                spA(oa, la);
            }
        };
        WAIT_V(13); BAR(); issue1(17, 2);
        WAIT_V(13); BARX();
        if (wid == 0) flag_raise(ws, fz);
        issueA(2 * pm + 1, 3); if (layer == 0) load5(4, 0);
        if (wid == 0) WAIT_IF(layer, 5, 9); else WAIT_IF(layer, 4, 8);
        BAR(); issueA(kord2(2), 0); if (layer == 0) load5(4, 1);
        BAR(); look(); issueA(kord2(3), 2);
        WAIT_V(4); need(3); BARX(); issueB(kord2(2), 0); issueB(kord2(3), 2); look();
        WAIT_IF(wid == 0, 6, 5); BAR(); issue(4, 1);
        WAIT_V(9); need(8);
        steps2(2, 11); look();
        WAIT_IF(wid == 0, 10, 9); BAR(); issue(14, 0);
        WAIT_V(9); need(16);
        steps2(12, 29);
        WAIT_V(9); BAR(); issue5(0); issue5(1); issue5(2);
        WAIT_V(15); BAR(); issue5(6); issue5(7);
        BAR(); issue5(3); put5(0, 9);
        unsigned vb;
        {
            vb = attn_vb(wid, lane);
            const unsigned fo = flag_out(fl + F_Y, 256, pm, cg, lane), fi = flag_in(fl + F_Y, 256, rh * 8 + c5, 16, lane);
            __builtin_amdgcn_s_setprio(3);
            WAIT_V(0); BARX();
            if (wid == 0) { flag_raise(ws, fo); flags_wait<Y_FIRST>(ws, fi); }
            __builtin_amdgcn_s_setprio(0);
            BARX();
        }
        put5(9, 13);
        BAR(); put5(13, 18);
        BAR(); put5(18, 24);
        BAR(); put5(24, 30);
        BAR(); put5(30, 36);
        BAR(); issue5(16); put5(36, 41);
        BAR(); issue5(17); put5(41, 45);
        WAIT_V(5); BAR();
        WAIT_V(0); BAR();
        if (layer == 0) { attn_issueB(lds, ws, hp, wid, vb, 0, 0); attn_issueB(lds, ws, hp, wid, vb, 1, 1); }
        BAR();
        if (layer == 0) {
            __builtin_amdgcn_s_setprio(3);
            const unsigned fo = flag_out(F_X2, 512, (rh >> 1) + wid, wid ? 16 + c5 : (rh & 1) * 8 + c5, wid == 0 || (wid == 1 && (rh & 1) && rh < 31) ? lane : 16);
            BARX();
            if (wid < 2) flag_raise(ws, fo);
        }
        TRACE_DUMP;
      }
        if (blockIdx.x == 0 && wid8 == 4) ST8(RSRC(ws), lane_id_fresh() ? 0x80000000u : (unsigned)OFF_EP, 0, ((u32x2){(unsigned)ep, (unsigned)(ep >> 32)}), 16);
        asm volatile("gm_code_end1:");
        return;
    }
#pragma unroll 1
  for (int layer = 0; layer < NET_LAYERS; ++layer) {
    NET_IDS
    TRACE_BEGIN;
    char* wl = ws + OFF_L0 + layer * SZ_L;
    const unsigned fl = layer * F_LAYER;
    const char* xin = wl + L_XB;
    char* y = wl + (layer ? L_Z2T : L_Y);
#undef STAMP_SEC
#define STAMP_SEC 0
    {
    STAMP_BEGIN;
    attn_consumer(lds, x0, wl, layer, pa, hp, wid, [&] { WAIT_V(0); prologue_done(lds, ws, ep, b, lane_id_fresh()); } STAMP_FWD);
    STAMP_END(1 + 5 * layer);
    }
#undef STAMP_SEC
#define STAMP_SEC 1
    STAMP_BEGIN;
    gmlp_body(lds, wid & 1, wid >> 1, pm, cg, wl + (layer ? L_Y : L_Z2T), y, bsp STAMP_FWD);
    {
        WAIT_V(0);
        BARX();
        STAMP(11);
        const int lane = lane_id_fresh(), r = lane & 15, q = lane >> 4;
        const int wk = wid >> 1, rt = rh * 4 + (wid & 1) * 2 + wk;
        const Res rx = proj_residual_load(xin, rt, c5, lane);
        const LAS char *pw[3], *ph[3];
#pragma unroll
        for (int i = 0; i < 3; ++i) {
            pw[i] = opaque(lds + 3 * i * K5_STAGE + wk * 4608 + lane * 16);
            ph[i] = opaque(lds + 3 * i * K5_STAGE + wk * 4608 + 4096 + q * 128 + (r & 7) * 16);
        }
        const auto rY = RSRC(y + (size_t)(rt & ~1) * 73728 + wk * 1024);
        const unsigned vy[2] = {pin((unsigned)(wk * 73728 + lane * 16)), pin((unsigned)((1 - wk) * 73728 + lane * 16))};
        int dk[4];
#pragma unroll
        for (int i = 0; i < 4; ++i) dk[i] = pin_s((kord5(4 + i) - 4 - i) * 4096);
        auto yoff = [&](int t) { return pin_s(t == 0 || t == 17 ? kord5(t) * 4096 : t * 4096 + dk[t & 3]); };
        bf16x8 af[5][4], wf[4][5];
        auto lda = [&](int so, int i) { return __builtin_bit_cast(bf16x8, __builtin_amdgcn_raw_buffer_load_b128(rY, vy[i & 1] + (i >> 1) * 2048, so, 16)); };
        auto rdw = [&](int t, int c, int n) {
            const int sl = (t + 6) & 7, o = (sl % 3) * K5_STAGE + c * 9216;
            wf[2 * (t & 1) + c][n] = n < 4 ? LDS_FRAG(pw[sl / 3] + o + n * 1024) : LDS_FRAG(ph[sl / 3] + o);
        };
#pragma unroll
        for (int n = 0; n < 10; ++n) rdw(0, n / 5, n % 5);
        int so[18];
#pragma unroll
        for (int i = 0; i < 5; ++i) so[i] = yoff(i);
        so[17] = yoff(17);
        f32x4 v[5], u[5];
        proj_residual(v, rx);
#pragma unroll
        for (int n = 0; n < 5; ++n) v[n] = pin(v[n]);
        LAS char* xo = (LAS char*)opaque(lds + wid * 4608 + lane * 16);
        LAS char* xo4 = (LAS char*)opaque(lds + wid * 4608 + 4096 + (lane & 31) * 16);
        const LAS char* xi = opaque(lds + (wid ^ 2) * 4608 + lane * 16);
        const LAS char* xi4 = opaque(lds + (wid ^ 2) * 4608 + 4096 + (lane & 31) * 16);
        const ProjOut po = layer ? proj_out_final(out, rt, c5, lane) : proj_out(wl + SZ_L + L_XA, wl + SZ_L + L_STA, rt, c5, lane);
        asm volatile("" :: "s"(rY));
        WAIT_L(0);
        BARX();
        STAMP(12);
#pragma unroll
        for (int i = 0; i < K5_PRE; ++i) { af[i >> 2][i & 3] = lda(so[i >> 2], i & 3); SCHED0(); if (i >= PACE0 && i + 1 < K5_PRE) PACE; SCHED0(); }
        static_for<18>([&](auto tc) {
            constexpr int t = tc.value;
            bf16x8 (&a)[4] = af[t % 5];
#pragma unroll
            for (int h = 0; h < 4; ++h) {
                const int c = t < 17 ? h >> 1 : h & 1, m = t < 17 ? h & 1 : 1 - (h >> 1);
                if (t < 17 ? m == 0 : h < 2) { if (t == 17 && h == 1) WAIT_L(0); else WAIT_L(5); }
#pragma unroll
                for (int n = 0; n < 5; ++n) {
                    f32x4& e = m ? u[n] : v[n];
                    if (t == 0 && h == 1) MFMA_0(wf[0][n], a[1], e); else MFMA_T(wf[2 * (t & 1) + c][n], a[2 * c + m], e);
                    SCHED0();
                    if (t < 17 && m == 0) rdw(t + 1, c, n);
                    if (t < 14 && m == 1 && !(n & 1)) { const int i = y_load_at(6 * t + 3 * c + n / 2); if (i >= 0) af[(i >> 2) % 5][i & 3] = lda(so[i >> 2], i & 3); }
                    if (t < 12 && h == 3 && n == 3) so[t + 5] = yoff(t + 5);
                    if (t == 17 && h * 5 + n >= 13 && h * 5 + n < 18) { const int i = h * 5 + n - 13; *(LAS f32x4*)(i < 4 ? xo + i * 1024 : xo4) = u[i]; }
                    SCHED0();
                }
            }
            if ((t & 1) && t < 17) BAR();
            SCHED0();
        });
        {
            WAIT_L0(); BARX();
#pragma unroll
            for (int n = 0; n < 5; ++n) u[n] = *(const LAS f32x4*)(n < 4 ? xi + n * 1024 : xi4);
            SCHED0();
            asm volatile("s_nop 1" : "+v"(v[0]), "+v"(v[1]), "+v"(v[2]), "+v"(v[3]), "+v"(v[4]));
#pragma unroll
            for (int n = 0; n < 5; ++n) v[n] += u[n];
        }
        STAMP(13);
        if (layer) proj_store<true>(v, po);
        else { proj_store(v, po); WAIT_V(0); BARX(); }
    }
    STAMP_END(3 + 5 * layer);
    TRACE_DUMP;
  }
  asm volatile("gm_code_end2:");
}

}

#define STOP_CHECK(n)

extern "C" void kernel_launch(void* const* d_in, const int* in_sizes, int n_in, void* d_out, int out_size, void* d_ws, size_t ws_size,
                              hipStream_t stream) {
    (void)in_sizes; (void)n_in; (void)out_size; (void)ws_size;
    const u16* x = (const u16*)d_in[0];
    const u16* wqkv = (const u16*)d_in[1];
    const u16* wo = (const u16*)d_in[2];
    const u16* w_in = (const u16*)d_in[4];
    const u16* w_sp = (const u16*)d_in[5];
    const u16* b_sp = (const u16*)d_in[6];
    const u16* w_out = (const u16*)d_in[7];
    char* ws = (char*)d_ws;
    (void)hipFuncSetAttribute((const void*)k_net, hipFuncAttributeMaxDynamicSharedMemorySize, NET_LDS);
    k_net<<<256, 512, NET_LDS, stream>>>(ws, w_sp, b_sp, (char*)d_out, (const char*)x, (const char*)wqkv, wo, w_in, w_out DBG_LAUNCH(0));
}
```

```cpp
#include <hip/hip_runtime.h>
#include <stdint.h>

namespace {

typedef unsigned short u16;
typedef short bf16x8 __attribute__((ext_vector_type(8)));
typedef float f32x4 __attribute__((ext_vector_type(4)));
typedef float f32x2 __attribute__((ext_vector_type(2)));
typedef unsigned u32x4 __attribute__((ext_vector_type(4)));
typedef unsigned u32x2 __attribute__((ext_vector_type(2)));
typedef __bf16 bf16x2_t __attribute__((ext_vector_type(2)));
#define LAS __attribute__((address_space(3)))
#define DEVI __device__ __forceinline__

constexpr int S_ = 2048, D_ = 576, FH = 2304;

constexpr size_t SZ_TWQKV = (size_t)16 * 18 * 6 * 1024;
constexpr size_t SZ_TWT = (size_t)48 * 18 * 256;
constexpr size_t SZ_TWO = (size_t)8 * 18 * 4608;
constexpr size_t SZ_TWIN = (size_t)16 * 18 * 18 * 1024;
constexpr size_t SZ_TWOUT = (size_t)8 * 72 * 4608;
constexpr size_t SZ_X = (size_t)S_ * D_ * 2;
constexpr size_t SZ_Z = (size_t)S_ * FH * 2;
constexpr size_t SZ_ST = (size_t)S_ * 64;
constexpr size_t SZ_ZST = (size_t)16 * 16 * 128 * 8;
constexpr size_t OFF_TWQKV = 0;
constexpr size_t OFF_TWT = OFF_TWQKV + SZ_TWQKV;
constexpr size_t OFF_TWO = OFF_TWT + SZ_TWT;
constexpr size_t OFF_TWIN = OFF_TWO + SZ_TWO;
constexpr size_t OFF_TWOUT = OFF_TWIN + SZ_TWIN;
constexpr size_t OFF_CNT = OFF_TWOUT + SZ_TWOUT;
constexpr size_t SZ_CNT = 10 * 65536;
constexpr size_t OFF_FW = OFF_CNT + SZ_CNT;
constexpr size_t OFF_FL = OFF_FW + 262144;
constexpr size_t OFF_EP = OFF_FL + 4096;
constexpr size_t OFF_FL2 = OFF_EP + 4096;
constexpr size_t OFF_ZST = OFF_FL2 + 12288;
constexpr size_t OFF_L0 = OFF_ZST + 2 * SZ_ZST;
constexpr size_t L_XA = 0, L_STA = L_XA + SZ_X, L_O = L_STA + SZ_ST, L_XB = L_O + SZ_X, L_STB = L_XB + SZ_X, L_Z2T = L_STB + SZ_ST, L_Y = L_Z2T + SZ_Z;
constexpr size_t SZ_L = 0;
static_assert(OFF_L0 + L_Y + SZ_Z <= ((size_t)256 << 20), "workspace");
constexpr unsigned F_O = OFF_CNT, F_X1 = F_O + 65536, F_Z2 = F_O + 131072, F_Y = F_O + 196608, F_LAYER = 262144, F_X2 = F_O + 2 * F_LAYER;

DEVI unsigned pack2(float a, float b) { f32x2 v = {a, b}; bf16x2_t r = __builtin_convertvector(v, bf16x2_t); return __builtin_bit_cast(unsigned, r); }
DEVI float bflo(unsigned u) { return __uint_as_float(u << 16); }
DEVI float bfhi(unsigned u) { return __uint_as_float(u & 0xffff0000u); }
DEVI int slot2row(int sg) { return (sg & ~31) + 2 * (sg & 15) + ((sg >> 4) & 1); }
DEVI float fmax2(float a, float b) { return __builtin_amdgcn_fmed3f(a, b, __builtin_inff()); }
#define XQ4(OP, MID) \
    asm volatile("s_nop 1\n\tv_permlane32_swap_b32 %0, %1\n\tv_permlane32_swap_b32 %2, %3\n\t" OP " %0, %0, %1\n\t" OP " %2, %2, %3\n\t" \
                 "s_nop 1\n\tv_permlane16_swap_b32 %0, %2\n\t" OP " %0, %0, %2\n\t" MID \
                 "v_mov_b32 %2, %0\n\ts_nop 1\n\tv_permlane16_swap_b32 %0, %2\n\tv_mov_b32 %1, %0\n\tv_mov_b32 %3, %2\n\ts_nop 0\n\t" \
                 "v_permlane32_swap_b32 %0, %1\n\tv_permlane32_swap_b32 %2, %3" : "+v"(x[0]), "+v"(x[1]), "+v"(x[2]), "+v"(x[3]))
DEVI void xq4_max(float (&x)[4]) { XQ4("v_max_f32", ""); }
DEVI void xq4_sum(float (&x)[4]) { XQ4("v_add_f32", ""); }
DEVI void xq4_sum_rcp(float (&x)[4]) { XQ4("v_add_f32", "v_rcp_f32 %0, %0\n\ts_nop 0\n\t"); }
template <class T> DEVI T pin(T v) { asm volatile("" : "+v"(v)); return v; }
template <int V> struct IC { static constexpr int value = V; };
template <int N, int I = 0, class F> DEVI void static_for(F&& f) { if constexpr (I < N) { f(IC<I>{}); static_for<N, I + 1>(f); } }
template <class T> DEVI T* own_s(T* p) { T* r; asm volatile("s_mov_b64 %0, %1" : "=&s"(r) : "s"(p)); return r; }
DEVI int pin_s(int v) { asm volatile("" : "+s"(v)); return v; }
DEVI float xq_sum(float v) {
    auto a = __builtin_amdgcn_permlane32_swap(__float_as_uint(v), __float_as_uint(v), false, false);
    v = __uint_as_float(a[0]) + __uint_as_float(a[1]);
    auto b = __builtin_amdgcn_permlane16_swap(__float_as_uint(v), __float_as_uint(v), false, false);
    return __uint_as_float(b[0]) + __uint_as_float(b[1]);
}
#define RSRC(p) __builtin_amdgcn_make_buffer_rsrc((void*)(p), 0, 0x7fffffff, 0x00020000)
#define ST16(rs, vo, so, v, aux) __builtin_amdgcn_raw_buffer_store_b128((v), (rs), (vo) + (so), 0, aux)
#define ST8(rs, vo, so, v, aux) __builtin_amdgcn_raw_buffer_store_b64((v), (rs), (vo), (so), aux)
#define WAIT_V(n) asm volatile("s_waitcnt vmcnt(%0)" ::"i"(n) : "memory")
#define WAIT_L0() asm volatile("s_waitcnt lgkmcnt(0)" ::: "memory")
#define BAR() do { asm volatile("" ::: "memory"); __builtin_amdgcn_s_barrier(); asm volatile("" ::: "memory"); } while (0)
#define BARX() do { __builtin_amdgcn_sched_barrier(0); BAR(); __builtin_amdgcn_sched_barrier(0); } while (0)
#define LOOP_ALIGN() asm volatile(".p2align 6")
#define SCHED0() __builtin_amdgcn_sched_barrier(0)
#define MFMA(a, b, c) __builtin_amdgcn_mfma_f32_16x16x32_bf16((a), (b), (c), 0, 0, 0)
#define MFMA_T(a, b, c) asm volatile("v_mfma_f32_16x16x32_bf16 %0, %1, %2, %0" : "+v"(c) : "v"(a), "v"(b))
#define MFMA_0(a, b, c) asm volatile("v_mfma_f32_16x16x32_bf16 %0, %1, %2, 0" : "=&v"(c) : "v"(a), "v"(b))
#define WAIT_L(n) __builtin_amdgcn_s_waitcnt(0xC07F | ((n) << 8))
#define LDS_FRAG(p) (*(const LAS bf16x8*)(p))
DEVI int lane_id_fresh() { int z = 0; asm volatile("" : "+v"(z)); return __builtin_amdgcn_mbcnt_hi(~0u, __builtin_amdgcn_mbcnt_lo(~0u, z)); }
DEVI const LAS char* opaque(const LAS char* p) { asm volatile("" : "+v"(p)); return p; }
#define DBG_ARG
#define DBG_LAUNCH(n)
#define STAMP(i)
#define STAMP_DECL
#define STAMP_BEGIN
#define STAMP_END(n)
#define STAMP_PARAM
#define STAMP_FWD

#define PST_PARAM
#define PST_FWD
#define PST_DECL
#define PST(i)
#define PST_DUMP(c, k)
#define TRACE_ATTR
#define TRACE_BEGIN
#define TRACE_DUMP

DEVI float add1(float a, float b) { float r; asm("v_add_f32 %0, %1, %2" : "=v"(r) : "v"(a), "v"(b)); return r; }
DEVI float rstd_of(float s, float q, float inv_n, float& mean) {
    float var;
    asm("v_mul_f32 %0, %2, %3\n\tv_mul_f32 %1, %2, %4\n\tv_fma_f32 %1, -%0, %0, %1" : "=&v"(mean), "=&v"(var) : "s"(inv_n), "v"(s), "v"(q));
    return __builtin_amdgcn_rsqf(fmax2(var, 0.f) + 1e-5f);
}
DEVI float ln_rstd(const f32x4 p) {
    float s, q, mean;
    asm("v_add_f32 %0, %2, %4\n\tv_add_f32 %1, %3, %5\n\ts_nop 0\n\t"
        "v_add_f32_dpp %0, %0, %0 quad_perm:[1,0,3,2] row_mask:0xf bank_mask:0xf\n\tv_add_f32_dpp %1, %1, %1 quad_perm:[1,0,3,2] row_mask:0xf bank_mask:0xf\n\ts_nop 0\n\t"
        "v_add_f32_dpp %0, %0, %0 quad_perm:[2,3,0,1] row_mask:0xf bank_mask:0xf\n\tv_add_f32_dpp %1, %1, %1 quad_perm:[2,3,0,1] row_mask:0xf bank_mask:0xf"
        : "=&v"(s), "=&v"(q) : "v"(p[0]), "v"(p[1]), "v"(p[2]), "v"(p[3]));
    return rstd_of(s, q, 1.0f / 576.0f, mean);
}

struct Res { u32x4 a, c; u32x2 e; };
DEVI void proj_residual(f32x4 (&v)[5], const Res& x) {
    v[0] = (f32x4){bflo(x.a.x), bfhi(x.a.x), bflo(x.a.y), bfhi(x.a.y)}; v[1] = (f32x4){bflo(x.a.z), bfhi(x.a.z), bflo(x.a.w), bfhi(x.a.w)};
    v[2] = (f32x4){bflo(x.c.x), bfhi(x.c.x), bflo(x.c.y), bfhi(x.c.y)}; v[3] = (f32x4){bflo(x.c.z), bfhi(x.c.z), bflo(x.c.w), bfhi(x.c.w)};
    v[4] = (f32x4){bflo(x.e.x), bfhi(x.e.x), bflo(x.e.y), bfhi(x.e.y)};
}
struct ProjOut { __amdgpu_buffer_rsrc_t x, st; unsigned v0, v1, v2, vs; };
DEVI ProjOut proj_out(char* xout, char* stout, int rt, int cg, int lane) {
    const int r = lane & 15, q = lane >> 4;
    ProjOut p = {RSRC(xout + (size_t)rt * 18432 + cg * 2304), RSRC(stout + (size_t)rt * 1024 + cg * 8),
                 pin((unsigned)lane * 16u), pin((unsigned)lane * 16u + 1024u), pin((unsigned)(2048 + r * 16 + (q & 1) * 8)), pin((unsigned)(r * 64 + (q >> 1) * 4))};
    asm volatile("" :: "s"(p.x), "s"(p.st));
    return p;
}
DEVI ProjOut proj_out_final(char* out, int rt, int cg, int lane) {
    const int q = lane >> 4;
    const unsigned o = (unsigned)(slot2row(rt * 16 + (lane & 15)) * 1152 + cg * 144);
    ProjOut p = {RSRC(out), RSRC(out), pin(o + q * 16), pin(o + 64 + q * 16), pin(o + 128 + (q & 1) * 8), 0u};
    return p;
}
DEVI Res proj_residual_load(const ProjOut& p) {
    return {__builtin_amdgcn_raw_buffer_load_b128(p.x, p.v0, 0, 16), __builtin_amdgcn_raw_buffer_load_b128(p.x, p.v1, 0, 16), __builtin_amdgcn_raw_buffer_load_b64(p.x, p.v2, 0, 16)};
}
template <bool FINAL = false>
DEVI void proj_store(f32x4 (&v)[5], const ProjOut& p) {
    ST16(p.x, p.v0, 0, ((u32x4){pack2(v[0][0], v[0][1]), pack2(v[0][2], v[0][3]), pack2(v[1][0], v[1][1]), pack2(v[1][2], v[1][3])}), FINAL ? 0 : 16);
    SCHED0();
    asm volatile("" : "+v"(v[2]), "+v"(v[3]), "+v"(v[4]));
    ST16(p.x, p.v1, 0, ((u32x4){pack2(v[2][0], v[2][1]), pack2(v[2][2], v[2][3]), pack2(v[3][0], v[3][1]), pack2(v[3][2], v[3][3])}), FINAL ? 0 : 16);
    SCHED0();
    ST8(p.x, p.v2, 0, ((u32x2){pack2(v[4][0], v[4][1]), pack2(v[4][2], v[4][3])}), FINAL ? 0 : 16);
    SCHED0();
    if (!FINAL) {
        const f32x4 s4 = ((v[0] + v[1]) + (v[2] + v[3])) + v[4] * 0.5f;
        const f32x4 q4 = ((v[0] * v[0] + v[1] * v[1]) + (v[2] * v[2] + v[3] * v[3])) + v[4] * v[4] * 0.5f;
        auto x = __builtin_amdgcn_permlane32_swap(__float_as_uint((s4[0] + s4[2]) + (s4[1] + s4[3])), __float_as_uint((q4[0] + q4[2]) + (q4[1] + q4[3])), false, false);
        const float t = __uint_as_float(x[0]) + __uint_as_float(x[1]);
        auto y = __builtin_amdgcn_permlane16_swap(__float_as_uint(t), __float_as_uint(t), false, false);
        __builtin_amdgcn_raw_buffer_store_b32(__float_as_uint(__uint_as_float(y[0]) + __uint_as_float(y[1])), p.st, p.vs, 0, 16);
        SCHED0();
    }
}

DEVI void side_load(f32x4 (&d)[3], const char* p, const char* p2) {
    asm volatile("global_load_dwordx4 %0, %3, off sc1\n\tglobal_load_dwordx4 %1, %3, off offset:1024 sc1\n\tglobal_load_dwordx4 %2, %4, off sc1"
                 : "=&v"(d[0]), "=&v"(d[1]), "=&v"(d[2]) : "v"(p), "v"(p2) : "memory");
}
DEVI void side_fence(f32x4 (&d)[3]) { asm volatile("" : "+v"(d[0]), "+v"(d[1]), "+v"(d[2])); }
DEVI void lds_word_set(LAS char* p, unsigned v) { asm volatile("ds_write_b32 %0, %1" :: "v"((unsigned)(size_t)p), "v"(v) : "memory"); }
#define DMA_I(v, off, sc) "buffer_load_dwordx4 " v ", %1, %2 offen offset:" off sc " lds\n\t"
#define DMA5(sc) asm volatile("s_mov_b32 m0, %0\n\ts_mov_b32 exec_hi, 0\n\ts_nop 2\n\t" DMA_I("%3", "0", sc) "s_mov_b32 exec_hi, -1\n\t" \
    DMA_I("%3", "512", sc) DMA_I("%3", "1536", sc) DMA_I("%3", "2560", sc) DMA_I("%3", "3584", sc) :: "s"(la), "s"(rs), "s"(so), "v"(vo) : "memory")
#define DMA4(sc) asm volatile("s_mov_b32 m0, %0\n\ts_nop 3\n\t" DMA_I("%3", "0", sc) DMA_I("%4", "1024", sc) DMA_I("%5", "2048", sc) DMA_I("%6", "3072", sc) \
    :: "s"(la), "s"(rs), "s"(so), "v"(v0), "v"(v1), "v"(v2), "v"(v3) : "memory")
template <bool SC1> DEVI void dma5(unsigned la, __amdgpu_buffer_rsrc_t rs, int so, unsigned vo) { if (SC1) DMA5(" sc1"); else DMA5(""); }
template <bool SC1> DEVI void dma4(unsigned la, __amdgpu_buffer_rsrc_t rs, int so, unsigned v0, unsigned v1, unsigned v2, unsigned v3) { if (SC1) DMA4(" sc1"); else DMA4(""); }
#define DMA2(sc) asm volatile("s_mov_b32 m0, %0\n\ts_nop 3\n\t" DMA_I("%3", "0", sc) DMA_I("%4", "1024", sc) :: "s"(la), "s"(rs), "s"(so), "v"(v0), "v"(v1) : "memory")
template <bool SC1> DEVI void dma2(unsigned la, __amdgpu_buffer_rsrc_t rs, int so, unsigned v0, unsigned v1) { if (SC1) DMA2(" sc1"); else DMA2(""); }
DEVI void dma1(unsigned la, __amdgpu_buffer_rsrc_t rs, int so, unsigned v0) { asm volatile("s_mov_b32 m0, %0\n\ts_nop 3\n\t" DMA_I("%3", "0", "") :: "s"(la), "s"(rs), "s"(so), "v"(v0) : "memory"); }
#define WAIT_IF(c, a, b) do { if (c) WAIT_V(a); else WAIT_V(b); } while (0)
template <int T0, int T1, int P, int S0 = 0, class ISSUE>
DEVI void ring_steps(ISSUE&& issue) {
    int slot = (T0 + S0) % 3;
    LOOP_ALIGN();
#pragma unroll 1
    for (int t = T0; t < T1; ++t) {
        WAIT_V(P); BAR();
        issue(t + 3, slot);
        slot = slot == 2 ? 0 : slot + 1;
    }
}

DEVI unsigned flag_out(unsigned use, int pitch, int group, int writer, int lane) { return pin(lane < 16 ? use + (group * 16 + lane) * pitch + writer * 16 : 0x80000000u); }
DEVI unsigned flag_in(unsigned use, int pitch, int reader, int n, int lane) { return pin(use + reader * pitch + (lane < n ? lane : 0) * 16); }
DEVI void flag_raise(const char* ws, unsigned vo) { ST16(RSRC(ws), vo, 0, ((u32x4){1u, 1u, 1u, 1u}), 16); SCHED0(); }
template <int FIRST, int AGAIN = 1>
DEVI void flags_wait(const char* ws, unsigned vo) {
    unsigned v, n = 400000u;
    asm volatile("s_nop 4\n\ts_sleep %4\n"
                 "1:\n\tbuffer_load_dword %0, %2, %3, 0 offen sc1\n\ts_waitcnt vmcnt(0)\n\tv_cmp_eq_u32 vcc, 0, %0\n\ts_cbranch_vccz 2f\n\t"
                 "s_sleep %5\n\ts_sub_u32 %1, %1, 1\n\ts_cbranch_scc0 1b\n2:"
                 : "=&v"(v), "+s"(n) : "v"(vo), "s"(RSRC(ws)), "i"(FIRST), "i"(AGAIN) : "vcc", "scc", "memory");
    SCHED0();
}
template <int FIRST>
DEVI void epoch_wait(const char* ws, unsigned vo, unsigned long long ep) {
    unsigned long long v; unsigned n = 400000u;
    asm volatile("s_nop 4\n\ts_sleep %5\n"
                 "1:\n\tbuffer_load_dwordx2 %0, %2, %3, 0 offen sc1\n\ts_waitcnt vmcnt(0)\n\tv_cmp_ne_u64 vcc, %4, %0\n\ts_cbranch_vccz 2f\n\t"
                 "s_sleep 1\n\ts_sub_u32 %1, %1, 1\n\ts_cbranch_scc0 1b\n2:"
                 : "=&v"(v), "+s"(n) : "v"(vo), "s"(RSRC(ws)), "s"(ep), "i"(FIRST) : "vcc", "scc", "memory");
    SCHED0();
}
DEVI unsigned lds_arrive(LAS char* p) {
    unsigned v;
    asm volatile("s_mov_b64 exec, 1\n\tds_add_rtn_u32 %0, %1, %2\n\ts_mov_b64 exec, -1\n\ts_waitcnt lgkmcnt(0)" : "=&v"(v) : "v"((unsigned)(size_t)p), "v"(1u) : "memory");
    return __builtin_amdgcn_readfirstlane(v);
}
DEVI void lds_count_wait(LAS char* p, unsigned n) {
#pragma unroll 1
    for (unsigned spin = 0; spin < 40000000u; ++spin) {
        unsigned v;
        asm volatile("ds_read_b32 %0, %1\n\ts_waitcnt lgkmcnt(0)" : "=v"(v) : "v"((unsigned)(size_t)p) : "memory");
        if (__builtin_amdgcn_readfirstlane(v) >= n) break;
    }
}

constexpr int K1_STAGE = 32768;
constexpr int WO_OFF = 69632;
constexpr int G_OWN = 139264;
constexpr int G_PART = G_OWN + 18432;
constexpr int G_TAB = G_PART + 2048;
constexpr int K1_SCR = G_TAB + 1536;
constexpr int G_WORD = K1_SCR + 576;
constexpr int NET_LDS = G_WORD + 64;

#define WO_PRE 9
#define WO_W 33
#define REL_PRIO 0
#define W_FIRST 2
#define I_SLEEP 30
#define OJOB_SLEEP 20
#define PACE0 1
#define PACE __builtin_amdgcn_s_sleep(1)
#define POLLW 0
#define WO_EV 4
#define WIN_EARLY 0
#define WIN_LOOK 40
#define W3_SLEEP 0
#define O_FIRST 8
#define X1_FIRST 4
#define X2_FIRST 4
#define Y_FIRST 14
#define Z2_FIRST 10
DEVI int win_strip(int b) { return ((b - 112) & 7) * 18 + ((b - 112) >> 3); }
DEVI int win_third(int b, int w) { return b & 8 ? 3 - w : w - 1; }
DEVI unsigned attn_vb(int wid, int lane) {
    const int c = lane & 15;
    return pin((unsigned)(wid < 3 ? lane * 16 : (c < 8 ? (c >> 1) & 1 : 2) * 4608 + (lane >> 4) * 64 + (c < 8 ? (c >> 2) * 2 + (c & 1) : c & 3) * 16));
}
DEVI void attn_issueB(LAS char* lds, const char* ws, int hp, int wid, unsigned vb, int s, int slot) {
    const unsigned la = (unsigned)(size_t)lds + slot * K1_STAGE + 18432 + wid * 4096;
    if (wid < 3) dma4<true>(la, RSRC(ws), (int)OFF_TWQKV + hp * (18 * 6144) + wid * 4096 + s * 12288, vb, vb, vb, vb);
    else asm volatile("s_mov_b32 m0, %0\n\ts_nop 3\n\tbuffer_load_dwordx4 %4, %1, %2 offen sc1 lds\n\tbuffer_load_dwordx4 %4, %1, %3 offen offset:1024 sc1 lds"
                      :: "s"(la), "s"(RSRC(ws)), "s"((int)OFF_TWT + hp * 13824 + s * 512), "s"((int)OFF_TWT + hp * 13824 + s * 512 + 256 - 1024), "v"(vb) : "memory");
}
struct AttnX { __amdgpu_buffer_rsrc_t rs; unsigned v[4], vh[2]; int oA, oh, step; };
DEVI AttnX attn_x(const char* x0, const char* xa, int layer, int pm, int wid, int lane) {
    const int l3 = lane >> 3, pmh = pm > 0 ? pm : 1;
    const unsigned v = layer ? lane * 16 : 2 * l3 * 1152 + ((lane & 7) ^ l3) * 16 + 4096, d1 = layer ? 0 : 16 * 1152 - 1024, d2 = layer ? 16384 : 1152 - 2048;
    const unsigned vh = layer ? (l3 & 1) * 18432 + (lane >> 4) * 256 + (8 + (lane & 7)) * 16 : v + 16 * 1152;
    return {RSRC(layer ? xa : x0 - 4096), {pin(v), pin(v + d1), pin(v + d2), pin(v + d1 + d2)}, {pin(vh), pin(vh + (layer ? 0 : 1152 - 1024))},
            (pm * 8 + 2 * wid) * 18432, (pmh * 8 - 2) * 18432, layer ? 2048 : 128};
}
DEVI void attn_issueA(LAS char* lds, const AttnX& a, int wid, int s, int slot) {
    const unsigned la = (unsigned)(size_t)lds + slot * K1_STAGE;
    if (wid == 3) dma2<true>(la, a.rs, a.oh + s * a.step, a.vh[0], a.vh[1]);
    dma4<true>(la + 2048 + wid * 4096, a.rs, a.oA + s * a.step, a.v[0], a.v[1], a.v[2], a.v[3]);
}
template <class DONE, class NEXT>
DEVI void attn_producer(LAS char* lds, const char* ws, const char* x0, const char* wl, unsigned fl, unsigned long long ep, int layer, int pm, int hp, int wid, DONE&& done, NEXT&& next) {
    const int lane = lane_id_fresh();
    const int pmh = pm > 0 ? pm : 1;
    const unsigned vo = lane * 16;
    const AttnX ax = attn_x(x0, wl + L_XA, layer, pm, wid, lane);
    const unsigned vb = attn_vb(wid, lane), fd = pin((unsigned)OFF_FL + wid * 1024 + vo);
    auto issueB = [&](int s, int slot) { attn_issueB(lds, ws, hp, wid, vb, s, slot); };
    auto issue = [&](int s, int slot) { attn_issueA(lds, ax, wid, s, slot); issueB(s, slot); };
    const int hr = lane >> 2;
    const char* ps = pin(wl + L_STA + (size_t)(pm * 128 + wid * 32) * 64 + lane * 16);
    const char* psh = pin(wl + L_STA + (size_t)((pmh * 8 - 2 + (hr >> 3)) * 16 + 8 + (hr & 7)) * 64 + (lane & 3) * 16);
    if (layer == 0) {
        const unsigned fi = pin((unsigned)OFF_FW + (pm * 16 + hp) * 1024 + (lane < 6 ? lane : 0) * 128);
        done();
        if (wid == 0) epoch_wait<W_FIRST>(ws, fi, ep);
        BARX();
        issueB(0, 0); issueB(1, 1); issue(2, 2);
        WAIT_IF(wid == 3, 10, 12);
    } else {
        issueB(2, 2);
        const unsigned fi = flag_in(F_X2, 512, pm * 16 + hp, pm > 0 ? 24 : 16, lane);
        if (wid == 0) flags_wait<X2_FIRST>(ws, fi);
        BARX();
#pragma unroll
        for (int i = 0; i < 3; ++i) attn_issueA(lds, ax, wid, i, i);
        WAIT_IF(wid == 3, 12, 8);
    }
    __builtin_amdgcn_s_setprio(0);
    BAR();
    if (layer) WAIT_IF(wid == 3, 6, 4);
    next(-1);
    ring_steps<0, 4, 8>(issue);
    {
        unsigned long long done;
        if (layer == 0) asm volatile("s_nop 4\n\tbuffer_load_dwordx2 %0, %1, %2, 0 offen sc1" : "+v"(done) : "v"(fd), "s"(RSRC(ws)) : "memory");
        WAIT_IF(layer, 8, 9); BAR(); issue(7, 1);
        WAIT_V(8);
        if (layer == 0) { asm volatile("" : "+v"(done)); if (__any(done != ep)) epoch_wait<0>(ws, fd, ep); }
        BAR(); issue(8, 2);
    }
    const auto rW = RSRC(ws + OFF_TWO + (size_t)(hp >> 1) * 82944 + wid * 4096);
    auto issueW = [&](int i) { dma4<true>((unsigned)(size_t)lds + WO_OFF + (i * 4 + wid) * 4096, rW, i * 16384, vo, vo, vo, vo); };
    {
        f32x4 sp[3];
        side_load(sp, ps, psh);
        WAIT_V(11); BAR(); issueW(2); issueW(3);
        WAIT_V(8); BARX();
        side_fence(sp);
#pragma unroll
        for (int i = 0; i < 3; ++i) lds_word_set(lds + K1_SCR + (i < 2 ? 16 + wid * 32 + i * 16 + hr : hr) * 4, __float_as_uint(ln_rstd(sp[i])));
        WAIT_L0();
    }
    BAR();
    next(0);
    issueW(0); issueW(1); issueW(4);
    if (wid == 0) dma2<true>((unsigned)(size_t)lds + WO_OFF + 80896, rW, 80896, vo, vo);
    next(1);
    const unsigned fo = flag_out(fl + F_O, 256, pm, hp, lane), fi = flag_in(fl + F_O, 256, pm * 16 + hp, 16, lane);
    WAIT_V(0);
    if (wid == POLLW) __builtin_amdgcn_s_setprio(3);
    BARX();
    next(2);
    if (wid == POLLW) { flag_raise(ws, fo); flags_wait<O_FIRST>(ws, fi); }
    __builtin_amdgcn_s_setprio(0);
    BARX();
}

#define STAMP_SEC 0
template <class DONE>
DEVI void attn_consumer(LAS char* lds, const char* x0, char* wl, int layer, int pm, int hp, int wid, DONE&& done STAMP_PARAM) {
    const int lane = lane_id_fresh(), r = lane & 15, q = lane >> 4;
    const char* xa = wl + L_XA;
    char* o = wl + L_O;
    const int T = wid & 1, h = wid >> 1;
    const LAS char *ph[2], *pa[2];
#pragma unroll
    for (int kk = 0; kk < 2; ++kk) {
        const int sw = ((4 * kk + q) ^ (r & 7)) * 16;
        ph[kk] = opaque(lds + (layer ? kk * 1024 + q * 256 + (r ^ (T ? 0 : 8)) * 16 : (r ^ (T ? 0 : 8)) * 128 + sw));
        pa[kk] = opaque(lds + T * 2048 + (layer ? kk * 1024 + lane * 16 : r * 128 + sw));
    }
    const LAS char* pb = lds + h * 3072 + lane * 16;
    const LAS char* pt = lds + lane * 16;
    f32x4 acc[5][4];
#pragma unroll
    for (int m = 0; m < 5; ++m)
#pragma unroll
        for (int n = 0; n < 4; ++n) if (m + n) acc[m][n] = pin((f32x4){0.f, 0.f, 0.f, 0.f});
    bf16x8 fa[2][5], fb[2][4];
    auto rd1 = [&](int kk, int i) {
        if (i < 3) fb[kk][i] = LDS_FRAG(pb + 18432 + kk * 6144 + i * 1024);
        else if (i == 3) fb[kk][3] = LDS_FRAG(pt + 30720 + kk * 1024);
        else if (i == 4) fa[kk][0] = LDS_FRAG(ph[kk]);
        else if (i < 9) fa[kk][i - 4] = LDS_FRAG(pa[kk] + 2048 + (i - 5) * 4096);
    };
    f32x4 mb, mb0;
#pragma unroll
    for (int j = 0; j < 4; ++j) {
        const bool ok = ((r - 4 * q - j) & 15) < 8;
        mb[j] = ok ? 0.f : -__builtin_inff();
        mb0[j] = (ok && (pm > 0 || 4 * q + j <= r)) ? 0.f : -__builtin_inff();
    }
    mb = pin(mb); mb0 = pin(mb0);
    const unsigned mp0 = pin((4 * q > r ? 0xffffu : 0u) | (4 * q + 1 > r ? 0xffff0000u : 0u)), mp1 = pin((4 * q + 2 > r ? 0xffffu : 0u) | (4 * q + 3 > r ? 0xffff0000u : 0u));
    const unsigned qtm = pin(q == h ? 0xffffffffu : 0u);
    const unsigned vom = pin((unsigned)(r * 16 + hp * 1024 + (2 * h + (q >> 1)) * 256 + (q & 1) * 8)), vot = pin((unsigned)(r * 16 + 16384 + (hp >> 1) * 256 + (hp & 1) * 8 + h * 4));
    const LAS char* tk0 = opaque(lds + K1_SCR + ((q * 4) ^ (T ? 0 : 8)) * 4);
    const LAS char* tk = opaque(lds + K1_SCR + ((1 + T) * 16 + q * 4) * 4);
    const LAS char* tq = opaque(lds + K1_SCR + ((1 + T) * 16 + r) * 4);
    pb = opaque(pb); pt = opaque(pt);
    STAMP(2);
    const int rt = pm * 8 + (hp & 1) * 4 + wid, cg = hp >> 1;
    const ProjOut pr = layer ? proj_out((char*)xa, (char*)xa, rt, cg, lane) : proj_out_final((char*)x0, rt, cg, lane);
    BAR();
    if (layer == 0) done();
    BARX();
    STAMP(3);
#pragma unroll
    for (int i = 0; i < 9; ++i) rd1(0, i);
    SCHED0();
    const Res rx = proj_residual_load(pr);
    SCHED0();
    int slot = 0, left = 9;
    LOOP_ALIGN();
#pragma unroll 1
    do {
        int d;
#pragma unroll
        for (int kk = 0; kk < 2; ++kk) {
            WAIT_L(0);
            if (kk) BAR();
            SCHED0();
#pragma unroll
            for (int i = 1; i < 20; ++i) {
                const int m = i >> 2, n = i & 3;
                const bf16x8 &A = fa[kk][m], &B = fb[kk][n];
                acc[m][n] = n != 2 ? MFMA(B, A, acc[m][n]) : MFMA(A, B, acc[m][n]);
                SCHED0();
                if (i <= 9) rd1(kk ^ 1, i - 1);
                if (kk == 0) {
                    if (i == 10) d = pin_s(slot == 2 ? -2 * K1_STAGE : K1_STAGE);
                    if (i == 11 || i == 12) ph[i & 1] = opaque(ph[i & 1] + d);
                    if (i == 13 || i == 14) pa[i & 1] = opaque(pa[i & 1] + d);
                    if (i == 15) pb = opaque(pb + d);
                    if (i == 16) pt = opaque(pt + d);
                } else {
                    if (i == 11) slot = pin_s(slot == 2 ? 0 : slot + 1);
                    if (i == 13) left = pin_s(left - 1);
                }
                SCHED0();
            }
        }
    } while (left);
    STAMP(4);
    u32x2 ts[5], tf[5];
    {
        LAS char* xb = lds + wid * 320;
#pragma unroll
        for (int m = 0; m < 5; ++m) ts[m] = (u32x2){pack2(acc[m][3][0], acc[m][3][1]), pack2(acc[m][3][2], acc[m][3][3])};
        if (q == 2) {
#pragma unroll
            for (int m = 0; m < 5; ++m) {
                const unsigned w = h ? ts[m].y : ts[m].x;
                *(LAS u16*)(xb + m * 64 + r * 2) = (u16)w; *(LAS u16*)(xb + m * 64 + 32 + r * 2) = (u16)(w >> 16);
            }
        }
        asm volatile("" ::: "memory");
#pragma unroll
        for (int m = 0; m < 5; ++m) tf[m] = *(const LAS u32x2*)(xb + m * 64 + (r & 1) * 32 + q * 8);
    }
    {
        f32x4 rk[5]; float rq[4];
        rk[0] = *(const LAS f32x4*)(tk0);
#pragma unroll
        for (int g = 0; g < 4; ++g) { rk[g + 1] = *(const LAS f32x4*)(tk + g * 128); rq[g] = *(const LAS float*)(tq + g * 128); }
        const f32x4 zero = {0.f, 0.f, 0.f, 0.f};
        u32x4 kf[5];
#pragma unroll
        for (int m = 0; m < 5; ++m) kf[m] = (u32x4){pack2(acc[m][1][0], acc[m][1][1]), pack2(acc[m][1][2], acc[m][1][3]), ts[m].y, 0u};
        f32x4 sp[4], sc[4];
#pragma unroll
        for (int g = 0; g < 4; ++g) {
            const u32x4 qf = {pack2(acc[g + 1][0][0], acc[g + 1][0][1]), pack2(acc[g + 1][0][2], acc[g + 1][0][3]), ts[g + 1].x & qtm, 0u};
            sp[g] = MFMA(__builtin_bit_cast(bf16x8, kf[g]), __builtin_bit_cast(bf16x8, qf), zero);
            sc[g] = MFMA(__builtin_bit_cast(bf16x8, kf[g + 1]), __builtin_bit_cast(bf16x8, qf), zero);
        }
        u32x2 vf[5];
#pragma unroll
        for (int m = 0; m < 5; ++m) vf[m] = (u32x2){pack2(acc[m][2][0], acc[m][2][1]), pack2(acc[m][2][2], acc[m][2][3])};
        f32x4 rs[4], t[4]; float mx[4], sm[4];
#pragma unroll
        for (int g = 0; g < 4; ++g) {
#pragma unroll
            for (int j = 0; j < 4; ++j) { const bool pv = 4 * q + j > r; rs[g][j] = pv ? rk[g][j] : rk[g + 1][j]; t[g][j] = pv ? sp[g][j] : sc[g][j]; }
            t[g] = (t[g] * rs[g]) * (rq[g] * (0.23570226039551584f * 1.4426950408889634f)) + (g == 0 ? mb0 : mb);
            mx[g] = fmaxf(fmaxf(t[g][0], t[g][1]), fmaxf(t[g][2], t[g][3]));
        }
        xq4_max(mx);
#pragma unroll
        for (int g = 0; g < 4; ++g) {
            t[g] = t[g] - mx[g];
#pragma unroll
            for (int j = 0; j < 4; ++j) t[g][j] = __builtin_amdgcn_exp2f(t[g][j]);
            sm[g] = (t[g][0] + t[g][2]) + (t[g][1] + t[g][3]);
        }
        f32x4 om[4], ot[4];
#pragma unroll
        for (int g = 0; g < 4; ++g) {
            const f32x4 p = t[g] * rs[g];
            const unsigned p01 = pack2(p[0], p[1]), p23 = pack2(p[2], p[3]);
            const u32x4 pf = {p01 & mp0, p23 & mp1, p01 & ~mp0, p23 & ~mp1};
            om[g] = MFMA(__builtin_bit_cast(bf16x8, ((u32x4){vf[g].x, vf[g].y, vf[g + 1].x, vf[g + 1].y})), __builtin_bit_cast(bf16x8, pf), zero);
            ot[g] = MFMA(__builtin_bit_cast(bf16x8, ((u32x4){tf[g].x, tf[g].y, tf[g + 1].x, tf[g + 1].y})), __builtin_bit_cast(bf16x8, pf), zero);
        }
        xq4_sum_rcp(sm);
        const auto ro = RSRC(o + (size_t)(pm * 8 + T) * 18432);
        unsigned tw2[4];
#pragma unroll
        for (int g = 0; g < 4; ++g) {
            const f32x4 v = om[g] * sm[g];
            __builtin_amdgcn_raw_buffer_store_b64((u32x2){pack2(v[0], v[1]), pack2(v[2], v[3])}, ro, vom, g * 36864, 16);
            const f32x2 w = ot[g].xy * sm[g];
            tw2[g] = pack2(w.x, w.y);
        }
        if (q == 2) {
#pragma unroll
            for (int g = 0; g < 4; ++g) __builtin_amdgcn_raw_buffer_store_b32(tw2[g], ro, vot, g * 36864, 16);
        }
    }
    STAMP(5);
    {
        WAIT_V(0); BARX();
        STAMP(6);
        f32x4 v[5];
        proj_residual(v, rx);
        const LAS char* pw = opaque(lds + WO_OFF + lane * 16);
        const LAS char* pw2 = opaque(lds + WO_OFF + 41472 + lane * 16);
        const LAS char* ph2 = opaque(lds + WO_OFF + 4096 + q * 128 + (r & 7) * 16);
        const LAS char* ph3 = opaque(lds + WO_OFF + 41472 + 4096 + q * 128 + (r & 7) * 16);
        bf16x8 wf[90];
        auto rdw = [&](int i) {
            const int kc = i / 5, n = i % 5;
            wf[i] = n < 4 ? LDS_FRAG((kc < 9 ? pw : pw2) + (kc % 9) * 4608 + n * 1024) : LDS_FRAG((kc < 9 ? ph2 : ph3) + (kc % 9) * 4608);
        };
#pragma unroll
        for (int i = 0; i < WO_W; ++i) rdw(i);
#pragma unroll
        for (int n = 0; n < 5; ++n) v[n] = pin(v[n]);
        const auto rO = RSRC(o + (size_t)rt * 18432);
        const ProjOut po = proj_out(wl + L_XB, wl + L_STB, rt, cg, lane);
        asm volatile("" :: "s"(rO));
        WAIT_L(0);
        __builtin_amdgcn_s_setprio(REL_PRIO);
        BARX();
        STAMP(7);
        bf16x8 af[18];
        auto ldo = [&](int kc) { af[kc] = __builtin_bit_cast(bf16x8, __builtin_amdgcn_raw_buffer_load_b128(rO, lane * 16 + (kc & 3) * 1024, (kc >> 2) * 4096, 16)); };
#pragma unroll
        for (int kc = 0; kc < WO_PRE; ++kc) { ldo(kc); SCHED0(); if (kc >= PACE0 && kc + 1 < WO_PRE) PACE; SCHED0(); }
        static_for<90>([&](auto ic) {
            constexpr int i = ic.value, kc = i / 5, n = i % 5, j = WO_W + 2 * (i / 3) + i % 3;
            MFMA_T(wf[i], af[kc], v[n]);
            SCHED0();
            if (i % 3 < 2 && j < 90) rdw(j);
            if (i % WO_EV == 2 && i / WO_EV + WO_PRE < 18) ldo(i / WO_EV + WO_PRE);
            SCHED0();
        });
        asm volatile("s_nop 4" : "+v"(v[0]), "+v"(v[1]));
        STAMP(8);
        proj_store(v, po);
        __builtin_amdgcn_s_setprio(0);
    }
}

DEVI f32x2 gelu2(f32x2 v) {
    const f32x2 w = v * (v * v * -0.10294324f + -2.3022082f);
    const f32x2 d = (f32x2){__builtin_amdgcn_exp2f(w.x), __builtin_amdgcn_exp2f(w.y)} + 1.0f;
    return v * (f32x2){__builtin_amdgcn_rcpf(d.x), __builtin_amdgcn_rcpf(d.y)};
}
DEVI f32x4 gelu4(f32x4 v) { const f32x2 a = gelu2(v.xy), b = gelu2(v.zw); return (f32x4){a.x, a.y, b.x, b.y}; }
DEVI void rowsum16x8(const f32x4 a, const f32x4 b, float& o0, float& o1) {
    float t0, t1, t2, t3;
    asm volatile(
        "s_nop 1\n\t"
        "v_add_f32_dpp %2, %6, %6 row_ror:8 row_mask:0xf bank_mask:0x3\n\t"
        "v_add_f32_dpp %3, %7, %7 row_ror:8 row_mask:0xf bank_mask:0x3\n\t"
        "v_add_f32_dpp %4, %10, %10 row_ror:8 row_mask:0xf bank_mask:0x3\n\t"
        "v_add_f32_dpp %5, %11, %11 row_ror:8 row_mask:0xf bank_mask:0x3\n\t"
        "v_add_f32_dpp %2, %8, %8 row_ror:8 row_mask:0xf bank_mask:0xc\n\t"
        "v_add_f32_dpp %3, %9, %9 row_ror:8 row_mask:0xf bank_mask:0xc\n\t"
        "v_add_f32_dpp %4, %12, %12 row_ror:8 row_mask:0xf bank_mask:0xc\n\t"
        "v_add_f32_dpp %5, %13, %13 row_ror:8 row_mask:0xf bank_mask:0xc\n\t"
        "v_add_f32_dpp %0, %2, %2 row_half_mirror row_mask:0xf bank_mask:0x5\n\t"
        "s_nop 0\n\t"
        "v_add_f32_dpp %1, %4, %4 row_half_mirror row_mask:0xf bank_mask:0x5\n\t"
        "v_add_f32_dpp %0, %3, %3 row_half_mirror row_mask:0xf bank_mask:0xa\n\t"
        "v_add_f32_dpp %1, %5, %5 row_half_mirror row_mask:0xf bank_mask:0xa\n\t"
        "s_nop 0\n\t"
        "v_add_f32_dpp %0, %0, %0 quad_perm:[1,0,3,2] row_mask:0xf bank_mask:0xf\n\t"
        "v_add_f32_dpp %1, %1, %1 quad_perm:[1,0,3,2] row_mask:0xf bank_mask:0xf\n\t"
        "s_nop 0\n\t"
        "v_add_f32_dpp %0, %0, %0 quad_perm:[2,3,0,1] row_mask:0xf bank_mask:0xf\n\t"
        "v_add_f32_dpp %1, %1, %1 quad_perm:[2,3,0,1] row_mask:0xf bank_mask:0xf\n\t"
        : "=&v"(o0), "=&v"(o1), "=&v"(t0), "=&v"(t1), "=&v"(t2), "=&v"(t3)
        : "v"(a[0]), "v"(a[1]), "v"(a[2]), "v"(a[3]), "v"(b[0]), "v"(b[1]), "v"(b[2]), "v"(b[3]));
}

#define K5_PRE 9
#define RH_MAP 0
#define NET_LAYERS 2
constexpr int G_STAGE = 34816;
constexpr int K5_STAGE = 18432, K5_NS = 8;

constexpr int y_load_at(int slot) {
    int next = K5_PRE;
    for (int s = 0;; ++s) { const bool go = next < 18 + 2 * (s / 3); if (s == slot) return go ? next : -1; next += go; }
}

struct GTile { f32x4 a[4][4], e[2]; };
struct GFrag { bf16x8 a[2][4], b[2][5]; const LAS char *pa[2][2], *pb, *p8; int slot; };
DEVI void g_rd(GFrag& f, int kk, int i) {
    if (i < 4) f.a[kk][i] = LDS_FRAG(f.pa[kk][i >> 1] + (i & 1) * 2048);
    else if (i < 8) f.b[kk][i - 4] = LDS_FRAG(f.pb + (kk * 9 + i - 4) * 1024);
    else f.b[kk][4] = LDS_FRAG(f.p8 + kk * 9216);
}
template <int NS, int G, bool ROWS_IN_REGS>
DEVI void g_steps(GTile& c, GFrag& f, int n) {
    LOOP_ALIGN();
#pragma unroll 1
    do {
        int d;
#pragma unroll
        for (int kk = 0; kk < 2; ++kk) {
            WAIT_L(0);
            if (kk) BAR();
            SCHED0();
#pragma unroll
            for (int i = 0; i < 18; ++i) {
                const int nn = i >> 2, m = i & 3;
                f32x4& e = nn < 4 ? c.a[m][nn] : c.e[m & 1];
                e = ROWS_IN_REGS ? MFMA(f.a[kk][m], f.b[kk][nn], e) : MFMA(f.b[kk][nn], f.a[kk][m], e);
                SCHED0();
                if (i < 9) g_rd(f, kk ^ 1, i);
                if (kk == 0) {
                    if (i == 9) d = pin_s(f.slot == NS - 1 ? -(NS - 1) * G : G);
                    if (i >= 10 && i < 14) f.pa[(i - 10) >> 1][i & 1] = opaque(f.pa[(i - 10) >> 1][i & 1] + d);
                    if (i == 14) f.pb = opaque(f.pb + d);
                    if (i == 15) f.p8 = opaque(f.p8 + d);
                } else {
                    if (i == 10) f.slot = pin_s(f.slot == NS - 1 ? 0 : f.slot + 1);
                    if (i == 12) n = pin_s(n - 1);
                }
                SCHED0();
            }
        }
    } while (n);
}
DEVI void keep_s(__amdgpu_buffer_rsrc_t r) { asm volatile("" :: "s"(r)); }
DEVI void keep4(const f32x4 (&c)[4]) { asm volatile("" :: "v"(c[0]), "v"(c[1]), "v"(c[2]), "v"(c[3])); }
template <int D, int DB, bool FIRST, class HOOK>
DEVI void g_step_u(GTile& c, GFrag& f, const f32x4 (&c0)[4], HOOK&& hook) {
    static_for<36>([&](auto ic) {
        constexpr int kk = ic.value / 18, i = ic.value % 18, n = i >> 2, m = i & 3;
        if (i == 0) { WAIT_L(0); if (kk) BAR(); SCHED0(); }
        f32x4& e = n < 4 ? c.a[m][n] : c.e[m & 1];
        if (FIRST && kk == 0) asm volatile("v_mfma_f32_16x16x32_bf16 %0, %1, %2, %3" : "=&v"(e) : "v"(f.b[kk][n]), "v"(f.a[kk][m]), "v"(c0[m]));
        else MFMA_T(f.b[kk][n], f.a[kk][m], e);
        SCHED0();
        if (i < 9) g_rd(f, kk ^ 1, i);
        if (kk == 0) {
            if (i >= 10 && i < 14) f.pa[(i - 10) >> 1][i & 1] = opaque(f.pa[(i - 10) >> 1][i & 1] + D);
            if (i == 14) f.pb = opaque(f.pb + DB);
            if (i == 15) f.p8 = opaque(f.p8 + DB);
        }
        if (FIRST && ic.value == 20) keep4(c0);
        hook(ic);
        SCHED0();
    });
}
#define MF(d, a, b) "v_mfma_f32_16x16x32_bf16 " d ", " a ", " b ", " d "\n\t"
template <int D, int DB>
DEVI void g_step_gelu(GTile& c, GFrag& f, f32x4& z, float r, float c1) {
#pragma unroll
    for (int kk = 0; kk < 2; ++kk) {
        float va = z[2 * kk], vb = z[2 * kk + 1], ta, tb;
        WAIT_L(0); if (kk) BAR(); SCHED0();
#pragma unroll
        for (int i = 0; i < 9; ++i) {
            MFMA_T(f.b[kk][i >> 2], f.a[kk][i & 3], c.a[i & 3][i >> 2]);
            if (i == 5) asm volatile("v_mul_f32 %0, %0, %1" : "+v"(va) : "v"(r));
            if (i == 6) asm volatile("v_mul_f32 %0, %0, %1" : "+v"(vb) : "v"(r));
            if (i == 7) asm volatile("v_mul_f32 %0, %1, %1" : "=v"(ta) : "v"(va));
            if (i == 8) asm volatile("v_mul_f32 %0, %1, %1" : "=v"(tb) : "v"(vb));
            SCHED0();
            g_rd(f, kk ^ 1, i);
            SCHED0();
        }
        asm volatile(MF("%0", "%5", "%6") "v_fmaak_f32 %3, %3, %11, 0xc0135761\n\tv_fmaak_f32 %4, %4, %11, 0xc0135761\n\t"
                     MF("%1", "%5", "%7") "v_mul_f32 %3, %3, %9\n\tv_mul_f32 %4, %4, %10\n\t" MF("%2", "%5", "%8") "v_exp_f32 %3, %3"
                     : "+v"(c.a[1][2]), "+v"(c.a[2][2]), "+v"(c.a[3][2]), "+v"(ta), "+v"(tb)
                     : "v"(f.b[kk][2]), "v"(f.a[kk][1]), "v"(f.a[kk][2]), "v"(f.a[kk][3]), "v"(va), "v"(vb), "v"(c1));
        SCHED0(); f.pa[kk][0] = opaque(f.pa[kk][0] + D); SCHED0();
        asm volatile(MF("%0", "%6", "%7") "v_exp_f32 %5, %5\n\t" MF("%1", "%6", "%8") "v_add_f32 %4, 1.0, %4\n\tv_add_f32 %5, 1.0, %5\n\t"
                     MF("%2", "%6", "%9") "v_rcp_f32 %4, %4\n\t" MF("%3", "%6", "%10") "v_rcp_f32 %5, %5"
                     : "+v"(c.a[0][3]), "+v"(c.a[1][3]), "+v"(c.a[2][3]), "+v"(c.a[3][3]), "+v"(ta), "+v"(tb)
                     : "v"(f.b[kk][3]), "v"(f.a[kk][0]), "v"(f.a[kk][1]), "v"(f.a[kk][2]), "v"(f.a[kk][3]));
        SCHED0(); f.pa[kk][1] = opaque(f.pa[kk][1] + D); SCHED0();
        asm volatile(MF("%0", "%4", "%5") "v_mul_f32 %2, %2, %7\n\tv_mul_f32 %3, %3, %8\n\t" MF("%1", "%4", "%6")
                     : "+v"(c.e[0]), "+v"(c.e[1]), "+v"(va), "+v"(vb) : "v"(f.b[kk][4]), "v"(f.a[kk][0]), "v"(f.a[kk][1]), "v"(ta), "v"(tb));
        SCHED0();
        if (kk == 0) { f.pb = opaque(f.pb + DB); f.p8 = opaque(f.p8 + DB); }
        SCHED0();
        z[2 * kk] = va; z[2 * kk + 1] = vb;
    }
}
DEVI void g_fill(GTile& c, const float (&e)[4]) {
#pragma unroll
    for (int m = 0; m < 4; ++m) {
#pragma unroll
        for (int n = 0; n < 4; ++n) c.a[m][n] = pin((f32x4){e[m], e[m], e[m], e[m]});
        if (m < 2) c.e[m] = pin((f32x4){e[m], e[m], e[m], e[m]});
    }
}

#undef STAMP_SEC
#define STAMP_SEC 1
DEVI void gmlp_body(LAS char* lds, int wm, int wn, int pm, int cg, char* z2t, char* y, const u16* __restrict__ bsp STAMP_PARAM) {
    const int mlo = wm * 4 + wn * 2, mhi = wm * 4 + (1 - wn) * 2;
    const float zero4[4] = {0.f, 0.f, 0.f, 0.f};
    GTile z2, z1, acc;
    GFrag f;
    {
        const int lane = lane_id_fresh();
#pragma unroll
        for (int kk = 0; kk < 2; ++kk) { f.pa[kk][0] = lds + mlo * 2048 + kk * 1024 + lane * 16; f.pa[kk][1] = lds + mhi * 2048 + kk * 1024 + lane * 16; }
        f.pb = lds + 16384 + wn * 4096 + lane * 16; f.p8 = lds + 16384 + 8192 + lane * 16; f.slot = 0;
    }
    g_fill(z2, zero4); g_fill(z1, zero4);
#pragma unroll
    for (int i = 0; i < 4; ++i) f.pa[i >> 1][i & 1] = opaque(f.pa[i >> 1][i & 1]);
    f.pb = opaque(f.pb); f.p8 = opaque(f.p8);
    SCHED0();
    WAIT_V(0); BARX();
    BAR();
    BARX();
    STAMP(2);
#pragma unroll
    for (int i = 0; i < 9; ++i) g_rd(f, 0, i);
    g_steps<4, G_STAGE, true>(z2, f, 9);
    STAMP(3);
    {
        const int lane = lane_id_fresh(), r = lane & 15, q = lane >> 4;
        f32x4 ra[4];
#pragma unroll
        for (int m = 0; m < 4; ++m) ra[m] = *(const LAS f32x4*)(lds + G_TAB + ((m < 2 ? mlo : mhi) + (m & 1)) * 64 + q * 16);
        SCHED0();
#pragma unroll
        for (int m = 0; m < 4; ++m) {
            f32x4 s1, s2;
#pragma unroll
            for (int n = 0; n < 4; ++n) {
                const f32x4 g = gelu4(z2.a[m][n] * ra[m]);
                z2.a[m][n] = g;
                if (n == 0) { s1 = g; s2 = g * g; } else { s1 += g; s2 += g * g; }
            }
            if (m < 2) { const f32x4 g = gelu4(z2.e[m] * ra[m]); z2.e[m] = g; s1 += g; s2 += g * g; }
            float u0, u1;
            rowsum16x8(s1, s2, u0, u1);
            if ((r & 3) == 0) *(LAS f32x2*)(lds + G_PART + wn * 1024 + ((m < 2 ? mlo : mhi) + (m & 1)) * 128 + (q * 4 + (r >> 2)) * 8) = (f32x2){u0, u1};
            SCHED0();
        }
    }
    WAIT_L0(); BAR();
    STAMP(4);
    g_steps<4, G_STAGE, false>(z1, f, 4);
    unsigned b0, b1;
    f32x4 c0[4];
    g_step_u<G_STAGE, G_STAGE, false>(z1, f, c0, [&](auto ic) {
        if (ic.value != 18) return;
        STAMP(5);
        const int lane = lane_id_fresh(), q = lane >> 4;
        b0 = __builtin_amdgcn_raw_buffer_load_b32(RSRC(bsp), (lane & 15) * 4, pm * 256 + mlo * 32, 0);
        b1 = __builtin_amdgcn_raw_buffer_load_b32(RSRC(bsp), (lane & 15) * 4, pm * 256 + mhi * 32, 0);
#pragma unroll
        for (int g = 0; g < 2; ++g) {
            const auto rz2 = RSRC(z2t + (size_t)pm * 589824 + cg * 36864 + ((g ? mhi : mlo) >> 1) * 9216);
            const LAS char* tab = lds + G_TAB + (g ? mhi : mlo) * 64 + q * 16;
            LAS char* own = lds + (wm ? G_OWN : G_STAGE + 16384) + (g ^ wn) * 9216 + lane * 16;
            f32x4 ra[2], rb[2];
#pragma unroll
            for (int T = 0; T < 2; ++T) { ra[T] = *(const LAS f32x4*)(tab + 512 + T * 64); rb[T] = *(const LAS f32x4*)(tab + 1024 + T * 64); }
#pragma unroll
            for (int n = 0; n < (g ? 4 : 5); ++n) {
                const f32x4 v0 = (n < 4 ? z2.a[2 * g][n & 3] : z2.e[0]) * ra[0] + rb[0], v1 = (n < 4 ? z2.a[2 * g + 1][n & 3] : z2.e[1]) * ra[1] + rb[1];
                const u32x4 o = {pack2(v0[0], v1[0]), pack2(v0[1], v1[1]), pack2(v0[2], v1[2]), pack2(v0[3], v1[3])};
                ST16(rz2, lane * 16, n < 4 ? wn * 4096 + n * 1024 : 8192, o, 16);
                *(LAS u32x4*)(own + (n < 4 ? wn * 4096 + n * 1024 : 8192)) = o;
            }
        }
        STAMP(6);
    });
    static_for<3>([&](auto sc) {
        constexpr int D = sc.value == 0 ? G_STAGE : sc.value == 1 ? -3 * G_STAGE : 2 * G_STAGE;
        g_step_u<D, D, false>(z1, f, c0, [&](auto ic) { if (ic.value == 17 && sc.value == 1) WAIT_V(0); });
    });
    float ra[4], c1;
    {
        int lane, da[2];
        g_step_u<-G_STAGE, -G_STAGE, false>(z1, f, c0, [&](auto ic) {
            constexpr int i = ic.value;
            if (i == 9) lane = pin(lane_id_fresh());
            if (i == 14 || i == 15) da[i & 1] = pin((lane & 15) * 128 + (((4 * (i & 1) + (lane >> 4)) ^ (lane & 7)) * 16) - lane * 16 - (i & 1) * 1024);
            if (i == 16 || i == 17) f.pa[0][i & 1] = opaque(f.pa[0][i & 1] + da[0]);
            if (i == 27 || i == 28) f.pa[1][i & 1] = opaque(f.pa[1][i & 1] + da[1]);
            if (i >= 29 && i < 33) {
                constexpr int m = (i - 29) & 3;
                const float e = m & 1 ? bfhi(m < 2 ? b0 : b1) : bflo(m < 2 ? b0 : b1);
                c0[m] = pin((f32x4){e, e, e, e});
            }
            if (i == 33) {
#pragma unroll
                for (int m = 0; m < 4; ++m) ra[m] = *(const LAS float*)(lds + G_TAB + ((m < 2 ? mlo : mhi) + (m & 1)) * 64 + (lane & 15) * 4);
                c1 = pin(-0.10294324f);
            }
        });
    }
    STAMP(7);
    g_step_u<2 * G_STAGE, G_OWN - G_STAGE - 16384, true>(acc, f, c0, [](auto) {});
    static_for<18>([&](auto sc) {
        constexpr int st = sc.value, m = st < 16 ? st >> 2 : st & 1;
        constexpr int D = st == 0 ? -3 * G_STAGE : st % 3 == 1 ? 2 * G_STAGE : -G_STAGE;
        g_step_gelu<D, st == 0 ? 16384 - G_OWN : D>(acc, f, st < 16 ? z1.a[m][st & 3] : z1.e[m], ra[m], c1);
    });
    f.slot = 1;
    STAMP(9);
    g_steps<3, -G_STAGE, false>(acc, f, 12);
    __amdgpu_buffer_rsrc_t ry;
    unsigned vy[4], ve[2];
    int lane;
    u32x4 o = {0u, 0u, 0u, 0u};
    auto y_part = [&](auto jc) {
        constexpr int j = jc.value, m = j / 10, k = j % 10, e = (k & 1) * 2;
        const f32x4 &a = k < 8 ? acc.a[m][k >> 1] : acc.e[m & 1], &z = k < 8 ? z1.a[m][k >> 1] : z1.e[m & 1];
        float t0, t1;
        asm volatile("v_mul_f32 %1, %3, %5\n\tv_mul_f32 %2, %4, %6\n\tv_cvt_pk_bf16_f32 %0, %1, %2" : "=v"(o[k & 3]), "=&v"(t0), "=&v"(t1) : "v"(a[e]), "v"(a[e + 1]), "v"(z[e]), "v"(z[e + 1]));
        if (k == 3 || k == 7) ST16(ry, vy[m] + (k >> 2) * 1024, 0, o, 16);
        if (k == 9) ST8(ry, ve[m & 1], 0, ((u32x2){o[0], o[1]}), 16);
    };
    WAIT_L(0);
    SCHED0();
    static_for<36>([&](auto ic) {
        constexpr int i = ic.value, kk = i < 14 ? 0 : i < 28 ? 1 : i >= 32, t = i < 28 ? i % 14 : 14 + (i & 3), m = t < 10 ? t / 5 : 2 + (t - 10) / 4, n = t < 10 ? t % 5 : (t - 10) & 3;
        if (i == 14) { WAIT_L(0); BAR(); SCHED0(); }
        f32x4& e = n < 4 ? acc.a[m][n & 3] : acc.e[m & 1];
        MFMA_T(f.b[kk][n], f.a[kk][m], e);
        SCHED0();
        if (i < 9) g_rd(f, 1, i);
        if (i == 10) { ry = RSRC(own_s(y) + (size_t)pin_s(pm * 8 * 73728 + cg * 4608)); keep_s(ry); }
        if (i == 12) { lane = pin(lane_id_fresh()); vy[0] = pin((unsigned)(mlo * 73728 + wn * 2048 + lane * 16)); }
        if (i == 14) ve[0] = pin((unsigned)(mlo * 73728 + 4096 + (lane >> 5) * 256 + (lane & 15) * 16 + ((lane >> 4) & 1) * 8));
        if (i == 15) { vy[1] = pin(vy[0] + 73728); ve[1] = pin(ve[0] + 73728); }
        if (i == 16) { vy[2] = pin(vy[0] + (mhi - mlo) * 73728); vy[3] = pin(vy[2] + 73728); asm volatile("s_nop 4"); }
        if constexpr (i >= 17) y_part(IC<i - 17>{});
        SCHED0();
    });
    STAMP(10);
    y_part(IC<19>{});
    SCHED0();
#pragma unroll
    for (int i = 0; i < 4; ++i) {
        const int m = 2 + (i >> 1), p = i & 1;
        if (i == 2) asm volatile("" : "+v"(acc.a[3][0]), "+v"(acc.a[3][1]), "+v"(acc.a[3][2]), "+v"(acc.a[3][3]));
        const f32x4 a = acc.a[m][2 * p] * z1.a[m][2 * p], c = acc.a[m][2 * p + 1] * z1.a[m][2 * p + 1];
        ST16(ry, vy[m] + p * 1024, 0, ((u32x4){pack2(a[0], a[1]), pack2(a[2], a[3]), pack2(c[0], c[1]), pack2(c[2], c[3])}), 16);
        SCHED0();
    }
}

#define NET_IDS \
    int b = __builtin_amdgcn_readfirstlane(blockIdx.x), wid = wid8 & 3; \
    asm volatile("" : "+s"(b), "+s"(wid));     \
    const int xcd = b & 7, idx = b >> 3; \
    const int pa = (xcd >> 1) * 4 + (idx & 3), hp = (xcd & 1) * 8 + (idx >> 2); \
    const int pm = (xcd >> 2) * 8 + (idx & 7), cg = (xcd & 3) * 4 + (idx >> 3); \
    const int rh = RH_MAP ? xcd * 4 + (idx & 3) : (xcd >> 1) * 8 + (idx & 7), c5 = RH_MAP ? idx >> 2 : (xcd & 1) * 4 + (idx >> 3); \
    const int lead = (idx + (idx >> 3)) & 3; \
    auto kord = [&](int t, int n) { \
        const int k = t + ((int)(0x1F000000u << (((t - lead) & 3) * 4)) >> 28); \
        return (unsigned)k < (unsigned)n ? k : t; \
    }; \
    auto kord5 = [&](int t) { \
        const int k = t + ((int)(0x1F000000u << (((t - (RH_MAP ? c5 : lead)) & 3) * 4)) >> 28); \
        return (unsigned)k < 18u ? k : t; \
    };

typedef short s16x4 __attribute__((ext_vector_type(4)));
DEVI u32x4 tr_frag(const LAS char* p, int four_rows) {
    const u32x2 a = __builtin_bit_cast(u32x2, __builtin_amdgcn_ds_read_tr16_b64_v4i16((LAS s16x4*)p));
    const u32x2 b = __builtin_bit_cast(u32x2, __builtin_amdgcn_ds_read_tr16_b64_v4i16((LAS s16x4*)(p + four_rows)));
    return (u32x4){a.x, a.y, b.x, b.y};
}
DEVI float dot2(unsigned v, unsigned w, float acc) { return __builtin_amdgcn_fdot2_f32_bf16(__builtin_bit_cast(bf16x2_t, v), __builtin_bit_cast(bf16x2_t, w), acc, false); }
DEVI float sum2(unsigned v, float acc) { return dot2(v, 0x3f803f80u, acc); }
struct Sel { unsigned lo, hi, nlo, nhi; };
DEVI Sel make_sel() { return {(unsigned)pin_s(0x00003f80), (unsigned)pin_s(0x3f800000), (unsigned)pin_s(0x0000bf80), (unsigned)pin_s((int)0xbf800000u)}; }
DEVI unsigned centre2(unsigned v, f32x2& e, float mean, const Sel& k) {
    const float a = dot2(k.lo, v, e.x), b = dot2(k.hi, v, e.y);
    const unsigned r = pack2(a, b);
    e = (f32x2){dot2(k.nlo, r, a), dot2(k.nhi, r, b)} - mean;
    return r;
}
typedef u32x4 Retile[9];
template <int PITCH>
DEVI void retile_out_make(Retile& o, const LAS char* img, int lane) {
    const int c = lane & 15, q = lane >> 4, p = lane & 3;
    const LAS char* src = img + (q * 8 + (c >> 2)) * PITCH;
#pragma unroll
    for (int i = 0; i < 8; ++i) o[i] = tr_frag(src + p * 16 + (i >> 2) * 144 + ((i >> 1) & 1) * 64 + (i & 1) * 8, 4 * PITCH);
    o[8] = tr_frag(src + 128 + (p >> 1) * 144 + (p & 1) * 8, 4 * PITCH);
}
DEVI void retile_out_store(const Retile& o, char* dst, int nkc, int kc, int w, int lane) {
    const int c = lane & 15, q = lane >> 4;
    const auto rd = RSRC(dst + (size_t)(2 * w * nkc + kc) * 4608);
#pragma unroll
    for (int i = 0; i < 8; ++i) ST16(rd, lane * 16 + (i & 3) * 1024, (i >> 2) * nkc * 4608, o[i], 16);
    ST16(rd, (c >> 3) * nkc * 4608 + 4096 + q * 128 + (c & 7) * 16, 0, o[8], 16);
}

DEVI float dpp_ror(float v, int n) { return n == 8 ? __builtin_amdgcn_update_dpp(0.f, v, 0x128, 0xf, 0xf, true) : n == 4 ? __builtin_amdgcn_update_dpp(0.f, v, 0x124, 0xf, 0xf, true)
                                          : n == 2 ? __builtin_amdgcn_update_dpp(0.f, v, 0x122, 0xf, 0xf, true) : __builtin_amdgcn_update_dpp(0.f, v, 0x121, 0xf, 0xf, true); }
DEVI unsigned long long epoch_of(const char* ws) {
    SCHED0();
    const unsigned long long ep = *(const __attribute__((address_space(4))) unsigned long long*)(ws + pin_s((int)OFF_EP)) + 1ull;
    asm volatile("" :: "s"(ep));
    return ep;
}
DEVI void prologue_done(LAS char* lds, char* ws, unsigned long long ep, int b, int lane) {
    if (lds_arrive(lds + G_WORD + 8) == 7) ST16(RSRC(ws), lane ? 0x80000000u : (unsigned)OFF_FL + b * 16, 0, ((u32x4){(unsigned)ep, (unsigned)(ep >> 32), 0u, 0u}), 16);
}
DEVI void centre_frag(u32x4& f, f32x2 (&er)[4], float mean, const Sel& k) {
#pragma unroll
    for (int e = 0; e < 4; ++e) f[e] = centre2(f[e], er[e], mean, k);
}
template <int N>
DEVI void column_means(LAS char* lds, LAS char* cnt, int me, int lane, float a, float b, float& ma, float& mb) {
    LAS float* red = (LAS float*)(lds + G_PART);
    auto x = __builtin_amdgcn_permlane16_swap(__float_as_uint(a), __float_as_uint(b), false, false);
    red[me * 64 + lane] = __uint_as_float(x[0]) + __uint_as_float(x[1]);
    if (lds_arrive(cnt) < N - 1) lds_count_wait(cnt, N);
    float t = 0.f;
#pragma unroll
    for (int i = 0; i < N; ++i) t += red[i * 64 + lane];
    x = __builtin_amdgcn_permlane32_swap(__float_as_uint(t), __float_as_uint(t), false, false);
    t = (__uint_as_float(x[0]) + __uint_as_float(x[1])) * (1.0f / 576.0f);
    x = __builtin_amdgcn_permlane16_swap(__float_as_uint(t), __float_as_uint(t), false, false);
    ma = __uint_as_float(x[0]); mb = __uint_as_float(x[1]);
}
template <class ARGS>
DEVI unsigned long long prologue(LAS char* lds, char*& ws, const char*& x0, const char*& wqkv, const u16*& wo, const u16*& w_in, int b, int pa, int wid8, ARGS&& args PST_PARAM) {
    PST_DECL;
    const int lane = lane_id_fresh(), c = lane & 15, q = lane >> 4, w = wid8 & 3;
    const int jw = w - 1 + 3 * (((wid8 >> 2) ^ (b >> 3)) & 1);
    const bool wjob = b >= 16 && b < 112 && w;
    const int uo = (wid8 - 1 - (wid8 >> 2)) * 16 + b;
    const bool ojob = b < 16 && w && uo < 72;
    const unsigned l0 = (unsigned)(size_t)lds;
    LAS char* img = lds + 2 * K1_STAGE;
    const int uw = b - 16, head = (uw & 7) * 4 + ((uw >> 3) & 3), part = uw >> 5, hp = head >> 1, odd = head & 1;
    const int c0 = part * 1152 + hp * 72 + odd * 40;
    {
        const int so = pin_s(jw * 96 * 3456 + (c0 & ~15));
        const unsigned v = (lane & 3) < ((head & 3) == 3 ? 2 : 3) ? (lane >> 2) * 3456 + (lane & 3) * 16 : 0x80000000u, d = 16 * 3456 - 1024;
        const unsigned v0 = pin(v), v1 = pin(v + d), v2 = pin(v + 2 * d), v3 = pin(v + 3 * d);
        const int e = ((b - 112) >> 3) * 192 + wid8 * 64 + lane, row = (e * 10923) >> 16, r = e - row * 6;
        const unsigned vp = pin((unsigned)(row * 3456 + (r >> 1) * 1152 + (r & 1) * 128));
        const unsigned la = pin_s(l0 + 2 * K1_STAGE + jw * 6144);
        args(0);
        if (b >= 112 && wid8 < 3) dma1(l0 + G_OWN, RSRC(wqkv), (b & 7) * 128, vp);
        if (wjob) { dma4<false>(la, RSRC(wqkv), so, v0, v1, v2, v3); dma2<false>(la + 4096, RSRC(wqkv), so + 64 * 3456, v0, v1); }
        args(1);
    }
    if (ojob) {
        const auto rs = RSRC(wo + (uo & 3) * 144);
#pragma unroll
        for (int j = 0; j < 9; ++j) {
            const int i = lane + 64 * j, rl = (i * 3641) >> 16, kp = (uo >> 2) * 32 + rl;
            const int row = kp < 512 ? 18 * (kp >> 4) + (kp & 15) + 2 * ((kp >> 4) & 1) : 36 * ((kp - 512) >> 2) + 16 + (kp & 3);
            dma1(l0 + 2 * K1_STAGE + wid8 * 9216 + j * 1024, rs, 0, (unsigned)(row * 1152 + (i - rl * 18) * 16));
        }
    }
    if (wid8 == 4) { lds_word_set(lds + G_WORD + c * 4, 0u); WAIT_L0(); }
    BAR();
    unsigned long long ep;
    if (wid8 == 4) ep = epoch_of(ws);
    if (wid8 == 4 && b >= 16 && b < 112) lds_count_wait(lds + G_WORD + 12, 12);
    if (wid8 > 4 && b >= 112) {
        __builtin_amdgcn_s_sleep(I_SLEEP);
        const unsigned v = (lane >> 2) * 9216 + (lane & 3) * 16, d = 16 * 9216 - 1024;
#pragma unroll
        for (int i = 0; i < 3; ++i) dma4<false>(l0 + 3 * K1_STAGE + (w - 1) * 12288 + i * 4096, RSRC(w_in + win_strip(b) * 32), (win_third(b, w) * 192 + i * 64) * 9216, v, v + d, v + 2 * d, v + 3 * d);
    }
    if (wid8 == 4) {
        AttnX ax = attn_x(x0, x0, 0, pa, 0, lane);
#pragma unroll
        for (int s = 0; s < 2; ++s)
#pragma unroll
            for (int i = 0; i < 4; ++i) { ax.oA = (pa * 8 + 2 * i) * 18432; attn_issueA(lds, ax, i, s, s); }
    }
    u32x4 xv[9];
    if (wid8 == 0) {
        const unsigned v = (lane >> 3) * 2304 + (lane & 7) * 16;
        const auto rx = RSRC(x0 + (size_t)slot2row(b * 8) * 1152);
#pragma unroll
        for (int i = 0; i < 9; ++i) asm volatile("s_nop 4\n\tbuffer_load_dwordx4 %0, %1, %2, 0 offen offset:%3" : "+v"(xv[i]) : "v"(v), "s"(rx), "i"(i * 128) : "memory");
        const auto rw = RSRC(ws);
        const u32x4 ones = {~0u, ~0u, ~0u, ~0u}, zero = {0u, 0u, 0u, 0u};
        const unsigned vo = lane * 16;
        ST16(rw, vo, (unsigned)OFF_ZST + b * 2048, ones, 16); ST16(rw, vo, (unsigned)OFF_ZST + b * 2048 + 1024, ones, 16);
        ST16(rw, vo, (unsigned)OFF_CNT + b * 2560, zero, 16); ST16(rw, vo, (unsigned)OFF_CNT + b * 2560 + 1024, zero, 16);
        ST16(rw, vo, lane < 32 ? (unsigned)OFF_CNT + b * 2560 + 2048 : (unsigned)(OFF_L0 + L_STB) + b * 512 - 512, zero, 16);
        ST16(rw, vo, (unsigned)(OFF_L0 + L_O) + (b >> 1) * 18432 + 16384 + (b & 1) * 1024, zero, 16);
        ST16(rw, b < 12 ? vo : 0x80000000u, (unsigned)OFF_FL2 + b * 1024, zero, 16);
    }
    PST(1);
    if (wjob) {
        const Sel k = make_sel();
        const LAS char* s0 = opaque(img + jw * 6144 + (q * 8 + (c >> 2)) * 64 + (c0 & 15) + (c & 3) * 8);
        const LAS char* s1 = opaque(img + jw * 6144 + (q * 8 + (c >> 2)) * 64 + (c0 & 15) + 32 + min(c & 3, 2) * 2048);
        const auto rd = RSRC(ws + OFF_TWQKV + (size_t)(hp * 18 + jw * 3) * 6144 + (3 * odd + part) * 1024), rd2 = RSRC(ws + OFF_TWT + (size_t)((hp * 3 + part) * 18 + jw * 3) * 256);
        const unsigned vd2 = pin(c < 12 && !odd ? (unsigned)((c >> 2) * 256 + q * 64 + (c & 3) * 16) : 0x80000000u);
        asm volatile("" :: "s"(rd), "s"(rd2));
        WAIT_V(0);
        PST(2);
        u32x4 f[4];
        float sm[4] = {0.f, 0.f, 0.f, 0.f}, m0, m1;
#pragma unroll
        for (int i = 0; i < 4; ++i) f[i] = tr_frag(i < 3 ? s0 + i * 2048 : s1, 256);
#pragma unroll
        for (int e = 0; e < 16; ++e) sm[(e >= 12) * 2 + (e & 1)] = sum2(f[e >> 2][e & 3], sm[(e >= 12) * 2 + (e & 1)]);
        sm[2] = c < 12 ? sm[2] + sm[3] : 0.f;
        sm[2] += dpp_ror(sm[2], 4); sm[2] += dpp_ror(sm[2], 8);
        column_means<6>(lds, lds + G_WORD + 12, jw, lane, sm[0] + sm[1], sm[2], m0, m1);
        PST(3);
        f32x2 e0[4] = {{-m0, -m0}, {-m0, -m0}, {-m0, -m0}, {-m0, -m0}}, e1[4] = {{-m1, -m1}, {-m1, -m1}, {-m1, -m1}, {-m1, -m1}};
#pragma unroll
        for (int i = 0; i < 3; ++i) { centre_frag(f[i], e0, m0, k); ST16(rd, lane * 16, i * 6144, f[i], 16); }
        centre_frag(f[3], e1, m1, k); ST16(rd2, vd2, 0, f[3], 16);
        PST(4);
        lds_arrive(lds + G_WORD + 12);
    }
    if (wid8 != 4) ep = epoch_of(ws);
    {
        if (wjob) {
            const unsigned fo = pin((unsigned)OFF_FW + ((lane >> 3) * 16 + hp) * 1024 + (3 * odd + part) * 128 + (lane & 7) * 16);
            WAIT_V(0);
            PST(5);
            if (lds_arrive(lds + G_WORD + 4) == 5) {
                const u32x4 e4 = {(unsigned)ep, (unsigned)(ep >> 32), (unsigned)ep, (unsigned)(ep >> 32)};
                ST16(RSRC(ws), fo, 0, e4, 16); ST16(RSRC(ws), fo + 131072, 0, e4, 16);
            }
        }
    }
    if (wid8 == 0) {
        WAIT_V(7);
        asm volatile("" : "+v"(xv[0]), "+v"(xv[1]), "+v"(xv[2]), "+v"(xv[3]), "+v"(xv[4]), "+v"(xv[5]), "+v"(xv[6]), "+v"(xv[7]), "+v"(xv[8]));
        float s = 0.f, q2 = 0.f;
#pragma unroll
        for (int e = 0; e < 36; ++e) { s = sum2(xv[e >> 2][e & 3], s); q2 = dot2(xv[e >> 2][e & 3], xv[e >> 2][e & 3], q2); }
        ST8(RSRC(ws + OFF_L0 + L_STA + (size_t)b * 512), lane * 8, 0, ((u32x2){__float_as_uint(s), __float_as_uint(q2)}), 16);
    }
    if (ojob) {
        WAIT_V(0);
        Retile rt;
        retile_out_make<288>(rt, img + wid8 * 9216, lane); retile_out_store(rt, ws + OFF_TWO, 18, uo >> 2, uo & 3, lane);
    }
    PST(7);
    PST_DUMP(wid8 == 0 || wid8 == 4 || wid8 == 5, wid8 ? wid8 : 2);
    return ep;
}

__global__ __launch_bounds__(512, 2) TRACE_ATTR void k_net(char* ws, const u16* wsp, const u16* bsp, char* out, const char* x0, const char* wqkv, const u16* wo,
                                                             const u16* w_in, const u16* w_out DBG_ARG) {
    extern __shared__ __attribute__((aligned(16))) char smem[];
    LAS char* lds = (LAS char*)smem;
    asm volatile("gm_code_begin:");
    if (blockIdx.x < 16) {
        unsigned long long pc; int d0, len;
        asm volatile("s_getpc_b64 %0\n1:\n\ts_mov_b32 %1, gm_code_begin-1b\n\ts_mov_b32 %2, gm_code_end1-gm_code_begin\n\ts_max_i32 %2, %2, gm_code_end2-gm_code_begin"
                     : "=s"(pc), "=s"(d0), "=s"(len) :: "scc");
        const auto rs = __builtin_amdgcn_make_buffer_rsrc((void*)((pc + d0) & ~15ull), 0, len, 0x00020000);
        dma1((unsigned)(size_t)lds + G_OWN, rs, 0, (threadIdx.x * 2 + (blockIdx.x >> 3)) * 128);
    }
    STAMP_DECL;
    const int wid8 = __builtin_amdgcn_readfirstlane(threadIdx.x >> 6);
    unsigned long long ep;
    {
        NET_IDS
        ep = prologue(lds, ws, x0, wqkv, wo, w_in, b, pa, wid8, [&](int part) {
            if (part == 0) { asm volatile("" :: "s"(wqkv), "s"(w_in), "s"(w_out)); return; }
            ws = own_s(ws); wsp = own_s(wsp); bsp = own_s(bsp); out = own_s(out); x0 = own_s(x0); wo = own_s(wo); w_out = own_s(w_out);
        } PST_FWD);
    }
    const char *twin = ws + OFF_TWIN, *twout = ws + OFF_TWOUT;
    if (wid8 >= 4) {
        u32x4 wr[5][9];
#pragma unroll 1
      for (int layer = 0; layer < NET_LAYERS; ++layer) {
        NET_IDS
        TRACE_BEGIN;
        char* wl = ws + OFF_L0 + layer * SZ_L;
        const unsigned fl = layer * F_LAYER;
        const char *xin = wl + L_XB, *st = wl + L_STB, *z2t = wl + (layer ? L_Y : L_Z2T);
        unsigned long long* zst = (unsigned long long*)(ws + OFF_ZST + layer * SZ_ZST);
        const int lane = lane_id_fresh(), l3 = lane >> 3;
        const unsigned vo = lane * 16;
        const unsigned l0 = (unsigned)(size_t)lds, lA = l0 + wid * 4096, lB = l0 + 16384 + wid * 4608;
        const auto rX = RSRC(xin + (size_t)(pm * 8 + 2 * wid) * 18432), rI = RSRC(twin + (size_t)cg * (18 * 18432) + wid * 4608);
        auto issue1B = [&](int g, int slot) { const int k = kord(g < 9 ? g : (g < 13 ? g - 4 : g - 13), 9); dma5<true>(lB + slot * G_STAGE, rI, (g < 9 ? 9 + k : k) * 18432, vo); };
        auto issue1A = [&](int g, int slot) { dma4<true>(lA + slot * G_STAGE, rX, kord(g < 9 ? g : g - 13, 9) * 2048, vo, vo, vo + 16384, vo + 16384); };
        auto issue1 = [&](int g, int slot) { issue1B(g, slot); issue1A(g, slot); };
        const auto rA = RSRC((const char*)wsp + (size_t)(pm * 128 + wid * 32) * 4096), rB = RSRC(z2t + cg * 36864 + wid * 4608);
        const unsigned voA = (unsigned)(2 * l3 * 4096 + ((lane & 7) ^ l3) * 16);
        auto issueB = [&](int k, int slot) { dma5<true>(lB + slot * G_STAGE, rB, (k >> 1) * 589824 + (k & 1) * 18432, vo); };
        const auto rW = RSRC(twout + (size_t)c5 * (72 * 4608));
        auto issue5 = [&](int t) { dma5<true>(l0 + ((t + 6) & 7) * K5_STAGE + wid * 4608, rW, kord5(t) * K5_STAGE + wid * 4608, vo); };
        auto load5 = [&](int p, int i) {
            const int t0 = p ? 2 * p + 6 : 4, ow = kord5(t0 + i) * K5_STAGE + wid * 4096;
            asm volatile("s_nop 4\n\tbuffer_load_dwordx4 %0, %4, %5, %6 offen sc1\n\tbuffer_load_dwordx4 %1, %4, %5, %6 offen offset:1024 sc1\n\t"
                         "buffer_load_dwordx4 %2, %4, %5, %6 offen offset:2048 sc1\n\tbuffer_load_dwordx4 %3, %4, %5, %6 offen offset:3072 sc1"
                         : "+v"(wr[p][4 * i]), "+v"(wr[p][4 * i + 1]), "+v"(wr[p][4 * i + 2]), "+v"(wr[p][4 * i + 3]) : "v"(vo), "s"(rW), "s"(ow) : "memory");
            if (i) {
                const int oe = kord5(t0 + (wid >> 1)) * K5_STAGE + 16384 + (wid & 1) * 1024;
                asm volatile("s_nop 4\n\tbuffer_load_dwordx4 %0, %1, %2, %3 offen sc1" : "+v"(wr[p][8]) : "v"(vo), "s"(rW), "s"(oe) : "memory");
            }
        };
        auto put5 = [&](int w0, int w1) {
#pragma unroll
            for (int w = w0; w < w1; ++w) {
                const int p = w / 9, i = w % 9 == 4 ? -1 : w % 9 - (w % 9 > 4);
                LAS char* sl = lds + (((p ? 2 * p + 6 : 4) + 6) & 7) * K5_STAGE + lane * 16;
                if (i < 0) *(LAS u32x4*)(sl + (wid >> 1) * K5_STAGE + 16384 + (wid & 1) * 1024) = wr[p][8];
                else *(LAS u32x4*)(sl + (i >> 2) * K5_STAGE + wid * 4096 + (i & 3) * 1024) = wr[p][i];
            }
            WAIT_L0();
        };
        const int row = wid * 32 + (lane & 31);
        auto step1 = [&](int t) { BAR(); issue1(t + 4, t & 3); };
        const char* ps;
        unsigned fd, fd2;
        {
            unsigned fo, fi;
            const bool oj = !layer && wid && b < 96, ij = !layer && wid && b >= 112, early = win_strip(b) >= WIN_EARLY;
            const int ojob = b * 3 + wid - 1, oimg = 2048 + (wid - 1) * 9216;
            float m0, m1;
            unsigned fw;
            auto win_store = [&](int h) {
                const int u = win_strip(b) * 2 + h, half = u >= 144, g = u - half * 144, ug = g / 9, j = g - ug * 9, c = lane & 15;
                const auto rd = RSRC(ws + OFF_TWIN + (size_t)ug * (18 * 18432) + (half * 18 + win_third(b, wid) * 6) * 9216);
                const unsigned vd = (lane >> 4) * 256 + (j < 8 ? ((j & ~1) + (c >> 3)) * 1024 + ((j & 1) * 8 + (c & 7)) * 16 : 8192 + c * 16);
#pragma unroll
                for (int i = 0; i < 6; ++i) ST16(rd, vd, i * 9216, wr[1 + h][i], 16);
            };
            auto win_half = [&](int h) {
                const Sel k = make_sel();
                const float m = h ? m1 : m0;
                f32x2 er[4] = {{-m, -m}, {-m, -m}, {-m, -m}, {-m, -m}};
#pragma unroll
                for (int i = 0; i < 6; ++i) centre_frag(wr[1 + h][i], er, m, k);
            };
            attn_producer(lds, ws, x0, wl, fl, ep, layer, pa, hp, wid, [&] { if (b < 16) WAIT_V(0); prologue_done(lds, ws, ep, b, lane); }, [&](int part) {
                if (part == -1 && ij) {
#pragma unroll
                    for (int h = 0; h < 2; ++h) {
                        const LAS char* src = lds + 3 * K1_STAGE + (wid - 1) * 12288 + ((lane >> 4) * 8 + ((lane & 15) >> 2)) * 64 + h * 32
                                              + ((win_strip(b) * 2 + h) % 9 < 8 ? (lane & 1) * 16 + (lane & 2) * 4 : (lane & 3) * 8);
#pragma unroll
                        for (int i = 0; i < 6; ++i) wr[1 + h][i] = tr_frag(src + i * 2048, 256);
                    }
                    WAIT_L(0);
                }
                if (part == 0 && oj) {
                    const auto rs = RSRC(w_out + (size_t)(ojob >> 2) * 32 * 576 + (ojob & 3) * 144);
#pragma unroll
                    for (int j = 0; j < 9; ++j) { const int i = lane + 64 * j; dma1(l0 + oimg + j * 1024, rs, 0, (unsigned)(((i * 3641) >> 16) * (1152 - 288) + i * 16)); }
                }
                if (part == 1) {
                    if (layer) { issue1B(0, 0); issue1B(1, 1); }
                    fo = flag_out(fl + F_X1, 256, pa, hp, lane); fi = flag_in(fl + F_X1, 256, pm * 16 + cg, 16, lane);
                    ps = pin(st + (size_t)(pm * 128 + wid * 32) * 64 + lane * 16);
                    fd = pin((unsigned)OFF_FL2 + wid * 1024 + vo);
                    fd2 = pin((unsigned)OFF_FL2 + (lane < 32 ? 256 + lane : 288 + ((cg * 9) >> 1) * 3 + min(lane & 15, 14)) * 16);
                    fw = pin((unsigned)OFF_FL2 + (288 + (72 + ((cg * 9) >> 1)) * 3 + min(lane & 15, 14)) * 16);
                    if (ij) {
                        float sm[4] = {0.f, 0.f, 0.f, 0.f};
#pragma unroll
                        for (int e = 0; e < 48; ++e) sm[(e >= 24) * 2 + (e & 1)] = sum2(wr[1 + (e >= 24)][(e % 24) >> 2][e & 3], sm[(e >= 24) * 2 + (e & 1)]);
                        column_means<3>(lds, lds + G_WORD, wid - 1, lane, sm[0] + sm[1], sm[2] + sm[3], m0, m1);
                        win_half(0);
                    }
                }
                if (part == 2 && ij) { if (early) win_store(0); win_half(1); if (early) win_store(1); }
                if (part == 2 && oj) { retile_out_make<288>(wr[1], lds + oimg, lane); WAIT_L(0); }
            });
            if (oj || ij) {
                if (oj || !early) {
                    __builtin_amdgcn_s_sleep(OJOB_SLEEP);
                    if (oj) retile_out_store(wr[1], ws + OFF_TWOUT, 72, ojob >> 2, ojob & 3, lane); else { win_store(0); win_store(1); }
                }
                WAIT_V(0);
                flag_raise(ws, lane ? 0x80000000u : (unsigned)OFF_FL2 + (oj ? ojob : 288 + win_strip(b) * 3 + wid - 1) * 16);
            }
            if (layer == 0) {
                if (ij) __builtin_amdgcn_s_sleep(WIN_LOOK - 12); else if (!oj) __builtin_amdgcn_s_sleep(WIN_LOOK);
                flags_wait<0, 8>(ws, fw);
            }
            BARX();
            if (wid == 0) flag_raise(ws, fo);
            if (layer == 0) { issue1B(0, 0); issue1B(1, 1); }
            issue1B(2, 2); issue1B(3, 3);
            asm volatile("" :: "s"(rX));
            if (wid == 0) { if (layer) __builtin_amdgcn_s_sleep(X1_FIRST); flags_wait<0>(ws, fi); }
            BARX();
        }
        issue1A(0, 0); issue1A(1, 1); issue1A(2, 2);
        WAIT_V(8); BAR();
        issue1A(3, 3);
        {
            f32x4 sp[3];
            WAIT_V(8); step1(0);
            WAIT_V(13); step1(1);
            WAIT_V(18); step1(2);
            unsigned got[2];
            if (layer == 0) asm volatile("s_nop 4\n\tbuffer_load_dword %0, %2, %4, 0 offen sc1\n\tbuffer_load_dword %1, %3, %4, 0 offen sc1" : "+v"(got[0]), "+v"(got[1]) : "v"(fd), "v"(fd2), "s"(RSRC(ws)) : "memory");
            WAIT_IF(layer, 18, 20); step1(3);
            WAIT_IF(layer, 18, 20); step1(4);
            WAIT_V(18);
            if (layer == 0) {
                asm volatile("" : "+v"(got[0]), "+v"(got[1]));
                if (__any((got[0] & got[1]) == 0u)) { flags_wait<0>(ws, fd); flags_wait<0>(ws, fd2); }
            }
            BAR(); issue1B(9, 1); side_load(sp, ps, ps); if (layer == 0) load5(3, 0);
            WAIT_IF(layer, 17, 21); BAR(); issue1B(10, 2); if (layer == 0) load5(3, 1);
            WAIT_IF(layer, 5, 14); BARX();
            side_fence(sp);
#pragma unroll
            for (int i = 0; i < 2; ++i) lds_word_set(lds + G_TAB + (wid * 32 + i * 16 + (lane >> 2)) * 4, __float_as_uint(ln_rstd(sp[i])));
            issue1B(11, 3);
            WAIT_L0();
        }
        BAR(); issue1B(12, 0);
        if (layer == 1) {
            const auto rd = RSRC(out + (size_t)b * 9216 + wid * 2304);
            ST16(rd, vo, 0, ((u32x4){0u, 0u, 0u, 0u}), 16); ST16(rd, vo, 1024, ((u32x4){0u, 0u, 0u, 0u}), 16);
            if (lane < 16) ST16(rd, vo, 2048, ((u32x4){0u, 0u, 0u, 0u}), 16);
        }
        if (layer == 0) {
#pragma unroll
            for (int p = 0; p < 6; ++p) load5(p >> 1, p & 1);
        }
        WAIT_V(0);
        __builtin_amdgcn_s_setprio(3);
        const char* src = pin((const char*)zst + (size_t)(pm * 16 + (lane >> 4) * 4) * 1024 + wid * 256 + (lane & 15) * 16);
        const unsigned tw = pin(l0 + G_TAB + 512 + (lane >> 5) * 512 + (wid * 32 + (lane & 15) * 2 + ((lane >> 4) & 1)) * 4);
        const unsigned fz = flag_out(fl + F_Z2, 256, cg, pm, lane), fzi = pin(fl + F_Z2 + (cg * 16 + pm) * 256 + ((lane ^ pm) & 8) * 16 + (lane & 7) * 16);
        const unsigned vA[4] = {pin(voA), pin(voA + 16 * 4096 - 1024), pin(voA + 4096 - 2048), pin(voA + 17 * 4096 - 3072)};
        auto spA = [&](int so, unsigned la) { dma4<false>(la, rA, so, vA[0], vA[1], vA[2], vA[3]); };
        auto issueA = [&](int k, int slot) { spA(k * 128, lA + slot * G_STAGE); };
        {
            const LAS char* pp = opaque(lds + G_PART + row * 8);
            const auto rs = RSRC(zst + (size_t)(pm * 16 + cg) * 128);
            const unsigned vp = pin(lane < 32 ? (unsigned)row * 8u : 0x80000000u);
            asm volatile("" :: "s"(rs));
            BARX();
            const f32x2 a = *(const LAS f32x2*)pp, c = *(const LAS f32x2*)(pp + 1024);
            ST8(rs, vp, 0, ((u32x2){__float_as_uint(add1(a.x, c.x)), min(__float_as_uint(add1(a.y, c.y)), 0xfffffffeu)}), 16);
            SCHED0();
        }
        __builtin_amdgcn_s_setprio(0);
        {
            f32x4 g[4];
            float sm[4];
            auto gather = [&] {
                asm volatile("global_load_dwordx4 %0, %4, off sc1\n\tglobal_load_dwordx4 %1, %4, off offset:1024 sc1\n\t"
                             "global_load_dwordx4 %2, %4, off offset:2048 sc1\n\tglobal_load_dwordx4 %3, %4, off offset:3072 sc1"
                             : "=&v"(g[0]), "=&v"(g[1]), "=&v"(g[2]), "=&v"(g[3]) : "v"(src) : "memory");
            };
            auto total = [&] {
                asm volatile("" : "+v"(g[0]), "+v"(g[1]), "+v"(g[2]), "+v"(g[3]));
#pragma unroll
                for (int i = 0; i < 4; ++i) sm[i] = add1(add1(g[0][i], g[1][i]), add1(g[2][i], g[3][i]));
            };
            step1(9); step1(10);
            BARX(); gather(); issue1(15, 3);
            WAIT_V(22); BARX();
            __builtin_amdgcn_s_setprio(3);
            WAIT_V(9);
            total();
            if (__any(__builtin_isunordered(sm[1], sm[3]))) {
#pragma unroll 1
                for (unsigned spin = 0; spin < 400000u; ++spin) {
                    bool ok = true;
#pragma unroll
                    for (int i = 0; i < 4; ++i) ok &= __float_as_uint(g[i][1]) != 0xffffffffu && __float_as_uint(g[i][3]) != 0xffffffffu;
                    if (__all(ok)) break;
                    gather();
                    WAIT_V(0);
                    total();
                }
            }
            xq4_sum(sm);
            const bool odd = lane & 16;
            float mean;
            const float rs = rstd_of(odd ? sm[2] : sm[0], odd ? sm[3] : sm[1], 1.0f / 2304.0f, mean);
            asm volatile("ds_write_b32 %0, %1" :: "v"(tw), "v"(lane < 32 ? rs : -mean * rs) : "memory");
            __builtin_amdgcn_s_setprio(0);
            issue1B(16, 0);
            WAIT_L0();
        }
        BARX(); issue1A(16, 0); issueA(2 * pm, 1);
        auto kord2 = [&](int t) { const int j = kord(t - 2, 30); return ((j < 2 * (pm & 7) ? j : j + 2) + 2 * (pm & 8)) & 31; };
        unsigned seen;
        auto look = [&] { if (wid == 0) asm volatile("s_nop 4\n\tbuffer_load_dword %0, %1, %2, 0 offen sc1" : "+v"(seen) : "v"(fzi), "s"(RSRC(ws)) : "memory"); };
        auto need = [&](int n) {
            if (wid == 0) {
                asm volatile("" : "+v"(seen));
                if (__ballot(seen == 0u) & ((1ull << n) - 1ull)) flags_wait<0>(ws, lane < n ? fzi : fl + F_Z2 + (cg * 16 + pm) * 256 + pm * 16);
            }
        };
        auto issue = [&](int t, int sl) { const int k = kord2(t); issueB(k, sl); issueA(k, sl); };
        auto steps2 = [&](int t0, int t1) {
            int sl = (2 * t0 + 2) % 3 * G_STAGE, k = kord2(t0 + 3), ob = (k >> 1) * 589824 + (k & 1) * 18432;
            LOOP_ALIGN();
#pragma unroll 1
            for (int t = t0; t < t1; ++t) {
                WAIT_V(9); BARX();
                dma5<true>(lB + sl, rB, ob, vo);
                const int oa = pin_s(k * 128);
                const unsigned la = pin_s(lA + sl);
                k = pin_s(kord2(t + 4)); ob = pin_s((k >> 1) * 589824 + (k & 1) * 18432);
                sl = pin_s(sl == 0 ? 2 * G_STAGE : sl - G_STAGE);
                spA(oa, la);
            }
        };
        WAIT_V(13); BAR(); issue1(17, 2);
        WAIT_V(13); BARX();
        if (wid == 0) flag_raise(ws, fz);
        issueA(2 * pm + 1, 3); if (layer == 0) load5(4, 0);
        if (wid == 0) WAIT_IF(layer, 5, 9); else WAIT_IF(layer, 4, 8);
        BAR(); issueA(kord2(2), 0); if (layer == 0) load5(4, 1);
        BAR(); look(); issueA(kord2(3), 2);
        WAIT_V(4); need(3); BARX(); issueB(kord2(2), 0); issueB(kord2(3), 2); look();
        WAIT_IF(wid == 0, 6, 5); BAR(); issue(4, 1);
        WAIT_V(9); need(8);
        steps2(2, 11); look();
        WAIT_IF(wid == 0, 10, 9); BAR(); issue(14, 0);
        WAIT_V(9); need(16);
        steps2(12, 29);
        WAIT_V(9); BAR(); issue5(0); issue5(1); issue5(2);
        WAIT_V(15); BAR(); issue5(6); issue5(7);
        BAR(); __builtin_amdgcn_s_sleep(W3_SLEEP); issue5(3); put5(0, 9);
        unsigned vb;
        {
            vb = attn_vb(wid, lane);
            const unsigned fo = flag_out(fl + F_Y, 256, pm, cg, lane), fi = flag_in(fl + F_Y, 256, rh * 8 + c5, 16, lane);
            __builtin_amdgcn_s_setprio(3);
            WAIT_V(0); BARX();
            if (wid == 0) { flag_raise(ws, fo); flags_wait<Y_FIRST>(ws, fi); }
            __builtin_amdgcn_s_setprio(0);
            BARX();
        }
        put5(9, 13);
        BAR(); put5(13, 18);
        BAR(); put5(18, 24);
        BAR(); put5(24, 30);
        BAR(); put5(30, 36);
        BAR(); issue5(16); put5(36, 41);
        BAR(); issue5(17); put5(41, 45);
        WAIT_V(5); BAR();
        WAIT_V(0); BAR();
        if (layer == 0) { attn_issueB(lds, ws, hp, wid, vb, 0, 0); attn_issueB(lds, ws, hp, wid, vb, 1, 1); }
        BAR();
        if (layer == 0) {
            __builtin_amdgcn_s_setprio(3);
            const unsigned fo = flag_out(F_X2, 512, (rh >> 1) + wid, wid ? 16 + c5 : (rh & 1) * 8 + c5, wid == 0 || (wid == 1 && (rh & 1) && rh < 31) ? lane : 16);
            BARX();
            if (wid < 2) flag_raise(ws, fo);
        }
        TRACE_DUMP;
      }
        if (blockIdx.x == 0 && wid8 == 4) ST8(RSRC(ws), lane_id_fresh() ? 0x80000000u : (unsigned)OFF_EP, 0, ((u32x2){(unsigned)ep, (unsigned)(ep >> 32)}), 16);
        asm volatile("gm_code_end1:");
        return;
    }
#pragma unroll 1
  for (int layer = 0; layer < NET_LAYERS; ++layer) {
    NET_IDS
    TRACE_BEGIN;
    char* wl = ws + OFF_L0 + layer * SZ_L;
    const unsigned fl = layer * F_LAYER;
    const char* xin = wl + L_XB;
    char* y = wl + (layer ? L_Z2T : L_Y);
#undef STAMP_SEC
#define STAMP_SEC 0
    {
    STAMP_BEGIN;
    attn_consumer(lds, x0, wl, layer, pa, hp, wid, [&] { WAIT_V(0); prologue_done(lds, ws, ep, b, lane_id_fresh()); } STAMP_FWD);
    STAMP_END(1 + 5 * layer);
    }
#undef STAMP_SEC
#define STAMP_SEC 1
    STAMP_BEGIN;
    gmlp_body(lds, wid & 1, wid >> 1, pm, cg, wl + (layer ? L_Y : L_Z2T), y, bsp STAMP_FWD);
    {
        const int lane = lane_id_fresh(), r = lane & 15, q = lane >> 4;
        const int wk = wid >> 1, rt = rh * 4 + (wid & 1) * 2 + wk;
        const ProjOut pr = proj_out((char*)xin, (char*)xin, rt, c5, lane);
        const LAS char *pw[3], *ph[3];
#pragma unroll
        for (int i = 0; i < 3; ++i) {
            pw[i] = opaque(lds + 3 * i * K5_STAGE + wk * 4608 + lane * 16);
            ph[i] = opaque(lds + 3 * i * K5_STAGE + wk * 4608 + 4096 + q * 128 + (r & 7) * 16);
        }
        const auto rY = RSRC(y + (size_t)(rt & ~1) * 73728 + wk * 1024);
        const unsigned vy[2] = {pin((unsigned)(wk * 73728 + lane * 16)), pin((unsigned)((1 - wk) * 73728 + lane * 16))};
        int dk[4];
#pragma unroll
        for (int i = 0; i < 4; ++i) dk[i] = pin_s((kord5(4 + i) - 4 - i) * 4096);
        auto yoff = [&](int t) { return pin_s(t == 0 || t == 17 ? kord5(t) * 4096 : t * 4096 + dk[t & 3]); };
        bf16x8 af[5][4], wf[4][5];
        auto lda = [&](int so, int i) { return __builtin_bit_cast(bf16x8, __builtin_amdgcn_raw_buffer_load_b128(rY, vy[i & 1] + (i >> 1) * 2048, so, 16)); };
        auto rdw = [&](int t, int c, int n) {
            const int sl = (t + 6) & 7, o = (sl % 3) * K5_STAGE + c * 9216;
            wf[2 * (t & 1) + c][n] = n < 4 ? LDS_FRAG(pw[sl / 3] + o + n * 1024) : LDS_FRAG(ph[sl / 3] + o);
        };
        int so[18];
#pragma unroll
        for (int i = 0; i < 5; ++i) so[i] = yoff(i);
        so[17] = yoff(17);
        LAS char* xo = (LAS char*)opaque(lds + wid * 4608 + lane * 16);
        LAS char* xo4 = (LAS char*)opaque(lds + wid * 4608 + 4096 + (lane & 31) * 16);
        const LAS char* xi = opaque(lds + (wid ^ 2) * 4608 + lane * 16);
        const LAS char* xi4 = opaque(lds + (wid ^ 2) * 4608 + 4096 + (lane & 31) * 16);
        const ProjOut po = layer ? proj_out_final(out, rt, c5, lane) : proj_out(wl + SZ_L + L_XA, wl + SZ_L + L_STA, rt, c5, lane);
        asm volatile("" :: "s"(rY));
        SCHED0();
        WAIT_V(0);
        BARX();
        STAMP(11);
        const Res rx = proj_residual_load(pr);
#pragma unroll
        for (int n = 0; n < 10; ++n) rdw(0, n / 5, n % 5);
        f32x4 v[5], u[5];
        proj_residual(v, rx);
#pragma unroll
        for (int n = 0; n < 5; ++n) v[n] = pin(v[n]);
        WAIT_L(0);
        BARX();
        STAMP(12);
#pragma unroll
        for (int i = 0; i < K5_PRE; ++i) { af[i >> 2][i & 3] = lda(so[i >> 2], i & 3); SCHED0(); if (i >= PACE0 && i + 1 < K5_PRE) PACE; SCHED0(); }
        static_for<18>([&](auto tc) {
            constexpr int t = tc.value;
            bf16x8 (&a)[4] = af[t % 5];
#pragma unroll
            for (int h = 0; h < 4; ++h) {
                const int c = t < 17 ? h >> 1 : h & 1, m = t < 17 ? h & 1 : 1 - (h >> 1);
                if (t < 17 ? m == 0 : h < 2) { if (t == 17 && h == 1) WAIT_L(0); else WAIT_L(5); }
#pragma unroll
                for (int n = 0; n < 5; ++n) {
                    f32x4& e = m ? u[n] : v[n];
                    if (t == 0 && h == 1) MFMA_0(wf[0][n], a[1], e); else MFMA_T(wf[2 * (t & 1) + c][n], a[2 * c + m], e);
                    SCHED0();
                    if (t < 17 && m == 0) rdw(t + 1, c, n);
                    if (t < 14 && m == 1 && !(n & 1)) { const int i = y_load_at(6 * t + 3 * c + n / 2); if (i >= 0) af[(i >> 2) % 5][i & 3] = lda(so[i >> 2], i & 3); }
                    if (t < 12 && h == 3 && n == 3) so[t + 5] = yoff(t + 5);
                    if (t == 17 && h * 5 + n >= 13 && h * 5 + n < 18) { const int i = h * 5 + n - 13; *(LAS f32x4*)(i < 4 ? xo + i * 1024 : xo4) = u[i]; }
                    SCHED0();
                }
            }
            if ((t & 1) && t < 17) BAR();
            SCHED0();
        });
        {
            WAIT_L0(); BARX();
#pragma unroll
            for (int n = 0; n < 5; ++n) u[n] = *(const LAS f32x4*)(n < 4 ? xi + n * 1024 : xi4);
            SCHED0();
            asm volatile("s_nop 1" : "+v"(v[0]), "+v"(v[1]), "+v"(v[2]), "+v"(v[3]), "+v"(v[4]));
#pragma unroll
            for (int n = 0; n < 5; ++n) v[n] += u[n];
        }
        STAMP(13);
        if (layer) proj_store<true>(v, po);
        else { proj_store(v, po); WAIT_V(0); BARX(); }
    }
    STAMP_END(3 + 5 * layer);
    TRACE_DUMP;
  }
  asm volatile("gm_code_end2:");
}

}

#define STOP_CHECK(n)

extern "C" void kernel_launch(void* const* d_in, const int* in_sizes, int n_in, void* d_out, int out_size, void* d_ws, size_t ws_size,
                              hipStream_t stream) {
    (void)in_sizes; (void)n_in; (void)out_size; (void)ws_size;
    const u16* x = (const u16*)d_in[0];
    const u16* wqkv = (const u16*)d_in[1];
    const u16* wo = (const u16*)d_in[2];
    const u16* w_in = (const u16*)d_in[4];
    const u16* w_sp = (const u16*)d_in[5];
    const u16* b_sp = (const u16*)d_in[6];
    const u16* w_out = (const u16*)d_in[7];
    char* ws = (char*)d_ws;
    (void)hipFuncSetAttribute((const void*)k_net, hipFuncAttributeMaxDynamicSharedMemorySize, NET_LDS);
    k_net<<<256, 512, NET_LDS, stream>>>(ws, w_sp, b_sp, (char*)d_out, (const char*)x, (const char*)wqkv, wo, w_in, w_out DBG_LAUNCH(0));
}
```

```cpp
#include <hip/hip_runtime.h>
#include <stdint.h>

namespace {

typedef unsigned short u16;
typedef short bf16x8 __attribute__((ext_vector_type(8)));
typedef float f32x4 __attribute__((ext_vector_type(4)));
typedef float f32x2 __attribute__((ext_vector_type(2)));
typedef unsigned u32x4 __attribute__((ext_vector_type(4)));
typedef unsigned u32x2 __attribute__((ext_vector_type(2)));
typedef __bf16 bf16x2_t __attribute__((ext_vector_type(2)));
#define LAS __attribute__((address_space(3)))
#define DEVI __device__ __forceinline__

constexpr int S_ = 2048, D_ = 576, FH = 2304;

constexpr size_t SZ_TWQKV = (size_t)16 * 18 * 6 * 1024;
constexpr size_t SZ_TWT = (size_t)48 * 18 * 256;
constexpr size_t SZ_TWO = (size_t)8 * 18 * 4608;
constexpr size_t SZ_TWIN = (size_t)16 * 18 * 18 * 1024;
constexpr size_t SZ_TWOUT = (size_t)8 * 72 * 4608;
constexpr size_t SZ_X = (size_t)S_ * D_ * 2;
constexpr size_t SZ_Z = (size_t)S_ * FH * 2;
constexpr size_t SZ_ST = (size_t)S_ * 64;
constexpr size_t SZ_ZST = (size_t)16 * 16 * 128 * 8;
constexpr size_t OFF_TWQKV = 0;
constexpr size_t OFF_TWT = OFF_TWQKV + SZ_TWQKV;
constexpr size_t OFF_TWO = OFF_TWT + SZ_TWT;
constexpr size_t OFF_TWIN = OFF_TWO + SZ_TWO;
constexpr size_t OFF_TWOUT = OFF_TWIN + SZ_TWIN;
constexpr size_t OFF_CNT = OFF_TWOUT + SZ_TWOUT;
constexpr size_t SZ_CNT = 10 * 65536;
constexpr size_t OFF_FW = OFF_CNT + SZ_CNT;
constexpr size_t OFF_FL = OFF_FW + 262144;
constexpr size_t OFF_EP = OFF_FL + 4096;
constexpr size_t OFF_FL2 = OFF_EP + 4096;
constexpr size_t OFF_ZST = OFF_FL2 + 12288;
constexpr size_t OFF_L0 = OFF_ZST + 2 * SZ_ZST;
constexpr size_t L_XA = 0, L_STA = L_XA + SZ_X, L_O = L_STA + SZ_ST, L_XB = L_O + SZ_X, L_STB = L_XB + SZ_X, L_Z2T = L_STB + SZ_ST, L_Y = L_Z2T + SZ_Z;
constexpr size_t SZ_L = 0;
static_assert(OFF_L0 + L_Y + SZ_Z <= ((size_t)256 << 20), "workspace");
constexpr unsigned F_O = OFF_CNT, F_X1 = F_O + 65536, F_Z2 = F_O + 131072, F_Y = F_O + 196608, F_LAYER = 262144, F_X2 = F_O + 2 * F_LAYER;

DEVI unsigned pack2(float a, float b) { f32x2 v = {a, b}; bf16x2_t r = __builtin_convertvector(v, bf16x2_t); return __builtin_bit_cast(unsigned, r); }
DEVI float bflo(unsigned u) { return __uint_as_float(u << 16); }
DEVI float bfhi(unsigned u) { return __uint_as_float(u & 0xffff0000u); }
DEVI int slot2row(int sg) { return (sg & ~31) + 2 * (sg & 15) + ((sg >> 4) & 1); }
DEVI float fmax2(float a, float b) { return __builtin_amdgcn_fmed3f(a, b, __builtin_inff()); }
#define XQ4(OP, MID) \
    asm volatile("s_nop 1\n\tv_permlane32_swap_b32 %0, %1\n\tv_permlane32_swap_b32 %2, %3\n\t" OP " %0, %0, %1\n\t" OP " %2, %2, %3\n\t" \
                 "s_nop 1\n\tv_permlane16_swap_b32 %0, %2\n\t" OP " %0, %0, %2\n\t" MID \
                 "v_mov_b32 %2, %0\n\ts_nop 1\n\tv_permlane16_swap_b32 %0, %2\n\tv_mov_b32 %1, %0\n\tv_mov_b32 %3, %2\n\ts_nop 0\n\t" \
                 "v_permlane32_swap_b32 %0, %1\n\tv_permlane32_swap_b32 %2, %3" : "+v"(x[0]), "+v"(x[1]), "+v"(x[2]), "+v"(x[3]))
DEVI void xq4_max(float (&x)[4]) { XQ4("v_max_f32", ""); }
DEVI void xq4_sum(float (&x)[4]) { XQ4("v_add_f32", ""); }
DEVI void xq4_sum_rcp(float (&x)[4]) { XQ4("v_add_f32", "v_rcp_f32 %0, %0\n\ts_nop 0\n\t"); }
template <class T> DEVI T pin(T v) { asm volatile("" : "+v"(v)); return v; }
template <int V> struct IC { static constexpr int value = V; };
template <int N, int I = 0, class F> DEVI void static_for(F&& f) { if constexpr (I < N) { f(IC<I>{}); static_for<N, I + 1>(f); } }
template <class T> DEVI T* own_s(T* p) { T* r; asm volatile("s_mov_b64 %0, %1" : "=&s"(r) : "s"(p)); return r; }
DEVI int pin_s(int v) { asm volatile("" : "+s"(v)); return v; }
DEVI float xq_sum(float v) {
    auto a = __builtin_amdgcn_permlane32_swap(__float_as_uint(v), __float_as_uint(v), false, false);
    v = __uint_as_float(a[0]) + __uint_as_float(a[1]);
    auto b = __builtin_amdgcn_permlane16_swap(__float_as_uint(v), __float_as_uint(v), false, false);
    return __uint_as_float(b[0]) + __uint_as_float(b[1]);
}
#define RSRC(p) __builtin_amdgcn_make_buffer_rsrc((void*)(p), 0, 0x7fffffff, 0x00020000)
#define ST16(rs, vo, so, v, aux) __builtin_amdgcn_raw_buffer_store_b128((v), (rs), (vo) + (so), 0, aux)
#define ST8(rs, vo, so, v, aux) __builtin_amdgcn_raw_buffer_store_b64((v), (rs), (vo), (so), aux)
#define WAIT_V(n) asm volatile("s_waitcnt vmcnt(%0)" ::"i"(n) : "memory")
#define WAIT_L0() asm volatile("s_waitcnt lgkmcnt(0)" ::: "memory")
#define BAR() do { asm volatile("" ::: "memory"); __builtin_amdgcn_s_barrier(); asm volatile("" ::: "memory"); } while (0)
#define BARX() do { __builtin_amdgcn_sched_barrier(0); BAR(); __builtin_amdgcn_sched_barrier(0); } while (0)
#define LOOP_ALIGN() asm volatile(".p2align 6")
#define SCHED0() __builtin_amdgcn_sched_barrier(0)
#define MFMA(a, b, c) __builtin_amdgcn_mfma_f32_16x16x32_bf16((a), (b), (c), 0, 0, 0)
#define MFMA_T(a, b, c) asm volatile("v_mfma_f32_16x16x32_bf16 %0, %1, %2, %0" : "+v"(c) : "v"(a), "v"(b))
#define MFMA_0(a, b, c) asm volatile("v_mfma_f32_16x16x32_bf16 %0, %1, %2, 0" : "=&v"(c) : "v"(a), "v"(b))
#define WAIT_L(n) __builtin_amdgcn_s_waitcnt(0xC07F | ((n) << 8))
#define LDS_FRAG(p) (*(const LAS bf16x8*)(p))
DEVI int lane_id_fresh() { int z = 0; asm volatile("" : "+v"(z)); return __builtin_amdgcn_mbcnt_hi(~0u, __builtin_amdgcn_mbcnt_lo(~0u, z)); }
DEVI const LAS char* opaque(const LAS char* p) { asm volatile("" : "+v"(p)); return p; }
#define DBG_ARG
#define DBG_LAUNCH(n)
#define STAMP(i)
#define STAMP_DECL
#define STAMP_BEGIN
#define STAMP_END(n)
#define STAMP_PARAM
#define STAMP_FWD

#define PST_PARAM
#define PST_FWD
#define PST_DECL
#define PST(i)
#define PST_DUMP(c, k)
#define TRACE_ATTR
#define TRACE_BEGIN
#define TRACE_DUMP

DEVI float add1(float a, float b) { float r; asm("v_add_f32 %0, %1, %2" : "=v"(r) : "v"(a), "v"(b)); return r; }
DEVI float rstd_of(float s, float q, float inv_n, float& mean) {
    float var;
    asm("v_mul_f32 %0, %2, %3\n\tv_mul_f32 %1, %2, %4\n\tv_fma_f32 %1, -%0, %0, %1" : "=&v"(mean), "=&v"(var) : "s"(inv_n), "v"(s), "v"(q));
    return __builtin_amdgcn_rsqf(fmax2(var, 0.f) + 1e-5f);
}
DEVI float ln_rstd(const f32x4 p) {
    float s, q, mean;
    asm("v_add_f32 %0, %2, %4\n\tv_add_f32 %1, %3, %5\n\ts_nop 0\n\t"
        "v_add_f32_dpp %0, %0, %0 quad_perm:[1,0,3,2] row_mask:0xf bank_mask:0xf\n\tv_add_f32_dpp %1, %1, %1 quad_perm:[1,0,3,2] row_mask:0xf bank_mask:0xf\n\ts_nop 0\n\t"
        "v_add_f32_dpp %0, %0, %0 quad_perm:[2,3,0,1] row_mask:0xf bank_mask:0xf\n\tv_add_f32_dpp %1, %1, %1 quad_perm:[2,3,0,1] row_mask:0xf bank_mask:0xf"
        : "=&v"(s), "=&v"(q) : "v"(p[0]), "v"(p[1]), "v"(p[2]), "v"(p[3]));
    return rstd_of(s, q, 1.0f / 576.0f, mean);
}

struct Res { u32x4 a, c; u32x2 e; };
DEVI void proj_residual(f32x4 (&v)[5], const Res& x) {
    v[0] = (f32x4){bflo(x.a.x), bfhi(x.a.x), bflo(x.a.y), bfhi(x.a.y)}; v[1] = (f32x4){bflo(x.a.z), bfhi(x.a.z), bflo(x.a.w), bfhi(x.a.w)};
    v[2] = (f32x4){bflo(x.c.x), bfhi(x.c.x), bflo(x.c.y), bfhi(x.c.y)}; v[3] = (f32x4){bflo(x.c.z), bfhi(x.c.z), bflo(x.c.w), bfhi(x.c.w)};
    v[4] = (f32x4){bflo(x.e.x), bfhi(x.e.x), bflo(x.e.y), bfhi(x.e.y)};
}
struct ProjOut { __amdgpu_buffer_rsrc_t x, st; unsigned v0, v1, v2, vs; };
DEVI ProjOut proj_out(char* xout, char* stout, int rt, int cg, int lane) {
    const int r = lane & 15, q = lane >> 4;
    ProjOut p = {RSRC(xout + (size_t)rt * 18432 + cg * 2304), RSRC(stout + (size_t)rt * 1024 + cg * 8),
                 pin((unsigned)lane * 16u), pin((unsigned)lane * 16u + 1024u), pin((unsigned)(2048 + r * 16 + (q & 1) * 8)), pin((unsigned)(r * 64 + (q >> 1) * 4))};
    asm volatile("" :: "s"(p.x), "s"(p.st));
    return p;
}
DEVI ProjOut proj_out_final(char* out, int rt, int cg, int lane) {
    const int q = lane >> 4;
    const unsigned o = (unsigned)(slot2row(rt * 16 + (lane & 15)) * 1152 + cg * 144);
    ProjOut p = {RSRC(out), RSRC(out), pin(o + q * 16), pin(o + 64 + q * 16), pin(o + 128 + (q & 1) * 8), 0u};
    return p;
}
DEVI Res proj_residual_load(const ProjOut& p) {
    return {__builtin_amdgcn_raw_buffer_load_b128(p.x, p.v0, 0, 16), __builtin_amdgcn_raw_buffer_load_b128(p.x, p.v1, 0, 16), __builtin_amdgcn_raw_buffer_load_b64(p.x, p.v2, 0, 16)};
}
template <bool FINAL = false>
DEVI void proj_store(f32x4 (&v)[5], const ProjOut& p) {
    ST16(p.x, p.v0, 0, ((u32x4){pack2(v[0][0], v[0][1]), pack2(v[0][2], v[0][3]), pack2(v[1][0], v[1][1]), pack2(v[1][2], v[1][3])}), FINAL ? 0 : 16);
    SCHED0();
    asm volatile("" : "+v"(v[2]), "+v"(v[3]), "+v"(v[4]));
    ST16(p.x, p.v1, 0, ((u32x4){pack2(v[2][0], v[2][1]), pack2(v[2][2], v[2][3]), pack2(v[3][0], v[3][1]), pack2(v[3][2], v[3][3])}), FINAL ? 0 : 16);
    SCHED0();
    ST8(p.x, p.v2, 0, ((u32x2){pack2(v[4][0], v[4][1]), pack2(v[4][2], v[4][3])}), FINAL ? 0 : 16);
    SCHED0();
    if (!FINAL) {
        const f32x4 s4 = ((v[0] + v[1]) + (v[2] + v[3])) + v[4] * 0.5f;
        const f32x4 q4 = ((v[0] * v[0] + v[1] * v[1]) + (v[2] * v[2] + v[3] * v[3])) + v[4] * v[4] * 0.5f;
        auto x = __builtin_amdgcn_permlane32_swap(__float_as_uint((s4[0] + s4[2]) + (s4[1] + s4[3])), __float_as_uint((q4[0] + q4[2]) + (q4[1] + q4[3])), false, false);
        const float t = __uint_as_float(x[0]) + __uint_as_float(x[1]);
        auto y = __builtin_amdgcn_permlane16_swap(__float_as_uint(t), __float_as_uint(t), false, false);
        __builtin_amdgcn_raw_buffer_store_b32(__float_as_uint(__uint_as_float(y[0]) + __uint_as_float(y[1])), p.st, p.vs, 0, 16);
        SCHED0();
    }
}

DEVI void side_load(f32x4 (&d)[3], const char* p, const char* p2) {
    asm volatile("global_load_dwordx4 %0, %3, off sc1\n\tglobal_load_dwordx4 %1, %3, off offset:1024 sc1\n\tglobal_load_dwordx4 %2, %4, off sc1"
                 : "=&v"(d[0]), "=&v"(d[1]), "=&v"(d[2]) : "v"(p), "v"(p2) : "memory");
}
DEVI void side_fence(f32x4 (&d)[3]) { asm volatile("" : "+v"(d[0]), "+v"(d[1]), "+v"(d[2])); }
DEVI void lds_word_set(LAS char* p, unsigned v) { asm volatile("ds_write_b32 %0, %1" :: "v"((unsigned)(size_t)p), "v"(v) : "memory"); }
#define DMA_I(v, off, sc) "buffer_load_dwordx4 " v ", %1, %2 offen offset:" off sc " lds\n\t"
#define DMA5(sc) asm volatile("s_mov_b32 m0, %0\n\ts_mov_b32 exec_hi, 0\n\ts_nop 2\n\t" DMA_I("%3", "0", sc) "s_mov_b32 exec_hi, -1\n\t" \
    DMA_I("%3", "512", sc) DMA_I("%3", "1536", sc) DMA_I("%3", "2560", sc) DMA_I("%3", "3584", sc) :: "s"(la), "s"(rs), "s"(so), "v"(vo) : "memory")
#define DMA4(sc) asm volatile("s_mov_b32 m0, %0\n\ts_nop 3\n\t" DMA_I("%3", "0", sc) DMA_I("%4", "1024", sc) DMA_I("%5", "2048", sc) DMA_I("%6", "3072", sc) \
    :: "s"(la), "s"(rs), "s"(so), "v"(v0), "v"(v1), "v"(v2), "v"(v3) : "memory")
template <bool SC1> DEVI void dma5(unsigned la, __amdgpu_buffer_rsrc_t rs, int so, unsigned vo) { if (SC1) DMA5(" sc1"); else DMA5(""); }
template <bool SC1> DEVI void dma4(unsigned la, __amdgpu_buffer_rsrc_t rs, int so, unsigned v0, unsigned v1, unsigned v2, unsigned v3) { if (SC1) DMA4(" sc1"); else DMA4(""); }
#define DMA2(sc) asm volatile("s_mov_b32 m0, %0\n\ts_nop 3\n\t" DMA_I("%3", "0", sc) DMA_I("%4", "1024", sc) :: "s"(la), "s"(rs), "s"(so), "v"(v0), "v"(v1) : "memory")
template <bool SC1> DEVI void dma2(unsigned la, __amdgpu_buffer_rsrc_t rs, int so, unsigned v0, unsigned v1) { if (SC1) DMA2(" sc1"); else DMA2(""); }
DEVI void dma1(unsigned la, __amdgpu_buffer_rsrc_t rs, int so, unsigned v0) { asm volatile("s_mov_b32 m0, %0\n\ts_nop 3\n\t" DMA_I("%3", "0", "") :: "s"(la), "s"(rs), "s"(so), "v"(v0) : "memory"); }
#define WAIT_IF(c, a, b) do { if (c) WAIT_V(a); else WAIT_V(b); } while (0)
template <int T0, int T1, int P, int S0 = 0, class ISSUE>
DEVI void ring_steps(ISSUE&& issue) {
    int slot = (T0 + S0) % 3;
    LOOP_ALIGN();
#pragma unroll 1
    for (int t = T0; t < T1; ++t) {
        WAIT_V(P); BAR();
        issue(t + 3, slot);
        slot = slot == 2 ? 0 : slot + 1;
    }
}

DEVI unsigned flag_out(unsigned use, int pitch, int group, int writer, int lane) { return pin(lane < 16 ? use + (group * 16 + lane) * pitch + writer * 16 : 0x80000000u); }
DEVI unsigned flag_in(unsigned use, int pitch, int reader, int n, int lane) { return pin(use + reader * pitch + (lane < n ? lane : 0) * 16); }
DEVI void flag_raise(const char* ws, unsigned vo) { ST16(RSRC(ws), vo, 0, ((u32x4){1u, 1u, 1u, 1u}), 16); SCHED0(); }
template <int FIRST, int AGAIN = 1>
DEVI void flags_wait(const char* ws, unsigned vo) {
    unsigned v, n = 400000u;
    asm volatile("s_nop 4\n\ts_sleep %4\n"
                 "1:\n\tbuffer_load_dword %0, %2, %3, 0 offen sc1\n\ts_waitcnt vmcnt(0)\n\tv_cmp_eq_u32 vcc, 0, %0\n\ts_cbranch_vccz 2f\n\t"
                 "s_sleep %5\n\ts_sub_u32 %1, %1, 1\n\ts_cbranch_scc0 1b\n2:"
                 : "=&v"(v), "+s"(n) : "v"(vo), "s"(RSRC(ws)), "i"(FIRST), "i"(AGAIN) : "vcc", "scc", "memory");
    SCHED0();
}
template <int FIRST>
DEVI void epoch_wait(const char* ws, unsigned vo, unsigned long long ep) {
    unsigned long long v; unsigned n = 400000u;
    asm volatile("s_nop 4\n\ts_sleep %5\n"
                 "1:\n\tbuffer_load_dwordx2 %0, %2, %3, 0 offen sc1\n\ts_waitcnt vmcnt(0)\n\tv_cmp_ne_u64 vcc, %4, %0\n\ts_cbranch_vccz 2f\n\t"
                 "s_sleep 1\n\ts_sub_u32 %1, %1, 1\n\ts_cbranch_scc0 1b\n2:"
                 : "=&v"(v), "+s"(n) : "v"(vo), "s"(RSRC(ws)), "s"(ep), "i"(FIRST) : "vcc", "scc", "memory");
    SCHED0();
}
DEVI unsigned lds_arrive(LAS char* p) {
    unsigned v;
    asm volatile("s_mov_b64 exec, 1\n\tds_add_rtn_u32 %0, %1, %2\n\ts_mov_b64 exec, -1\n\ts_waitcnt lgkmcnt(0)" : "=&v"(v) : "v"((unsigned)(size_t)p), "v"(1u) : "memory");
    return __builtin_amdgcn_readfirstlane(v);
}
DEVI void lds_count_wait(LAS char* p, unsigned n) {
#pragma unroll 1
    for (unsigned spin = 0; spin < 40000000u; ++spin) {
        unsigned v;
        asm volatile("ds_read_b32 %0, %1\n\ts_waitcnt lgkmcnt(0)" : "=v"(v) : "v"((unsigned)(size_t)p) : "memory");
        if (__builtin_amdgcn_readfirstlane(v) >= n) break;
    }
}

constexpr int K1_STAGE = 32768;
constexpr int WO_OFF = 69632;
constexpr int G_OWN = 139264;
constexpr int G_PART = G_OWN + 18432;
constexpr int G_TAB = G_PART + 2048;
constexpr int K1_SCR = G_TAB + 1536;
constexpr int G_WORD = K1_SCR + 576;
constexpr int NET_LDS = G_WORD + 64;

#define WO_PRE 9
#define WO_W 33
#define REL_PRIO 0
#define W_FIRST 2
#define I_SLEEP 30
#define OJOB_SLEEP 20
#define PACE0 1
#define PACE __builtin_amdgcn_s_sleep(1)
#define POLLW 0
#define WO_EV 4
#define WIN_EARLY 0
#define WIN_LOOK 40
#define W3_SLEEP 0
#define O_FIRST 8
#define X1_FIRST 4
#define X2_FIRST 4
#define Y_FIRST 14
#define Z2_FIRST 10
DEVI int win_strip(int b) { return ((b - 112) & 7) * 18 + ((b - 112) >> 3); }
DEVI int win_third(int b, int w) { return b & 8 ? 3 - w : w - 1; }
DEVI unsigned attn_vb(int wid, int lane) {
    const int c = lane & 15;
    return pin((unsigned)(wid < 3 ? lane * 16 : (c < 8 ? (c >> 1) & 1 : 2) * 4608 + (lane >> 4) * 64 + (c < 8 ? (c >> 2) * 2 + (c & 1) : c & 3) * 16));
}
DEVI void attn_issueB(LAS char* lds, const char* ws, int hp, int wid, unsigned vb, int s, int slot) {
    const unsigned la = (unsigned)(size_t)lds + slot * K1_STAGE + 18432 + wid * 4096;
    if (wid < 3) dma4<true>(la, RSRC(ws), (int)OFF_TWQKV + hp * (18 * 6144) + wid * 4096 + s * 12288, vb, vb, vb, vb);
    else asm volatile("s_mov_b32 m0, %0\n\ts_nop 3\n\tbuffer_load_dwordx4 %4, %1, %2 offen sc1 lds\n\tbuffer_load_dwordx4 %4, %1, %3 offen offset:1024 sc1 lds"
                      :: "s"(la), "s"(RSRC(ws)), "s"((int)OFF_TWT + hp * 13824 + s * 512), "s"((int)OFF_TWT + hp * 13824 + s * 512 + 256 - 1024), "v"(vb) : "memory");
}
struct AttnX { __amdgpu_buffer_rsrc_t rs; unsigned v[4], vh[2]; int oA, oh, step; };
DEVI AttnX attn_x(const char* x0, const char* xa, int layer, int pm, int wid, int lane) {
    const int l3 = lane >> 3, pmh = pm > 0 ? pm : 1;
    const unsigned v = layer ? lane * 16 : 2 * l3 * 1152 + ((lane & 7) ^ l3) * 16 + 4096, d1 = layer ? 0 : 16 * 1152 - 1024, d2 = layer ? 16384 : 1152 - 2048;
    const unsigned vh = layer ? (l3 & 1) * 18432 + (lane >> 4) * 256 + (8 + (lane & 7)) * 16 : v + 16 * 1152;
    return {RSRC(layer ? xa : x0 - 4096), {pin(v), pin(v + d1), pin(v + d2), pin(v + d1 + d2)}, {pin(vh), pin(vh + (layer ? 0 : 1152 - 1024))},
            (pm * 8 + 2 * wid) * 18432, (pmh * 8 - 2) * 18432, layer ? 2048 : 128};
}
DEVI void attn_issueA(LAS char* lds, const AttnX& a, int wid, int s, int slot) {
    const unsigned la = (unsigned)(size_t)lds + slot * K1_STAGE;
    if (wid == 3) dma2<true>(la, a.rs, a.oh + s * a.step, a.vh[0], a.vh[1]);
    dma4<true>(la + 2048 + wid * 4096, a.rs, a.oA + s * a.step, a.v[0], a.v[1], a.v[2], a.v[3]);
}
template <class DONE, class NEXT>
DEVI void attn_producer(LAS char* lds, const char* ws, const char* x0, const char* wl, unsigned fl, unsigned long long ep, int layer, int pm, int hp, int wid, DONE&& done, NEXT&& next) {
    const int lane = lane_id_fresh();
    const int pmh = pm > 0 ? pm : 1;
    const unsigned vo = lane * 16;
    const AttnX ax = attn_x(x0, wl + L_XA, layer, pm, wid, lane);
    const unsigned vb = attn_vb(wid, lane), fd = pin((unsigned)OFF_FL + wid * 1024 + vo);
    auto issueB = [&](int s, int slot) { attn_issueB(lds, ws, hp, wid, vb, s, slot); };
    auto issue = [&](int s, int slot) { attn_issueA(lds, ax, wid, s, slot); issueB(s, slot); };
    const int hr = lane >> 2;
    const char* ps = pin(wl + L_STA + (size_t)(pm * 128 + wid * 32) * 64 + lane * 16);
    const char* psh = pin(wl + L_STA + (size_t)((pmh * 8 - 2 + (hr >> 3)) * 16 + 8 + (hr & 7)) * 64 + (lane & 3) * 16);
    if (layer == 0) {
        const unsigned fi = pin((unsigned)OFF_FW + (pm * 16 + hp) * 1024 + (lane < 6 ? lane : 0) * 128);
        done();
        if (wid == 0) { epoch_wait<W_FIRST>(ws, fi, ep); issueB(0, 0); }
        BARX();
        if (wid) issueB(0, 0);
        issueB(1, 1); issue(2, 2);
        WAIT_IF(wid == 3, 10, 12);
    } else {
        issueB(2, 2);
        const unsigned fi = flag_in(F_X2, 512, pm * 16 + hp, pm > 0 ? 24 : 16, lane);
        if (wid == 0) {
            flags_wait<X2_FIRST>(ws, fi);
            AttnX a0 = ax;
#pragma unroll
            for (int i = 0; i < 4; ++i) { attn_issueA(lds, a0, i, 0, 0); a0.oA += 36864; }
        }
        BARX();
        attn_issueA(lds, ax, wid, 1, 1); attn_issueA(lds, ax, wid, 2, 2);
        WAIT_IF(wid == 3, 12, 8);
    }
    __builtin_amdgcn_s_setprio(0);
    BAR();
    if (layer) WAIT_IF(wid == 3, 6, 4);
    next(-1);
    ring_steps<0, 4, 8>(issue);
    {
        unsigned long long done;
        if (layer == 0) asm volatile("s_nop 4\n\tbuffer_load_dwordx2 %0, %1, %2, 0 offen sc1" : "+v"(done) : "v"(fd), "s"(RSRC(ws)) : "memory");
        WAIT_IF(layer, 8, 9); BAR(); issue(7, 1);
        WAIT_V(8);
        if (layer == 0) { asm volatile("" : "+v"(done)); if (__any(done != ep)) epoch_wait<0>(ws, fd, ep); }
        BAR(); issue(8, 2);
    }
    const auto rW = RSRC(ws + OFF_TWO + (size_t)(hp >> 1) * 82944 + wid * 4096);
    auto issueW = [&](int i) { dma4<true>((unsigned)(size_t)lds + WO_OFF + (i * 4 + wid) * 4096, rW, i * 16384, vo, vo, vo, vo); };
    {
        f32x4 sp[3];
        side_load(sp, ps, psh);
        WAIT_V(11); BAR(); issueW(2); issueW(3);
        WAIT_V(8); BARX();
        side_fence(sp);
#pragma unroll
        for (int i = 0; i < 3; ++i) lds_word_set(lds + K1_SCR + (i < 2 ? 16 + wid * 32 + i * 16 + hr : hr) * 4, __float_as_uint(ln_rstd(sp[i])));
        WAIT_L0();
    }
    BAR();
    next(0);
    issueW(0); issueW(1); issueW(4);
    if (wid == 0) dma2<true>((unsigned)(size_t)lds + WO_OFF + 80896, rW, 80896, vo, vo);
    next(1);
    const unsigned fo = flag_out(fl + F_O, 256, pm, hp, lane), fi = flag_in(fl + F_O, 256, pm * 16 + hp, 16, lane);
    WAIT_V(0);
    if (wid == POLLW) __builtin_amdgcn_s_setprio(3);
    BARX();
    next(2);
    if (wid == POLLW) { flag_raise(ws, fo); flags_wait<O_FIRST>(ws, fi); }
    __builtin_amdgcn_s_setprio(0);
    BARX();
}

#define STAMP_SEC 0
template <class DONE>
DEVI void attn_consumer(LAS char* lds, const char* x0, char* wl, int layer, int pm, int hp, int wid, DONE&& done STAMP_PARAM) {
    const int lane = lane_id_fresh(), r = lane & 15, q = lane >> 4;
    const char* xa = wl + L_XA;
    char* o = wl + L_O;
    const int T = wid & 1, h = wid >> 1;
    const LAS char *ph[2], *pa[2];
#pragma unroll
    for (int kk = 0; kk < 2; ++kk) {
        const int sw = ((4 * kk + q) ^ (r & 7)) * 16;
        ph[kk] = opaque(lds + (layer ? kk * 1024 + q * 256 + (r ^ (T ? 0 : 8)) * 16 : (r ^ (T ? 0 : 8)) * 128 + sw));
        pa[kk] = opaque(lds + T * 2048 + (layer ? kk * 1024 + lane * 16 : r * 128 + sw));
    }
    const LAS char* pb = lds + h * 3072 + lane * 16;
    const LAS char* pt = lds + lane * 16;
    f32x4 acc[5][4];
#pragma unroll
    for (int m = 0; m < 5; ++m)
#pragma unroll
        for (int n = 0; n < 4; ++n) if (m + n) acc[m][n] = pin((f32x4){0.f, 0.f, 0.f, 0.f});
    bf16x8 fa[2][5], fb[2][4];
    auto rd1 = [&](int kk, int i) {
        if (i < 3) fb[kk][i] = LDS_FRAG(pb + 18432 + kk * 6144 + i * 1024);
        else if (i == 3) fb[kk][3] = LDS_FRAG(pt + 30720 + kk * 1024);
        else if (i == 4) fa[kk][0] = LDS_FRAG(ph[kk]);
        else if (i < 9) fa[kk][i - 4] = LDS_FRAG(pa[kk] + 2048 + (i - 5) * 4096);
    };
    f32x4 mb, mb0;
#pragma unroll
    for (int j = 0; j < 4; ++j) {
        const bool ok = ((r - 4 * q - j) & 15) < 8;
        mb[j] = ok ? 0.f : -__builtin_inff();
        mb0[j] = (ok && (pm > 0 || 4 * q + j <= r)) ? 0.f : -__builtin_inff();
    }
    mb = pin(mb); mb0 = pin(mb0);
    const unsigned mp0 = pin((4 * q > r ? 0xffffu : 0u) | (4 * q + 1 > r ? 0xffff0000u : 0u)), mp1 = pin((4 * q + 2 > r ? 0xffffu : 0u) | (4 * q + 3 > r ? 0xffff0000u : 0u));
    const unsigned qtm = pin(q == h ? 0xffffffffu : 0u);
    const unsigned vom = pin((unsigned)(r * 16 + hp * 1024 + (2 * h + (q >> 1)) * 256 + (q & 1) * 8)), vot = pin((unsigned)(r * 16 + 16384 + (hp >> 1) * 256 + (hp & 1) * 8 + h * 4));
    const LAS char* tk0 = opaque(lds + K1_SCR + ((q * 4) ^ (T ? 0 : 8)) * 4);
    const LAS char* tk = opaque(lds + K1_SCR + ((1 + T) * 16 + q * 4) * 4);
    const LAS char* tq = opaque(lds + K1_SCR + ((1 + T) * 16 + r) * 4);
    pb = opaque(pb); pt = opaque(pt);
    STAMP(2);
    const int rt = pm * 8 + (hp & 1) * 4 + wid, cg = hp >> 1;
    const ProjOut pr = layer ? proj_out((char*)xa, (char*)xa, rt, cg, lane) : proj_out_final((char*)x0, rt, cg, lane);
    BAR();
    if (layer == 0) done();
    BARX();
    STAMP(3);
#pragma unroll
    for (int i = 0; i < 9; ++i) rd1(0, i);
    SCHED0();
    const Res rx = proj_residual_load(pr);
    SCHED0();
    int slot = 0, left = 9;
    LOOP_ALIGN();
#pragma unroll 1
    do {
        int d;
#pragma unroll
        for (int kk = 0; kk < 2; ++kk) {
            WAIT_L(0);
            if (kk) BAR();
            SCHED0();
#pragma unroll
            for (int i = 1; i < 20; ++i) {
                const int m = i >> 2, n = i & 3;
                const bf16x8 &A = fa[kk][m], &B = fb[kk][n];
                acc[m][n] = n != 2 ? MFMA(B, A, acc[m][n]) : MFMA(A, B, acc[m][n]);
                SCHED0();
                if (i <= 9) rd1(kk ^ 1, i - 1);
                if (kk == 0) {
                    if (i == 10) d = pin_s(slot == 2 ? -2 * K1_STAGE : K1_STAGE);
                    if (i == 11 || i == 12) ph[i & 1] = opaque(ph[i & 1] + d);
                    if (i == 13 || i == 14) pa[i & 1] = opaque(pa[i & 1] + d);
                    if (i == 15) pb = opaque(pb + d);
                    if (i == 16) pt = opaque(pt + d);
                } else {
                    if (i == 11) slot = pin_s(slot == 2 ? 0 : slot + 1);
                    if (i == 13) left = pin_s(left - 1);
                }
                SCHED0();
            }
        }
    } while (left);
    STAMP(4);
    u32x2 ts[5], tf[5];
    {
        LAS char* xb = lds + wid * 320;
#pragma unroll
        for (int m = 0; m < 5; ++m) ts[m] = (u32x2){pack2(acc[m][3][0], acc[m][3][1]), pack2(acc[m][3][2], acc[m][3][3])};
        if (q == 2) {
#pragma unroll
            for (int m = 0; m < 5; ++m) {
                const unsigned w = h ? ts[m].y : ts[m].x;
                *(LAS u16*)(xb + m * 64 + r * 2) = (u16)w; *(LAS u16*)(xb + m * 64 + 32 + r * 2) = (u16)(w >> 16);
            }
        }
        asm volatile("" ::: "memory");
#pragma unroll
        for (int m = 0; m < 5; ++m) tf[m] = *(const LAS u32x2*)(xb + m * 64 + (r & 1) * 32 + q * 8);
    }
    {
        f32x4 rk[5]; float rq[4];
        rk[0] = *(const LAS f32x4*)(tk0);
#pragma unroll
        for (int g = 0; g < 4; ++g) { rk[g + 1] = *(const LAS f32x4*)(tk + g * 128); rq[g] = *(const LAS float*)(tq + g * 128); }
        const f32x4 zero = {0.f, 0.f, 0.f, 0.f};
        u32x4 kf[5];
#pragma unroll
        for (int m = 0; m < 5; ++m) kf[m] = (u32x4){pack2(acc[m][1][0], acc[m][1][1]), pack2(acc[m][1][2], acc[m][1][3]), ts[m].y, 0u};
        f32x4 sp[4], sc[4];
#pragma unroll
        for (int g = 0; g < 4; ++g) {
            const u32x4 qf = {pack2(acc[g + 1][0][0], acc[g + 1][0][1]), pack2(acc[g + 1][0][2], acc[g + 1][0][3]), ts[g + 1].x & qtm, 0u};
            sp[g] = MFMA(__builtin_bit_cast(bf16x8, kf[g]), __builtin_bit_cast(bf16x8, qf), zero);
            sc[g] = MFMA(__builtin_bit_cast(bf16x8, kf[g + 1]), __builtin_bit_cast(bf16x8, qf), zero);
        }
        u32x2 vf[5];
#pragma unroll
        for (int m = 0; m < 5; ++m) vf[m] = (u32x2){pack2(acc[m][2][0], acc[m][2][1]), pack2(acc[m][2][2], acc[m][2][3])};
        f32x4 rs[4], t[4]; float mx[4], sm[4];
#pragma unroll
        for (int g = 0; g < 4; ++g) {
#pragma unroll
            for (int j = 0; j < 4; ++j) { const bool pv = 4 * q + j > r; rs[g][j] = pv ? rk[g][j] : rk[g + 1][j]; t[g][j] = pv ? sp[g][j] : sc[g][j]; }
            t[g] = (t[g] * rs[g]) * (rq[g] * (0.23570226039551584f * 1.4426950408889634f)) + (g == 0 ? mb0 : mb);
            mx[g] = fmaxf(fmaxf(t[g][0], t[g][1]), fmaxf(t[g][2], t[g][3]));
        }
        xq4_max(mx);
#pragma unroll
        for (int g = 0; g < 4; ++g) {
            t[g] = t[g] - mx[g];
#pragma unroll
            for (int j = 0; j < 4; ++j) t[g][j] = __builtin_amdgcn_exp2f(t[g][j]);
            sm[g] = (t[g][0] + t[g][2]) + (t[g][1] + t[g][3]);
        }
        f32x4 om[4], ot[4];
#pragma unroll
        for (int g = 0; g < 4; ++g) {
            const f32x4 p = t[g] * rs[g];
            const unsigned p01 = pack2(p[0], p[1]), p23 = pack2(p[2], p[3]);
            const u32x4 pf = {p01 & mp0, p23 & mp1, p01 & ~mp0, p23 & ~mp1};
            om[g] = MFMA(__builtin_bit_cast(bf16x8, ((u32x4){vf[g].x, vf[g].y, vf[g + 1].x, vf[g + 1].y})), __builtin_bit_cast(bf16x8, pf), zero);
            ot[g] = MFMA(__builtin_bit_cast(bf16x8, ((u32x4){tf[g].x, tf[g].y, tf[g + 1].x, tf[g + 1].y})), __builtin_bit_cast(bf16x8, pf), zero);
        }
        xq4_sum_rcp(sm);
        const auto ro = RSRC(o + (size_t)(pm * 8 + T) * 18432);
        unsigned tw2[4];
#pragma unroll
        for (int g = 0; g < 4; ++g) {
            const f32x4 v = om[g] * sm[g];
            __builtin_amdgcn_raw_buffer_store_b64((u32x2){pack2(v[0], v[1]), pack2(v[2], v[3])}, ro, vom, g * 36864, 16);
            const f32x2 w = ot[g].xy * sm[g];
            tw2[g] = pack2(w.x, w.y);
        }
        if (q == 2) {
#pragma unroll
            for (int g = 0; g < 4; ++g) __builtin_amdgcn_raw_buffer_store_b32(tw2[g], ro, vot, g * 36864, 16);
        }
    }
    STAMP(5);
    {
        WAIT_V(0); BARX();
        STAMP(6);
        f32x4 v[5];
        proj_residual(v, rx);
        const LAS char* pw = opaque(lds + WO_OFF + lane * 16);
        const LAS char* pw2 = opaque(lds + WO_OFF + 41472 + lane * 16);
        const LAS char* ph2 = opaque(lds + WO_OFF + 4096 + q * 128 + (r & 7) * 16);
        const LAS char* ph3 = opaque(lds + WO_OFF + 41472 + 4096 + q * 128 + (r & 7) * 16);
        bf16x8 wf[90];
        auto rdw = [&](int i) {
            const int kc = i / 5, n = i % 5;
            wf[i] = n < 4 ? LDS_FRAG((kc < 9 ? pw : pw2) + (kc % 9) * 4608 + n * 1024) : LDS_FRAG((kc < 9 ? ph2 : ph3) + (kc % 9) * 4608);
        };
#pragma unroll
        for (int i = 0; i < WO_W; ++i) rdw(i);
#pragma unroll
        for (int n = 0; n < 5; ++n) v[n] = pin(v[n]);
        const auto rO = RSRC(o + (size_t)rt * 18432);
        const ProjOut po = proj_out(wl + L_XB, wl + L_STB, rt, cg, lane);
        asm volatile("" :: "s"(rO));
        WAIT_L(0);
        __builtin_amdgcn_s_setprio(REL_PRIO);
        BARX();
        STAMP(7);
        bf16x8 af[18];
        auto ldo = [&](int kc) { af[kc] = __builtin_bit_cast(bf16x8, __builtin_amdgcn_raw_buffer_load_b128(rO, lane * 16 + (kc & 3) * 1024, (kc >> 2) * 4096, 16)); };
#pragma unroll
        for (int kc = 0; kc < WO_PRE; ++kc) { ldo(kc); SCHED0(); if (kc >= PACE0 && kc + 1 < WO_PRE) PACE; SCHED0(); }
        static_for<90>([&](auto ic) {
            constexpr int i = ic.value, kc = i / 5, n = i % 5, j = WO_W + 2 * (i / 3) + i % 3;
            MFMA_T(wf[i], af[kc], v[n]);
            SCHED0();
            if (i % 3 < 2 && j < 90) rdw(j);
            if (i % WO_EV == 2 && i / WO_EV + WO_PRE < 18) ldo(i / WO_EV + WO_PRE);
            SCHED0();
        });
        asm volatile("s_nop 4" : "+v"(v[0]), "+v"(v[1]));
        STAMP(8);
        proj_store(v, po);
        __builtin_amdgcn_s_setprio(0);
    }
}

DEVI f32x2 gelu2(f32x2 v) {
    const f32x2 w = v * (v * v * -0.10294324f + -2.3022082f);
    const f32x2 d = (f32x2){__builtin_amdgcn_exp2f(w.x), __builtin_amdgcn_exp2f(w.y)} + 1.0f;
    return v * (f32x2){__builtin_amdgcn_rcpf(d.x), __builtin_amdgcn_rcpf(d.y)};
}
DEVI f32x4 gelu4(f32x4 v) { const f32x2 a = gelu2(v.xy), b = gelu2(v.zw); return (f32x4){a.x, a.y, b.x, b.y}; }
DEVI void rowsum16x8(const f32x4 a, const f32x4 b, float& o0, float& o1) {
    float t0, t1, t2, t3;
    asm volatile(
        "s_nop 1\n\t"
        "v_add_f32_dpp %2, %6, %6 row_ror:8 row_mask:0xf bank_mask:0x3\n\t"
        "v_add_f32_dpp %3, %7, %7 row_ror:8 row_mask:0xf bank_mask:0x3\n\t"
        "v_add_f32_dpp %4, %10, %10 row_ror:8 row_mask:0xf bank_mask:0x3\n\t"
        "v_add_f32_dpp %5, %11, %11 row_ror:8 row_mask:0xf bank_mask:0x3\n\t"
        "v_add_f32_dpp %2, %8, %8 row_ror:8 row_mask:0xf bank_mask:0xc\n\t"
        "v_add_f32_dpp %3, %9, %9 row_ror:8 row_mask:0xf bank_mask:0xc\n\t"
        "v_add_f32_dpp %4, %12, %12 row_ror:8 row_mask:0xf bank_mask:0xc\n\t"
        "v_add_f32_dpp %5, %13, %13 row_ror:8 row_mask:0xf bank_mask:0xc\n\t"
        "v_add_f32_dpp %0, %2, %2 row_half_mirror row_mask:0xf bank_mask:0x5\n\t"
        "s_nop 0\n\t"
        "v_add_f32_dpp %1, %4, %4 row_half_mirror row_mask:0xf bank_mask:0x5\n\t"
        "v_add_f32_dpp %0, %3, %3 row_half_mirror row_mask:0xf bank_mask:0xa\n\t"
        "v_add_f32_dpp %1, %5, %5 row_half_mirror row_mask:0xf bank_mask:0xa\n\t"
        "s_nop 0\n\t"
        "v_add_f32_dpp %0, %0, %0 quad_perm:[1,0,3,2] row_mask:0xf bank_mask:0xf\n\t"
        "v_add_f32_dpp %1, %1, %1 quad_perm:[1,0,3,2] row_mask:0xf bank_mask:0xf\n\t"
        "s_nop 0\n\t"
        "v_add_f32_dpp %0, %0, %0 quad_perm:[2,3,0,1] row_mask:0xf bank_mask:0xf\n\t"
        "v_add_f32_dpp %1, %1, %1 quad_perm:[2,3,0,1] row_mask:0xf bank_mask:0xf\n\t"
        : "=&v"(o0), "=&v"(o1), "=&v"(t0), "=&v"(t1), "=&v"(t2), "=&v"(t3)
        : "v"(a[0]), "v"(a[1]), "v"(a[2]), "v"(a[3]), "v"(b[0]), "v"(b[1]), "v"(b[2]), "v"(b[3]));
}

#define K5_PRE 9
#define RH_MAP 0
#define NET_LAYERS 2
constexpr int G_STAGE = 34816;
constexpr int K5_STAGE = 18432, K5_NS = 8;

constexpr int y_load_at(int slot) {
    int next = K5_PRE;
    for (int s = 0;; ++s) { const bool go = next < 18 + 2 * (s / 3); if (s == slot) return go ? next : -1; next += go; }
}

struct GTile { f32x4 a[4][4], e[2]; };
struct GFrag { bf16x8 a[2][4], b[2][5]; const LAS char *pa[2][2], *pb, *p8; int slot; };
DEVI void g_rd(GFrag& f, int kk, int i) {
    if (i < 4) f.a[kk][i] = LDS_FRAG(f.pa[kk][i >> 1] + (i & 1) * 2048);
    else if (i < 8) f.b[kk][i - 4] = LDS_FRAG(f.pb + (kk * 9 + i - 4) * 1024);
    else f.b[kk][4] = LDS_FRAG(f.p8 + kk * 9216);
}
template <int NS, int G, bool ROWS_IN_REGS>
DEVI void g_steps(GTile& c, GFrag& f, int n) {
    LOOP_ALIGN();
#pragma unroll 1
    do {
        int d;
#pragma unroll
        for (int kk = 0; kk < 2; ++kk) {
            WAIT_L(0);
            if (kk) BAR();
            SCHED0();
#pragma unroll
            for (int i = 0; i < 18; ++i) {
                const int nn = i >> 2, m = i & 3;
                f32x4& e = nn < 4 ? c.a[m][nn] : c.e[m & 1];
                e = ROWS_IN_REGS ? MFMA(f.a[kk][m], f.b[kk][nn], e) : MFMA(f.b[kk][nn], f.a[kk][m], e);
                SCHED0();
                if (i < 9) g_rd(f, kk ^ 1, i);
                if (kk == 0) {
                    if (i == 9) d = pin_s(f.slot == NS - 1 ? -(NS - 1) * G : G);
                    if (i >= 10 && i < 14) f.pa[(i - 10) >> 1][i & 1] = opaque(f.pa[(i - 10) >> 1][i & 1] + d);
                    if (i == 14) f.pb = opaque(f.pb + d);
                    if (i == 15) f.p8 = opaque(f.p8 + d);
                } else {
                    if (i == 10) f.slot = pin_s(f.slot == NS - 1 ? 0 : f.slot + 1);
                    if (i == 12) n = pin_s(n - 1);
                }
                SCHED0();
            }
        }
    } while (n);
}
DEVI void keep_s(__amdgpu_buffer_rsrc_t r) { asm volatile("" :: "s"(r)); }
DEVI void keep4(const f32x4 (&c)[4]) { asm volatile("" :: "v"(c[0]), "v"(c[1]), "v"(c[2]), "v"(c[3])); }
template <int D, int DB, bool FIRST, class HOOK>
DEVI void g_step_u(GTile& c, GFrag& f, const f32x4 (&c0)[4], HOOK&& hook) {
    static_for<36>([&](auto ic) {
        constexpr int kk = ic.value / 18, i = ic.value % 18, n = i >> 2, m = i & 3;
        if (i == 0) { WAIT_L(0); if (kk) BAR(); SCHED0(); }
        f32x4& e = n < 4 ? c.a[m][n] : c.e[m & 1];
        if (FIRST && kk == 0) asm volatile("v_mfma_f32_16x16x32_bf16 %0, %1, %2, %3" : "=&v"(e) : "v"(f.b[kk][n]), "v"(f.a[kk][m]), "v"(c0[m]));
        else MFMA_T(f.b[kk][n], f.a[kk][m], e);
        SCHED0();
        if (i < 9) g_rd(f, kk ^ 1, i);
        if (kk == 0) {
            if (i >= 10 && i < 14) f.pa[(i - 10) >> 1][i & 1] = opaque(f.pa[(i - 10) >> 1][i & 1] + D);
            if (i == 14) f.pb = opaque(f.pb + DB);
            if (i == 15) f.p8 = opaque(f.p8 + DB);
        }
        if (FIRST && ic.value == 20) keep4(c0);
        hook(ic);
        SCHED0();
    });
}
#define MF(d, a, b) "v_mfma_f32_16x16x32_bf16 " d ", " a ", " b ", " d "\n\t"
template <int D, int DB>
DEVI void g_step_gelu(GTile& c, GFrag& f, f32x4& z, float r, float c1) {
#pragma unroll
    for (int kk = 0; kk < 2; ++kk) {
        float va = z[2 * kk], vb = z[2 * kk + 1], ta, tb;
        WAIT_L(0); if (kk) BAR(); SCHED0();
#pragma unroll
        for (int i = 0; i < 9; ++i) {
            MFMA_T(f.b[kk][i >> 2], f.a[kk][i & 3], c.a[i & 3][i >> 2]);
            if (i == 5) asm volatile("v_mul_f32 %0, %0, %1" : "+v"(va) : "v"(r));
            if (i == 6) asm volatile("v_mul_f32 %0, %0, %1" : "+v"(vb) : "v"(r));
            if (i == 7) asm volatile("v_mul_f32 %0, %1, %1" : "=v"(ta) : "v"(va));
            if (i == 8) asm volatile("v_mul_f32 %0, %1, %1" : "=v"(tb) : "v"(vb));
            SCHED0();
            g_rd(f, kk ^ 1, i);
            SCHED0();
        }
        asm volatile(MF("%0", "%5", "%6") "v_fmaak_f32 %3, %3, %11, 0xc0135761\n\tv_fmaak_f32 %4, %4, %11, 0xc0135761\n\t"
                     MF("%1", "%5", "%7") "v_mul_f32 %3, %3, %9\n\tv_mul_f32 %4, %4, %10\n\t" MF("%2", "%5", "%8") "v_exp_f32 %3, %3"
                     : "+v"(c.a[1][2]), "+v"(c.a[2][2]), "+v"(c.a[3][2]), "+v"(ta), "+v"(tb)
                     : "v"(f.b[kk][2]), "v"(f.a[kk][1]), "v"(f.a[kk][2]), "v"(f.a[kk][3]), "v"(va), "v"(vb), "v"(c1));
        SCHED0(); f.pa[kk][0] = opaque(f.pa[kk][0] + D); SCHED0();
        asm volatile(MF("%0", "%6", "%7") "v_exp_f32 %5, %5\n\t" MF("%1", "%6", "%8") "v_add_f32 %4, 1.0, %4\n\tv_add_f32 %5, 1.0, %5\n\t"
                     MF("%2", "%6", "%9") "v_rcp_f32 %4, %4\n\t" MF("%3", "%6", "%10") "v_rcp_f32 %5, %5"
                     : "+v"(c.a[0][3]), "+v"(c.a[1][3]), "+v"(c.a[2][3]), "+v"(c.a[3][3]), "+v"(ta), "+v"(tb)
                     : "v"(f.b[kk][3]), "v"(f.a[kk][0]), "v"(f.a[kk][1]), "v"(f.a[kk][2]), "v"(f.a[kk][3]));
        SCHED0(); f.pa[kk][1] = opaque(f.pa[kk][1] + D); SCHED0();
        asm volatile(MF("%0", "%4", "%5") "v_mul_f32 %2, %2, %7\n\tv_mul_f32 %3, %3, %8\n\t" MF("%1", "%4", "%6")
                     : "+v"(c.e[0]), "+v"(c.e[1]), "+v"(va), "+v"(vb) : "v"(f.b[kk][4]), "v"(f.a[kk][0]), "v"(f.a[kk][1]), "v"(ta), "v"(tb));
        SCHED0();
        if (kk == 0) { f.pb = opaque(f.pb + DB); f.p8 = opaque(f.p8 + DB); }
        SCHED0();
        z[2 * kk] = va; z[2 * kk + 1] = vb;
    }
}
DEVI void g_fill(GTile& c, const float (&e)[4]) {
#pragma unroll
    for (int m = 0; m < 4; ++m) {
#pragma unroll
        for (int n = 0; n < 4; ++n) c.a[m][n] = pin((f32x4){e[m], e[m], e[m], e[m]});
        if (m < 2) c.e[m] = pin((f32x4){e[m], e[m], e[m], e[m]});
    }
}

#undef STAMP_SEC
#define STAMP_SEC 1
DEVI void gmlp_body(LAS char* lds, int wm, int wn, int pm, int cg, char* z2t, char* y, const u16* __restrict__ bsp STAMP_PARAM) {
    const int mlo = wm * 4 + wn * 2, mhi = wm * 4 + (1 - wn) * 2;
    const float zero4[4] = {0.f, 0.f, 0.f, 0.f};
    GTile z2, z1, acc;
    GFrag f;
    {
        const int lane = lane_id_fresh();
#pragma unroll
        for (int kk = 0; kk < 2; ++kk) { f.pa[kk][0] = lds + mlo * 2048 + kk * 1024 + lane * 16; f.pa[kk][1] = lds + mhi * 2048 + kk * 1024 + lane * 16; }
        f.pb = lds + 16384 + wn * 4096 + lane * 16; f.p8 = lds + 16384 + 8192 + lane * 16; f.slot = 0;
    }
    g_fill(z2, zero4); g_fill(z1, zero4);
#pragma unroll
    for (int i = 0; i < 4; ++i) f.pa[i >> 1][i & 1] = opaque(f.pa[i >> 1][i & 1]);
    f.pb = opaque(f.pb); f.p8 = opaque(f.p8);
    SCHED0();
    WAIT_V(0); BARX();
    BAR();
    BARX();
    STAMP(2);
#pragma unroll
    for (int i = 0; i < 9; ++i) g_rd(f, 0, i);
    g_steps<4, G_STAGE, true>(z2, f, 9);
    STAMP(3);
    {
        const int lane = lane_id_fresh(), r = lane & 15, q = lane >> 4;
        f32x4 ra[4];
#pragma unroll
        for (int m = 0; m < 4; ++m) ra[m] = *(const LAS f32x4*)(lds + G_TAB + ((m < 2 ? mlo : mhi) + (m & 1)) * 64 + q * 16);
        SCHED0();
#pragma unroll
        for (int m = 0; m < 4; ++m) {
            f32x4 s1, s2;
#pragma unroll
            for (int n = 0; n < 4; ++n) {
                const f32x4 g = gelu4(z2.a[m][n] * ra[m]);
                z2.a[m][n] = g;
                if (n == 0) { s1 = g; s2 = g * g; } else { s1 += g; s2 += g * g; }
            }
            if (m < 2) { const f32x4 g = gelu4(z2.e[m] * ra[m]); z2.e[m] = g; s1 += g; s2 += g * g; }
            float u0, u1;
            rowsum16x8(s1, s2, u0, u1);
            if ((r & 3) == 0) *(LAS f32x2*)(lds + G_PART + wn * 1024 + ((m < 2 ? mlo : mhi) + (m & 1)) * 128 + (q * 4 + (r >> 2)) * 8) = (f32x2){u0, u1};
            SCHED0();
        }
    }
    WAIT_L0(); BAR();
    STAMP(4);
    g_steps<4, G_STAGE, false>(z1, f, 4);
    unsigned b0, b1;
    f32x4 c0[4];
    g_step_u<G_STAGE, G_STAGE, false>(z1, f, c0, [&](auto ic) {
        if (ic.value != 18) return;
        STAMP(5);
        const int lane = lane_id_fresh(), q = lane >> 4;
        b0 = __builtin_amdgcn_raw_buffer_load_b32(RSRC(bsp), (lane & 15) * 4, pm * 256 + mlo * 32, 0);
        b1 = __builtin_amdgcn_raw_buffer_load_b32(RSRC(bsp), (lane & 15) * 4, pm * 256 + mhi * 32, 0);
#pragma unroll
        for (int g = 0; g < 2; ++g) {
            const auto rz2 = RSRC(z2t + (size_t)pm * 589824 + cg * 36864 + ((g ? mhi : mlo) >> 1) * 9216);
            const LAS char* tab = lds + G_TAB + (g ? mhi : mlo) * 64 + q * 16;
            LAS char* own = lds + (wm ? G_OWN : G_STAGE + 16384) + (g ^ wn) * 9216 + lane * 16;
            f32x4 ra[2], rb[2];
#pragma unroll
            for (int T = 0; T < 2; ++T) { ra[T] = *(const LAS f32x4*)(tab + 512 + T * 64); rb[T] = *(const LAS f32x4*)(tab + 1024 + T * 64); }
#pragma unroll
            for (int n = 0; n < (g ? 4 : 5); ++n) {
                const f32x4 v0 = (n < 4 ? z2.a[2 * g][n & 3] : z2.e[0]) * ra[0] + rb[0], v1 = (n < 4 ? z2.a[2 * g + 1][n & 3] : z2.e[1]) * ra[1] + rb[1];
                const u32x4 o = {pack2(v0[0], v1[0]), pack2(v0[1], v1[1]), pack2(v0[2], v1[2]), pack2(v0[3], v1[3])};
                ST16(rz2, lane * 16, n < 4 ? wn * 4096 + n * 1024 : 8192, o, 16);
                *(LAS u32x4*)(own + (n < 4 ? wn * 4096 + n * 1024 : 8192)) = o;
            }
        }
        STAMP(6);
    });
    static_for<3>([&](auto sc) {
        constexpr int D = sc.value == 0 ? G_STAGE : sc.value == 1 ? -3 * G_STAGE : 2 * G_STAGE;
        g_step_u<D, D, false>(z1, f, c0, [&](auto ic) { if (ic.value == 17 && sc.value == 1) WAIT_V(0); });
    });
    float ra[4], c1;
    {
        int lane, da[2];
        g_step_u<-G_STAGE, -G_STAGE, false>(z1, f, c0, [&](auto ic) {
            constexpr int i = ic.value;
            if (i == 9) lane = pin(lane_id_fresh());
            if (i == 14 || i == 15) da[i & 1] = pin((lane & 15) * 128 + (((4 * (i & 1) + (lane >> 4)) ^ (lane & 7)) * 16) - lane * 16 - (i & 1) * 1024);
            if (i == 16 || i == 17) f.pa[0][i & 1] = opaque(f.pa[0][i & 1] + da[0]);
            if (i == 27 || i == 28) f.pa[1][i & 1] = opaque(f.pa[1][i & 1] + da[1]);
            if (i >= 29 && i < 33) {
                constexpr int m = (i - 29) & 3;
                const float e = m & 1 ? bfhi(m < 2 ? b0 : b1) : bflo(m < 2 ? b0 : b1);
                c0[m] = pin((f32x4){e, e, e, e});
            }
            if (i == 33) {
#pragma unroll
                for (int m = 0; m < 4; ++m) ra[m] = *(const LAS float*)(lds + G_TAB + ((m < 2 ? mlo : mhi) + (m & 1)) * 64 + (lane & 15) * 4);
                c1 = pin(-0.10294324f);
            }
        });
    }
    STAMP(7);
    g_step_u<2 * G_STAGE, G_OWN - G_STAGE - 16384, true>(acc, f, c0, [](auto) {});
    static_for<18>([&](auto sc) {
        constexpr int st = sc.value, m = st < 16 ? st >> 2 : st & 1;
        constexpr int D = st == 0 ? -3 * G_STAGE : st % 3 == 1 ? 2 * G_STAGE : -G_STAGE;
        g_step_gelu<D, st == 0 ? 16384 - G_OWN : D>(acc, f, st < 16 ? z1.a[m][st & 3] : z1.e[m], ra[m], c1);
    });
    f.slot = 1;
    STAMP(9);
    g_steps<3, -G_STAGE, false>(acc, f, 12);
    __amdgpu_buffer_rsrc_t ry;
    unsigned vy[4], ve[2];
    int lane;
    u32x4 o = {0u, 0u, 0u, 0u};
    auto y_part = [&](auto jc) {
        constexpr int j = jc.value, m = j / 10, k = j % 10, e = (k & 1) * 2;
        const f32x4 &a = k < 8 ? acc.a[m][k >> 1] : acc.e[m & 1], &z = k < 8 ? z1.a[m][k >> 1] : z1.e[m & 1];
        float t0, t1;
        asm volatile("v_mul_f32 %1, %3, %5\n\tv_mul_f32 %2, %4, %6\n\tv_cvt_pk_bf16_f32 %0, %1, %2" : "=v"(o[k & 3]), "=&v"(t0), "=&v"(t1) : "v"(a[e]), "v"(a[e + 1]), "v"(z[e]), "v"(z[e + 1]));
        if (k == 3 || k == 7) ST16(ry, vy[m] + (k >> 2) * 1024, 0, o, 16);
        if (k == 9) ST8(ry, ve[m & 1], 0, ((u32x2){o[0], o[1]}), 16);
    };
    WAIT_L(0);
    SCHED0();
    static_for<36>([&](auto ic) {
        constexpr int i = ic.value, kk = i < 14 ? 0 : i < 28 ? 1 : i >= 32, t = i < 28 ? i % 14 : 14 + (i & 3), m = t < 10 ? t / 5 : 2 + (t - 10) / 4, n = t < 10 ? t % 5 : (t - 10) & 3;
        if (i == 14) { WAIT_L(0); BAR(); SCHED0(); }
        f32x4& e = n < 4 ? acc.a[m][n & 3] : acc.e[m & 1];
        MFMA_T(f.b[kk][n], f.a[kk][m], e);
        SCHED0();
        if (i < 9) g_rd(f, 1, i);
        if (i == 10) { ry = RSRC(own_s(y) + (size_t)pin_s(pm * 8 * 73728 + cg * 4608)); keep_s(ry); }
        if (i == 12) { lane = pin(lane_id_fresh()); vy[0] = pin((unsigned)(mlo * 73728 + wn * 2048 + lane * 16)); }
        if (i == 14) ve[0] = pin((unsigned)(mlo * 73728 + 4096 + (lane >> 5) * 256 + (lane & 15) * 16 + ((lane >> 4) & 1) * 8));
        if (i == 15) { vy[1] = pin(vy[0] + 73728); ve[1] = pin(ve[0] + 73728); }
        if (i == 16) { vy[2] = pin(vy[0] + (mhi - mlo) * 73728); vy[3] = pin(vy[2] + 73728); asm volatile("s_nop 4"); }
        if constexpr (i >= 17) y_part(IC<i - 17>{});
        SCHED0();
    });
    STAMP(10);
    y_part(IC<19>{});
    SCHED0();
#pragma unroll
    for (int i = 0; i < 4; ++i) {
        const int m = 2 + (i >> 1), p = i & 1;
        if (i == 2) asm volatile("" : "+v"(acc.a[3][0]), "+v"(acc.a[3][1]), "+v"(acc.a[3][2]), "+v"(acc.a[3][3]));
        const f32x4 a = acc.a[m][2 * p] * z1.a[m][2 * p], c = acc.a[m][2 * p + 1] * z1.a[m][2 * p + 1];
        ST16(ry, vy[m] + p * 1024, 0, ((u32x4){pack2(a[0], a[1]), pack2(a[2], a[3]), pack2(c[0], c[1]), pack2(c[2], c[3])}), 16);
        SCHED0();
    }
}

#define NET_IDS \
    int b = __builtin_amdgcn_readfirstlane(blockIdx.x), wid = wid8 & 3; \
    asm volatile("" : "+s"(b), "+s"(wid));     \
    const int xcd = b & 7, idx = b >> 3; \
    const int pa = (xcd >> 1) * 4 + (idx & 3), hp = (xcd & 1) * 8 + (idx >> 2); \
    const int pm = (xcd >> 2) * 8 + (idx & 7), cg = (xcd & 3) * 4 + (idx >> 3); \
    const int rh = RH_MAP ? xcd * 4 + (idx & 3) : (xcd >> 1) * 8 + (idx & 7), c5 = RH_MAP ? idx >> 2 : (xcd & 1) * 4 + (idx >> 3); \
    const int lead = (idx + (idx >> 3)) & 3; \
    auto kord = [&](int t, int n) { \
        const int k = t + ((int)(0x1F000000u << (((t - lead) & 3) * 4)) >> 28); \
        return (unsigned)k < (unsigned)n ? k : t; \
    }; \
    auto kord5 = [&](int t) { \
        const int k = t + ((int)(0x1F000000u << (((t - (RH_MAP ? c5 : lead)) & 3) * 4)) >> 28); \
        return (unsigned)k < 18u ? k : t; \
    };

typedef short s16x4 __attribute__((ext_vector_type(4)));
DEVI u32x4 tr_frag(const LAS char* p, int four_rows) {
    const u32x2 a = __builtin_bit_cast(u32x2, __builtin_amdgcn_ds_read_tr16_b64_v4i16((LAS s16x4*)p));
    const u32x2 b = __builtin_bit_cast(u32x2, __builtin_amdgcn_ds_read_tr16_b64_v4i16((LAS s16x4*)(p + four_rows)));
    return (u32x4){a.x, a.y, b.x, b.y};
}
DEVI float dot2(unsigned v, unsigned w, float acc) { return __builtin_amdgcn_fdot2_f32_bf16(__builtin_bit_cast(bf16x2_t, v), __builtin_bit_cast(bf16x2_t, w), acc, false); }
DEVI float sum2(unsigned v, float acc) { return dot2(v, 0x3f803f80u, acc); }
struct Sel { unsigned lo, hi, nlo, nhi; };
DEVI Sel make_sel() { return {(unsigned)pin_s(0x00003f80), (unsigned)pin_s(0x3f800000), (unsigned)pin_s(0x0000bf80), (unsigned)pin_s((int)0xbf800000u)}; }
DEVI unsigned centre2(unsigned v, f32x2& e, float mean, const Sel& k) {
    const float a = dot2(k.lo, v, e.x), b = dot2(k.hi, v, e.y);
    const unsigned r = pack2(a, b);
    e = (f32x2){dot2(k.nlo, r, a), dot2(k.nhi, r, b)} - mean;
    return r;
}
typedef u32x4 Retile[9];
template <int PITCH>
DEVI void retile_out_make(Retile& o, const LAS char* img, int lane) {
    const int c = lane & 15, q = lane >> 4, p = lane & 3;
    const LAS char* src = img + (q * 8 + (c >> 2)) * PITCH;
#pragma unroll
    for (int i = 0; i < 8; ++i) o[i] = tr_frag(src + p * 16 + (i >> 2) * 144 + ((i >> 1) & 1) * 64 + (i & 1) * 8, 4 * PITCH);
    o[8] = tr_frag(src + 128 + (p >> 1) * 144 + (p & 1) * 8, 4 * PITCH);
}
DEVI void retile_out_store(const Retile& o, char* dst, int nkc, int kc, int w, int lane) {
    const int c = lane & 15, q = lane >> 4;
    const auto rd = RSRC(dst + (size_t)(2 * w * nkc + kc) * 4608);
#pragma unroll
    for (int i = 0; i < 8; ++i) ST16(rd, lane * 16 + (i & 3) * 1024, (i >> 2) * nkc * 4608, o[i], 16);
    ST16(rd, (c >> 3) * nkc * 4608 + 4096 + q * 128 + (c & 7) * 16, 0, o[8], 16);
}

DEVI float dpp_ror(float v, int n) { return n == 8 ? __builtin_amdgcn_update_dpp(0.f, v, 0x128, 0xf, 0xf, true) : n == 4 ? __builtin_amdgcn_update_dpp(0.f, v, 0x124, 0xf, 0xf, true)
                                          : n == 2 ? __builtin_amdgcn_update_dpp(0.f, v, 0x122, 0xf, 0xf, true) : __builtin_amdgcn_update_dpp(0.f, v, 0x121, 0xf, 0xf, true); }
DEVI unsigned long long epoch_of(const char* ws) {
    SCHED0();
    const unsigned long long ep = *(const __attribute__((address_space(4))) unsigned long long*)(ws + pin_s((int)OFF_EP)) + 1ull;
    asm volatile("" :: "s"(ep));
    return ep;
}
DEVI void prologue_done(LAS char* lds, char* ws, unsigned long long ep, int b, int lane) {
    if (lds_arrive(lds + G_WORD + 8) == 7) ST16(RSRC(ws), lane ? 0x80000000u : (unsigned)OFF_FL + b * 16, 0, ((u32x4){(unsigned)ep, (unsigned)(ep >> 32), 0u, 0u}), 16);
}
DEVI void centre_frag(u32x4& f, f32x2 (&er)[4], float mean, const Sel& k) {
#pragma unroll
    for (int e = 0; e < 4; ++e) f[e] = centre2(f[e], er[e], mean, k);
}
template <int N>
DEVI void column_means(LAS char* lds, LAS char* cnt, int me, int lane, float a, float b, float& ma, float& mb) {
    LAS float* red = (LAS float*)(lds + G_PART);
    auto x = __builtin_amdgcn_permlane16_swap(__float_as_uint(a), __float_as_uint(b), false, false);
    red[me * 64 + lane] = __uint_as_float(x[0]) + __uint_as_float(x[1]);
    if (lds_arrive(cnt) < N - 1) lds_count_wait(cnt, N);
    float t = 0.f;
#pragma unroll
    for (int i = 0; i < N; ++i) t += red[i * 64 + lane];
    x = __builtin_amdgcn_permlane32_swap(__float_as_uint(t), __float_as_uint(t), false, false);
    t = (__uint_as_float(x[0]) + __uint_as_float(x[1])) * (1.0f / 576.0f);
    x = __builtin_amdgcn_permlane16_swap(__float_as_uint(t), __float_as_uint(t), false, false);
    ma = __uint_as_float(x[0]); mb = __uint_as_float(x[1]);
}
template <class ARGS>
DEVI unsigned long long prologue(LAS char* lds, char*& ws, const char*& x0, const char*& wqkv, const u16*& wo, const u16*& w_in, int b, int pa, int wid8, ARGS&& args PST_PARAM) {
    PST_DECL;
    const int lane = lane_id_fresh(), c = lane & 15, q = lane >> 4, w = wid8 & 3;
    const int jw = w - 1 + 3 * (((wid8 >> 2) ^ (b >> 3)) & 1);
    const bool wjob = b >= 16 && b < 112 && w;
    const int uo = (wid8 - 1 - (wid8 >> 2)) * 16 + b;
    const bool ojob = b < 16 && w && uo < 72;
    const unsigned l0 = (unsigned)(size_t)lds;
    LAS char* img = lds + 2 * K1_STAGE;
    const int uw = b - 16, head = (uw & 7) * 4 + ((uw >> 3) & 3), part = uw >> 5, hp = head >> 1, odd = head & 1;
    const int c0 = part * 1152 + hp * 72 + odd * 40;
    {
        const int so = pin_s(jw * 96 * 3456 + (c0 & ~15));
        const unsigned v = (lane & 3) < ((head & 3) == 3 ? 2 : 3) ? (lane >> 2) * 3456 + (lane & 3) * 16 : 0x80000000u, d = 16 * 3456 - 1024;
        const unsigned v0 = pin(v), v1 = pin(v + d), v2 = pin(v + 2 * d), v3 = pin(v + 3 * d);
        const int e = ((b - 112) >> 3) * 192 + wid8 * 64 + lane, row = (e * 10923) >> 16, r = e - row * 6;
        const unsigned vp = pin((unsigned)(row * 3456 + (r >> 1) * 1152 + (r & 1) * 128));
        const unsigned la = pin_s(l0 + 2 * K1_STAGE + jw * 6144);
        args(0);
        if (b >= 112 && wid8 < 3) dma1(l0 + G_OWN, RSRC(wqkv), (b & 7) * 128, vp);
        if (wjob) { dma4<false>(la, RSRC(wqkv), so, v0, v1, v2, v3); dma2<false>(la + 4096, RSRC(wqkv), so + 64 * 3456, v0, v1); }
        args(1);
    }
    if (ojob) {
        const auto rs = RSRC(wo + (uo & 3) * 144);
#pragma unroll
        for (int j = 0; j < 9; ++j) {
            const int i = lane + 64 * j, rl = (i * 3641) >> 16, kp = (uo >> 2) * 32 + rl;
            const int row = kp < 512 ? 18 * (kp >> 4) + (kp & 15) + 2 * ((kp >> 4) & 1) : 36 * ((kp - 512) >> 2) + 16 + (kp & 3);
            dma1(l0 + 2 * K1_STAGE + wid8 * 9216 + j * 1024, rs, 0, (unsigned)(row * 1152 + (i - rl * 18) * 16));
        }
    }
    if (wid8 == 4) { lds_word_set(lds + G_WORD + c * 4, 0u); WAIT_L0(); }
    BAR();
    unsigned long long ep;
    if (wid8 == 4) ep = epoch_of(ws);
    if (wid8 == 4 && b >= 16 && b < 112) lds_count_wait(lds + G_WORD + 12, 12);
    if (wid8 > 4 && b >= 112) {
        __builtin_amdgcn_s_sleep(I_SLEEP);
        const unsigned v = (lane >> 2) * 9216 + (lane & 3) * 16, d = 16 * 9216 - 1024;
#pragma unroll
        for (int i = 0; i < 3; ++i) dma4<false>(l0 + 3 * K1_STAGE + (w - 1) * 12288 + i * 4096, RSRC(w_in + win_strip(b) * 32), (win_third(b, w) * 192 + i * 64) * 9216, v, v + d, v + 2 * d, v + 3 * d);
    }
    if (wid8 == 4) {
        AttnX ax = attn_x(x0, x0, 0, pa, 0, lane);
#pragma unroll
        for (int s = 0; s < 2; ++s)
#pragma unroll
            for (int i = 0; i < 4; ++i) { ax.oA = (pa * 8 + 2 * i) * 18432; attn_issueA(lds, ax, i, s, s); }
    }
    u32x4 xv[9];
    if (wid8 == 0) {
        const unsigned v = (lane >> 3) * 2304 + (lane & 7) * 16;
        const auto rx = RSRC(x0 + (size_t)slot2row(b * 8) * 1152);
#pragma unroll
        for (int i = 0; i < 9; ++i) asm volatile("s_nop 4\n\tbuffer_load_dwordx4 %0, %1, %2, 0 offen offset:%3" : "+v"(xv[i]) : "v"(v), "s"(rx), "i"(i * 128) : "memory");
        const auto rw = RSRC(ws);
        const u32x4 ones = {~0u, ~0u, ~0u, ~0u}, zero = {0u, 0u, 0u, 0u};
        const unsigned vo = lane * 16;
        ST16(rw, vo, (unsigned)OFF_ZST + b * 2048, ones, 16); ST16(rw, vo, (unsigned)OFF_ZST + b * 2048 + 1024, ones, 16);
        ST16(rw, vo, (unsigned)OFF_CNT + b * 2560, zero, 16); ST16(rw, vo, (unsigned)OFF_CNT + b * 2560 + 1024, zero, 16);
        ST16(rw, vo, lane < 32 ? (unsigned)OFF_CNT + b * 2560 + 2048 : (unsigned)(OFF_L0 + L_STB) + b * 512 - 512, zero, 16);
        ST16(rw, vo, (unsigned)(OFF_L0 + L_O) + (b >> 1) * 18432 + 16384 + (b & 1) * 1024, zero, 16);
        ST16(rw, b < 12 ? vo : 0x80000000u, (unsigned)OFF_FL2 + b * 1024, zero, 16);
    }
    PST(1);
    if (wjob) {
        const Sel k = make_sel();
        const LAS char* s0 = opaque(img + jw * 6144 + (q * 8 + (c >> 2)) * 64 + (c0 & 15) + (c & 3) * 8);
        const LAS char* s1 = opaque(img + jw * 6144 + (q * 8 + (c >> 2)) * 64 + (c0 & 15) + 32 + min(c & 3, 2) * 2048);
        const auto rd = RSRC(ws + OFF_TWQKV + (size_t)(hp * 18 + jw * 3) * 6144 + (3 * odd + part) * 1024), rd2 = RSRC(ws + OFF_TWT + (size_t)((hp * 3 + part) * 18 + jw * 3) * 256);
        const unsigned vd2 = pin(c < 12 && !odd ? (unsigned)((c >> 2) * 256 + q * 64 + (c & 3) * 16) : 0x80000000u);
        asm volatile("" :: "s"(rd), "s"(rd2));
        WAIT_V(0);
        PST(2);
        u32x4 f[4];
        float sm[4] = {0.f, 0.f, 0.f, 0.f}, m0, m1;
#pragma unroll
        for (int i = 0; i < 4; ++i) f[i] = tr_frag(i < 3 ? s0 + i * 2048 : s1, 256);
#pragma unroll
        for (int e = 0; e < 16; ++e) sm[(e >= 12) * 2 + (e & 1)] = sum2(f[e >> 2][e & 3], sm[(e >= 12) * 2 + (e & 1)]);
        sm[2] = c < 12 ? sm[2] + sm[3] : 0.f;
        sm[2] += dpp_ror(sm[2], 4); sm[2] += dpp_ror(sm[2], 8);
        column_means<6>(lds, lds + G_WORD + 12, jw, lane, sm[0] + sm[1], sm[2], m0, m1);
        PST(3);
        f32x2 e0[4] = {{-m0, -m0}, {-m0, -m0}, {-m0, -m0}, {-m0, -m0}}, e1[4] = {{-m1, -m1}, {-m1, -m1}, {-m1, -m1}, {-m1, -m1}};
#pragma unroll
        for (int i = 0; i < 3; ++i) { centre_frag(f[i], e0, m0, k); ST16(rd, lane * 16, i * 6144, f[i], 16); }
        centre_frag(f[3], e1, m1, k); ST16(rd2, vd2, 0, f[3], 16);
        PST(4);
        lds_arrive(lds + G_WORD + 12);
    }
    if (wid8 != 4) ep = epoch_of(ws);
    {
        if (wjob) {
            const unsigned fo = pin((unsigned)OFF_FW + ((lane >> 3) * 16 + hp) * 1024 + (3 * odd + part) * 128 + (lane & 7) * 16);
            WAIT_V(0);
            PST(5);
            if (lds_arrive(lds + G_WORD + 4) == 5) {
                const u32x4 e4 = {(unsigned)ep, (unsigned)(ep >> 32), (unsigned)ep, (unsigned)(ep >> 32)};
                ST16(RSRC(ws), fo, 0, e4, 16); ST16(RSRC(ws), fo + 131072, 0, e4, 16);
            }
        }
    }
    if (wid8 == 0) {
        WAIT_V(7);
        asm volatile("" : "+v"(xv[0]), "+v"(xv[1]), "+v"(xv[2]), "+v"(xv[3]), "+v"(xv[4]), "+v"(xv[5]), "+v"(xv[6]), "+v"(xv[7]), "+v"(xv[8]));
        float s = 0.f, q2 = 0.f;
#pragma unroll
        for (int e = 0; e < 36; ++e) { s = sum2(xv[e >> 2][e & 3], s); q2 = dot2(xv[e >> 2][e & 3], xv[e >> 2][e & 3], q2); }
        ST8(RSRC(ws + OFF_L0 + L_STA + (size_t)b * 512), lane * 8, 0, ((u32x2){__float_as_uint(s), __float_as_uint(q2)}), 16);
    }
    if (ojob) {
        WAIT_V(0);
        Retile rt;
        retile_out_make<288>(rt, img + wid8 * 9216, lane); retile_out_store(rt, ws + OFF_TWO, 18, uo >> 2, uo & 3, lane);
    }
    PST(7);
    PST_DUMP(wid8 == 0 || wid8 == 4 || wid8 == 5, wid8 ? wid8 : 2);
    return ep;
}

__global__ __launch_bounds__(512, 2) TRACE_ATTR void k_net(char* ws, const u16* wsp, const u16* bsp, char* out, const char* x0, const char* wqkv, const u16* wo,
                                                             const u16* w_in, const u16* w_out DBG_ARG) {
    extern __shared__ __attribute__((aligned(16))) char smem[];
    LAS char* lds = (LAS char*)smem;
    asm volatile("gm_code_begin:");
    if (blockIdx.x < 16) {
        unsigned long long pc; int d0, len;
        asm volatile("s_getpc_b64 %0\n1:\n\ts_mov_b32 %1, gm_code_begin-1b\n\ts_mov_b32 %2, gm_code_end1-gm_code_begin\n\ts_max_i32 %2, %2, gm_code_end2-gm_code_begin"
                     : "=s"(pc), "=s"(d0), "=s"(len) :: "scc");
        const auto rs = __builtin_amdgcn_make_buffer_rsrc((void*)((pc + d0) & ~15ull), 0, len, 0x00020000);
        dma1((unsigned)(size_t)lds + G_OWN, rs, 0, (threadIdx.x * 2 + (blockIdx.x >> 3)) * 128);
    }
    STAMP_DECL;
    const int wid8 = __builtin_amdgcn_readfirstlane(threadIdx.x >> 6);
    unsigned long long ep;
    {
        NET_IDS
        ep = prologue(lds, ws, x0, wqkv, wo, w_in, b, pa, wid8, [&](int part) {
            if (part == 0) { asm volatile("" :: "s"(wqkv), "s"(w_in), "s"(w_out)); return; }
            ws = own_s(ws); wsp = own_s(wsp); bsp = own_s(bsp); out = own_s(out); x0 = own_s(x0); wo = own_s(wo); w_out = own_s(w_out);
        } PST_FWD);
    }
    const char *twin = ws + OFF_TWIN, *twout = ws + OFF_TWOUT;
    if (wid8 >= 4) {
        u32x4 wr[5][9];
#pragma unroll 1
      for (int layer = 0; layer < NET_LAYERS; ++layer) {
        NET_IDS
        TRACE_BEGIN;
        char* wl = ws + OFF_L0 + layer * SZ_L;
        const unsigned fl = layer * F_LAYER;
        const char *xin = wl + L_XB, *st = wl + L_STB, *z2t = wl + (layer ? L_Y : L_Z2T);
        unsigned long long* zst = (unsigned long long*)(ws + OFF_ZST + layer * SZ_ZST);
        const int lane = lane_id_fresh(), l3 = lane >> 3;
        const unsigned vo = lane * 16;
        const unsigned l0 = (unsigned)(size_t)lds, lA = l0 + wid * 4096, lB = l0 + 16384 + wid * 4608;
        const auto rX = RSRC(xin + (size_t)(pm * 8 + 2 * wid) * 18432), rI = RSRC(twin + (size_t)cg * (18 * 18432) + wid * 4608);
        auto issue1B = [&](int g, int slot) { const int k = kord(g < 9 ? g : (g < 13 ? g - 4 : g - 13), 9); dma5<true>(lB + slot * G_STAGE, rI, (g < 9 ? 9 + k : k) * 18432, vo); };
        auto issue1A = [&](int g, int slot) { dma4<true>(lA + slot * G_STAGE, rX, kord(g < 9 ? g : g - 13, 9) * 2048, vo, vo, vo + 16384, vo + 16384); };
        auto issue1 = [&](int g, int slot) { issue1B(g, slot); issue1A(g, slot); };
        const auto rA = RSRC((const char*)wsp + (size_t)(pm * 128 + wid * 32) * 4096), rB = RSRC(z2t + cg * 36864 + wid * 4608);
        const unsigned voA = (unsigned)(2 * l3 * 4096 + ((lane & 7) ^ l3) * 16);
        auto issueB = [&](int k, int slot) { dma5<true>(lB + slot * G_STAGE, rB, (k >> 1) * 589824 + (k & 1) * 18432, vo); };
        const auto rW = RSRC(twout + (size_t)c5 * (72 * 4608));
        auto issue5 = [&](int t) { dma5<true>(l0 + ((t + 6) & 7) * K5_STAGE + wid * 4608, rW, kord5(t) * K5_STAGE + wid * 4608, vo); };
        auto load5 = [&](int p, int i) {
            const int t0 = p ? 2 * p + 6 : 4, ow = kord5(t0 + i) * K5_STAGE + wid * 4096;
            asm volatile("s_nop 4\n\tbuffer_load_dwordx4 %0, %4, %5, %6 offen sc1\n\tbuffer_load_dwordx4 %1, %4, %5, %6 offen offset:1024 sc1\n\t"
                         "buffer_load_dwordx4 %2, %4, %5, %6 offen offset:2048 sc1\n\tbuffer_load_dwordx4 %3, %4, %5, %6 offen offset:3072 sc1"
                         : "+v"(wr[p][4 * i]), "+v"(wr[p][4 * i + 1]), "+v"(wr[p][4 * i + 2]), "+v"(wr[p][4 * i + 3]) : "v"(vo), "s"(rW), "s"(ow) : "memory");
            if (i) {
                const int oe = kord5(t0 + (wid >> 1)) * K5_STAGE + 16384 + (wid & 1) * 1024;
                asm volatile("s_nop 4\n\tbuffer_load_dwordx4 %0, %1, %2, %3 offen sc1" : "+v"(wr[p][8]) : "v"(vo), "s"(rW), "s"(oe) : "memory");
            }
        };
        auto put5 = [&](int w0, int w1) {
#pragma unroll
            for (int w = w0; w < w1; ++w) {
                const int p = w / 9, i = w % 9 == 4 ? -1 : w % 9 - (w % 9 > 4);
                LAS char* sl = lds + (((p ? 2 * p + 6 : 4) + 6) & 7) * K5_STAGE + lane * 16;
                if (i < 0) *(LAS u32x4*)(sl + (wid >> 1) * K5_STAGE + 16384 + (wid & 1) * 1024) = wr[p][8];
                else *(LAS u32x4*)(sl + (i >> 2) * K5_STAGE + wid * 4096 + (i & 3) * 1024) = wr[p][i];
            }
            WAIT_L0();
        };
        const int row = wid * 32 + (lane & 31);
        auto step1 = [&](int t) { BAR(); issue1(t + 4, t & 3); };
        const char* ps;
        unsigned fd, fd2;
        {
            unsigned fo, fi;
            const bool oj = !layer && wid && b < 96, ij = !layer && wid && b >= 112, early = win_strip(b) >= WIN_EARLY;
            const int ojob = b * 3 + wid - 1, oimg = 2048 + (wid - 1) * 9216;
            float m0, m1;
            unsigned fw;
            auto win_store = [&](int h) {
                const int u = win_strip(b) * 2 + h, half = u >= 144, g = u - half * 144, ug = g / 9, j = g - ug * 9, c = lane & 15;
                const auto rd = RSRC(ws + OFF_TWIN + (size_t)ug * (18 * 18432) + (half * 18 + win_third(b, wid) * 6) * 9216);
                const unsigned vd = (lane >> 4) * 256 + (j < 8 ? ((j & ~1) + (c >> 3)) * 1024 + ((j & 1) * 8 + (c & 7)) * 16 : 8192 + c * 16);
#pragma unroll
                for (int i = 0; i < 6; ++i) ST16(rd, vd, i * 9216, wr[1 + h][i], 16);
            };
            auto win_half = [&](int h) {
                const Sel k = make_sel();
                const float m = h ? m1 : m0;
                f32x2 er[4] = {{-m, -m}, {-m, -m}, {-m, -m}, {-m, -m}};
#pragma unroll
                for (int i = 0; i < 6; ++i) centre_frag(wr[1 + h][i], er, m, k);
            };
            attn_producer(lds, ws, x0, wl, fl, ep, layer, pa, hp, wid, [&] { if (b < 16) WAIT_V(0); prologue_done(lds, ws, ep, b, lane); }, [&](int part) {
                if (part == -1 && ij) {
#pragma unroll
                    for (int h = 0; h < 2; ++h) {
                        const LAS char* src = lds + 3 * K1_STAGE + (wid - 1) * 12288 + ((lane >> 4) * 8 + ((lane & 15) >> 2)) * 64 + h * 32
                                              + ((win_strip(b) * 2 + h) % 9 < 8 ? (lane & 1) * 16 + (lane & 2) * 4 : (lane & 3) * 8);
#pragma unroll
                        for (int i = 0; i < 6; ++i) wr[1 + h][i] = tr_frag(src + i * 2048, 256);
                    }
                    WAIT_L(0);
                }
                if (part == 0 && oj) {
                    const auto rs = RSRC(w_out + (size_t)(ojob >> 2) * 32 * 576 + (ojob & 3) * 144);
#pragma unroll
                    for (int j = 0; j < 9; ++j) { const int i = lane + 64 * j; dma1(l0 + oimg + j * 1024, rs, 0, (unsigned)(((i * 3641) >> 16) * (1152 - 288) + i * 16)); }
                }
                if (part == 1) {
                    if (layer) { issue1B(0, 0); issue1B(1, 1); }
                    fo = flag_out(fl + F_X1, 256, pa, hp, lane); fi = flag_in(fl + F_X1, 256, pm * 16 + cg, 16, lane);
                    ps = pin(st + (size_t)(pm * 128 + wid * 32) * 64 + lane * 16);
                    fd = pin((unsigned)OFF_FL2 + wid * 1024 + vo);
                    fd2 = pin((unsigned)OFF_FL2 + (lane < 32 ? 256 + lane : 288 + ((cg * 9) >> 1) * 3 + min(lane & 15, 14)) * 16);
                    fw = pin((unsigned)OFF_FL2 + (288 + (72 + ((cg * 9) >> 1)) * 3 + min(lane & 15, 14)) * 16);
                    if (ij) {
                        float sm[4] = {0.f, 0.f, 0.f, 0.f};
#pragma unroll
                        for (int e = 0; e < 48; ++e) sm[(e >= 24) * 2 + (e & 1)] = sum2(wr[1 + (e >= 24)][(e % 24) >> 2][e & 3], sm[(e >= 24) * 2 + (e & 1)]);
                        column_means<3>(lds, lds + G_WORD, wid - 1, lane, sm[0] + sm[1], sm[2] + sm[3], m0, m1);
                        win_half(0);
                    }
                }
                if (part == 2 && ij) { if (early) win_store(0); win_half(1); if (early) win_store(1); }
                if (part == 2 && oj) { retile_out_make<288>(wr[1], lds + oimg, lane); WAIT_L(0); }
            });
            if (oj || ij) {
                if (oj || !early) {
                    __builtin_amdgcn_s_sleep(OJOB_SLEEP);
                    if (oj) retile_out_store(wr[1], ws + OFF_TWOUT, 72, ojob >> 2, ojob & 3, lane); else { win_store(0); win_store(1); }
                }
                WAIT_V(0);
                flag_raise(ws, lane ? 0x80000000u : (unsigned)OFF_FL2 + (oj ? ojob : 288 + win_strip(b) * 3 + wid - 1) * 16);
            }
            if (layer == 0) {
                if (ij) __builtin_amdgcn_s_sleep(WIN_LOOK - 12); else if (!oj) __builtin_amdgcn_s_sleep(WIN_LOOK);
                flags_wait<0, 8>(ws, fw);
            }
            BARX();
            if (wid == 0) flag_raise(ws, fo);
            if (layer == 0) { issue1B(0, 0); issue1B(1, 1); }
            issue1B(2, 2); issue1B(3, 3);
            asm volatile("" :: "s"(rX));
            const int o0 = pin_s(kord(0, 9) * 2048);
            const unsigned v2 = pin(vo + 16384);
            if (wid == 0) { if (layer) __builtin_amdgcn_s_sleep(X1_FIRST); flags_wait<0>(ws, fi);
#pragma unroll
                for (int i = 0; i < 4; ++i) dma4<true>(lA + i * 4096, rX, o0 + i * 36864, vo, vo, v2, v2);
            }
            BARX();
        }
        issue1A(1, 1); issue1A(2, 2);
        WAIT_V(8); BAR();
        issue1A(3, 3);
        {
            f32x4 sp[3];
            WAIT_V(8); step1(0);
            WAIT_V(13); step1(1);
            WAIT_V(18); step1(2);
            unsigned got[2];
            if (layer == 0) asm volatile("s_nop 4\n\tbuffer_load_dword %0, %2, %4, 0 offen sc1\n\tbuffer_load_dword %1, %3, %4, 0 offen sc1" : "+v"(got[0]), "+v"(got[1]) : "v"(fd), "v"(fd2), "s"(RSRC(ws)) : "memory");
            WAIT_IF(layer, 18, 20); step1(3);
            WAIT_IF(layer, 18, 20); step1(4);
            WAIT_V(18);
            if (layer == 0) {
                asm volatile("" : "+v"(got[0]), "+v"(got[1]));
                if (__any((got[0] & got[1]) == 0u)) { flags_wait<0>(ws, fd); flags_wait<0>(ws, fd2); }
            }
            BAR(); issue1B(9, 1); side_load(sp, ps, ps); if (layer == 0) load5(3, 0);
            WAIT_IF(layer, 17, 21); BAR(); issue1B(10, 2); if (layer == 0) load5(3, 1);
            WAIT_IF(layer, 5, 14); BARX();
            side_fence(sp);
#pragma unroll
            for (int i = 0; i < 2; ++i) lds_word_set(lds + G_TAB + (wid * 32 + i * 16 + (lane >> 2)) * 4, __float_as_uint(ln_rstd(sp[i])));
            issue1B(11, 3);
            WAIT_L0();
        }
        BAR(); issue1B(12, 0);
        if (layer == 1) {
            const auto rd = RSRC(out + (size_t)b * 9216 + wid * 2304);
            ST16(rd, vo, 0, ((u32x4){0u, 0u, 0u, 0u}), 16); ST16(rd, vo, 1024, ((u32x4){0u, 0u, 0u, 0u}), 16);
            if (lane < 16) ST16(rd, vo, 2048, ((u32x4){0u, 0u, 0u, 0u}), 16);
        }
        if (layer == 0) {
#pragma unroll
            for (int p = 0; p < 6; ++p) load5(p >> 1, p & 1);
        }
        WAIT_V(0);
        __builtin_amdgcn_s_setprio(3);
        const char* src = pin((const char*)zst + (size_t)(pm * 16 + (lane >> 4) * 4) * 1024 + wid * 256 + (lane & 15) * 16);
        const unsigned tw = pin(l0 + G_TAB + 512 + (lane >> 5) * 512 + (wid * 32 + (lane & 15) * 2 + ((lane >> 4) & 1)) * 4);
        const unsigned fz = flag_out(fl + F_Z2, 256, cg, pm, lane), fzi = pin(fl + F_Z2 + (cg * 16 + pm) * 256 + ((lane ^ pm) & 8) * 16 + (lane & 7) * 16);
        const unsigned vA[4] = {pin(voA), pin(voA + 16 * 4096 - 1024), pin(voA + 4096 - 2048), pin(voA + 17 * 4096 - 3072)};
        auto spA = [&](int so, unsigned la) { dma4<false>(la, rA, so, vA[0], vA[1], vA[2], vA[3]); };
        auto issueA = [&](int k, int slot) { spA(k * 128, lA + slot * G_STAGE); };
        {
            const LAS char* pp = opaque(lds + G_PART + row * 8);
            const auto rs = RSRC(zst + (size_t)(pm * 16 + cg) * 128);
            const unsigned vp = pin(lane < 32 ? (unsigned)row * 8u : 0x80000000u);
            asm volatile("" :: "s"(rs));
            BARX();
            const f32x2 a = *(const LAS f32x2*)pp, c = *(const LAS f32x2*)(pp + 1024);
            ST8(rs, vp, 0, ((u32x2){__float_as_uint(add1(a.x, c.x)), min(__float_as_uint(add1(a.y, c.y)), 0xfffffffeu)}), 16);
            SCHED0();
        }
        __builtin_amdgcn_s_setprio(0);
        {
            f32x4 g[4];
            float sm[4];
            auto gather = [&] {
                asm volatile("global_load_dwordx4 %0, %4, off sc1\n\tglobal_load_dwordx4 %1, %4, off offset:1024 sc1\n\t"
                             "global_load_dwordx4 %2, %4, off offset:2048 sc1\n\tglobal_load_dwordx4 %3, %4, off offset:3072 sc1"
                             : "=&v"(g[0]), "=&v"(g[1]), "=&v"(g[2]), "=&v"(g[3]) : "v"(src) : "memory");
            };
            auto total = [&] {
                asm volatile("" : "+v"(g[0]), "+v"(g[1]), "+v"(g[2]), "+v"(g[3]));
#pragma unroll
                for (int i = 0; i < 4; ++i) sm[i] = add1(add1(g[0][i], g[1][i]), add1(g[2][i], g[3][i]));
            };
            step1(9); step1(10);
            BARX(); gather(); issue1(15, 3);
            WAIT_V(22); BARX();
            __builtin_amdgcn_s_setprio(3);
            WAIT_V(9);
            total();
            if (__any(__builtin_isunordered(sm[1], sm[3]))) {
#pragma unroll 1
                for (unsigned spin = 0; spin < 400000u; ++spin) {
                    bool ok = true;
#pragma unroll
                    for (int i = 0; i < 4; ++i) ok &= __float_as_uint(g[i][1]) != 0xffffffffu && __float_as_uint(g[i][3]) != 0xffffffffu;
                    if (__all(ok)) break;
                    gather();
                    WAIT_V(0);
                    total();
                }
            }
            xq4_sum(sm);
            const bool odd = lane & 16;
            float mean;
            const float rs = rstd_of(odd ? sm[2] : sm[0], odd ? sm[3] : sm[1], 1.0f / 2304.0f, mean);
            asm volatile("ds_write_b32 %0, %1" :: "v"(tw), "v"(lane < 32 ? rs : -mean * rs) : "memory");
            __builtin_amdgcn_s_setprio(0);
            issue1B(16, 0);
            WAIT_L0();
        }
        BARX(); issue1A(16, 0); issueA(2 * pm, 1);
        auto kord2 = [&](int t) { const int j = kord(t - 2, 30); return ((j < 2 * (pm & 7) ? j : j + 2) + 2 * (pm & 8)) & 31; };
        unsigned seen;
        auto look = [&] { if (wid == 0) asm volatile("s_nop 4\n\tbuffer_load_dword %0, %1, %2, 0 offen sc1" : "+v"(seen) : "v"(fzi), "s"(RSRC(ws)) : "memory"); };
        auto need = [&](int n) {
            if (wid == 0) {
                asm volatile("" : "+v"(seen));
                if (__ballot(seen == 0u) & ((1ull << n) - 1ull)) flags_wait<0>(ws, lane < n ? fzi : fl + F_Z2 + (cg * 16 + pm) * 256 + pm * 16);
            }
        };
        auto issue = [&](int t, int sl) { const int k = kord2(t); issueB(k, sl); issueA(k, sl); };
        auto steps2 = [&](int t0, int t1) {
            int sl = (2 * t0 + 2) % 3 * G_STAGE, k = kord2(t0 + 3), ob = (k >> 1) * 589824 + (k & 1) * 18432;
            LOOP_ALIGN();
#pragma unroll 1
            for (int t = t0; t < t1; ++t) {
                WAIT_V(9); BARX();
                dma5<true>(lB + sl, rB, ob, vo);
                const int oa = pin_s(k * 128);
                const unsigned la = pin_s(lA + sl);
                k = pin_s(kord2(t + 4)); ob = pin_s((k >> 1) * 589824 + (k & 1) * 18432);
                sl = pin_s(sl == 0 ? 2 * G_STAGE : sl - G_STAGE);
                spA(oa, la);
            }
        };
        WAIT_V(13); BAR(); issue1(17, 2);
        WAIT_V(13); BARX();
        if (wid == 0) flag_raise(ws, fz);
        issueA(2 * pm + 1, 3); if (layer == 0) load5(4, 0);
        if (wid == 0) WAIT_IF(layer, 5, 9); else WAIT_IF(layer, 4, 8);
        BAR(); issueA(kord2(2), 0); if (layer == 0) load5(4, 1);
        BAR(); look(); issueA(kord2(3), 2);
        WAIT_V(4); need(3); BARX(); issueB(kord2(2), 0); issueB(kord2(3), 2); look();
        WAIT_IF(wid == 0, 6, 5); BAR(); issue(4, 1);
        WAIT_V(9); need(8);
        steps2(2, 11); look();
        WAIT_IF(wid == 0, 10, 9); BAR(); issue(14, 0);
        WAIT_V(9); need(16);
        steps2(12, 29);
        WAIT_V(9); BAR(); issue5(0); issue5(1); issue5(2);
        WAIT_V(15); BAR(); issue5(6); issue5(7);
        BAR(); __builtin_amdgcn_s_sleep(W3_SLEEP); issue5(3); put5(0, 9);
        unsigned vb;
        {
            vb = attn_vb(wid, lane);
            const unsigned fo = flag_out(fl + F_Y, 256, pm, cg, lane), fi = flag_in(fl + F_Y, 256, rh * 8 + c5, 16, lane);
            __builtin_amdgcn_s_setprio(3);
            WAIT_V(0); BARX();
            if (wid == 0) { flag_raise(ws, fo); flags_wait<Y_FIRST>(ws, fi); }
            __builtin_amdgcn_s_setprio(0);
            BARX();
        }
        put5(9, 13);
        BAR(); put5(13, 18);
        BAR(); put5(18, 24);
        BAR(); put5(24, 30);
        BAR(); put5(30, 36);
        BAR(); issue5(16); put5(36, 41);
        BAR(); issue5(17); put5(41, 45);
        WAIT_V(5); BAR();
        WAIT_V(0); BAR();
        if (layer == 0) { attn_issueB(lds, ws, hp, wid, vb, 0, 0); attn_issueB(lds, ws, hp, wid, vb, 1, 1); }
        BAR();
        if (layer == 0) {
            __builtin_amdgcn_s_setprio(3);
            const unsigned fo = flag_out(F_X2, 512, (rh >> 1) + wid, wid ? 16 + c5 : (rh & 1) * 8 + c5, wid == 0 || (wid == 1 && (rh & 1) && rh < 31) ? lane : 16);
            BARX();
            if (wid < 2) flag_raise(ws, fo);
        }
        TRACE_DUMP;
      }
        if (blockIdx.x == 0 && wid8 == 4) ST8(RSRC(ws), lane_id_fresh() ? 0x80000000u : (unsigned)OFF_EP, 0, ((u32x2){(unsigned)ep, (unsigned)(ep >> 32)}), 16);
        asm volatile("gm_code_end1:");
        return;
    }
#pragma unroll 1
  for (int layer = 0; layer < NET_LAYERS; ++layer) {
    NET_IDS
    TRACE_BEGIN;
    char* wl = ws + OFF_L0 + layer * SZ_L;
    const unsigned fl = layer * F_LAYER;
    const char* xin = wl + L_XB;
    char* y = wl + (layer ? L_Z2T : L_Y);
#undef STAMP_SEC
#define STAMP_SEC 0
    {
    STAMP_BEGIN;
    attn_consumer(lds, x0, wl, layer, pa, hp, wid, [&] { WAIT_V(0); prologue_done(lds, ws, ep, b, lane_id_fresh()); } STAMP_FWD);
    STAMP_END(1 + 5 * layer);
    }
#undef STAMP_SEC
#define STAMP_SEC 1
    STAMP_BEGIN;
    gmlp_body(lds, wid & 1, wid >> 1, pm, cg, wl + (layer ? L_Y : L_Z2T), y, bsp STAMP_FWD);
    {
        const int lane = lane_id_fresh(), r = lane & 15, q = lane >> 4;
        const int wk = wid >> 1, rt = rh * 4 + (wid & 1) * 2 + wk;
        const ProjOut pr = proj_out((char*)xin, (char*)xin, rt, c5, lane);
        const LAS char *pw[3], *ph[3];
#pragma unroll
        for (int i = 0; i < 3; ++i) {
            pw[i] = opaque(lds + 3 * i * K5_STAGE + wk * 4608 + lane * 16);
            ph[i] = opaque(lds + 3 * i * K5_STAGE + wk * 4608 + 4096 + q * 128 + (r & 7) * 16);
        }
        const auto rY = RSRC(y + (size_t)(rt & ~1) * 73728 + wk * 1024);
        const unsigned vy[2] = {pin((unsigned)(wk * 73728 + lane * 16)), pin((unsigned)((1 - wk) * 73728 + lane * 16))};
        int dk[4];
#pragma unroll
        for (int i = 0; i < 4; ++i) dk[i] = pin_s((kord5(4 + i) - 4 - i) * 4096);
        auto yoff = [&](int t) { return pin_s(t == 0 || t == 17 ? kord5(t) * 4096 : t * 4096 + dk[t & 3]); };
        bf16x8 af[5][4], wf[4][5];
        auto lda = [&](int so, int i) { return __builtin_bit_cast(bf16x8, __builtin_amdgcn_raw_buffer_load_b128(rY, vy[i & 1] + (i >> 1) * 2048, so, 16)); };
        auto rdw = [&](int t, int c, int n) {
            const int sl = (t + 6) & 7, o = (sl % 3) * K5_STAGE + c * 9216;
            wf[2 * (t & 1) + c][n] = n < 4 ? LDS_FRAG(pw[sl / 3] + o + n * 1024) : LDS_FRAG(ph[sl / 3] + o);
        };
        int so[18];
#pragma unroll
        for (int i = 0; i < 5; ++i) so[i] = yoff(i);
        so[17] = yoff(17);
        LAS char* xo = (LAS char*)opaque(lds + wid * 4608 + lane * 16);
        LAS char* xo4 = (LAS char*)opaque(lds + wid * 4608 + 4096 + (lane & 31) * 16);
        const LAS char* xi = opaque(lds + (wid ^ 2) * 4608 + lane * 16);
        const LAS char* xi4 = opaque(lds + (wid ^ 2) * 4608 + 4096 + (lane & 31) * 16);
        const ProjOut po = layer ? proj_out_final(out, rt, c5, lane) : proj_out(wl + SZ_L + L_XA, wl + SZ_L + L_STA, rt, c5, lane);
        asm volatile("" :: "s"(rY));
        SCHED0();
        WAIT_V(0);
        BARX();
        STAMP(11);
        const Res rx = proj_residual_load(pr);
#pragma unroll
        for (int n = 0; n < 10; ++n) rdw(0, n / 5, n % 5);
        f32x4 v[5], u[5];
        proj_residual(v, rx);
#pragma unroll
        for (int n = 0; n < 5; ++n) v[n] = pin(v[n]);
        WAIT_L(0);
        BARX();
        STAMP(12);
#pragma unroll
        for (int i = 0; i < K5_PRE; ++i) { af[i >> 2][i & 3] = lda(so[i >> 2], i & 3); SCHED0(); if (i >= PACE0 && i + 1 < K5_PRE) PACE; SCHED0(); }
        static_for<18>([&](auto tc) {
            constexpr int t = tc.value;
            bf16x8 (&a)[4] = af[t % 5];
#pragma unroll
            for (int h = 0; h < 4; ++h) {
                const int c = t < 17 ? h >> 1 : h & 1, m = t < 17 ? h & 1 : 1 - (h >> 1);
                if (t < 17 ? m == 0 : h < 2) { if (t == 17 && h == 1) WAIT_L(0); else WAIT_L(5); }
#pragma unroll
                for (int n = 0; n < 5; ++n) {
                    f32x4& e = m ? u[n] : v[n];
                    if (t == 0 && h == 1) MFMA_0(wf[0][n], a[1], e); else MFMA_T(wf[2 * (t & 1) + c][n], a[2 * c + m], e);
                    SCHED0();
                    if (t < 17 && m == 0) rdw(t + 1, c, n);
                    if (t < 14 && m == 1 && !(n & 1)) { const int i = y_load_at(6 * t + 3 * c + n / 2); if (i >= 0) af[(i >> 2) % 5][i & 3] = lda(so[i >> 2], i & 3); }
                    if (t < 12 && h == 3 && n == 3) so[t + 5] = yoff(t + 5);
                    if (t == 17 && h * 5 + n >= 13 && h * 5 + n < 18) { const int i = h * 5 + n - 13; *(LAS f32x4*)(i < 4 ? xo + i * 1024 : xo4) = u[i]; }
                    SCHED0();
                }
            }
            if ((t & 1) && t < 17) BAR();
            SCHED0();
        });
        {
            WAIT_L0(); BARX();
#pragma unroll
            for (int n = 0; n < 5; ++n) u[n] = *(const LAS f32x4*)(n < 4 ? xi + n * 1024 : xi4);
            SCHED0();
            asm volatile("s_nop 1" : "+v"(v[0]), "+v"(v[1]), "+v"(v[2]), "+v"(v[3]), "+v"(v[4]));
#pragma unroll
            for (int n = 0; n < 5; ++n) v[n] += u[n];
        }
        STAMP(13);
        if (layer) proj_store<true>(v, po);
        else { proj_store(v, po); WAIT_V(0); BARX(); }
    }
    STAMP_END(3 + 5 * layer);
    TRACE_DUMP;
  }
  asm volatile("gm_code_end2:");
}

}

#define STOP_CHECK(n)

extern "C" void kernel_launch(void* const* d_in, const int* in_sizes, int n_in, void* d_out, int out_size, void* d_ws, size_t ws_size,
                              hipStream_t stream) {
    (void)in_sizes; (void)n_in; (void)out_size; (void)ws_size;
    const u16* x = (const u16*)d_in[0];
    const u16* wqkv = (const u16*)d_in[1];
    const u16* wo = (const u16*)d_in[2];
    const u16* w_in = (const u16*)d_in[4];
    const u16* w_sp = (const u16*)d_in[5];
    const u16* b_sp = (const u16*)d_in[6];
    const u16* w_out = (const u16*)d_in[7];
    char* ws = (char*)d_ws;
    (void)hipFuncSetAttribute((const void*)k_net, hipFuncAttributeMaxDynamicSharedMemorySize, NET_LDS);
    k_net<<<256, 512, NET_LDS, stream>>>(ws, w_sp, b_sp, (char*)d_out, (const char*)x, (const char*)wqkv, wo, w_in, w_out DBG_LAUNCH(0));
}
```

```cpp
#include <hip/hip_runtime.h>
#include <stdint.h>

namespace {

typedef unsigned short u16;
typedef short bf16x8 __attribute__((ext_vector_type(8)));
typedef float f32x4 __attribute__((ext_vector_type(4)));
typedef float f32x2 __attribute__((ext_vector_type(2)));
typedef unsigned u32x4 __attribute__((ext_vector_type(4)));
typedef unsigned u32x2 __attribute__((ext_vector_type(2)));
typedef __bf16 bf16x2_t __attribute__((ext_vector_type(2)));
#define LAS __attribute__((address_space(3)))
#define DEVI __device__ __forceinline__

constexpr int S_ = 2048, D_ = 576, FH = 2304;

constexpr size_t SZ_TWQKV = (size_t)16 * 18 * 6 * 1024;
constexpr size_t SZ_TWT = (size_t)48 * 18 * 256;
constexpr size_t SZ_TWO = (size_t)8 * 18 * 4608;
constexpr size_t SZ_TWIN = (size_t)16 * 18 * 18 * 1024;
constexpr size_t SZ_TWOUT = (size_t)8 * 72 * 4608;
constexpr size_t SZ_X = (size_t)S_ * D_ * 2;
constexpr size_t SZ_Z = (size_t)S_ * FH * 2;
constexpr size_t SZ_ST = (size_t)S_ * 64;
constexpr size_t SZ_ZST = (size_t)16 * 16 * 128 * 8;
constexpr size_t OFF_TWQKV = 0;
constexpr size_t OFF_TWT = OFF_TWQKV + SZ_TWQKV;
constexpr size_t OFF_TWO = OFF_TWT + SZ_TWT;
constexpr size_t OFF_TWIN = OFF_TWO + SZ_TWO;
constexpr size_t OFF_TWOUT = OFF_TWIN + SZ_TWIN;
constexpr size_t OFF_CNT = OFF_TWOUT + SZ_TWOUT;
constexpr size_t SZ_CNT = 10 * 65536;
constexpr size_t OFF_FW = OFF_CNT + SZ_CNT;
constexpr size_t OFF_FL = OFF_FW + 262144;
constexpr size_t OFF_EP = OFF_FL + 4096;
constexpr size_t OFF_FL2 = OFF_EP + 4096;
constexpr size_t OFF_ZST = OFF_FL2 + 12288;
constexpr size_t OFF_L0 = OFF_ZST + 2 * SZ_ZST;
constexpr size_t L_XA = 0, L_STA = L_XA + SZ_X, L_O = L_STA + SZ_ST, L_XB = L_O + SZ_X, L_STB = L_XB + SZ_X, L_Z2T = L_STB + SZ_ST, L_Y = L_Z2T + SZ_Z;
constexpr size_t SZ_L = 0;
static_assert(OFF_L0 + L_Y + SZ_Z <= ((size_t)256 << 20), "workspace");
constexpr unsigned F_O = OFF_CNT, F_X1 = F_O + 65536, F_Z2 = F_O + 131072, F_Y = F_O + 196608, F_LAYER = 262144, F_X2 = F_O + 2 * F_LAYER;

DEVI unsigned pack2(float a, float b) { f32x2 v = {a, b}; bf16x2_t r = __builtin_convertvector(v, bf16x2_t); return __builtin_bit_cast(unsigned, r); }
DEVI float bflo(unsigned u) { return __uint_as_float(u << 16); }
DEVI float bfhi(unsigned u) { return __uint_as_float(u & 0xffff0000u); }
DEVI int slot2row(int sg) { return (sg & ~31) + 2 * (sg & 15) + ((sg >> 4) & 1); }
DEVI float fmax2(float a, float b) { return __builtin_amdgcn_fmed3f(a, b, __builtin_inff()); }
#define XQ4(OP, MID) \
    asm volatile("s_nop 1\n\tv_permlane32_swap_b32 %0, %1\n\tv_permlane32_swap_b32 %2, %3\n\t" OP " %0, %0, %1\n\t" OP " %2, %2, %3\n\t" \
                 "s_nop 1\n\tv_permlane16_swap_b32 %0, %2\n\t" OP " %0, %0, %2\n\t" MID \
                 "v_mov_b32 %2, %0\n\ts_nop 1\n\tv_permlane16_swap_b32 %0, %2\n\tv_mov_b32 %1, %0\n\tv_mov_b32 %3, %2\n\ts_nop 0\n\t" \
                 "v_permlane32_swap_b32 %0, %1\n\tv_permlane32_swap_b32 %2, %3" : "+v"(x[0]), "+v"(x[1]), "+v"(x[2]), "+v"(x[3]))
DEVI void xq4_max(float (&x)[4]) { XQ4("v_max_f32", ""); }
DEVI void xq4_sum(float (&x)[4]) { XQ4("v_add_f32", ""); }
DEVI void xq4_sum_rcp(float (&x)[4]) { XQ4("v_add_f32", "v_rcp_f32 %0, %0\n\ts_nop 0\n\t"); }
template <class T> DEVI T pin(T v) { asm volatile("" : "+v"(v)); return v; }
template <int V> struct IC { static constexpr int value = V; };
template <int N, int I = 0, class F> DEVI void static_for(F&& f) { if constexpr (I < N) { f(IC<I>{}); static_for<N, I + 1>(f); } }
template <class T> DEVI T* own_s(T* p) { T* r; asm volatile("s_mov_b64 %0, %1" : "=&s"(r) : "s"(p)); return r; }
DEVI int pin_s(int v) { asm volatile("" : "+s"(v)); return v; }
DEVI float xq_sum(float v) {
    auto a = __builtin_amdgcn_permlane32_swap(__float_as_uint(v), __float_as_uint(v), false, false);
    v = __uint_as_float(a[0]) + __uint_as_float(a[1]);
    auto b = __builtin_amdgcn_permlane16_swap(__float_as_uint(v), __float_as_uint(v), false, false);
    return __uint_as_float(b[0]) + __uint_as_float(b[1]);
}
#define RSRC(p) __builtin_amdgcn_make_buffer_rsrc((void*)(p), 0, 0x7fffffff, 0x00020000)
#define ST16(rs, vo, so, v, aux) __builtin_amdgcn_raw_buffer_store_b128((v), (rs), (vo) + (so), 0, aux)
#define ST8(rs, vo, so, v, aux) __builtin_amdgcn_raw_buffer_store_b64((v), (rs), (vo), (so), aux)
#define WAIT_V(n) asm volatile("s_waitcnt vmcnt(%0)" ::"i"(n) : "memory")
#define WAIT_L0() asm volatile("s_waitcnt lgkmcnt(0)" ::: "memory")
#define BAR() do { asm volatile("" ::: "memory"); __builtin_amdgcn_s_barrier(); asm volatile("" ::: "memory"); } while (0)
#define BARX() do { __builtin_amdgcn_sched_barrier(0); BAR(); __builtin_amdgcn_sched_barrier(0); } while (0)
#define LOOP_ALIGN() asm volatile(".p2align 6")
#define SCHED0() __builtin_amdgcn_sched_barrier(0)
#define MFMA(a, b, c) __builtin_amdgcn_mfma_f32_16x16x32_bf16((a), (b), (c), 0, 0, 0)
#define MFMA_T(a, b, c) asm volatile("v_mfma_f32_16x16x32_bf16 %0, %1, %2, %0" : "+v"(c) : "v"(a), "v"(b))
#define MFMA_0(a, b, c) asm volatile("v_mfma_f32_16x16x32_bf16 %0, %1, %2, 0" : "=&v"(c) : "v"(a), "v"(b))
#define WAIT_L(n) __builtin_amdgcn_s_waitcnt(0xC07F | ((n) << 8))
#define LDS_FRAG(p) (*(const LAS bf16x8*)(p))
DEVI int lane_id_fresh() { int z = 0; asm volatile("" : "+v"(z)); return __builtin_amdgcn_mbcnt_hi(~0u, __builtin_amdgcn_mbcnt_lo(~0u, z)); }
DEVI const LAS char* opaque(const LAS char* p) { asm volatile("" : "+v"(p)); return p; }
#define DBG_ARG
#define DBG_LAUNCH(n)
#define STAMP(i)
#define STAMP_DECL
#define STAMP_BEGIN
#define STAMP_END(n)
#define STAMP_PARAM
#define STAMP_FWD

#define PST_PARAM
#define PST_FWD
#define PST_DECL
#define PST(i)
#define PST_DUMP(c, k)
#define TRACE_ATTR
#define TRACE_BEGIN
#define TRACE_DUMP

DEVI float add1(float a, float b) { float r; asm("v_add_f32 %0, %1, %2" : "=v"(r) : "v"(a), "v"(b)); return r; }
DEVI float rstd_of(float s, float q, float inv_n, float& mean) {
    float var;
    asm("v_mul_f32 %0, %2, %3\n\tv_mul_f32 %1, %2, %4\n\tv_fma_f32 %1, -%0, %0, %1" : "=&v"(mean), "=&v"(var) : "s"(inv_n), "v"(s), "v"(q));
    return __builtin_amdgcn_rsqf(fmax2(var, 0.f) + 1e-5f);
}
DEVI float ln_rstd(const f32x4 p) {
    float s, q, mean;
    asm("v_add_f32 %0, %2, %4\n\tv_add_f32 %1, %3, %5\n\ts_nop 0\n\t"
        "v_add_f32_dpp %0, %0, %0 quad_perm:[1,0,3,2] row_mask:0xf bank_mask:0xf\n\tv_add_f32_dpp %1, %1, %1 quad_perm:[1,0,3,2] row_mask:0xf bank_mask:0xf\n\ts_nop 0\n\t"
        "v_add_f32_dpp %0, %0, %0 quad_perm:[2,3,0,1] row_mask:0xf bank_mask:0xf\n\tv_add_f32_dpp %1, %1, %1 quad_perm:[2,3,0,1] row_mask:0xf bank_mask:0xf"
        : "=&v"(s), "=&v"(q) : "v"(p[0]), "v"(p[1]), "v"(p[2]), "v"(p[3]));
    return rstd_of(s, q, 1.0f / 576.0f, mean);
}

struct Res { u32x4 a, c; u32x2 e; };
DEVI void proj_residual(f32x4 (&v)[5], const Res& x) {
    v[0] = (f32x4){bflo(x.a.x), bfhi(x.a.x), bflo(x.a.y), bfhi(x.a.y)}; v[1] = (f32x4){bflo(x.a.z), bfhi(x.a.z), bflo(x.a.w), bfhi(x.a.w)};
    v[2] = (f32x4){bflo(x.c.x), bfhi(x.c.x), bflo(x.c.y), bfhi(x.c.y)}; v[3] = (f32x4){bflo(x.c.z), bfhi(x.c.z), bflo(x.c.w), bfhi(x.c.w)};
    v[4] = (f32x4){bflo(x.e.x), bfhi(x.e.x), bflo(x.e.y), bfhi(x.e.y)};
}
struct ProjOut { __amdgpu_buffer_rsrc_t x, st; unsigned v0, v1, v2, vs; };
DEVI ProjOut proj_out(char* xout, char* stout, int rt, int cg, int lane) {
    const int r = lane & 15, q = lane >> 4;
    ProjOut p = {RSRC(xout + (size_t)rt * 18432 + cg * 2304), RSRC(stout + (size_t)rt * 1024 + cg * 8),
                 pin((unsigned)lane * 16u), pin((unsigned)lane * 16u + 1024u), pin((unsigned)(2048 + r * 16 + (q & 1) * 8)), pin((unsigned)(r * 64 + (q >> 1) * 4))};
    asm volatile("" :: "s"(p.x), "s"(p.st));
    return p;
}
DEVI ProjOut proj_out_final(char* out, int rt, int cg, int lane) {
    const int q = lane >> 4;
    const unsigned o = (unsigned)(slot2row(rt * 16 + (lane & 15)) * 1152 + cg * 144);
    ProjOut p = {RSRC(out), RSRC(out), pin(o + q * 16), pin(o + 64 + q * 16), pin(o + 128 + (q & 1) * 8), 0u};
    return p;
}
DEVI Res proj_residual_load(const ProjOut& p) {
    return {__builtin_amdgcn_raw_buffer_load_b128(p.x, p.v0, 0, 16), __builtin_amdgcn_raw_buffer_load_b128(p.x, p.v1, 0, 16), __builtin_amdgcn_raw_buffer_load_b64(p.x, p.v2, 0, 16)};
}
template <bool FINAL = false>
DEVI void proj_store(f32x4 (&v)[5], const ProjOut& p) {
    ST16(p.x, p.v0, 0, ((u32x4){pack2(v[0][0], v[0][1]), pack2(v[0][2], v[0][3]), pack2(v[1][0], v[1][1]), pack2(v[1][2], v[1][3])}), FINAL ? 0 : 16);
    SCHED0();
    asm volatile("" : "+v"(v[2]), "+v"(v[3]), "+v"(v[4]));
    ST16(p.x, p.v1, 0, ((u32x4){pack2(v[2][0], v[2][1]), pack2(v[2][2], v[2][3]), pack2(v[3][0], v[3][1]), pack2(v[3][2], v[3][3])}), FINAL ? 0 : 16);
    SCHED0();
    ST8(p.x, p.v2, 0, ((u32x2){pack2(v[4][0], v[4][1]), pack2(v[4][2], v[4][3])}), FINAL ? 0 : 16);
    SCHED0();
    if (!FINAL) {
        const f32x4 s4 = ((v[0] + v[1]) + (v[2] + v[3])) + v[4] * 0.5f;
        const f32x4 q4 = ((v[0] * v[0] + v[1] * v[1]) + (v[2] * v[2] + v[3] * v[3])) + v[4] * v[4] * 0.5f;
        auto x = __builtin_amdgcn_permlane32_swap(__float_as_uint((s4[0] + s4[2]) + (s4[1] + s4[3])), __float_as_uint((q4[0] + q4[2]) + (q4[1] + q4[3])), false, false);
        const float t = __uint_as_float(x[0]) + __uint_as_float(x[1]);
        auto y = __builtin_amdgcn_permlane16_swap(__float_as_uint(t), __float_as_uint(t), false, false);
        __builtin_amdgcn_raw_buffer_store_b32(__float_as_uint(__uint_as_float(y[0]) + __uint_as_float(y[1])), p.st, p.vs, 0, 16);
        SCHED0();
    }
}

DEVI void side_load(f32x4 (&d)[3], const char* p, const char* p2) {
    asm volatile("global_load_dwordx4 %0, %3, off sc1\n\tglobal_load_dwordx4 %1, %3, off offset:1024 sc1\n\tglobal_load_dwordx4 %2, %4, off sc1"
                 : "=&v"(d[0]), "=&v"(d[1]), "=&v"(d[2]) : "v"(p), "v"(p2) : "memory");
}
DEVI void side_fence(f32x4 (&d)[3]) { asm volatile("" : "+v"(d[0]), "+v"(d[1]), "+v"(d[2])); }
DEVI void lds_word_set(LAS char* p, unsigned v) { asm volatile("ds_write_b32 %0, %1" :: "v"((unsigned)(size_t)p), "v"(v) : "memory"); }
#define DMA_I(v, off, sc) "buffer_load_dwordx4 " v ", %1, %2 offen offset:" off sc " lds\n\t"
#define DMA5(sc) asm volatile("s_mov_b32 m0, %0\n\ts_mov_b32 exec_hi, 0\n\ts_nop 2\n\t" DMA_I("%3", "0", sc) "s_mov_b32 exec_hi, -1\n\t" \
    DMA_I("%3", "512", sc) DMA_I("%3", "1536", sc) DMA_I("%3", "2560", sc) DMA_I("%3", "3584", sc) :: "s"(la), "s"(rs), "s"(so), "v"(vo) : "memory")
#define DMA4(sc) asm volatile("s_mov_b32 m0, %0\n\ts_nop 3\n\t" DMA_I("%3", "0", sc) DMA_I("%4", "1024", sc) DMA_I("%5", "2048", sc) DMA_I("%6", "3072", sc) \
    :: "s"(la), "s"(rs), "s"(so), "v"(v0), "v"(v1), "v"(v2), "v"(v3) : "memory")
template <bool SC1> DEVI void dma5(unsigned la, __amdgpu_buffer_rsrc_t rs, int so, unsigned vo) { if (SC1) DMA5(" sc1"); else DMA5(""); }
template <bool SC1> DEVI void dma4(unsigned la, __amdgpu_buffer_rsrc_t rs, int so, unsigned v0, unsigned v1, unsigned v2, unsigned v3) { if (SC1) DMA4(" sc1"); else DMA4(""); }
#define DMA2(sc) asm volatile("s_mov_b32 m0, %0\n\ts_nop 3\n\t" DMA_I("%3", "0", sc) DMA_I("%4", "1024", sc) :: "s"(la), "s"(rs), "s"(so), "v"(v0), "v"(v1) : "memory")
template <bool SC1> DEVI void dma2(unsigned la, __amdgpu_buffer_rsrc_t rs, int so, unsigned v0, unsigned v1) { if (SC1) DMA2(" sc1"); else DMA2(""); }
DEVI void dma1(unsigned la, __amdgpu_buffer_rsrc_t rs, int so, unsigned v0) { asm volatile("s_mov_b32 m0, %0\n\ts_nop 3\n\t" DMA_I("%3", "0", "") :: "s"(la), "s"(rs), "s"(so), "v"(v0) : "memory"); }
#define WAIT_IF(c, a, b) do { if (c) WAIT_V(a); else WAIT_V(b); } while (0)
template <int T0, int T1, int P, int S0 = 0, class ISSUE>
DEVI void ring_steps(ISSUE&& issue) {
    int slot = (T0 + S0) % 3;
    LOOP_ALIGN();
#pragma unroll 1
    for (int t = T0; t < T1; ++t) {
        WAIT_V(P); BAR();
        issue(t + 3, slot);
        slot = slot == 2 ? 0 : slot + 1;
    }
}

DEVI unsigned flag_out(unsigned use, int pitch, int group, int writer, int lane) { return pin(lane < 16 ? use + (group * 16 + lane) * pitch + writer * 16 : 0x80000000u); }
DEVI unsigned flag_in(unsigned use, int pitch, int reader, int n, int lane) { return pin(use + reader * pitch + (lane < n ? lane : 0) * 16); }
DEVI void flag_raise(const char* ws, unsigned vo) { ST16(RSRC(ws), vo, 0, ((u32x4){1u, 1u, 1u, 1u}), 16); SCHED0(); }
template <int FIRST, int AGAIN = 1>
DEVI void flags_wait(const char* ws, unsigned vo) {
    unsigned v, n = 400000u;
    asm volatile("s_nop 4\n\ts_sleep %4\n"
                 "1:\n\tbuffer_load_dword %0, %2, %3, 0 offen sc1\n\ts_waitcnt vmcnt(0)\n\tv_cmp_eq_u32 vcc, 0, %0\n\ts_cbranch_vccz 2f\n\t"
                 "s_sleep %5\n\ts_sub_u32 %1, %1, 1\n\ts_cbranch_scc0 1b\n2:"
                 : "=&v"(v), "+s"(n) : "v"(vo), "s"(RSRC(ws)), "i"(FIRST), "i"(AGAIN) : "vcc", "scc", "memory");
    SCHED0();
}
template <int FIRST>
DEVI void epoch_wait(const char* ws, unsigned vo, unsigned long long ep) {
    unsigned long long v; unsigned n = 400000u;
    asm volatile("s_nop 4\n\ts_sleep %5\n"
                 "1:\n\tbuffer_load_dwordx2 %0, %2, %3, 0 offen sc1\n\ts_waitcnt vmcnt(0)\n\tv_cmp_ne_u64 vcc, %4, %0\n\ts_cbranch_vccz 2f\n\t"
                 "s_sleep 1\n\ts_sub_u32 %1, %1, 1\n\ts_cbranch_scc0 1b\n2:"
                 : "=&v"(v), "+s"(n) : "v"(vo), "s"(RSRC(ws)), "s"(ep), "i"(FIRST) : "vcc", "scc", "memory");
    SCHED0();
}
DEVI unsigned lds_arrive(LAS char* p) {
    unsigned v;
    asm volatile("s_mov_b64 exec, 1\n\tds_add_rtn_u32 %0, %1, %2\n\ts_mov_b64 exec, -1\n\ts_waitcnt lgkmcnt(0)" : "=&v"(v) : "v"((unsigned)(size_t)p), "v"(1u) : "memory");
    return __builtin_amdgcn_readfirstlane(v);
}
DEVI void lds_count_wait(LAS char* p, unsigned n) {
#pragma unroll 1
    for (unsigned spin = 0; spin < 40000000u; ++spin) {
        unsigned v;
        asm volatile("ds_read_b32 %0, %1\n\ts_waitcnt lgkmcnt(0)" : "=v"(v) : "v"((unsigned)(size_t)p) : "memory");
        if (__builtin_amdgcn_readfirstlane(v) >= n) break;
    }
}

constexpr int K1_STAGE = 32768;
constexpr int WO_OFF = 69632;
constexpr int G_OWN = 139264;
constexpr int G_PART = G_OWN + 18432;
constexpr int G_TAB = G_PART + 2048;
constexpr int K1_SCR = G_TAB + 1536;
constexpr int G_WORD = K1_SCR + 576;
constexpr int G_JUNK = G_WORD + 64;
constexpr int NET_LDS = G_JUNK + 1024;
DEVI __amdgpu_buffer_rsrc_t code_rsrc() {
    unsigned long long pc; int d0, len;
    asm volatile("s_getpc_b64 %0\n1:\n\ts_mov_b32 %1, gm_code_begin-1b\n\ts_mov_b32 %2, gm_code_end1-gm_code_begin\n\ts_max_i32 %2, %2, gm_code_end2-gm_code_begin"
                 : "=s"(pc), "=s"(d0), "=s"(len) :: "scc");
    return __builtin_amdgcn_make_buffer_rsrc((void*)((pc + d0) & ~4095ull), 0, len + ((unsigned)(pc + d0) & 4095), 0x00020000);
}

#define WO_PRE 9
#define WO_W 33
#define REL_PRIO 0
#define W_FIRST 2
#define I_SLEEP 30
#define OJOB_SLEEP 20
#define PACE0 1
#define PACE __builtin_amdgcn_s_sleep(1)
#define POLLW 0
#define WO_EV 4
#define WIN_EARLY 0
#define WIN_LOOK 40
#define W3_SLEEP 0
#define O_FIRST 8
#define X1_FIRST 4
#define X2_FIRST 4
#define Y_FIRST 14
#define Z2_FIRST 10
DEVI int win_strip(int b) { return ((b - 112) & 7) * 18 + ((b - 112) >> 3); }
DEVI int win_third(int b, int w) { return b & 8 ? 3 - w : w - 1; }
DEVI unsigned attn_vb(int wid, int lane) {
    const int c = lane & 15;
    return pin((unsigned)(wid < 3 ? lane * 16 : (c < 8 ? (c >> 1) & 1 : 2) * 4608 + (lane >> 4) * 64 + (c < 8 ? (c >> 2) * 2 + (c & 1) : c & 3) * 16));
}
DEVI void attn_issueB(LAS char* lds, const char* ws, int hp, int wid, unsigned vb, int s, int slot) {
    const unsigned la = (unsigned)(size_t)lds + slot * K1_STAGE + 18432 + wid * 4096;
    if (wid < 3) dma4<true>(la, RSRC(ws), (int)OFF_TWQKV + hp * (18 * 6144) + wid * 4096 + s * 12288, vb, vb, vb, vb);
    else asm volatile("s_mov_b32 m0, %0\n\ts_nop 3\n\tbuffer_load_dwordx4 %4, %1, %2 offen sc1 lds\n\tbuffer_load_dwordx4 %4, %1, %3 offen offset:1024 sc1 lds"
                      :: "s"(la), "s"(RSRC(ws)), "s"((int)OFF_TWT + hp * 13824 + s * 512), "s"((int)OFF_TWT + hp * 13824 + s * 512 + 256 - 1024), "v"(vb) : "memory");
}
struct AttnX { __amdgpu_buffer_rsrc_t rs; unsigned v[4], vh[2]; int oA, oh, step; };
DEVI AttnX attn_x(const char* x0, const char* xa, int layer, int pm, int wid, int lane) {
    const int l3 = lane >> 3, pmh = pm > 0 ? pm : 1;
    const unsigned v = layer ? lane * 16 : 2 * l3 * 1152 + ((lane & 7) ^ l3) * 16 + 4096, d1 = layer ? 0 : 16 * 1152 - 1024, d2 = layer ? 16384 : 1152 - 2048;
    const unsigned vh = layer ? (l3 & 1) * 18432 + (lane >> 4) * 256 + (8 + (lane & 7)) * 16 : v + 16 * 1152;
    return {RSRC(layer ? xa : x0 - 4096), {pin(v), pin(v + d1), pin(v + d2), pin(v + d1 + d2)}, {pin(vh), pin(vh + (layer ? 0 : 1152 - 1024))},
            (pm * 8 + 2 * wid) * 18432, (pmh * 8 - 2) * 18432, layer ? 2048 : 128};
}
DEVI void attn_issueA(LAS char* lds, const AttnX& a, int wid, int s, int slot) {
    const unsigned la = (unsigned)(size_t)lds + slot * K1_STAGE;
    if (wid == 3) dma2<true>(la, a.rs, a.oh + s * a.step, a.vh[0], a.vh[1]);
    dma4<true>(la + 2048 + wid * 4096, a.rs, a.oA + s * a.step, a.v[0], a.v[1], a.v[2], a.v[3]);
}
template <class DONE, class NEXT>
DEVI void attn_producer(LAS char* lds, const char* ws, const char* x0, const char* wl, unsigned fl, unsigned long long ep, int layer, int pm, int hp, int wid, DONE&& done, NEXT&& next) {
    const int lane = lane_id_fresh();
    const int pmh = pm > 0 ? pm : 1;
    const unsigned vo = lane * 16;
    const AttnX ax = attn_x(x0, wl + L_XA, layer, pm, wid, lane);
    const unsigned vb = attn_vb(wid, lane), fd = pin((unsigned)OFF_FL + wid * 1024 + vo);
    auto issueB = [&](int s, int slot) { attn_issueB(lds, ws, hp, wid, vb, s, slot); };
    auto issue = [&](int s, int slot) { attn_issueA(lds, ax, wid, s, slot); issueB(s, slot); };
    const int hr = lane >> 2;
    const char* ps = pin(wl + L_STA + (size_t)(pm * 128 + wid * 32) * 64 + lane * 16);
    const char* psh = pin(wl + L_STA + (size_t)((pmh * 8 - 2 + (hr >> 3)) * 16 + 8 + (hr & 7)) * 64 + (lane & 3) * 16);
    if (layer == 0) {
        const unsigned fi = pin((unsigned)OFF_FW + (pm * 16 + hp) * 1024 + (lane < 6 ? lane : 0) * 128);
        done();
        if (wid == 0) { epoch_wait<W_FIRST>(ws, fi, ep); issueB(0, 0); }
        BARX();
        if (wid) issueB(0, 0);
        issueB(1, 1); issue(2, 2);
        WAIT_IF(wid == 3, 10, 12);
    } else {
        issueB(2, 2);
        const unsigned fi = flag_in(F_X2, 512, pm * 16 + hp, pm > 0 ? 24 : 16, lane);
        if (wid == 0) {
            flags_wait<X2_FIRST>(ws, fi);
            AttnX a0 = ax;
#pragma unroll
            for (int i = 0; i < 4; ++i) { attn_issueA(lds, a0, i, 0, 0); a0.oA += 36864; }
        }
        BARX();
        attn_issueA(lds, ax, wid, 1, 1); attn_issueA(lds, ax, wid, 2, 2);
        WAIT_IF(wid == 3, 12, 8);
    }
    __builtin_amdgcn_s_setprio(0);
    BAR();
    if (layer) WAIT_IF(wid == 3, 6, 4);
    next(-1);
    ring_steps<0, 4, 8>(issue);
    {
        unsigned long long done;
        if (layer == 0) asm volatile("s_nop 4\n\tbuffer_load_dwordx2 %0, %1, %2, 0 offen sc1" : "+v"(done) : "v"(fd), "s"(RSRC(ws)) : "memory");
        WAIT_IF(layer, 8, 9); BAR(); issue(7, 1);
        WAIT_V(8);
        if (layer == 0) { asm volatile("" : "+v"(done)); if (__any(done != ep)) epoch_wait<0>(ws, fd, ep); }
        BAR(); issue(8, 2);
    }
    const auto rW = RSRC(ws + OFF_TWO + (size_t)(hp >> 1) * 82944 + wid * 4096);
    auto issueW = [&](int i) { dma4<true>((unsigned)(size_t)lds + WO_OFF + (i * 4 + wid) * 4096, rW, i * 16384, vo, vo, vo, vo); };
    {
        f32x4 sp[3];
        side_load(sp, ps, psh);
        WAIT_V(11); BAR(); issueW(2); issueW(3);
        WAIT_V(8); BARX();
        side_fence(sp);
#pragma unroll
        for (int i = 0; i < 3; ++i) lds_word_set(lds + K1_SCR + (i < 2 ? 16 + wid * 32 + i * 16 + hr : hr) * 4, __float_as_uint(ln_rstd(sp[i])));
        WAIT_L0();
    }
    BAR();
    next(0);
    issueW(0); issueW(1); issueW(4);
    if (wid == 0) dma2<true>((unsigned)(size_t)lds + WO_OFF + 80896, rW, 80896, vo, vo);
    next(1);
    const unsigned fo = flag_out(fl + F_O, 256, pm, hp, lane), fi = flag_in(fl + F_O, 256, pm * 16 + hp, 16, lane);
    WAIT_V(0);
    if (wid == POLLW) __builtin_amdgcn_s_setprio(3);
    BARX();
    next(2);
    if (wid == POLLW) { flag_raise(ws, fo); flags_wait<O_FIRST>(ws, fi); }
    __builtin_amdgcn_s_setprio(0);
    BARX();
}

#define STAMP_SEC 0
template <class DONE>
DEVI void attn_consumer(LAS char* lds, const char* x0, char* wl, int layer, int pm, int hp, int wid, DONE&& done STAMP_PARAM) {
    const int lane = lane_id_fresh(), r = lane & 15, q = lane >> 4;
    const char* xa = wl + L_XA;
    char* o = wl + L_O;
    const int T = wid & 1, h = wid >> 1;
    const LAS char *ph[2], *pa[2];
#pragma unroll
    for (int kk = 0; kk < 2; ++kk) {
        const int sw = ((4 * kk + q) ^ (r & 7)) * 16;
        ph[kk] = opaque(lds + (layer ? kk * 1024 + q * 256 + (r ^ (T ? 0 : 8)) * 16 : (r ^ (T ? 0 : 8)) * 128 + sw));
        pa[kk] = opaque(lds + T * 2048 + (layer ? kk * 1024 + lane * 16 : r * 128 + sw));
    }
    const LAS char* pb = lds + h * 3072 + lane * 16;
    const LAS char* pt = lds + lane * 16;
    f32x4 acc[5][4];
#pragma unroll
    for (int m = 0; m < 5; ++m)
#pragma unroll
        for (int n = 0; n < 4; ++n) if (m + n) acc[m][n] = pin((f32x4){0.f, 0.f, 0.f, 0.f});
    bf16x8 fa[2][5], fb[2][4];
    auto rd1 = [&](int kk, int i) {
        if (i < 3) fb[kk][i] = LDS_FRAG(pb + 18432 + kk * 6144 + i * 1024);
        else if (i == 3) fb[kk][3] = LDS_FRAG(pt + 30720 + kk * 1024);
        else if (i == 4) fa[kk][0] = LDS_FRAG(ph[kk]);
        else if (i < 9) fa[kk][i - 4] = LDS_FRAG(pa[kk] + 2048 + (i - 5) * 4096);
    };
    f32x4 mb, mb0;
#pragma unroll
    for (int j = 0; j < 4; ++j) {
        const bool ok = ((r - 4 * q - j) & 15) < 8;
        mb[j] = ok ? 0.f : -__builtin_inff();
        mb0[j] = (ok && (pm > 0 || 4 * q + j <= r)) ? 0.f : -__builtin_inff();
    }
    mb = pin(mb); mb0 = pin(mb0);
    const unsigned mp0 = pin((4 * q > r ? 0xffffu : 0u) | (4 * q + 1 > r ? 0xffff0000u : 0u)), mp1 = pin((4 * q + 2 > r ? 0xffffu : 0u) | (4 * q + 3 > r ? 0xffff0000u : 0u));
    const unsigned qtm = pin(q == h ? 0xffffffffu : 0u);
    const unsigned vom = pin((unsigned)(r * 16 + hp * 1024 + (2 * h + (q >> 1)) * 256 + (q & 1) * 8)), vot = pin((unsigned)(r * 16 + 16384 + (hp >> 1) * 256 + (hp & 1) * 8 + h * 4));
    const LAS char* tk0 = opaque(lds + K1_SCR + ((q * 4) ^ (T ? 0 : 8)) * 4);
    const LAS char* tk = opaque(lds + K1_SCR + ((1 + T) * 16 + q * 4) * 4);
    const LAS char* tq = opaque(lds + K1_SCR + ((1 + T) * 16 + r) * 4);
    pb = opaque(pb); pt = opaque(pt);
    STAMP(2);
    const int rt = pm * 8 + (hp & 1) * 4 + wid, cg = hp >> 1;
    const ProjOut pr = layer ? proj_out((char*)xa, (char*)xa, rt, cg, lane) : proj_out_final((char*)x0, rt, cg, lane);
    BAR();
    if (layer == 0) done();
    BARX();
    STAMP(3);
#pragma unroll
    for (int i = 0; i < 9; ++i) rd1(0, i);
    SCHED0();
    const Res rx = proj_residual_load(pr);
    SCHED0();
    int slot = 0, left = 9;
    LOOP_ALIGN();
#pragma unroll 1
    do {
        int d;
#pragma unroll
        for (int kk = 0; kk < 2; ++kk) {
            WAIT_L(0);
            if (kk) BAR();
            SCHED0();
#pragma unroll
            for (int i = 1; i < 20; ++i) {
                const int m = i >> 2, n = i & 3;
                const bf16x8 &A = fa[kk][m], &B = fb[kk][n];
                acc[m][n] = n != 2 ? MFMA(B, A, acc[m][n]) : MFMA(A, B, acc[m][n]);
                SCHED0();
                if (i <= 9) rd1(kk ^ 1, i - 1);
                if (kk == 0) {
                    if (i == 10) d = pin_s(slot == 2 ? -2 * K1_STAGE : K1_STAGE);
                    if (i == 11 || i == 12) ph[i & 1] = opaque(ph[i & 1] + d);
                    if (i == 13 || i == 14) pa[i & 1] = opaque(pa[i & 1] + d);
                    if (i == 15) pb = opaque(pb + d);
                    if (i == 16) pt = opaque(pt + d);
                } else {
                    if (i == 11) slot = pin_s(slot == 2 ? 0 : slot + 1);
                    if (i == 13) left = pin_s(left - 1);
                }
                SCHED0();
            }
        }
    } while (left);
    STAMP(4);
    u32x2 ts[5], tf[5];
    {
        LAS char* xb = lds + wid * 320;
#pragma unroll
        for (int m = 0; m < 5; ++m) ts[m] = (u32x2){pack2(acc[m][3][0], acc[m][3][1]), pack2(acc[m][3][2], acc[m][3][3])};
        if (q == 2) {
#pragma unroll
            for (int m = 0; m < 5; ++m) {
                const unsigned w = h ? ts[m].y : ts[m].x;
                *(LAS u16*)(xb + m * 64 + r * 2) = (u16)w; *(LAS u16*)(xb + m * 64 + 32 + r * 2) = (u16)(w >> 16);
            }
        }
        asm volatile("" ::: "memory");
#pragma unroll
        for (int m = 0; m < 5; ++m) tf[m] = *(const LAS u32x2*)(xb + m * 64 + (r & 1) * 32 + q * 8);
    }
    {
        f32x4 rk[5]; float rq[4];
        rk[0] = *(const LAS f32x4*)(tk0);
#pragma unroll
        for (int g = 0; g < 4; ++g) { rk[g + 1] = *(const LAS f32x4*)(tk + g * 128); rq[g] = *(const LAS float*)(tq + g * 128); }
        const f32x4 zero = {0.f, 0.f, 0.f, 0.f};
        u32x4 kf[5];
#pragma unroll
        for (int m = 0; m < 5; ++m) kf[m] = (u32x4){pack2(acc[m][1][0], acc[m][1][1]), pack2(acc[m][1][2], acc[m][1][3]), ts[m].y, 0u};
        f32x4 sp[4], sc[4];
#pragma unroll
        for (int g = 0; g < 4; ++g) {
            const u32x4 qf = {pack2(acc[g + 1][0][0], acc[g + 1][0][1]), pack2(acc[g + 1][0][2], acc[g + 1][0][3]), ts[g + 1].x & qtm, 0u};
            sp[g] = MFMA(__builtin_bit_cast(bf16x8, kf[g]), __builtin_bit_cast(bf16x8, qf), zero);
            sc[g] = MFMA(__builtin_bit_cast(bf16x8, kf[g + 1]), __builtin_bit_cast(bf16x8, qf), zero);
        }
        u32x2 vf[5];
#pragma unroll
        for (int m = 0; m < 5; ++m) vf[m] = (u32x2){pack2(acc[m][2][0], acc[m][2][1]), pack2(acc[m][2][2], acc[m][2][3])};
        f32x4 rs[4], t[4]; float mx[4], sm[4];
#pragma unroll
        for (int g = 0; g < 4; ++g) {
#pragma unroll
            for (int j = 0; j < 4; ++j) { const bool pv = 4 * q + j > r; rs[g][j] = pv ? rk[g][j] : rk[g + 1][j]; t[g][j] = pv ? sp[g][j] : sc[g][j]; }
            t[g] = (t[g] * rs[g]) * (rq[g] * (0.23570226039551584f * 1.4426950408889634f)) + (g == 0 ? mb0 : mb);
            mx[g] = fmaxf(fmaxf(t[g][0], t[g][1]), fmaxf(t[g][2], t[g][3]));
        }
        xq4_max(mx);
#pragma unroll
        for (int g = 0; g < 4; ++g) {
            t[g] = t[g] - mx[g];
#pragma unroll
            for (int j = 0; j < 4; ++j) t[g][j] = __builtin_amdgcn_exp2f(t[g][j]);
            sm[g] = (t[g][0] + t[g][2]) + (t[g][1] + t[g][3]);
        }
        f32x4 om[4], ot[4];
#pragma unroll
        for (int g = 0; g < 4; ++g) {
            const f32x4 p = t[g] * rs[g];
            const unsigned p01 = pack2(p[0], p[1]), p23 = pack2(p[2], p[3]);
            const u32x4 pf = {p01 & mp0, p23 & mp1, p01 & ~mp0, p23 & ~mp1};
            om[g] = MFMA(__builtin_bit_cast(bf16x8, ((u32x4){vf[g].x, vf[g].y, vf[g + 1].x, vf[g + 1].y})), __builtin_bit_cast(bf16x8, pf), zero);
            ot[g] = MFMA(__builtin_bit_cast(bf16x8, ((u32x4){tf[g].x, tf[g].y, tf[g + 1].x, tf[g + 1].y})), __builtin_bit_cast(bf16x8, pf), zero);
        }
        xq4_sum_rcp(sm);
        const auto ro = RSRC(o + (size_t)(pm * 8 + T) * 18432);
        unsigned tw2[4];
#pragma unroll
        for (int g = 0; g < 4; ++g) {
            const f32x4 v = om[g] * sm[g];
            __builtin_amdgcn_raw_buffer_store_b64((u32x2){pack2(v[0], v[1]), pack2(v[2], v[3])}, ro, vom, g * 36864, 16);
            const f32x2 w = ot[g].xy * sm[g];
            tw2[g] = pack2(w.x, w.y);
        }
        if (q == 2) {
#pragma unroll
            for (int g = 0; g < 4; ++g) __builtin_amdgcn_raw_buffer_store_b32(tw2[g], ro, vot, g * 36864, 16);
        }
    }
    STAMP(5);
    {
        WAIT_V(0); BARX();
        STAMP(6);
        f32x4 v[5];
        proj_residual(v, rx);
        const LAS char* pw = opaque(lds + WO_OFF + lane * 16);
        const LAS char* pw2 = opaque(lds + WO_OFF + 41472 + lane * 16);
        const LAS char* ph2 = opaque(lds + WO_OFF + 4096 + q * 128 + (r & 7) * 16);
        const LAS char* ph3 = opaque(lds + WO_OFF + 41472 + 4096 + q * 128 + (r & 7) * 16);
        bf16x8 wf[90];
        auto rdw = [&](int i) {
            const int kc = i / 5, n = i % 5;
            wf[i] = n < 4 ? LDS_FRAG((kc < 9 ? pw : pw2) + (kc % 9) * 4608 + n * 1024) : LDS_FRAG((kc < 9 ? ph2 : ph3) + (kc % 9) * 4608);
        };
#pragma unroll
        for (int i = 0; i < WO_W; ++i) rdw(i);
#pragma unroll
        for (int n = 0; n < 5; ++n) v[n] = pin(v[n]);
        const auto rO = RSRC(o + (size_t)rt * 18432);
        const ProjOut po = proj_out(wl + L_XB, wl + L_STB, rt, cg, lane);
        asm volatile("" :: "s"(rO));
        WAIT_L(0);
        __builtin_amdgcn_s_setprio(REL_PRIO);
        BARX();
        STAMP(7);
        bf16x8 af[18];
        auto ldo = [&](int kc) { af[kc] = __builtin_bit_cast(bf16x8, __builtin_amdgcn_raw_buffer_load_b128(rO, lane * 16 + (kc & 3) * 1024, (kc >> 2) * 4096, 16)); };
#pragma unroll
        for (int kc = 0; kc < WO_PRE; ++kc) { ldo(kc); SCHED0(); if (kc >= PACE0 && kc + 1 < WO_PRE) PACE; SCHED0(); }
        static_for<90>([&](auto ic) {
            constexpr int i = ic.value, kc = i / 5, n = i % 5, j = WO_W + 2 * (i / 3) + i % 3;
            MFMA_T(wf[i], af[kc], v[n]);
            SCHED0();
            if (i % 3 < 2 && j < 90) rdw(j);
            if (i % WO_EV == 2 && i / WO_EV + WO_PRE < 18) ldo(i / WO_EV + WO_PRE);
            SCHED0();
        });
        asm volatile("s_nop 4" : "+v"(v[0]), "+v"(v[1]));
        STAMP(8);
        proj_store(v, po);
        __builtin_amdgcn_s_setprio(0);
    }
}

DEVI f32x2 gelu2(f32x2 v) {
    const f32x2 w = v * (v * v * -0.10294324f + -2.3022082f);
    const f32x2 d = (f32x2){__builtin_amdgcn_exp2f(w.x), __builtin_amdgcn_exp2f(w.y)} + 1.0f;
    return v * (f32x2){__builtin_amdgcn_rcpf(d.x), __builtin_amdgcn_rcpf(d.y)};
}
DEVI f32x4 gelu4(f32x4 v) { const f32x2 a = gelu2(v.xy), b = gelu2(v.zw); return (f32x4){a.x, a.y, b.x, b.y}; }
DEVI void rowsum16x8(const f32x4 a, const f32x4 b, float& o0, float& o1) {
    float t0, t1, t2, t3;
    asm volatile(
        "s_nop 1\n\t"
        "v_add_f32_dpp %2, %6, %6 row_ror:8 row_mask:0xf bank_mask:0x3\n\t"
        "v_add_f32_dpp %3, %7, %7 row_ror:8 row_mask:0xf bank_mask:0x3\n\t"
        "v_add_f32_dpp %4, %10, %10 row_ror:8 row_mask:0xf bank_mask:0x3\n\t"
        "v_add_f32_dpp %5, %11, %11 row_ror:8 row_mask:0xf bank_mask:0x3\n\t"
        "v_add_f32_dpp %2, %8, %8 row_ror:8 row_mask:0xf bank_mask:0xc\n\t"
        "v_add_f32_dpp %3, %9, %9 row_ror:8 row_mask:0xf bank_mask:0xc\n\t"
        "v_add_f32_dpp %4, %12, %12 row_ror:8 row_mask:0xf bank_mask:0xc\n\t"
        "v_add_f32_dpp %5, %13, %13 row_ror:8 row_mask:0xf bank_mask:0xc\n\t"
        "v_add_f32_dpp %0, %2, %2 row_half_mirror row_mask:0xf bank_mask:0x5\n\t"
        "s_nop 0\n\t"
        "v_add_f32_dpp %1, %4, %4 row_half_mirror row_mask:0xf bank_mask:0x5\n\t"
        "v_add_f32_dpp %0, %3, %3 row_half_mirror row_mask:0xf bank_mask:0xa\n\t"
        "v_add_f32_dpp %1, %5, %5 row_half_mirror row_mask:0xf bank_mask:0xa\n\t"
        "s_nop 0\n\t"
        "v_add_f32_dpp %0, %0, %0 quad_perm:[1,0,3,2] row_mask:0xf bank_mask:0xf\n\t"
        "v_add_f32_dpp %1, %1, %1 quad_perm:[1,0,3,2] row_mask:0xf bank_mask:0xf\n\t"
        "s_nop 0\n\t"
        "v_add_f32_dpp %0, %0, %0 quad_perm:[2,3,0,1] row_mask:0xf bank_mask:0xf\n\t"
        "v_add_f32_dpp %1, %1, %1 quad_perm:[2,3,0,1] row_mask:0xf bank_mask:0xf\n\t"
        : "=&v"(o0), "=&v"(o1), "=&v"(t0), "=&v"(t1), "=&v"(t2), "=&v"(t3)
        : "v"(a[0]), "v"(a[1]), "v"(a[2]), "v"(a[3]), "v"(b[0]), "v"(b[1]), "v"(b[2]), "v"(b[3]));
}

#define K5_PRE 9
#define RH_MAP 0
#define NET_LAYERS 2
constexpr int G_STAGE = 34816;
constexpr int K5_STAGE = 18432, K5_NS = 8;

constexpr int y_load_at(int slot) {
    int next = K5_PRE;
    for (int s = 0;; ++s) { const bool go = next < 18 + 2 * (s / 3); if (s == slot) return go ? next : -1; next += go; }
}

struct GTile { f32x4 a[4][4], e[2]; };
struct GFrag { bf16x8 a[2][4], b[2][5]; const LAS char *pa[2][2], *pb, *p8; int slot; };
DEVI void g_rd(GFrag& f, int kk, int i) {
    if (i < 4) f.a[kk][i] = LDS_FRAG(f.pa[kk][i >> 1] + (i & 1) * 2048);
    else if (i < 8) f.b[kk][i - 4] = LDS_FRAG(f.pb + (kk * 9 + i - 4) * 1024);
    else f.b[kk][4] = LDS_FRAG(f.p8 + kk * 9216);
}
template <int NS, int G, bool ROWS_IN_REGS>
DEVI void g_steps(GTile& c, GFrag& f, int n) {
    LOOP_ALIGN();
#pragma unroll 1
    do {
        int d;
#pragma unroll
        for (int kk = 0; kk < 2; ++kk) {
            WAIT_L(0);
            if (kk) BAR();
            SCHED0();
#pragma unroll
            for (int i = 0; i < 18; ++i) {
                const int nn = i >> 2, m = i & 3;
                f32x4& e = nn < 4 ? c.a[m][nn] : c.e[m & 1];
                e = ROWS_IN_REGS ? MFMA(f.a[kk][m], f.b[kk][nn], e) : MFMA(f.b[kk][nn], f.a[kk][m], e);
                SCHED0();
                if (i < 9) g_rd(f, kk ^ 1, i);
                if (kk == 0) {
                    if (i == 9) d = pin_s(f.slot == NS - 1 ? -(NS - 1) * G : G);
                    if (i >= 10 && i < 14) f.pa[(i - 10) >> 1][i & 1] = opaque(f.pa[(i - 10) >> 1][i & 1] + d);
                    if (i == 14) f.pb = opaque(f.pb + d);
                    if (i == 15) f.p8 = opaque(f.p8 + d);
                } else {
                    if (i == 10) f.slot = pin_s(f.slot == NS - 1 ? 0 : f.slot + 1);
                    if (i == 12) n = pin_s(n - 1);
                }
                SCHED0();
            }
        }
    } while (n);
}
DEVI void keep_s(__amdgpu_buffer_rsrc_t r) { asm volatile("" :: "s"(r)); }
DEVI void keep4(const f32x4 (&c)[4]) { asm volatile("" :: "v"(c[0]), "v"(c[1]), "v"(c[2]), "v"(c[3])); }
template <int D, int DB, bool FIRST, class HOOK>
DEVI void g_step_u(GTile& c, GFrag& f, const f32x4 (&c0)[4], HOOK&& hook) {
    static_for<36>([&](auto ic) {
        constexpr int kk = ic.value / 18, i = ic.value % 18, n = i >> 2, m = i & 3;
        if (i == 0) { WAIT_L(0); if (kk) BAR(); SCHED0(); }
        f32x4& e = n < 4 ? c.a[m][n] : c.e[m & 1];
        if (FIRST && kk == 0) asm volatile("v_mfma_f32_16x16x32_bf16 %0, %1, %2, %3" : "=&v"(e) : "v"(f.b[kk][n]), "v"(f.a[kk][m]), "v"(c0[m]));
        else MFMA_T(f.b[kk][n], f.a[kk][m], e);
        SCHED0();
        if (i < 9) g_rd(f, kk ^ 1, i);
        if (kk == 0) {
            if (i >= 10 && i < 14) f.pa[(i - 10) >> 1][i & 1] = opaque(f.pa[(i - 10) >> 1][i & 1] + D);
            if (i == 14) f.pb = opaque(f.pb + DB);
            if (i == 15) f.p8 = opaque(f.p8 + DB);
        }
        if (FIRST && ic.value == 20) keep4(c0);
        hook(ic);
        SCHED0();
    });
}
#define MF(d, a, b) "v_mfma_f32_16x16x32_bf16 " d ", " a ", " b ", " d "\n\t"
template <int D, int DB>
DEVI void g_step_gelu(GTile& c, GFrag& f, f32x4& z, float r, float c1) {
#pragma unroll
    for (int kk = 0; kk < 2; ++kk) {
        float va = z[2 * kk], vb = z[2 * kk + 1], ta, tb;
        WAIT_L(0); if (kk) BAR(); SCHED0();
#pragma unroll
        for (int i = 0; i < 9; ++i) {
            MFMA_T(f.b[kk][i >> 2], f.a[kk][i & 3], c.a[i & 3][i >> 2]);
            if (i == 5) asm volatile("v_mul_f32 %0, %0, %1" : "+v"(va) : "v"(r));
            if (i == 6) asm volatile("v_mul_f32 %0, %0, %1" : "+v"(vb) : "v"(r));
            if (i == 7) asm volatile("v_mul_f32 %0, %1, %1" : "=v"(ta) : "v"(va));
            if (i == 8) asm volatile("v_mul_f32 %0, %1, %1" : "=v"(tb) : "v"(vb));
            SCHED0();
            g_rd(f, kk ^ 1, i);
            SCHED0();
        }
        asm volatile(MF("%0", "%5", "%6") "v_fmaak_f32 %3, %3, %11, 0xc0135761\n\tv_fmaak_f32 %4, %4, %11, 0xc0135761\n\t"
                     MF("%1", "%5", "%7") "v_mul_f32 %3, %3, %9\n\tv_mul_f32 %4, %4, %10\n\t" MF("%2", "%5", "%8") "v_exp_f32 %3, %3"
                     : "+v"(c.a[1][2]), "+v"(c.a[2][2]), "+v"(c.a[3][2]), "+v"(ta), "+v"(tb)
                     : "v"(f.b[kk][2]), "v"(f.a[kk][1]), "v"(f.a[kk][2]), "v"(f.a[kk][3]), "v"(va), "v"(vb), "v"(c1));
        SCHED0(); f.pa[kk][0] = opaque(f.pa[kk][0] + D); SCHED0();
        asm volatile(MF("%0", "%6", "%7") "v_exp_f32 %5, %5\n\t" MF("%1", "%6", "%8") "v_add_f32 %4, 1.0, %4\n\tv_add_f32 %5, 1.0, %5\n\t"
                     MF("%2", "%6", "%9") "v_rcp_f32 %4, %4\n\t" MF("%3", "%6", "%10") "v_rcp_f32 %5, %5"
                     : "+v"(c.a[0][3]), "+v"(c.a[1][3]), "+v"(c.a[2][3]), "+v"(c.a[3][3]), "+v"(ta), "+v"(tb)
                     : "v"(f.b[kk][3]), "v"(f.a[kk][0]), "v"(f.a[kk][1]), "v"(f.a[kk][2]), "v"(f.a[kk][3]));
        SCHED0(); f.pa[kk][1] = opaque(f.pa[kk][1] + D); SCHED0();
        asm volatile(MF("%0", "%4", "%5") "v_mul_f32 %2, %2, %7\n\tv_mul_f32 %3, %3, %8\n\t" MF("%1", "%4", "%6")
                     : "+v"(c.e[0]), "+v"(c.e[1]), "+v"(va), "+v"(vb) : "v"(f.b[kk][4]), "v"(f.a[kk][0]), "v"(f.a[kk][1]), "v"(ta), "v"(tb));
        SCHED0();
        if (kk == 0) { f.pb = opaque(f.pb + DB); f.p8 = opaque(f.p8 + DB); }
        SCHED0();
        z[2 * kk] = va; z[2 * kk + 1] = vb;
    }
}
DEVI void g_fill(GTile& c, const float (&e)[4]) {
#pragma unroll
    for (int m = 0; m < 4; ++m) {
#pragma unroll
        for (int n = 0; n < 4; ++n) c.a[m][n] = pin((f32x4){e[m], e[m], e[m], e[m]});
        if (m < 2) c.e[m] = pin((f32x4){e[m], e[m], e[m], e[m]});
    }
}

#undef STAMP_SEC
#define STAMP_SEC 1
DEVI void gmlp_body(LAS char* lds, int wm, int wn, int pm, int cg, char* z2t, char* y, const u16* __restrict__ bsp STAMP_PARAM) {
    const int mlo = wm * 4 + wn * 2, mhi = wm * 4 + (1 - wn) * 2;
    const float zero4[4] = {0.f, 0.f, 0.f, 0.f};
    GTile z2, z1, acc;
    GFrag f;
    {
        const int lane = lane_id_fresh();
#pragma unroll
        for (int kk = 0; kk < 2; ++kk) { f.pa[kk][0] = lds + mlo * 2048 + kk * 1024 + lane * 16; f.pa[kk][1] = lds + mhi * 2048 + kk * 1024 + lane * 16; }
        f.pb = lds + 16384 + wn * 4096 + lane * 16; f.p8 = lds + 16384 + 8192 + lane * 16; f.slot = 0;
    }
    g_fill(z2, zero4); g_fill(z1, zero4);
#pragma unroll
    for (int i = 0; i < 4; ++i) f.pa[i >> 1][i & 1] = opaque(f.pa[i >> 1][i & 1]);
    f.pb = opaque(f.pb); f.p8 = opaque(f.p8);
    SCHED0();
    WAIT_V(0); BARX();
    BAR();
    BARX();
    STAMP(2);
#pragma unroll
    for (int i = 0; i < 9; ++i) g_rd(f, 0, i);
    g_steps<4, G_STAGE, true>(z2, f, 9);
    STAMP(3);
    {
        const int lane = lane_id_fresh(), r = lane & 15, q = lane >> 4;
        f32x4 ra[4];
#pragma unroll
        for (int m = 0; m < 4; ++m) ra[m] = *(const LAS f32x4*)(lds + G_TAB + ((m < 2 ? mlo : mhi) + (m & 1)) * 64 + q * 16);
        SCHED0();
#pragma unroll
        for (int m = 0; m < 4; ++m) {
            f32x4 s1, s2;
#pragma unroll
            for (int n = 0; n < 4; ++n) {
                const f32x4 g = gelu4(z2.a[m][n] * ra[m]);
                z2.a[m][n] = g;
                if (n == 0) { s1 = g; s2 = g * g; } else { s1 += g; s2 += g * g; }
            }
            if (m < 2) { const f32x4 g = gelu4(z2.e[m] * ra[m]); z2.e[m] = g; s1 += g; s2 += g * g; }
            float u0, u1;
            rowsum16x8(s1, s2, u0, u1);
            if ((r & 3) == 0) *(LAS f32x2*)(lds + G_PART + wn * 1024 + ((m < 2 ? mlo : mhi) + (m & 1)) * 128 + (q * 4 + (r >> 2)) * 8) = (f32x2){u0, u1};
            SCHED0();
        }
    }
    WAIT_L0(); BAR();
    STAMP(4);
    g_steps<4, G_STAGE, false>(z1, f, 4);
    unsigned b0, b1;
    f32x4 c0[4];
    g_step_u<G_STAGE, G_STAGE, false>(z1, f, c0, [&](auto ic) {
        if (ic.value != 18) return;
        STAMP(5);
        const int lane = lane_id_fresh(), q = lane >> 4;
        b0 = __builtin_amdgcn_raw_buffer_load_b32(RSRC(bsp), (lane & 15) * 4, pm * 256 + mlo * 32, 0);
        b1 = __builtin_amdgcn_raw_buffer_load_b32(RSRC(bsp), (lane & 15) * 4, pm * 256 + mhi * 32, 0);
#pragma unroll
        for (int g = 0; g < 2; ++g) {
            const auto rz2 = RSRC(z2t + (size_t)pm * 589824 + cg * 36864 + ((g ? mhi : mlo) >> 1) * 9216);
            const LAS char* tab = lds + G_TAB + (g ? mhi : mlo) * 64 + q * 16;
            LAS char* own = lds + (wm ? G_OWN : G_STAGE + 16384) + (g ^ wn) * 9216 + lane * 16;
            f32x4 ra[2], rb[2];
#pragma unroll
            for (int T = 0; T < 2; ++T) { ra[T] = *(const LAS f32x4*)(tab + 512 + T * 64); rb[T] = *(const LAS f32x4*)(tab + 1024 + T * 64); }
#pragma unroll
            for (int n = 0; n < (g ? 4 : 5); ++n) {
                const f32x4 v0 = (n < 4 ? z2.a[2 * g][n & 3] : z2.e[0]) * ra[0] + rb[0], v1 = (n < 4 ? z2.a[2 * g + 1][n & 3] : z2.e[1]) * ra[1] + rb[1];
                const u32x4 o = {pack2(v0[0], v1[0]), pack2(v0[1], v1[1]), pack2(v0[2], v1[2]), pack2(v0[3], v1[3])};
                ST16(rz2, lane * 16, n < 4 ? wn * 4096 + n * 1024 : 8192, o, 16);
                *(LAS u32x4*)(own + (n < 4 ? wn * 4096 + n * 1024 : 8192)) = o;
            }
        }
        STAMP(6);
    });
    static_for<3>([&](auto sc) {
        constexpr int D = sc.value == 0 ? G_STAGE : sc.value == 1 ? -3 * G_STAGE : 2 * G_STAGE;
        g_step_u<D, D, false>(z1, f, c0, [&](auto ic) { if (ic.value == 17 && sc.value == 1) WAIT_V(0); });
    });
    float ra[4], c1;
    {
        int lane, da[2];
        g_step_u<-G_STAGE, -G_STAGE, false>(z1, f, c0, [&](auto ic) {
            constexpr int i = ic.value;
            if (i == 9) lane = pin(lane_id_fresh());
            if (i == 14 || i == 15) da[i & 1] = pin((lane & 15) * 128 + (((4 * (i & 1) + (lane >> 4)) ^ (lane & 7)) * 16) - lane * 16 - (i & 1) * 1024);
            if (i == 16 || i == 17) f.pa[0][i & 1] = opaque(f.pa[0][i & 1] + da[0]);
            if (i == 27 || i == 28) f.pa[1][i & 1] = opaque(f.pa[1][i & 1] + da[1]);
            if (i >= 29 && i < 33) {
                constexpr int m = (i - 29) & 3;
                const float e = m & 1 ? bfhi(m < 2 ? b0 : b1) : bflo(m < 2 ? b0 : b1);
                c0[m] = pin((f32x4){e, e, e, e});
            }
            if (i == 33) {
#pragma unroll
                for (int m = 0; m < 4; ++m) ra[m] = *(const LAS float*)(lds + G_TAB + ((m < 2 ? mlo : mhi) + (m & 1)) * 64 + (lane & 15) * 4);
                c1 = pin(-0.10294324f);
            }
        });
    }
    STAMP(7);
    g_step_u<2 * G_STAGE, G_OWN - G_STAGE - 16384, true>(acc, f, c0, [](auto) {});
    static_for<18>([&](auto sc) {
        constexpr int st = sc.value, m = st < 16 ? st >> 2 : st & 1;
        constexpr int D = st == 0 ? -3 * G_STAGE : st % 3 == 1 ? 2 * G_STAGE : -G_STAGE;
        g_step_gelu<D, st == 0 ? 16384 - G_OWN : D>(acc, f, st < 16 ? z1.a[m][st & 3] : z1.e[m], ra[m], c1);
    });
    f.slot = 1;
    STAMP(9);
    g_steps<3, -G_STAGE, false>(acc, f, 12);
    __amdgpu_buffer_rsrc_t ry;
    unsigned vy[4], ve[2];
    int lane;
    u32x4 o = {0u, 0u, 0u, 0u};
    auto y_part = [&](auto jc) {
        constexpr int j = jc.value, m = j / 10, k = j % 10, e = (k & 1) * 2;
        const f32x4 &a = k < 8 ? acc.a[m][k >> 1] : acc.e[m & 1], &z = k < 8 ? z1.a[m][k >> 1] : z1.e[m & 1];
        float t0, t1;
        asm volatile("v_mul_f32 %1, %3, %5\n\tv_mul_f32 %2, %4, %6\n\tv_cvt_pk_bf16_f32 %0, %1, %2" : "=v"(o[k & 3]), "=&v"(t0), "=&v"(t1) : "v"(a[e]), "v"(a[e + 1]), "v"(z[e]), "v"(z[e + 1]));
        if (k == 3 || k == 7) ST16(ry, vy[m] + (k >> 2) * 1024, 0, o, 16);
        if (k == 9) ST8(ry, ve[m & 1], 0, ((u32x2){o[0], o[1]}), 16);
    };
    WAIT_L(0);
    SCHED0();
    static_for<36>([&](auto ic) {
        constexpr int i = ic.value, kk = i < 14 ? 0 : i < 28 ? 1 : i >= 32, t = i < 28 ? i % 14 : 14 + (i & 3), m = t < 10 ? t / 5 : 2 + (t - 10) / 4, n = t < 10 ? t % 5 : (t - 10) & 3;
        if (i == 14) { WAIT_L(0); BAR(); SCHED0(); }
        f32x4& e = n < 4 ? acc.a[m][n & 3] : acc.e[m & 1];
        MFMA_T(f.b[kk][n], f.a[kk][m], e);
        SCHED0();
        if (i < 9) g_rd(f, 1, i);
        if (i == 10) { ry = RSRC(own_s(y) + (size_t)pin_s(pm * 8 * 73728 + cg * 4608)); keep_s(ry); }
        if (i == 12) { lane = pin(lane_id_fresh()); vy[0] = pin((unsigned)(mlo * 73728 + wn * 2048 + lane * 16)); }
        if (i == 14) ve[0] = pin((unsigned)(mlo * 73728 + 4096 + (lane >> 5) * 256 + (lane & 15) * 16 + ((lane >> 4) & 1) * 8));
        if (i == 15) { vy[1] = pin(vy[0] + 73728); ve[1] = pin(ve[0] + 73728); }
        if (i == 16) { vy[2] = pin(vy[0] + (mhi - mlo) * 73728); vy[3] = pin(vy[2] + 73728); asm volatile("s_nop 4"); }
        if constexpr (i >= 17) y_part(IC<i - 17>{});
        SCHED0();
    });
    STAMP(10);
    y_part(IC<19>{});
    SCHED0();
#pragma unroll
    for (int i = 0; i < 4; ++i) {
        const int m = 2 + (i >> 1), p = i & 1;
        if (i == 2) asm volatile("" : "+v"(acc.a[3][0]), "+v"(acc.a[3][1]), "+v"(acc.a[3][2]), "+v"(acc.a[3][3]));
        const f32x4 a = acc.a[m][2 * p] * z1.a[m][2 * p], c = acc.a[m][2 * p + 1] * z1.a[m][2 * p + 1];
        ST16(ry, vy[m] + p * 1024, 0, ((u32x4){pack2(a[0], a[1]), pack2(a[2], a[3]), pack2(c[0], c[1]), pack2(c[2], c[3])}), 16);
        SCHED0();
    }
}

#define NET_IDS \
    int b = __builtin_amdgcn_readfirstlane(blockIdx.x), wid = wid8 & 3; \
    asm volatile("" : "+s"(b), "+s"(wid));     \
    const int xcd = b & 7, idx = b >> 3; \
    const int pa = (xcd >> 1) * 4 + (idx & 3), hp = (xcd & 1) * 8 + (idx >> 2); \
    const int pm = (xcd >> 2) * 8 + (idx & 7), cg = (xcd & 3) * 4 + (idx >> 3); \
    const int rh = RH_MAP ? xcd * 4 + (idx & 3) : (xcd >> 1) * 8 + (idx & 7), c5 = RH_MAP ? idx >> 2 : (xcd & 1) * 4 + (idx >> 3); \
    const int lead = (idx + (idx >> 3)) & 3; \
    auto kord = [&](int t, int n) { \
        const int k = t + ((int)(0x1F000000u << (((t - lead) & 3) * 4)) >> 28); \
        return (unsigned)k < (unsigned)n ? k : t; \
    }; \
    auto kord5 = [&](int t) { \
        const int k = t + ((int)(0x1F000000u << (((t - (RH_MAP ? c5 : lead)) & 3) * 4)) >> 28); \
        return (unsigned)k < 18u ? k : t; \
    };

typedef short s16x4 __attribute__((ext_vector_type(4)));
DEVI u32x4 tr_frag(const LAS char* p, int four_rows) {
    const u32x2 a = __builtin_bit_cast(u32x2, __builtin_amdgcn_ds_read_tr16_b64_v4i16((LAS s16x4*)p));
    const u32x2 b = __builtin_bit_cast(u32x2, __builtin_amdgcn_ds_read_tr16_b64_v4i16((LAS s16x4*)(p + four_rows)));
    return (u32x4){a.x, a.y, b.x, b.y};
}
DEVI float dot2(unsigned v, unsigned w, float acc) { return __builtin_amdgcn_fdot2_f32_bf16(__builtin_bit_cast(bf16x2_t, v), __builtin_bit_cast(bf16x2_t, w), acc, false); }
DEVI float sum2(unsigned v, float acc) { return dot2(v, 0x3f803f80u, acc); }
struct Sel { unsigned lo, hi, nlo, nhi; };
DEVI Sel make_sel() { return {(unsigned)pin_s(0x00003f80), (unsigned)pin_s(0x3f800000), (unsigned)pin_s(0x0000bf80), (unsigned)pin_s((int)0xbf800000u)}; }
DEVI unsigned centre2(unsigned v, f32x2& e, float mean, const Sel& k) {
    const float a = dot2(k.lo, v, e.x), b = dot2(k.hi, v, e.y);
    const unsigned r = pack2(a, b);
    e = (f32x2){dot2(k.nlo, r, a), dot2(k.nhi, r, b)} - mean;
    return r;
}
typedef u32x4 Retile[9];
template <int PITCH>
DEVI void retile_out_make(Retile& o, const LAS char* img, int lane) {
    const int c = lane & 15, q = lane >> 4, p = lane & 3;
    const LAS char* src = img + (q * 8 + (c >> 2)) * PITCH;
#pragma unroll
    for (int i = 0; i < 8; ++i) o[i] = tr_frag(src + p * 16 + (i >> 2) * 144 + ((i >> 1) & 1) * 64 + (i & 1) * 8, 4 * PITCH);
    o[8] = tr_frag(src + 128 + (p >> 1) * 144 + (p & 1) * 8, 4 * PITCH);
}
DEVI void retile_out_store(const Retile& o, char* dst, int nkc, int kc, int w, int lane) {
    const int c = lane & 15, q = lane >> 4;
    const auto rd = RSRC(dst + (size_t)(2 * w * nkc + kc) * 4608);
#pragma unroll
    for (int i = 0; i < 8; ++i) ST16(rd, lane * 16 + (i & 3) * 1024, (i >> 2) * nkc * 4608, o[i], 16);
    ST16(rd, (c >> 3) * nkc * 4608 + 4096 + q * 128 + (c & 7) * 16, 0, o[8], 16);
}

DEVI float dpp_ror(float v, int n) { return n == 8 ? __builtin_amdgcn_update_dpp(0.f, v, 0x128, 0xf, 0xf, true) : n == 4 ? __builtin_amdgcn_update_dpp(0.f, v, 0x124, 0xf, 0xf, true)
                                          : n == 2 ? __builtin_amdgcn_update_dpp(0.f, v, 0x122, 0xf, 0xf, true) : __builtin_amdgcn_update_dpp(0.f, v, 0x121, 0xf, 0xf, true); }
DEVI unsigned long long epoch_of(const char* ws) {
    SCHED0();
    const unsigned long long ep = *(const __attribute__((address_space(4))) unsigned long long*)(ws + pin_s((int)OFF_EP)) + 1ull;
    asm volatile("" :: "s"(ep));
    return ep;
}
DEVI void prologue_done(LAS char* lds, char* ws, unsigned long long ep, int b, int lane) {
    if (lds_arrive(lds + G_WORD + 8) == 7) ST16(RSRC(ws), lane ? 0x80000000u : (unsigned)OFF_FL + b * 16, 0, ((u32x4){(unsigned)ep, (unsigned)(ep >> 32), 0u, 0u}), 16);
}
DEVI void centre_frag(u32x4& f, f32x2 (&er)[4], float mean, const Sel& k) {
#pragma unroll
    for (int e = 0; e < 4; ++e) f[e] = centre2(f[e], er[e], mean, k);
}
template <int N>
DEVI void column_means(LAS char* lds, LAS char* cnt, int me, int lane, float a, float b, float& ma, float& mb) {
    LAS float* red = (LAS float*)(lds + G_PART);
    auto x = __builtin_amdgcn_permlane16_swap(__float_as_uint(a), __float_as_uint(b), false, false);
    red[me * 64 + lane] = __uint_as_float(x[0]) + __uint_as_float(x[1]);
    if (lds_arrive(cnt) < N - 1) lds_count_wait(cnt, N);
    float t = 0.f;
#pragma unroll
    for (int i = 0; i < N; ++i) t += red[i * 64 + lane];
    x = __builtin_amdgcn_permlane32_swap(__float_as_uint(t), __float_as_uint(t), false, false);
    t = (__uint_as_float(x[0]) + __uint_as_float(x[1])) * (1.0f / 576.0f);
    x = __builtin_amdgcn_permlane16_swap(__float_as_uint(t), __float_as_uint(t), false, false);
    ma = __uint_as_float(x[0]); mb = __uint_as_float(x[1]);
}
template <class ARGS>
DEVI unsigned long long prologue(LAS char* lds, char*& ws, const char*& x0, const char*& wqkv, const u16*& wo, const u16*& w_in, int b, int pa, int wid8, ARGS&& args PST_PARAM) {
    PST_DECL;
    const int lane = lane_id_fresh(), c = lane & 15, q = lane >> 4, w = wid8 & 3;
    const int jw = w - 1 + 3 * (((wid8 >> 2) ^ (b >> 3)) & 1);
    const bool wjob = b >= 16 && b < 112 && w;
    const int uo = (wid8 - 1 - (wid8 >> 2)) * 16 + b;
    const bool ojob = b < 16 && w && uo < 72;
    const unsigned l0 = (unsigned)(size_t)lds;
    LAS char* img = lds + 2 * K1_STAGE;
    const int uw = b - 16, head = (uw & 7) * 4 + ((uw >> 3) & 3), part = uw >> 5, hp = head >> 1, odd = head & 1;
    const int c0 = part * 1152 + hp * 72 + odd * 40;
    {
        const int so = pin_s(jw * 96 * 3456 + (c0 & ~15));
        const unsigned v = (lane & 3) < ((head & 3) == 3 ? 2 : 3) ? (lane >> 2) * 3456 + (lane & 3) * 16 : 0x80000000u, d = 16 * 3456 - 1024;
        const unsigned v0 = pin(v), v1 = pin(v + d), v2 = pin(v + 2 * d), v3 = pin(v + 3 * d);
        const int e = ((b - 112) >> 3) * 192 + wid8 * 64 + lane, row = (e * 10923) >> 16, r = e - row * 6;
        const unsigned vp = pin((unsigned)(row * 3456 + (r >> 1) * 1152 + (r & 1) * 128));
        const unsigned la = pin_s(l0 + 2 * K1_STAGE + jw * 6144);
        args(0);
        if (b >= 112 && wid8 < 3) dma1(l0 + G_JUNK, RSRC(wqkv), (b & 7) * 128, vp);
        if (wjob) { dma4<false>(la, RSRC(wqkv), so, v0, v1, v2, v3); dma2<false>(la + 4096, RSRC(wqkv), so + 64 * 3456, v0, v1); }
        args(1);
    }
    if (ojob) {
        const auto rs = RSRC(wo + (uo & 3) * 144);
#pragma unroll
        for (int j = 0; j < 9; ++j) {
            const int i = lane + 64 * j, rl = (i * 3641) >> 16, kp = (uo >> 2) * 32 + rl;
            const int row = kp < 512 ? 18 * (kp >> 4) + (kp & 15) + 2 * ((kp >> 4) & 1) : 36 * ((kp - 512) >> 2) + 16 + (kp & 3);
            dma1(l0 + 2 * K1_STAGE + wid8 * 9216 + j * 1024, rs, 0, (unsigned)(row * 1152 + (i - rl * 18) * 16));
        }
    }
    if (wid8 == 4) { lds_word_set(lds + G_WORD + c * 4, 0u); WAIT_L0(); }
    BAR();
    unsigned long long ep;
    if (wid8 == 4) ep = epoch_of(ws);
    if (wid8 == 4 && b >= 16 && b < 112) lds_count_wait(lds + G_WORD + 12, 12);
    if (wid8 > 4 && b >= 112) {
        __builtin_amdgcn_s_sleep(I_SLEEP);
        const unsigned v = (lane >> 2) * 9216 + (lane & 3) * 16, d = 16 * 9216 - 1024;
#pragma unroll
        for (int i = 0; i < 3; ++i) dma4<false>(l0 + 3 * K1_STAGE + (w - 1) * 12288 + i * 4096, RSRC(w_in + win_strip(b) * 32), (win_third(b, w) * 192 + i * 64) * 9216, v, v + d, v + 2 * d, v + 3 * d);
    }
    if (wid8 == 4) {
        AttnX ax = attn_x(x0, x0, 0, pa, 0, lane);
#pragma unroll
        for (int s = 0; s < 2; ++s)
#pragma unroll
            for (int i = 0; i < 4; ++i) { ax.oA = (pa * 8 + 2 * i) * 18432; attn_issueA(lds, ax, i, s, s); }
    }
    u32x4 xv[9];
    if (wid8 == 0) {
        const unsigned v = (lane >> 3) * 2304 + (lane & 7) * 16;
        const auto rx = RSRC(x0 + (size_t)slot2row(b * 8) * 1152);
#pragma unroll
        for (int i = 0; i < 9; ++i) asm volatile("s_nop 4\n\tbuffer_load_dwordx4 %0, %1, %2, 0 offen offset:%3" : "+v"(xv[i]) : "v"(v), "s"(rx), "i"(i * 128) : "memory");
        const auto rw = RSRC(ws);
        const u32x4 ones = {~0u, ~0u, ~0u, ~0u}, zero = {0u, 0u, 0u, 0u};
        const unsigned vo = lane * 16;
        ST16(rw, vo, (unsigned)OFF_ZST + b * 2048, ones, 16); ST16(rw, vo, (unsigned)OFF_ZST + b * 2048 + 1024, ones, 16);
        ST16(rw, vo, (unsigned)OFF_CNT + b * 2560, zero, 16); ST16(rw, vo, (unsigned)OFF_CNT + b * 2560 + 1024, zero, 16);
        ST16(rw, vo, lane < 32 ? (unsigned)OFF_CNT + b * 2560 + 2048 : (unsigned)(OFF_L0 + L_STB) + b * 512 - 512, zero, 16);
        ST16(rw, vo, (unsigned)(OFF_L0 + L_O) + (b >> 1) * 18432 + 16384 + (b & 1) * 1024, zero, 16);
        ST16(rw, b < 12 ? vo : 0x80000000u, (unsigned)OFF_FL2 + b * 1024, zero, 16);
    }
    PST(1);
    if (wjob) {
        const Sel k = make_sel();
        const LAS char* s0 = opaque(img + jw * 6144 + (q * 8 + (c >> 2)) * 64 + (c0 & 15) + (c & 3) * 8);
        const LAS char* s1 = opaque(img + jw * 6144 + (q * 8 + (c >> 2)) * 64 + (c0 & 15) + 32 + min(c & 3, 2) * 2048);
        const auto rd = RSRC(ws + OFF_TWQKV + (size_t)(hp * 18 + jw * 3) * 6144 + (3 * odd + part) * 1024), rd2 = RSRC(ws + OFF_TWT + (size_t)((hp * 3 + part) * 18 + jw * 3) * 256);
        const unsigned vd2 = pin(c < 12 && !odd ? (unsigned)((c >> 2) * 256 + q * 64 + (c & 3) * 16) : 0x80000000u);
        asm volatile("" :: "s"(rd), "s"(rd2));
        WAIT_V(0);
        PST(2);
        u32x4 f[4];
        float sm[4] = {0.f, 0.f, 0.f, 0.f}, m0, m1;
#pragma unroll
        for (int i = 0; i < 4; ++i) f[i] = tr_frag(i < 3 ? s0 + i * 2048 : s1, 256);
#pragma unroll
        for (int e = 0; e < 16; ++e) sm[(e >= 12) * 2 + (e & 1)] = sum2(f[e >> 2][e & 3], sm[(e >= 12) * 2 + (e & 1)]);
        sm[2] = c < 12 ? sm[2] + sm[3] : 0.f;
        sm[2] += dpp_ror(sm[2], 4); sm[2] += dpp_ror(sm[2], 8);
        column_means<6>(lds, lds + G_WORD + 12, jw, lane, sm[0] + sm[1], sm[2], m0, m1);
        PST(3);
        f32x2 e0[4] = {{-m0, -m0}, {-m0, -m0}, {-m0, -m0}, {-m0, -m0}}, e1[4] = {{-m1, -m1}, {-m1, -m1}, {-m1, -m1}, {-m1, -m1}};
#pragma unroll
        for (int i = 0; i < 3; ++i) { centre_frag(f[i], e0, m0, k); ST16(rd, lane * 16, i * 6144, f[i], 16); }
        centre_frag(f[3], e1, m1, k); ST16(rd2, vd2, 0, f[3], 16);
        PST(4);
        lds_arrive(lds + G_WORD + 12);
    }
    if (wid8 != 4) ep = epoch_of(ws);
    {
        if (wjob) {
            const unsigned fo = pin((unsigned)OFF_FW + ((lane >> 3) * 16 + hp) * 1024 + (3 * odd + part) * 128 + (lane & 7) * 16);
            WAIT_V(0);
            PST(5);
            if (lds_arrive(lds + G_WORD + 4) == 5) {
                const u32x4 e4 = {(unsigned)ep, (unsigned)(ep >> 32), (unsigned)ep, (unsigned)(ep >> 32)};
                ST16(RSRC(ws), fo, 0, e4, 16); ST16(RSRC(ws), fo + 131072, 0, e4, 16);
            }
        }
    }
    if (wid8 == 0) {
        WAIT_V(7);
        asm volatile("" : "+v"(xv[0]), "+v"(xv[1]), "+v"(xv[2]), "+v"(xv[3]), "+v"(xv[4]), "+v"(xv[5]), "+v"(xv[6]), "+v"(xv[7]), "+v"(xv[8]));
        float s = 0.f, q2 = 0.f;
#pragma unroll
        for (int e = 0; e < 36; ++e) { s = sum2(xv[e >> 2][e & 3], s); q2 = dot2(xv[e >> 2][e & 3], xv[e >> 2][e & 3], q2); }
        ST8(RSRC(ws + OFF_L0 + L_STA + (size_t)b * 512), lane * 8, 0, ((u32x2){__float_as_uint(s), __float_as_uint(q2)}), 16);
    }
    if (ojob) {
        WAIT_V(0);
        Retile rt;
        retile_out_make<288>(rt, img + wid8 * 9216, lane); retile_out_store(rt, ws + OFF_TWO, 18, uo >> 2, uo & 3, lane);
    }
    PST(7);
    PST_DUMP(wid8 == 0 || wid8 == 4 || wid8 == 5, wid8 ? wid8 : 2);
    return ep;
}

__global__ __launch_bounds__(512, 2) TRACE_ATTR void k_net(char* ws, const u16* wsp, const u16* bsp, char* out, const char* x0, const char* wqkv, const u16* wo,
                                                             const u16* w_in, const u16* w_out DBG_ARG) {
    extern __shared__ __attribute__((aligned(16))) char smem[];
    LAS char* lds = (LAS char*)smem;
    asm volatile("gm_code_begin:");
    if (blockIdx.x < 16) dma1((unsigned)(size_t)lds + G_JUNK, code_rsrc(), 0, (threadIdx.x * 2 + (blockIdx.x >> 3)) * 128);
    STAMP_DECL;
    const int wid8 = __builtin_amdgcn_readfirstlane(threadIdx.x >> 6);
    unsigned long long ep;
    {
        NET_IDS
        ep = prologue(lds, ws, x0, wqkv, wo, w_in, b, pa, wid8, [&](int part) {
            if (part == 0) { asm volatile("" :: "s"(wqkv), "s"(w_in), "s"(w_out)); return; }
            ws = own_s(ws); wsp = own_s(wsp); bsp = own_s(bsp); out = own_s(out); x0 = own_s(x0); wo = own_s(wo); w_out = own_s(w_out);
        } PST_FWD);
    }
    const char *twin = ws + OFF_TWIN, *twout = ws + OFF_TWOUT;
    if (wid8 >= 4) {
        u32x4 wr[5][9];
#pragma unroll 1
      for (int layer = 0; layer < NET_LAYERS; ++layer) {
        NET_IDS
        TRACE_BEGIN;
        char* wl = ws + OFF_L0 + layer * SZ_L;
        const unsigned fl = layer * F_LAYER;
        const char *xin = wl + L_XB, *st = wl + L_STB, *z2t = wl + (layer ? L_Y : L_Z2T);
        unsigned long long* zst = (unsigned long long*)(ws + OFF_ZST + layer * SZ_ZST);
        const int lane = lane_id_fresh(), l3 = lane >> 3;
        const unsigned vo = lane * 16;
        const unsigned l0 = (unsigned)(size_t)lds, lA = l0 + wid * 4096, lB = l0 + 16384 + wid * 4608;
        const auto rX = RSRC(xin + (size_t)(pm * 8 + 2 * wid) * 18432), rI = RSRC(twin + (size_t)cg * (18 * 18432) + wid * 4608);
        auto issue1B = [&](int g, int slot) { const int k = kord(g < 9 ? g : (g < 13 ? g - 4 : g - 13), 9); dma5<true>(lB + slot * G_STAGE, rI, (g < 9 ? 9 + k : k) * 18432, vo); };
        auto issue1A = [&](int g, int slot) { dma4<true>(lA + slot * G_STAGE, rX, kord(g < 9 ? g : g - 13, 9) * 2048, vo, vo, vo + 16384, vo + 16384); };
        auto issue1 = [&](int g, int slot) { issue1B(g, slot); issue1A(g, slot); };
        const auto rA = RSRC((const char*)wsp + (size_t)(pm * 128 + wid * 32) * 4096), rB = RSRC(z2t + cg * 36864 + wid * 4608);
        const unsigned voA = (unsigned)(2 * l3 * 4096 + ((lane & 7) ^ l3) * 16);
        auto issueB = [&](int k, int slot) { dma5<true>(lB + slot * G_STAGE, rB, (k >> 1) * 589824 + (k & 1) * 18432, vo); };
        const auto rW = RSRC(twout + (size_t)c5 * (72 * 4608));
        auto issue5 = [&](int t) { dma5<true>(l0 + ((t + 6) & 7) * K5_STAGE + wid * 4608, rW, kord5(t) * K5_STAGE + wid * 4608, vo); };
        auto load5 = [&](int p, int i) {
            const int t0 = p ? 2 * p + 6 : 4, ow = kord5(t0 + i) * K5_STAGE + wid * 4096;
            asm volatile("s_nop 4\n\tbuffer_load_dwordx4 %0, %4, %5, %6 offen sc1\n\tbuffer_load_dwordx4 %1, %4, %5, %6 offen offset:1024 sc1\n\t"
                         "buffer_load_dwordx4 %2, %4, %5, %6 offen offset:2048 sc1\n\tbuffer_load_dwordx4 %3, %4, %5, %6 offen offset:3072 sc1"
                         : "+v"(wr[p][4 * i]), "+v"(wr[p][4 * i + 1]), "+v"(wr[p][4 * i + 2]), "+v"(wr[p][4 * i + 3]) : "v"(vo), "s"(rW), "s"(ow) : "memory");
            if (i) {
                const int oe = kord5(t0 + (wid >> 1)) * K5_STAGE + 16384 + (wid & 1) * 1024;
                asm volatile("s_nop 4\n\tbuffer_load_dwordx4 %0, %1, %2, %3 offen sc1" : "+v"(wr[p][8]) : "v"(vo), "s"(rW), "s"(oe) : "memory");
            }
        };
        auto put5 = [&](int w0, int w1) {
#pragma unroll
            for (int w = w0; w < w1; ++w) {
                const int p = w / 9, i = w % 9 == 4 ? -1 : w % 9 - (w % 9 > 4);
                LAS char* sl = lds + (((p ? 2 * p + 6 : 4) + 6) & 7) * K5_STAGE + lane * 16;
                if (i < 0) *(LAS u32x4*)(sl + (wid >> 1) * K5_STAGE + 16384 + (wid & 1) * 1024) = wr[p][8];
                else *(LAS u32x4*)(sl + (i >> 2) * K5_STAGE + wid * 4096 + (i & 3) * 1024) = wr[p][i];
            }
            WAIT_L0();
        };
        const int row = wid * 32 + (lane & 31);
        auto step1 = [&](int t) { BAR(); issue1(t + 4, t & 3); };
        const char* ps;
        unsigned fd, fd2;
        {
            unsigned fo, fi;
            const bool oj = !layer && wid && b < 96, ij = !layer && wid && b >= 112, early = win_strip(b) >= WIN_EARLY;
            const int ojob = b * 3 + wid - 1, oimg = 2048 + (wid - 1) * 9216;
            float m0, m1;
            unsigned fw;
            auto win_store = [&](int h) {
                const int u = win_strip(b) * 2 + h, half = u >= 144, g = u - half * 144, ug = g / 9, j = g - ug * 9, c = lane & 15;
                const auto rd = RSRC(ws + OFF_TWIN + (size_t)ug * (18 * 18432) + (half * 18 + win_third(b, wid) * 6) * 9216);
                const unsigned vd = (lane >> 4) * 256 + (j < 8 ? ((j & ~1) + (c >> 3)) * 1024 + ((j & 1) * 8 + (c & 7)) * 16 : 8192 + c * 16);
#pragma unroll
                for (int i = 0; i < 6; ++i) ST16(rd, vd, i * 9216, wr[1 + h][i], 16);
            };
            auto win_half = [&](int h) {
                const Sel k = make_sel();
                const float m = h ? m1 : m0;
                f32x2 er[4] = {{-m, -m}, {-m, -m}, {-m, -m}, {-m, -m}};
#pragma unroll
                for (int i = 0; i < 6; ++i) centre_frag(wr[1 + h][i], er, m, k);
            };
            attn_producer(lds, ws, x0, wl, fl, ep, layer, pa, hp, wid, [&] { if (b < 16) WAIT_V(0); prologue_done(lds, ws, ep, b, lane); }, [&](int part) {
                if (part == -1 && ij) {
#pragma unroll
                    for (int h = 0; h < 2; ++h) {
                        const LAS char* src = lds + 3 * K1_STAGE + (wid - 1) * 12288 + ((lane >> 4) * 8 + ((lane & 15) >> 2)) * 64 + h * 32
                                              + ((win_strip(b) * 2 + h) % 9 < 8 ? (lane & 1) * 16 + (lane & 2) * 4 : (lane & 3) * 8);
#pragma unroll
                        for (int i = 0; i < 6; ++i) wr[1 + h][i] = tr_frag(src + i * 2048, 256);
                    }
                    WAIT_L(0);
                }
                if (part == 0 && oj) {
                    const auto rs = RSRC(w_out + (size_t)(ojob >> 2) * 32 * 576 + (ojob & 3) * 144);
#pragma unroll
                    for (int j = 0; j < 9; ++j) { const int i = lane + 64 * j; dma1(l0 + oimg + j * 1024, rs, 0, (unsigned)(((i * 3641) >> 16) * (1152 - 288) + i * 16)); }
                }
                if (part == 1) {
                    if (layer) { issue1B(0, 0); issue1B(1, 1); }
                    fo = flag_out(fl + F_X1, 256, pa, hp, lane); fi = flag_in(fl + F_X1, 256, pm * 16 + cg, 16, lane);
                    ps = pin(st + (size_t)(pm * 128 + wid * 32) * 64 + lane * 16);
                    fd = pin((unsigned)OFF_FL2 + wid * 1024 + vo);
                    fd2 = pin((unsigned)OFF_FL2 + (lane < 32 ? 256 + lane : 288 + ((cg * 9) >> 1) * 3 + min(lane & 15, 14)) * 16);
                    fw = pin((unsigned)OFF_FL2 + (288 + (72 + ((cg * 9) >> 1)) * 3 + min(lane & 15, 14)) * 16);
                    if (ij) {
                        float sm[4] = {0.f, 0.f, 0.f, 0.f};
#pragma unroll
                        for (int e = 0; e < 48; ++e) sm[(e >= 24) * 2 + (e & 1)] = sum2(wr[1 + (e >= 24)][(e % 24) >> 2][e & 3], sm[(e >= 24) * 2 + (e & 1)]);
                        column_means<3>(lds, lds + G_WORD, wid - 1, lane, sm[0] + sm[1], sm[2] + sm[3], m0, m1);
                        win_half(0);
                    }
                }
                if (part == 2 && ij) { if (early) win_store(0); win_half(1); if (early) win_store(1); }
                if (part == 2 && oj) { retile_out_make<288>(wr[1], lds + oimg, lane); WAIT_L(0); }
            });
            if (oj || ij) {
                if (oj || !early) {
                    __builtin_amdgcn_s_sleep(OJOB_SLEEP);
                    if (oj) retile_out_store(wr[1], ws + OFF_TWOUT, 72, ojob >> 2, ojob & 3, lane); else { win_store(0); win_store(1); }
                }
                WAIT_V(0);
                flag_raise(ws, lane ? 0x80000000u : (unsigned)OFF_FL2 + (oj ? ojob : 288 + win_strip(b) * 3 + wid - 1) * 16);
            }
            if (layer == 0) {
                if (ij) __builtin_amdgcn_s_sleep(WIN_LOOK - 12); else if (!oj) __builtin_amdgcn_s_sleep(WIN_LOOK);
                flags_wait<0, 8>(ws, fw);
            }
            BARX();
            if (wid == 0) flag_raise(ws, fo);
            if (layer == 0) { issue1B(0, 0); issue1B(1, 1); }
            issue1B(2, 2); issue1B(3, 3);
            asm volatile("" :: "s"(rX));
            const int o0 = pin_s(kord(0, 9) * 2048);
            const unsigned v2 = pin(vo + 16384);
            if (wid == 0) { if (layer) __builtin_amdgcn_s_sleep(X1_FIRST); flags_wait<0>(ws, fi);
#pragma unroll
                for (int i = 0; i < 4; ++i) dma4<true>(lA + i * 4096, rX, o0 + i * 36864, vo, vo, v2, v2);
            }
            BARX();
        }
        issue1A(1, 1); issue1A(2, 2);
        WAIT_V(8); BAR();
        issue1A(3, 3);
        {
            f32x4 sp[3];
            WAIT_V(8); step1(0);
            WAIT_V(13); step1(1);
            WAIT_V(18); step1(2);
            unsigned got[2];
            if (layer == 0) asm volatile("s_nop 4\n\tbuffer_load_dword %0, %2, %4, 0 offen sc1\n\tbuffer_load_dword %1, %3, %4, 0 offen sc1" : "+v"(got[0]), "+v"(got[1]) : "v"(fd), "v"(fd2), "s"(RSRC(ws)) : "memory");
            WAIT_IF(layer, 18, 20); step1(3);
            WAIT_IF(layer, 18, 20); step1(4);
            WAIT_V(18);
            if (layer == 0) {
                asm volatile("" : "+v"(got[0]), "+v"(got[1]));
                if (__any((got[0] & got[1]) == 0u)) { flags_wait<0>(ws, fd); flags_wait<0>(ws, fd2); }
            }
            BAR(); issue1B(9, 1); side_load(sp, ps, ps); if (layer == 0) load5(3, 0);
            WAIT_IF(layer, 17, 21); BAR(); issue1B(10, 2); if (layer == 0) load5(3, 1);
            WAIT_IF(layer, 5, 14); BARX();
            side_fence(sp);
#pragma unroll
            for (int i = 0; i < 2; ++i) lds_word_set(lds + G_TAB + (wid * 32 + i * 16 + (lane >> 2)) * 4, __float_as_uint(ln_rstd(sp[i])));
            issue1B(11, 3);
            WAIT_L0();
        }
        BAR(); issue1B(12, 0);
        if (layer == 1) {
            const auto rd = RSRC(out + (size_t)b * 9216 + wid * 2304);
            ST16(rd, vo, 0, ((u32x4){0u, 0u, 0u, 0u}), 16); ST16(rd, vo, 1024, ((u32x4){0u, 0u, 0u, 0u}), 16);
            if (lane < 16) ST16(rd, vo, 2048, ((u32x4){0u, 0u, 0u, 0u}), 16);
        }
        if (layer == 0) {
#pragma unroll
            for (int p = 0; p < 6; ++p) load5(p >> 1, p & 1);
        }
        WAIT_V(0);
        __builtin_amdgcn_s_setprio(3);
        const char* src = pin((const char*)zst + (size_t)(pm * 16 + (lane >> 4) * 4) * 1024 + wid * 256 + (lane & 15) * 16);
        const unsigned tw = pin(l0 + G_TAB + 512 + (lane >> 5) * 512 + (wid * 32 + (lane & 15) * 2 + ((lane >> 4) & 1)) * 4);
        const unsigned fz = flag_out(fl + F_Z2, 256, cg, pm, lane), fzi = pin(fl + F_Z2 + (cg * 16 + pm) * 256 + ((lane ^ pm) & 8) * 16 + (lane & 7) * 16);
        const unsigned vA[4] = {pin(voA), pin(voA + 16 * 4096 - 1024), pin(voA + 4096 - 2048), pin(voA + 17 * 4096 - 3072)};
        auto spA = [&](int so, unsigned la) { dma4<false>(la, rA, so, vA[0], vA[1], vA[2], vA[3]); };
        auto issueA = [&](int k, int slot) { spA(k * 128, lA + slot * G_STAGE); };
        {
            const LAS char* pp = opaque(lds + G_PART + row * 8);
            const auto rs = RSRC(zst + (size_t)(pm * 16 + cg) * 128);
            const unsigned vp = pin(lane < 32 ? (unsigned)row * 8u : 0x80000000u);
            asm volatile("" :: "s"(rs));
            BARX();
            const f32x2 a = *(const LAS f32x2*)pp, c = *(const LAS f32x2*)(pp + 1024);
            ST8(rs, vp, 0, ((u32x2){__float_as_uint(add1(a.x, c.x)), min(__float_as_uint(add1(a.y, c.y)), 0xfffffffeu)}), 16);
            SCHED0();
        }
        __builtin_amdgcn_s_setprio(0);
        {
            f32x4 g[4];
            float sm[4];
            auto gather = [&] {
                asm volatile("global_load_dwordx4 %0, %4, off sc1\n\tglobal_load_dwordx4 %1, %4, off offset:1024 sc1\n\t"
                             "global_load_dwordx4 %2, %4, off offset:2048 sc1\n\tglobal_load_dwordx4 %3, %4, off offset:3072 sc1"
                             : "=&v"(g[0]), "=&v"(g[1]), "=&v"(g[2]), "=&v"(g[3]) : "v"(src) : "memory");
            };
            auto total = [&] {
                asm volatile("" : "+v"(g[0]), "+v"(g[1]), "+v"(g[2]), "+v"(g[3]));
#pragma unroll
                for (int i = 0; i < 4; ++i) sm[i] = add1(add1(g[0][i], g[1][i]), add1(g[2][i], g[3][i]));
            };
            step1(9); step1(10);
            BARX(); gather(); issue1(15, 3);
            WAIT_V(22); BARX();
            __builtin_amdgcn_s_setprio(3);
            WAIT_V(9);
            total();
            if (__any(__builtin_isunordered(sm[1], sm[3]))) {
#pragma unroll 1
                for (unsigned spin = 0; spin < 400000u; ++spin) {
                    bool ok = true;
#pragma unroll
                    for (int i = 0; i < 4; ++i) ok &= __float_as_uint(g[i][1]) != 0xffffffffu && __float_as_uint(g[i][3]) != 0xffffffffu;
                    if (__all(ok)) break;
                    gather();
                    WAIT_V(0);
                    total();
                }
            }
            xq4_sum(sm);
            const bool odd = lane & 16;
            float mean;
            const float rs = rstd_of(odd ? sm[2] : sm[0], odd ? sm[3] : sm[1], 1.0f / 2304.0f, mean);
            asm volatile("ds_write_b32 %0, %1" :: "v"(tw), "v"(lane < 32 ? rs : -mean * rs) : "memory");
            __builtin_amdgcn_s_setprio(0);
            issue1B(16, 0);
            WAIT_L0();
        }
        BARX(); issue1A(16, 0); issueA(2 * pm, 1);
        auto kord2 = [&](int t) { const int j = kord(t - 2, 30); return ((j < 2 * (pm & 7) ? j : j + 2) + 2 * (pm & 8)) & 31; };
        unsigned seen;
        auto look = [&] { if (wid == 0) asm volatile("s_nop 4\n\tbuffer_load_dword %0, %1, %2, 0 offen sc1" : "+v"(seen) : "v"(fzi), "s"(RSRC(ws)) : "memory"); };
        auto need = [&](int n) {
            if (wid == 0) {
                asm volatile("" : "+v"(seen));
                if (__ballot(seen == 0u) & ((1ull << n) - 1ull)) flags_wait<0>(ws, lane < n ? fzi : fl + F_Z2 + (cg * 16 + pm) * 256 + pm * 16);
            }
        };
        auto issue = [&](int t, int sl) { const int k = kord2(t); issueB(k, sl); issueA(k, sl); };
        auto steps2 = [&](int t0, int t1) {
            int sl = (2 * t0 + 2) % 3 * G_STAGE, k = kord2(t0 + 3), ob = (k >> 1) * 589824 + (k & 1) * 18432;
            LOOP_ALIGN();
#pragma unroll 1
            for (int t = t0; t < t1; ++t) {
                WAIT_V(9); BARX();
                dma5<true>(lB + sl, rB, ob, vo);
                const int oa = pin_s(k * 128);
                const unsigned la = pin_s(lA + sl);
                k = pin_s(kord2(t + 4)); ob = pin_s((k >> 1) * 589824 + (k & 1) * 18432);
                sl = pin_s(sl == 0 ? 2 * G_STAGE : sl - G_STAGE);
                spA(oa, la);
            }
        };
        WAIT_V(13); BAR(); issue1(17, 2);
        WAIT_V(13); BARX();
        if (wid == 0) flag_raise(ws, fz);
        issueA(2 * pm + 1, 3); if (layer == 0) load5(4, 0);
        if (wid == 0) WAIT_IF(layer, 5, 9); else WAIT_IF(layer, 4, 8);
        BAR(); issueA(kord2(2), 0); if (layer == 0) load5(4, 1);
        BAR(); look(); issueA(kord2(3), 2);
        WAIT_V(4); need(3); BARX(); issueB(kord2(2), 0); issueB(kord2(3), 2); look();
        WAIT_IF(wid == 0, 6, 5); BAR(); issue(4, 1);
        WAIT_V(9); need(8);
        steps2(2, 11); look();
        WAIT_IF(wid == 0, 10, 9); BAR(); issue(14, 0);
        WAIT_V(9); need(16);
        steps2(12, 29);
        WAIT_V(9); BAR(); issue5(0); issue5(1); issue5(2);
        WAIT_V(15); BAR(); issue5(6); issue5(7);
        BAR(); __builtin_amdgcn_s_sleep(W3_SLEEP); issue5(3); put5(0, 9);
        unsigned vb;
        {
            vb = attn_vb(wid, lane);
            const unsigned fo = flag_out(fl + F_Y, 256, pm, cg, lane), fi = flag_in(fl + F_Y, 256, rh * 8 + c5, 16, lane);
            __builtin_amdgcn_s_setprio(3);
            WAIT_V(0); BARX();
            if (wid == 0) { flag_raise(ws, fo); flags_wait<Y_FIRST>(ws, fi); }
            __builtin_amdgcn_s_setprio(0);
            BARX();
        }
        put5(9, 13);
        BAR(); put5(13, 18);
        BAR(); put5(18, 24);
        BAR(); put5(24, 30);
        BAR(); put5(30, 36);
        BAR(); issue5(16); put5(36, 41);
        BAR(); issue5(17); put5(41, 45);
        WAIT_V(5); BAR();
        WAIT_V(0); BAR();
        if (layer == 0) { attn_issueB(lds, ws, hp, wid, vb, 0, 0); attn_issueB(lds, ws, hp, wid, vb, 1, 1); }
        BAR();
        if (layer == 0) {
            __builtin_amdgcn_s_setprio(3);
            const unsigned fo = flag_out(F_X2, 512, (rh >> 1) + wid, wid ? 16 + c5 : (rh & 1) * 8 + c5, wid == 0 || (wid == 1 && (rh & 1) && rh < 31) ? lane : 16);
            BARX();
            if (wid < 2) flag_raise(ws, fo);
        }
        TRACE_DUMP;
      }
        if (blockIdx.x == 0 && wid8 == 4) ST8(RSRC(ws), lane_id_fresh() ? 0x80000000u : (unsigned)OFF_EP, 0, ((u32x2){(unsigned)ep, (unsigned)(ep >> 32)}), 16);
        asm volatile("gm_code_end1:");
        return;
    }
#pragma unroll 1
  for (int layer = 0; layer < NET_LAYERS; ++layer) {
    NET_IDS
    TRACE_BEGIN;
    char* wl = ws + OFF_L0 + layer * SZ_L;
    const unsigned fl = layer * F_LAYER;
    const char* xin = wl + L_XB;
    char* y = wl + (layer ? L_Z2T : L_Y);
#undef STAMP_SEC
#define STAMP_SEC 0
    {
    STAMP_BEGIN;
    attn_consumer(lds, x0, wl, layer, pa, hp, wid, [&] { WAIT_V(0); prologue_done(lds, ws, ep, b, lane_id_fresh()); } STAMP_FWD);
    STAMP_END(1 + 5 * layer);
    }
#undef STAMP_SEC
#define STAMP_SEC 1
    STAMP_BEGIN;
    gmlp_body(lds, wid & 1, wid >> 1, pm, cg, wl + (layer ? L_Y : L_Z2T), y, bsp STAMP_FWD);
    {
        const int lane = lane_id_fresh(), r = lane & 15, q = lane >> 4;
        const int wk = wid >> 1, rt = rh * 4 + (wid & 1) * 2 + wk;
        const ProjOut pr = proj_out((char*)xin, (char*)xin, rt, c5, lane);
        const LAS char *pw[3], *ph[3];
#pragma unroll
        for (int i = 0; i < 3; ++i) {
            pw[i] = opaque(lds + 3 * i * K5_STAGE + wk * 4608 + lane * 16);
            ph[i] = opaque(lds + 3 * i * K5_STAGE + wk * 4608 + 4096 + q * 128 + (r & 7) * 16);
        }
        const auto rY = RSRC(y + (size_t)(rt & ~1) * 73728 + wk * 1024);
        const unsigned vy[2] = {pin((unsigned)(wk * 73728 + lane * 16)), pin((unsigned)((1 - wk) * 73728 + lane * 16))};
        int dk[4];
#pragma unroll
        for (int i = 0; i < 4; ++i) dk[i] = pin_s((kord5(4 + i) - 4 - i) * 4096);
        auto yoff = [&](int t) { return pin_s(t == 0 || t == 17 ? kord5(t) * 4096 : t * 4096 + dk[t & 3]); };
        bf16x8 af[5][4], wf[4][5];
        auto lda = [&](int so, int i) { return __builtin_bit_cast(bf16x8, __builtin_amdgcn_raw_buffer_load_b128(rY, vy[i & 1] + (i >> 1) * 2048, so, 16)); };
        auto rdw = [&](int t, int c, int n) {
            const int sl = (t + 6) & 7, o = (sl % 3) * K5_STAGE + c * 9216;
            wf[2 * (t & 1) + c][n] = n < 4 ? LDS_FRAG(pw[sl / 3] + o + n * 1024) : LDS_FRAG(ph[sl / 3] + o);
        };
        int so[18];
#pragma unroll
        for (int i = 0; i < 5; ++i) so[i] = yoff(i);
        so[17] = yoff(17);
        LAS char* xo = (LAS char*)opaque(lds + wid * 4608 + lane * 16);
        LAS char* xo4 = (LAS char*)opaque(lds + wid * 4608 + 4096 + (lane & 31) * 16);
        const LAS char* xi = opaque(lds + (wid ^ 2) * 4608 + lane * 16);
        const LAS char* xi4 = opaque(lds + (wid ^ 2) * 4608 + 4096 + (lane & 31) * 16);
        const ProjOut po = layer ? proj_out_final(out, rt, c5, lane) : proj_out(wl + SZ_L + L_XA, wl + SZ_L + L_STA, rt, c5, lane);
        asm volatile("" :: "s"(rY));
        SCHED0();
        WAIT_V(0);
        BARX();
        STAMP(11);
        const Res rx = proj_residual_load(pr);
#pragma unroll
        for (int n = 0; n < 10; ++n) rdw(0, n / 5, n % 5);
        f32x4 v[5], u[5];
        proj_residual(v, rx);
#pragma unroll
        for (int n = 0; n < 5; ++n) v[n] = pin(v[n]);
        WAIT_L(0);
        BARX();
        STAMP(12);
#pragma unroll
        for (int i = 0; i < K5_PRE; ++i) { af[i >> 2][i & 3] = lda(so[i >> 2], i & 3); SCHED0(); if (i >= PACE0 && i + 1 < K5_PRE) PACE; SCHED0(); }
        static_for<18>([&](auto tc) {
            constexpr int t = tc.value;
            bf16x8 (&a)[4] = af[t % 5];
#pragma unroll
            for (int h = 0; h < 4; ++h) {
                const int c = t < 17 ? h >> 1 : h & 1, m = t < 17 ? h & 1 : 1 - (h >> 1);
                if (t < 17 ? m == 0 : h < 2) { if (t == 17 && h == 1) WAIT_L(0); else WAIT_L(5); }
#pragma unroll
                for (int n = 0; n < 5; ++n) {
                    f32x4& e = m ? u[n] : v[n];
                    if (t == 0 && h == 1) MFMA_0(wf[0][n], a[1], e); else MFMA_T(wf[2 * (t & 1) + c][n], a[2 * c + m], e);
                    SCHED0();
                    if (t < 17 && m == 0) rdw(t + 1, c, n);
                    if (t < 14 && m == 1 && !(n & 1)) { const int i = y_load_at(6 * t + 3 * c + n / 2); if (i >= 0) af[(i >> 2) % 5][i & 3] = lda(so[i >> 2], i & 3); }
                    if (t < 12 && h == 3 && n == 3) so[t + 5] = yoff(t + 5);
                    if (t == 17 && h * 5 + n >= 13 && h * 5 + n < 18) { const int i = h * 5 + n - 13; *(LAS f32x4*)(i < 4 ? xo + i * 1024 : xo4) = u[i]; }
                    SCHED0();
                }
            }
            if ((t & 1) && t < 17) BAR();
            SCHED0();
        });
        {
            WAIT_L0(); BARX();
#pragma unroll
            for (int n = 0; n < 5; ++n) u[n] = *(const LAS f32x4*)(n < 4 ? xi + n * 1024 : xi4);
            SCHED0();
            asm volatile("s_nop 1" : "+v"(v[0]), "+v"(v[1]), "+v"(v[2]), "+v"(v[3]), "+v"(v[4]));
#pragma unroll
            for (int n = 0; n < 5; ++n) v[n] += u[n];
        }
        STAMP(13);
        if (layer) proj_store<true>(v, po);
        else { proj_store(v, po); WAIT_V(0); BARX(); }
    }
    STAMP_END(3 + 5 * layer);
    TRACE_DUMP;
  }
  asm volatile("gm_code_end2:");
}

}

#define STOP_CHECK(n)

extern "C" void kernel_launch(void* const* d_in, const int* in_sizes, int n_in, void* d_out, int out_size, void* d_ws, size_t ws_size,
                              hipStream_t stream) {
    (void)in_sizes; (void)n_in; (void)out_size; (void)ws_size;
    const u16* x = (const u16*)d_in[0];
    const u16* wqkv = (const u16*)d_in[1];
    const u16* wo = (const u16*)d_in[2];
    const u16* w_in = (const u16*)d_in[4];
    const u16* w_sp = (const u16*)d_in[5];
    const u16* b_sp = (const u16*)d_in[6];
    const u16* w_out = (const u16*)d_in[7];
    char* ws = (char*)d_ws;
    (void)hipFuncSetAttribute((const void*)k_net, hipFuncAttributeMaxDynamicSharedMemorySize, NET_LDS);
    k_net<<<256, 512, NET_LDS, stream>>>(ws, w_sp, b_sp, (char*)d_out, (const char*)x, (const char*)wqkv, wo, w_in, w_out DBG_LAUNCH(0));
}
```

```cpp
#include <hip/hip_runtime.h>
#include <stdint.h>

namespace {

typedef unsigned short u16;
typedef short bf16x8 __attribute__((ext_vector_type(8)));
typedef float f32x4 __attribute__((ext_vector_type(4)));
typedef float f32x2 __attribute__((ext_vector_type(2)));
typedef unsigned u32x4 __attribute__((ext_vector_type(4)));
typedef unsigned u32x2 __attribute__((ext_vector_type(2)));
typedef short s16x4 __attribute__((ext_vector_type(4)));
typedef __bf16 bf16x2_t __attribute__((ext_vector_type(2)));
#define LAS __attribute__((address_space(3)))
#define DEVI __device__ __forceinline__

constexpr int S_ = 2048, D_ = 576, FH = 2304;

constexpr size_t SZ_TWQKV = (size_t)16 * 18 * 6 * 1024;
constexpr size_t SZ_TWT = (size_t)48 * 18 * 256;
constexpr size_t SZ_TWO = (size_t)8 * 18 * 4608;
constexpr size_t SZ_TWIN = (size_t)16 * 18 * 18 * 1024;
constexpr size_t SZ_TWOUT = (size_t)8 * 72 * 4608;
constexpr size_t SZ_X = (size_t)S_ * D_ * 2;
constexpr size_t SZ_Z = (size_t)S_ * FH * 2;
constexpr size_t SZ_ST = (size_t)S_ * 64;
constexpr size_t SZ_ZST = (size_t)16 * 16 * 128 * 8;
constexpr size_t OFF_TWQKV = 0;
constexpr size_t OFF_TWT = OFF_TWQKV + SZ_TWQKV;
constexpr size_t OFF_TWO = OFF_TWT + SZ_TWT;
constexpr size_t OFF_TWIN = OFF_TWO + SZ_TWO;
constexpr size_t OFF_TWOUT = OFF_TWIN + SZ_TWIN;
constexpr size_t OFF_CNT = OFF_TWOUT + SZ_TWOUT;
constexpr size_t SZ_CNT = 10 * 65536;
constexpr size_t OFF_FW = OFF_CNT + SZ_CNT;
constexpr size_t OFF_FL = OFF_FW + 262144;
constexpr size_t OFF_EP = OFF_FL + 4096;
constexpr size_t OFF_FL2 = OFF_EP + 4096;
constexpr size_t OFF_ZST = OFF_FL2 + 12288;
constexpr size_t OFF_L0 = OFF_ZST + 2 * SZ_ZST;
constexpr size_t L_XA = 0, L_STA = L_XA + SZ_X, L_O = L_STA + SZ_ST, L_XB = L_O + SZ_X, L_STB = L_XB + SZ_X, L_Z2T = L_STB + SZ_ST, L_Y = L_Z2T + SZ_Z;
constexpr size_t SZ_L = 0;
static_assert(OFF_L0 + L_Y + SZ_Z <= ((size_t)256 << 20), "workspace");
constexpr unsigned F_O = OFF_CNT, F_X1 = F_O + 65536, F_Z2 = F_O + 131072, F_Y = F_O + 196608, F_LAYER = 262144, F_X2 = F_O + 2 * F_LAYER;

DEVI unsigned pack2(float a, float b) { f32x2 v = {a, b}; bf16x2_t r = __builtin_convertvector(v, bf16x2_t); return __builtin_bit_cast(unsigned, r); }
DEVI float bflo(unsigned u) { return __uint_as_float(u << 16); }
DEVI float bfhi(unsigned u) { return __uint_as_float(u & 0xffff0000u); }
DEVI int slot2row(int sg) { return (sg & ~31) + 2 * (sg & 15) + ((sg >> 4) & 1); }
DEVI float fmax2(float a, float b) { return __builtin_amdgcn_fmed3f(a, b, __builtin_inff()); }
#define XQ4(OP, MID) \
    asm volatile("s_nop 1\n\tv_permlane32_swap_b32 %0, %1\n\tv_permlane32_swap_b32 %2, %3\n\t" OP " %0, %0, %1\n\t" OP " %2, %2, %3\n\t" \
                 "s_nop 1\n\tv_permlane16_swap_b32 %0, %2\n\t" OP " %0, %0, %2\n\t" MID \
                 "v_mov_b32 %2, %0\n\ts_nop 1\n\tv_permlane16_swap_b32 %0, %2\n\tv_mov_b32 %1, %0\n\tv_mov_b32 %3, %2\n\ts_nop 0\n\t" \
                 "v_permlane32_swap_b32 %0, %1\n\tv_permlane32_swap_b32 %2, %3" : "+v"(x[0]), "+v"(x[1]), "+v"(x[2]), "+v"(x[3]))
DEVI void xq4_max(float (&x)[4]) { XQ4("v_max_f32", ""); }
DEVI void xq4_sum(float (&x)[4]) { XQ4("v_add_f32", ""); }
DEVI void xq4_sum_rcp(float (&x)[4]) { XQ4("v_add_f32", "v_rcp_f32 %0, %0\n\ts_nop 0\n\t"); }
template <class T> DEVI T pin(T v) { asm volatile("" : "+v"(v)); return v; }
template <int V> struct IC { static constexpr int value = V; };
template <int N, int I = 0, class F> DEVI void static_for(F&& f) { if constexpr (I < N) { f(IC<I>{}); static_for<N, I + 1>(f); } }
template <class T> DEVI T* own_s(T* p) { T* r; asm volatile("s_mov_b64 %0, %1" : "=&s"(r) : "s"(p)); return r; }
DEVI int pin_s(int v) { asm volatile("" : "+s"(v)); return v; }
DEVI float xq_sum(float v) {
    auto a = __builtin_amdgcn_permlane32_swap(__float_as_uint(v), __float_as_uint(v), false, false);
    v = __uint_as_float(a[0]) + __uint_as_float(a[1]);
    auto b = __builtin_amdgcn_permlane16_swap(__float_as_uint(v), __float_as_uint(v), false, false);
    return __uint_as_float(b[0]) + __uint_as_float(b[1]);
}
#define RSRC(p) __builtin_amdgcn_make_buffer_rsrc((void*)(p), 0, 0x7fffffff, 0x00020000)
#define ST16(rs, vo, so, v, aux) __builtin_amdgcn_raw_buffer_store_b128((v), (rs), (vo) + (so), 0, aux)
#define ST8(rs, vo, so, v, aux) __builtin_amdgcn_raw_buffer_store_b64((v), (rs), (vo), (so), aux)
#define WAIT_V(n) asm volatile("s_waitcnt vmcnt(%0)" ::"i"(n) : "memory")
#define WAIT_L0() asm volatile("s_waitcnt lgkmcnt(0)" ::: "memory")
#define BAR() do { asm volatile("" ::: "memory"); __builtin_amdgcn_s_barrier(); asm volatile("" ::: "memory"); } while (0)
#define BARX() do { __builtin_amdgcn_sched_barrier(0); BAR(); __builtin_amdgcn_sched_barrier(0); } while (0)
#define LOOP_ALIGN() asm volatile(".p2align 6")
#define SCHED0() __builtin_amdgcn_sched_barrier(0)
#define MFMA(a, b, c) __builtin_amdgcn_mfma_f32_16x16x32_bf16((a), (b), (c), 0, 0, 0)
#define MFMA_T(a, b, c) asm volatile("v_mfma_f32_16x16x32_bf16 %0, %1, %2, %0" : "+v"(c) : "v"(a), "v"(b))
#define MFMA_0(a, b, c) asm volatile("v_mfma_f32_16x16x32_bf16 %0, %1, %2, 0" : "=&v"(c) : "v"(a), "v"(b))
#define WAIT_L(n) __builtin_amdgcn_s_waitcnt(0xC07F | ((n) << 8))
#define LDS_FRAG(p) (*(const LAS bf16x8*)(p))
DEVI int lane_id_fresh() { int z = 0; asm volatile("" : "+v"(z)); return __builtin_amdgcn_mbcnt_hi(~0u, __builtin_amdgcn_mbcnt_lo(~0u, z)); }
DEVI const LAS char* opaque(const LAS char* p) { asm volatile("" : "+v"(p)); return p; }
#define DBG_ARG
#define DBG_LAUNCH(n)
#define STAMP(i)
#define STAMP_DECL
#define STAMP_BEGIN
#define STAMP_END(n)
#define STAMP_PARAM
#define STAMP_FWD

#define PST_PARAM
#define PST_FWD
#define PST_DECL
#define PST(i)
#define PST_DUMP(c, k)
#define TRACE_ATTR
#define TRACE_BEGIN
#define TRACE_DUMP

DEVI float add1(float a, float b) { float r; asm("v_add_f32 %0, %1, %2" : "=v"(r) : "v"(a), "v"(b)); return r; }
DEVI float mul1(float a, float b) { float r; asm("v_mul_f32 %0, %1, %2" : "=v"(r) : "v"(a), "v"(b)); return r; }
DEVI float rstd_of(float s, float q, float inv_n, float& mean) {
    float var;
    asm("v_mul_f32 %0, %2, %3\n\tv_mul_f32 %1, %2, %4\n\tv_fma_f32 %1, -%0, %0, %1" : "=&v"(mean), "=&v"(var) : "s"(inv_n), "v"(s), "v"(q));
    return __builtin_amdgcn_rsqf(fmax2(var, 0.f) + 1e-5f);
}
DEVI float ln_rstd(const f32x4 p) {
    float s, q, mean;
    asm("v_add_f32 %0, %2, %4\n\tv_add_f32 %1, %3, %5\n\ts_nop 0\n\t"
        "v_add_f32_dpp %0, %0, %0 quad_perm:[1,0,3,2] row_mask:0xf bank_mask:0xf\n\tv_add_f32_dpp %1, %1, %1 quad_perm:[1,0,3,2] row_mask:0xf bank_mask:0xf\n\ts_nop 0\n\t"
        "v_add_f32_dpp %0, %0, %0 quad_perm:[2,3,0,1] row_mask:0xf bank_mask:0xf\n\tv_add_f32_dpp %1, %1, %1 quad_perm:[2,3,0,1] row_mask:0xf bank_mask:0xf"
        : "=&v"(s), "=&v"(q) : "v"(p[0]), "v"(p[1]), "v"(p[2]), "v"(p[3]));
    return rstd_of(s, q, 1.0f / 576.0f, mean);
}

struct Res { u32x4 a, c; u32x2 e; };
DEVI void proj_residual(f32x4 (&v)[5], const Res& x) {
    v[0] = (f32x4){bflo(x.a.x), bfhi(x.a.x), bflo(x.a.y), bfhi(x.a.y)}; v[1] = (f32x4){bflo(x.a.z), bfhi(x.a.z), bflo(x.a.w), bfhi(x.a.w)};
    v[2] = (f32x4){bflo(x.c.x), bfhi(x.c.x), bflo(x.c.y), bfhi(x.c.y)}; v[3] = (f32x4){bflo(x.c.z), bfhi(x.c.z), bflo(x.c.w), bfhi(x.c.w)};
    v[4] = (f32x4){bflo(x.e.x), bfhi(x.e.x), bflo(x.e.y), bfhi(x.e.y)};
}
struct ProjOut { __amdgpu_buffer_rsrc_t x, st; unsigned v0, v1, v2, vs; };
DEVI ProjOut proj_out(char* xout, char* stout, int rt, int cg, int lane) {
    const int r = lane & 15, q = lane >> 4;
    ProjOut p = {RSRC(xout + (size_t)rt * 18432 + cg * 2304), RSRC(stout + (size_t)rt * 1024 + cg * 8),
                 pin((unsigned)lane * 16u), pin((unsigned)lane * 16u + 1024u), pin((unsigned)(2048 + r * 16 + (q & 1) * 8)), pin((unsigned)(r * 64 + (q >> 1) * 4))};
    asm volatile("" :: "s"(p.x), "s"(p.st));
    return p;
}
DEVI ProjOut proj_out_final(char* out, int rt, int cg, int lane) {
    const int q = lane >> 4;
    const unsigned o = (unsigned)(slot2row(rt * 16 + (lane & 15)) * 1152 + cg * 144);
    ProjOut p = {RSRC(out), RSRC(out), pin(o + q * 16), pin(o + 64 + q * 16), pin(o + 128 + (q & 1) * 8), 0u};
    return p;
}
DEVI Res proj_residual_load(const ProjOut& p) {
    return {__builtin_amdgcn_raw_buffer_load_b128(p.x, p.v0, 0, 16), __builtin_amdgcn_raw_buffer_load_b128(p.x, p.v1, 0, 16), __builtin_amdgcn_raw_buffer_load_b64(p.x, p.v2, 0, 16)};
}
template <bool FINAL = false>
DEVI void proj_store(f32x4 (&v)[5], const ProjOut& p) {
    ST16(p.x, p.v0, 0, ((u32x4){pack2(v[0][0], v[0][1]), pack2(v[0][2], v[0][3]), pack2(v[1][0], v[1][1]), pack2(v[1][2], v[1][3])}), FINAL ? 0 : 16);
    SCHED0();
    asm volatile("" : "+v"(v[2]), "+v"(v[3]), "+v"(v[4]));
    ST16(p.x, p.v1, 0, ((u32x4){pack2(v[2][0], v[2][1]), pack2(v[2][2], v[2][3]), pack2(v[3][0], v[3][1]), pack2(v[3][2], v[3][3])}), FINAL ? 0 : 16);
    SCHED0();
    ST8(p.x, p.v2, 0, ((u32x2){pack2(v[4][0], v[4][1]), pack2(v[4][2], v[4][3])}), FINAL ? 0 : 16);
    SCHED0();
    if (!FINAL) {
        const f32x4 s4 = ((v[0] + v[1]) + (v[2] + v[3])) + v[4] * 0.5f;
        const f32x4 q4 = ((v[0] * v[0] + v[1] * v[1]) + (v[2] * v[2] + v[3] * v[3])) + v[4] * v[4] * 0.5f;
        auto x = __builtin_amdgcn_permlane32_swap(__float_as_uint((s4[0] + s4[2]) + (s4[1] + s4[3])), __float_as_uint((q4[0] + q4[2]) + (q4[1] + q4[3])), false, false);
        const float t = __uint_as_float(x[0]) + __uint_as_float(x[1]);
        auto y = __builtin_amdgcn_permlane16_swap(__float_as_uint(t), __float_as_uint(t), false, false);
        __builtin_amdgcn_raw_buffer_store_b32(__float_as_uint(__uint_as_float(y[0]) + __uint_as_float(y[1])), p.st, p.vs, 0, 16);
        SCHED0();
    }
}

DEVI void side_load(f32x4 (&d)[3], const char* p, const char* p2) {
    asm volatile("global_load_dwordx4 %0, %3, off sc1\n\tglobal_load_dwordx4 %1, %3, off offset:1024 sc1\n\tglobal_load_dwordx4 %2, %4, off sc1"
                 : "=&v"(d[0]), "=&v"(d[1]), "=&v"(d[2]) : "v"(p), "v"(p2) : "memory");
}
DEVI void side_fence(f32x4 (&d)[3]) { asm volatile("" : "+v"(d[0]), "+v"(d[1]), "+v"(d[2])); }
DEVI void lds_word_set(LAS char* p, unsigned v) { asm volatile("ds_write_b32 %0, %1" :: "v"((unsigned)(size_t)p), "v"(v) : "memory"); }
#define DMA_I(v, off, sc) "buffer_load_dwordx4 " v ", %1, %2 offen offset:" off sc " lds\n\t"
#define DMA5(sc) asm volatile("s_mov_b32 m0, %0\n\ts_mov_b32 exec_hi, 0\n\ts_nop 2\n\t" DMA_I("%3", "0", sc) "s_mov_b32 exec_hi, -1\n\t" \
    DMA_I("%3", "512", sc) DMA_I("%3", "1536", sc) DMA_I("%3", "2560", sc) DMA_I("%3", "3584", sc) :: "s"(la), "s"(rs), "s"(so), "v"(vo) : "memory")
#define DMA4(sc) asm volatile("s_mov_b32 m0, %0\n\ts_nop 3\n\t" DMA_I("%3", "0", sc) DMA_I("%4", "1024", sc) DMA_I("%5", "2048", sc) DMA_I("%6", "3072", sc) \
    :: "s"(la), "s"(rs), "s"(so), "v"(v0), "v"(v1), "v"(v2), "v"(v3) : "memory")
template <bool SC1> DEVI void dma5(unsigned la, __amdgpu_buffer_rsrc_t rs, int so, unsigned vo) { if (SC1) DMA5(" sc1"); else DMA5(""); }
template <bool SC1> DEVI void dma4(unsigned la, __amdgpu_buffer_rsrc_t rs, int so, unsigned v0, unsigned v1, unsigned v2, unsigned v3) { if (SC1) DMA4(" sc1"); else DMA4(""); }
#define DMA2(sc) asm volatile("s_mov_b32 m0, %0\n\ts_nop 3\n\t" DMA_I("%3", "0", sc) DMA_I("%4", "1024", sc) :: "s"(la), "s"(rs), "s"(so), "v"(v0), "v"(v1) : "memory")
template <bool SC1> DEVI void dma2(unsigned la, __amdgpu_buffer_rsrc_t rs, int so, unsigned v0, unsigned v1) { if (SC1) DMA2(" sc1"); else DMA2(""); }
DEVI void dma1(unsigned la, __amdgpu_buffer_rsrc_t rs, int so, unsigned v0) { asm volatile("s_mov_b32 m0, %0\n\ts_nop 3\n\t" DMA_I("%3", "0", "") :: "s"(la), "s"(rs), "s"(so), "v"(v0) : "memory"); }
#define WAIT_IF(c, a, b) do { if (c) WAIT_V(a); else WAIT_V(b); } while (0)
template <int T0, int T1, int P, int S0 = 0, class ISSUE>
DEVI void ring_steps(ISSUE&& issue) {
    int slot = (T0 + S0) % 3;
    LOOP_ALIGN();
#pragma unroll 1
    for (int t = T0; t < T1; ++t) {
        WAIT_V(P); BAR();
        issue(t + 3, slot);
        slot = slot == 2 ? 0 : slot + 1;
    }
}

DEVI unsigned flag_out(unsigned use, int pitch, int group, int writer, int lane) { return pin(lane < 16 ? use + (group * 16 + lane) * pitch + writer * 16 : 0x80000000u); }
DEVI unsigned flag_in(unsigned use, int pitch, int reader, int n, int lane) { return pin(use + reader * pitch + (lane < n ? lane : 0) * 16); }
DEVI void flag_raise(const char* ws, unsigned vo) { ST16(RSRC(ws), vo, 0, ((u32x4){1u, 1u, 1u, 1u}), 16); SCHED0(); }
template <int FIRST, int AGAIN = 1>
DEVI void flags_wait(const char* ws, unsigned vo) {
    unsigned v, n = 400000u;
    asm volatile("s_nop 4\n\ts_sleep %4\n"
                 "1:\n\tbuffer_load_dword %0, %2, %3, 0 offen sc1\n\ts_waitcnt vmcnt(0)\n\tv_cmp_eq_u32 vcc, 0, %0\n\ts_cbranch_vccz 2f\n\t"
                 "s_sleep %5\n\ts_sub_u32 %1, %1, 1\n\ts_cbranch_scc0 1b\n2:"
                 : "=&v"(v), "+s"(n) : "v"(vo), "s"(RSRC(ws)), "i"(FIRST), "i"(AGAIN) : "vcc", "scc", "memory");
    SCHED0();
}
template <int FIRST>
DEVI void epoch_wait(const char* ws, unsigned vo, unsigned long long ep) {
    unsigned long long v; unsigned n = 400000u;
    asm volatile("s_nop 4\n\ts_sleep %5\n"
                 "1:\n\tbuffer_load_dwordx2 %0, %2, %3, 0 offen sc1\n\ts_waitcnt vmcnt(0)\n\tv_cmp_ne_u64 vcc, %4, %0\n\ts_cbranch_vccz 2f\n\t"
                 "s_sleep 1\n\ts_sub_u32 %1, %1, 1\n\ts_cbranch_scc0 1b\n2:"
                 : "=&v"(v), "+s"(n) : "v"(vo), "s"(RSRC(ws)), "s"(ep), "i"(FIRST) : "vcc", "scc", "memory");
    SCHED0();
}
DEVI unsigned lds_arrive(LAS char* p) {
    unsigned v;
    asm volatile("s_mov_b64 exec, 1\n\tds_add_rtn_u32 %0, %1, %2\n\ts_mov_b64 exec, -1\n\ts_waitcnt lgkmcnt(0)" : "=&v"(v) : "v"((unsigned)(size_t)p), "v"(1u) : "memory");
    return __builtin_amdgcn_readfirstlane(v);
}
DEVI void lds_count_wait(LAS char* p, unsigned n) {
#pragma unroll 1
    for (unsigned spin = 0; spin < 40000000u; ++spin) {
        unsigned v;
        asm volatile("ds_read_b32 %0, %1\n\ts_waitcnt lgkmcnt(0)" : "=v"(v) : "v"((unsigned)(size_t)p) : "memory");
        if (__builtin_amdgcn_readfirstlane(v) >= n) break;
    }
}

constexpr int K1_STAGE = 32768;
constexpr int WO_OFF = 69632;
constexpr int G_OWN = 139264;
constexpr int G_PART = G_OWN + 18432;
constexpr int G_TAB = G_PART + 2048;
constexpr int K1_SCR = G_TAB + 1536;
constexpr int G_WORD = K1_SCR + 576;
constexpr int G_JUNK = G_WORD + 64;
constexpr int NET_LDS = G_JUNK + 1024;
DEVI __amdgpu_buffer_rsrc_t code_rsrc() {
    unsigned long long pc; int d0, len;
    asm volatile("s_getpc_b64 %0\n1:\n\ts_mov_b32 %1, gm_code_begin-1b\n\ts_mov_b32 %2, gm_code_end1-gm_code_begin\n\ts_max_i32 %2, %2, gm_code_end2-gm_code_begin"
                 : "=s"(pc), "=s"(d0), "=s"(len) :: "scc");
    return __builtin_amdgcn_make_buffer_rsrc((void*)((pc + d0) & ~4095ull), 0, len + ((unsigned)(pc + d0) & 4095), 0x00020000);
}

#define WO_PRE 9
#define WO_W 33
#define REL_PRIO 0
#define W_FIRST 2
#define I_SLEEP 30
#define OJOB_SLEEP 20
#define PACE0 1
#define PACE __builtin_amdgcn_s_sleep(1)
#define POLLW 0
#define WO_EV 4
#define WIN_EARLY 0
#define WIN_LOOK 40
#define W3_SLEEP 0
#define RSTD_PRIO 3
#define B01_POS 1
#define O_FIRST 8
#define X1_FIRST 4
#define X2_FIRST 4
#define Y_FIRST 14
#define Z2_FIRST 10
DEVI int win_strip(int b) { return ((b - 112) & 7) * 18 + ((b - 112) >> 3); }
DEVI int win_third(int b, int w) { return b & 8 ? 3 - w : w - 1; }
DEVI unsigned attn_vb(int wid, int lane) {
    const int c = lane & 15;
    return pin((unsigned)(wid < 3 ? lane * 16 : (c < 8 ? (c >> 1) & 1 : 2) * 4608 + (lane >> 4) * 64 + (c < 8 ? (c >> 2) * 2 + (c & 1) : c & 3) * 16));
}
DEVI void attn_issueB(LAS char* lds, const char* ws, int hp, int wid, unsigned vb, int s, int slot) {
    const unsigned la = (unsigned)(size_t)lds + slot * K1_STAGE + 18432 + wid * 4096;
    if (wid < 3) dma4<true>(la, RSRC(ws), (int)OFF_TWQKV + hp * (18 * 6144) + wid * 4096 + s * 12288, vb, vb, vb, vb);
    else asm volatile("s_mov_b32 m0, %0\n\ts_nop 3\n\tbuffer_load_dwordx4 %4, %1, %2 offen sc1 lds\n\tbuffer_load_dwordx4 %4, %1, %3 offen offset:1024 sc1 lds"
                      :: "s"(la), "s"(RSRC(ws)), "s"((int)OFF_TWT + hp * 13824 + s * 512), "s"((int)OFF_TWT + hp * 13824 + s * 512 + 256 - 1024), "v"(vb) : "memory");
}
struct AttnX { __amdgpu_buffer_rsrc_t rs; unsigned v[4], vh[2]; int oA, oh, step; };
DEVI AttnX attn_x(const char* x0, const char* xa, int layer, int pm, int wid, int lane) {
    const int l3 = lane >> 3, pmh = pm > 0 ? pm : 1;
    const unsigned v = layer ? lane * 16 : 2 * l3 * 1152 + ((lane & 7) ^ l3) * 16 + 4096, d1 = layer ? 0 : 16 * 1152 - 1024, d2 = layer ? 16384 : 1152 - 2048;
    const unsigned vh = layer ? (l3 & 1) * 18432 + (lane >> 4) * 256 + (8 + (lane & 7)) * 16 : v + 16 * 1152;
    return {RSRC(layer ? xa : x0 - 4096), {pin(v), pin(v + d1), pin(v + d2), pin(v + d1 + d2)}, {pin(vh), pin(vh + (layer ? 0 : 1152 - 1024))},
            (pm * 8 + 2 * wid) * 18432, (pmh * 8 - 2) * 18432, layer ? 2048 : 128};
}
DEVI void attn_issueA(LAS char* lds, const AttnX& a, int wid, int s, int slot) {
    const unsigned la = (unsigned)(size_t)lds + slot * K1_STAGE;
    if (wid == 3) dma2<true>(la, a.rs, a.oh + s * a.step, a.vh[0], a.vh[1]);
    dma4<true>(la + 2048 + wid * 4096, a.rs, a.oA + s * a.step, a.v[0], a.v[1], a.v[2], a.v[3]);
}
template <class DONE, class NEXT>
DEVI void attn_producer(LAS char* lds, const char* ws, const char* x0, const char* wl, unsigned fl, unsigned long long ep, int layer, int pm, int hp, int wid, DONE&& done, NEXT&& next) {
    const int lane = lane_id_fresh();
    const int pmh = pm > 0 ? pm : 1;
    const unsigned vo = lane * 16;
    const AttnX ax = attn_x(x0, wl + L_XA, layer, pm, wid, lane);
    const unsigned vb = attn_vb(wid, lane), fd = pin((unsigned)OFF_FL + wid * 1024 + vo);
    auto issueB = [&](int s, int slot) { attn_issueB(lds, ws, hp, wid, vb, s, slot); };
    auto issue = [&](int s, int slot) { attn_issueA(lds, ax, wid, s, slot); issueB(s, slot); };
    const int hr = lane >> 2;
    const char* ps = pin(wl + L_STA + (size_t)(pm * 128 + wid * 32) * 64 + lane * 16);
    const char* psh = pin(wl + L_STA + (size_t)((pmh * 8 - 2 + (hr >> 3)) * 16 + 8 + (hr & 7)) * 64 + (lane & 3) * 16);
    LAS char *tr = (LAS char*)opaque(lds + K1_SCR + (16 + wid * 32 + hr) * 4), *th = (LAS char*)opaque(lds + K1_SCR + hr * 4);
    if (layer == 0) {
        const unsigned fi = pin((unsigned)OFF_FW + (pm * 16 + hp) * 1024 + (lane < 6 ? lane : 0) * 128);
        done();
        if (wid == 0) { epoch_wait<W_FIRST>(ws, fi, ep); issueB(0, 0); }
        BARX();
        if (wid) issueB(0, 0);
        issueB(1, 1); issue(2, 2);
        WAIT_IF(wid == 3, 10, 12);
    } else {
        issueB(2, 2);
        const unsigned fi = flag_in(F_X2, 512, pm * 16 + hp, pm > 0 ? 24 : 16, lane);
        if (wid == 0) {
            flags_wait<X2_FIRST>(ws, fi);
            AttnX a0 = ax;
#pragma unroll
            for (int i = 0; i < 4; ++i) { attn_issueA(lds, a0, i, 0, 0); a0.oA += 36864; }
        }
        BARX();
        attn_issueA(lds, ax, wid, 1, 1); attn_issueA(lds, ax, wid, 2, 2);
        WAIT_IF(wid == 3, 12, 8);
    }
    __builtin_amdgcn_s_setprio(0);
    BAR();
    if (layer) WAIT_IF(wid == 3, 6, 4);
    next(-1);
    ring_steps<0, 4, 8>(issue);
    {
        unsigned long long done;
        if (layer == 0) asm volatile("s_nop 4\n\tbuffer_load_dwordx2 %0, %1, %2, 0 offen sc1" : "+v"(done) : "v"(fd), "s"(RSRC(ws)) : "memory");
        WAIT_IF(layer, 8, 9); BAR(); issue(7, 1);
        WAIT_V(8);
        if (layer == 0) { asm volatile("" : "+v"(done)); if (__any(done != ep)) epoch_wait<0>(ws, fd, ep); }
        BAR(); issue(8, 2);
    }
    const auto rW = RSRC(ws + OFF_TWO + (size_t)(hp >> 1) * 82944 + wid * 4096);
    auto issueW = [&](int i) { dma4<true>((unsigned)(size_t)lds + WO_OFF + (i * 4 + wid) * 4096, rW, i * 16384, vo, vo, vo, vo); };
    {
        f32x4 sp[3];
        side_load(sp, ps, psh);
        WAIT_V(11); BAR(); issueW(2); issueW(3);
        WAIT_V(8); BARX();
        __builtin_amdgcn_s_setprio(RSTD_PRIO);
        side_fence(sp);
#pragma unroll
        for (int i = 0; i < 3; ++i) lds_word_set(i < 2 ? tr + i * 64 : th, __float_as_uint(ln_rstd(sp[i])));
        __builtin_amdgcn_s_setprio(0);
        issueW(4);
        if (wid == 0) dma2<true>((unsigned)(size_t)lds + WO_OFF + 80896, rW, 80896, vo, vo);
        WAIT_L0();
    }
    BAR();
    next(0);
    issueW(0); issueW(1);
    next(1);
    const unsigned fo = flag_out(fl + F_O, 256, pm, hp, lane), fi = flag_in(fl + F_O, 256, pm * 16 + hp, 16, lane);
    WAIT_V(0);
    if (wid == POLLW) __builtin_amdgcn_s_setprio(3);
    BARX();
    if (wid == POLLW) flag_raise(ws, fo);
    next(2);
    if (wid == POLLW) { if (layer == 0 || B01_POS != 2) __builtin_amdgcn_s_sleep(O_FIRST); flags_wait<0>(ws, fi); }
    __builtin_amdgcn_s_setprio(0);
    BARX();
}

#define STAMP_SEC 0
template <class DONE>
DEVI void attn_consumer(LAS char* lds, const char* x0, char* wl, int layer, int pm, int hp, int wid, DONE&& done STAMP_PARAM) {
    const int lane = lane_id_fresh(), r = lane & 15, q = lane >> 4;
    const char* xa = wl + L_XA;
    char* o = wl + L_O;
    const int T = wid & 1, h = wid >> 1;
    const LAS char *ph[2], *pa[2];
#pragma unroll
    for (int kk = 0; kk < 2; ++kk) {
        const int sw = ((4 * kk + q) ^ (r & 7)) * 16;
        ph[kk] = opaque(lds + (layer ? kk * 1024 + q * 256 + (r ^ (T ? 0 : 8)) * 16 : (r ^ (T ? 0 : 8)) * 128 + sw));
        pa[kk] = opaque(lds + T * 2048 + (layer ? kk * 1024 + lane * 16 : r * 128 + sw));
    }
    const LAS char* pb = lds + h * 3072 + lane * 16;
    const LAS char* pt = lds + q * 256 + (r ^ (r < 8 ? 4 * h : 2 * h)) * 16;
    f32x4 acc[5][4];
#pragma unroll
    for (int m = 0; m < 5; ++m)
#pragma unroll
        for (int n = 0; n < 4; ++n) if (m + n) acc[m][n] = pin((f32x4){0.f, 0.f, 0.f, 0.f});
    bf16x8 fa[2][5], fb[2][4];
    auto rd1 = [&](int kk, int i) {
        if (i < 3) fb[kk][i] = LDS_FRAG(pb + 18432 + kk * 6144 + i * 1024);
        else if (i == 3) fb[kk][3] = LDS_FRAG(pt + 30720 + kk * 1024);
        else if (i == 4) fa[kk][0] = LDS_FRAG(ph[kk]);
        else if (i < 9) fa[kk][i - 4] = LDS_FRAG(pa[kk] + 2048 + (i - 5) * 4096);
    };
    f32x4 mb, mb0;
#pragma unroll
    for (int j = 0; j < 4; ++j) {
        const bool ok = ((r - 4 * q - j) & 15) < 8;
        mb[j] = ok ? 0.f : -__builtin_inff();
        mb0[j] = (ok && (pm > 0 || 4 * q + j <= r)) ? 0.f : -__builtin_inff();
    }
    mb = pin(mb); mb0 = pin(mb0);
    const unsigned mp0 = pin((4 * q > r ? 0xffffu : 0u) | (4 * q + 1 > r ? 0xffff0000u : 0u)), mp1 = pin((4 * q + 2 > r ? 0xffffu : 0u) | (4 * q + 3 > r ? 0xffff0000u : 0u));
    const unsigned qtm = pin(q == 0 ? 0xffffffffu : 0u);
    const unsigned vom = pin((unsigned)(r * 16 + hp * 1024 + (2 * h + (q >> 1)) * 256 + (q & 1) * 36864));
    const unsigned vot = pin((unsigned)(r * 16 + 16384 + (hp >> 1) * 256 + (hp & 1) * 8 + h * 4 + q * 36864));
    LAS char* xw = (LAS char*)opaque(lds + (q == 2 ? wid * 640 : G_JUNK) + r * 8);
    const LAS char* xr = opaque(lds + wid * 640 + (q * 4 + (r >> 2)) * 8);
    const LAS char* tk0 = opaque(lds + K1_SCR + ((q * 4) ^ (T ? 0 : 8)) * 4);
    const LAS char* tk = opaque(lds + K1_SCR + ((1 + T) * 16 + q * 4) * 4);
    const LAS char* tq = opaque(lds + K1_SCR + ((1 + T) * 16 + r) * 4);
    pb = opaque(pb); pt = opaque(pt);
    STAMP(2);
    const int rt = pm * 8 + (hp & 1) * 4 + wid, cg = hp >> 1;
    const ProjOut pr = layer ? proj_out((char*)xa, (char*)xa, rt, cg, lane) : proj_out_final((char*)x0, rt, cg, lane);
    BAR();
    if (layer == 0) done();
    BARX();
    STAMP(3);
#pragma unroll
    for (int i = 0; i < 9; ++i) rd1(0, i);
    SCHED0();
    const Res rx = proj_residual_load(pr);
    SCHED0();
    int slot = 0, left = 8;
    LOOP_ALIGN();
#pragma unroll 1
    do {
        int d;
#pragma unroll
        for (int kk = 0; kk < 2; ++kk) {
            WAIT_L(0);
            if (kk) BAR();
            SCHED0();
#pragma unroll
            for (int i = 1; i < 20; ++i) {
                const int m = i >> 2, n = i & 3;
                const bf16x8 &A = fa[kk][m], &B = fb[kk][n];
                acc[m][n] = n != 2 ? MFMA(B, A, acc[m][n]) : MFMA(A, B, acc[m][n]);
                SCHED0();
                if (i <= 9) rd1(kk ^ 1, i - 1);
                if (kk == 0) {
                    if (i == 10) d = pin_s(slot == 2 ? -2 * K1_STAGE : K1_STAGE);
                    if (i == 11 || i == 12) ph[i & 1] = opaque(ph[i & 1] + d);
                    if (i == 13 || i == 14) pa[i & 1] = opaque(pa[i & 1] + d);
                    if (i == 15) pb = opaque(pb + d);
                    if (i == 16) pt = opaque(pt + d);
                } else {
                    if (i == 11) slot = pin_s(slot == 2 ? 0 : slot + 1);
                    if (i == 13) left = pin_s(left - 1);
                }
                SCHED0();
            }
        }
    } while (left);
    {
        typedef unsigned u32x10 __attribute__((ext_vector_type(10)));
        f32x4 rk[5], rs[4], ks[4]; float rq[4];
        u32x2 ts[5];
        u32x4 kf[5], qf[4];
        u32x10 tf, vf;
        WAIT_L(0);
        SCHED0();
        static_for<38>([&](auto ic) {
            constexpr int i = ic.value, kk = i < 14 ? 0 : i < 28 ? 1 : i >= 33, t = i % 14, n = i >= 28 ? 2 : t < 5 ? 3 : t < 10 ? 1 : 0, m = i >= 28 ? (i - 28) % 5 : t < 10 ? t % 5 : t - 9;
            if (i == 14) { WAIT_L(0); BAR(); SCHED0(); }
            acc[m][n] = n != 2 ? MFMA(fb[kk][n], fa[kk][m], acc[m][n]) : MFMA(fa[kk][m], fb[kk][n], acc[m][n]);
            SCHED0();
            if (i < 9) rd1(1, i);
            if (i == 14) { rk[0] = *(const LAS f32x4*)(tk0); rk[1] = *(const LAS f32x4*)(tk); }
            if (i == 15) { rk[2] = *(const LAS f32x4*)(tk + 128); rk[3] = *(const LAS f32x4*)(tk + 256); }
            if (i == 16) {
                rk[4] = *(const LAS f32x4*)(tk + 384);
#pragma unroll
                for (int g = 0; g < 4; ++g) rq[g] = *(const LAS float*)(tq + g * 128);
            }
            if constexpr (i >= 19 && i < 24) {
                ts[i - 19] = (u32x2){pack2(acc[i - 19][3][0], acc[i - 19][3][1]), pack2(acc[i - 19][3][2], acc[i - 19][3][3])};
                *(LAS u32x2*)(xw + (i - 19) * 128) = ts[i - 19];
            }
            if constexpr (i >= 24 && i < 29) kf[i - 24] = pin((u32x4){pack2(acc[i - 24][1][0], acc[i - 24][1][1]), pack2(acc[i - 24][1][2], acc[i - 24][1][3]), ts[i - 24].x, ts[i - 24].y});
            if constexpr (i >= 29 && i < 33) qf[i - 29] = pin((u32x4){pack2(acc[i - 28][0][0], acc[i - 28][0][1]), pack2(acc[i - 28][0][2], acc[i - 28][0][3]), pin(0u), kf[i - 28].z & qtm});
            if (i == 33) {
#pragma unroll
                for (int e = 0; e < 5; ++e) {
                    const u32x2 w = __builtin_bit_cast(u32x2, __builtin_amdgcn_ds_read_tr16_b64_v4i16((LAS s16x4*)(xr + e * 128)));
                    tf[2 * e] = w.x; tf[2 * e + 1] = w.y;
                }
            }
            if constexpr (i >= 34) {
#pragma unroll
                for (int j = 0; j < 4; ++j) rs[i - 34][j] = 4 * q + j > r ? rk[i - 34][j] : rk[i - 33][j];
                rs[i - 34] = pin(rs[i - 34]);
            }
            SCHED0();
        });
        STAMP(4);
        const f32x4 zero = {0.f, 0.f, 0.f, 0.f};
        f32x4 sp[4], sc[4];
#pragma unroll
        for (int g = 0; g < 4; ++g) {
            sp[g] = MFMA(__builtin_bit_cast(bf16x8, kf[g]), __builtin_bit_cast(bf16x8, qf[g]), zero);
            sc[g] = MFMA(__builtin_bit_cast(bf16x8, kf[g + 1]), __builtin_bit_cast(bf16x8, qf[g]), zero);
            const float c = rq[g] * (0.23570226039551584f * 1.4426950408889634f);
#pragma unroll
            for (int j = 0; j < 4; ++j) ks[g][j] = mul1(rs[g][j], c);
            SCHED0();
        }
#pragma unroll
        for (int m = 0; m < 5; ++m) { vf[2 * m] = pack2(acc[m][2][0], acc[m][2][1]); vf[2 * m + 1] = pack2(acc[m][2][2], acc[m][2][3]); }
        vf = pin(vf); tf = pin(tf);
        f32x4 t[4]; float mx[4], sm[4];
#pragma unroll
        for (int g = 0; g < 4; ++g) {
#pragma unroll
            for (int j = 0; j < 4; ++j) t[g][j] = 4 * q + j > r ? sp[g][j] : sc[g][j];
            t[g] = t[g] * ks[g] + (g == 0 ? mb0 : mb);
            mx[g] = fmaxf(fmaxf(t[g][0], t[g][1]), fmaxf(t[g][2], t[g][3]));
        }
        xq4_max(mx);
#pragma unroll
        for (int g = 0; g < 4; ++g) {
            t[g] = t[g] - mx[g];
#pragma unroll
            for (int j = 0; j < 4; ++j) t[g][j] = __builtin_amdgcn_exp2f(t[g][j]);
            sm[g] = (t[g][0] + t[g][2]) + (t[g][1] + t[g][3]);
        }
        f32x4 om[4], ot[4];
        static_for<4>([&](auto gc) {
            constexpr int g = gc.value;
            const f32x4 p = t[g] * rs[g];
            const unsigned p01 = pack2(p[0], p[1]), p23 = pack2(p[2], p[3]);
            const u32x4 pf = {p01 & mp0, p23 & mp1, p01 & ~mp0, p23 & ~mp1};
            om[g] = MFMA(__builtin_bit_cast(bf16x8, (u32x4)__builtin_shufflevector(vf, vf, 2 * g, 2 * g + 1, 2 * g + 2, 2 * g + 3)), __builtin_bit_cast(bf16x8, pf), zero);
            ot[g] = MFMA(__builtin_bit_cast(bf16x8, (u32x4)__builtin_shufflevector(tf, tf, 2 * g, 2 * g + 1, 2 * g + 2, 2 * g + 3)), __builtin_bit_cast(bf16x8, pf), zero);
        });
        xq4_sum_rcp(sm);
        const auto ro = RSRC(o + (size_t)(pm * 8 + T) * 18432);
#pragma unroll
        for (int g = 0; g < 4; g += 2) {
            const f32x4 a = om[g] * sm[g], c = om[g + 1] * sm[g + 1];
            const auto x = __builtin_amdgcn_permlane16_swap(pack2(a[0], a[1]), pack2(c[0], c[1]), false, false);
            const auto y = __builtin_amdgcn_permlane16_swap(pack2(a[2], a[3]), pack2(c[2], c[3]), false, false);
            ST16(ro, vom, g * 36864, ((u32x4){x[0], y[0], x[1], y[1]}), 16);
        }
        unsigned tw[4];
#pragma unroll
        for (int g = 0; g < 4; ++g) { const f32x2 w = ot[g].xy * sm[g]; tw[g] = pack2(w.x, w.y); }
        __builtin_amdgcn_raw_buffer_store_b32(q < 2 ? (q ? tw[1] : tw[0]) : (q == 3 ? tw[3] : tw[2]), ro, vot, 0, 16);
    }
    STAMP(5);
    {
        WAIT_V(0); BARX();
        STAMP(6);
        f32x4 v[5];
        proj_residual(v, rx);
        const LAS char* pw = opaque(lds + WO_OFF + lane * 16);
        const LAS char* pw2 = opaque(lds + WO_OFF + 41472 + lane * 16);
        const LAS char* ph2 = opaque(lds + WO_OFF + 4096 + q * 128 + (r & 7) * 16);
        const LAS char* ph3 = opaque(lds + WO_OFF + 41472 + 4096 + q * 128 + (r & 7) * 16);
        bf16x8 wf[90];
        auto rdw = [&](int i) {
            const int kc = i / 5, n = i % 5;
            wf[i] = n < 4 ? LDS_FRAG((kc < 9 ? pw : pw2) + (kc % 9) * 4608 + n * 1024) : LDS_FRAG((kc < 9 ? ph2 : ph3) + (kc % 9) * 4608);
        };
#pragma unroll
        for (int i = 0; i < WO_W; ++i) rdw(i);
#pragma unroll
        for (int n = 0; n < 5; ++n) v[n] = pin(v[n]);
        const auto rO = RSRC(o + (size_t)rt * 18432);
        const ProjOut po = proj_out(wl + L_XB, wl + L_STB, rt, cg, lane);
        asm volatile("" :: "s"(rO));
        WAIT_L(0);
        __builtin_amdgcn_s_setprio(REL_PRIO);
        BARX();
        STAMP(7);
        bf16x8 af[18];
        auto ldo = [&](int kc) { af[kc] = __builtin_bit_cast(bf16x8, __builtin_amdgcn_raw_buffer_load_b128(rO, lane * 16 + (kc & 3) * 1024, (kc >> 2) * 4096, 16)); };
#pragma unroll
        for (int kc = 0; kc < WO_PRE; ++kc) { ldo(kc); SCHED0(); if (kc >= PACE0 && kc + 1 < WO_PRE) PACE; SCHED0(); }
        static_for<90>([&](auto ic) {
            constexpr int i = ic.value, kc = i / 5, n = i % 5, j = WO_W + 2 * (i / 3) + i % 3;
            MFMA_T(wf[i], af[kc], v[n]);
            SCHED0();
            if (i % 3 < 2 && j < 90) rdw(j);
            if (i % WO_EV == 2 && i / WO_EV + WO_PRE < 18) ldo(i / WO_EV + WO_PRE);
            SCHED0();
        });
        asm volatile("s_nop 4" : "+v"(v[0]), "+v"(v[1]));
        STAMP(8);
        proj_store(v, po);
        __builtin_amdgcn_s_setprio(0);
    }
}

DEVI f32x2 gelu2(f32x2 v) {
    const f32x2 w = v * (v * v * -0.10294324f + -2.3022082f);
    const f32x2 d = (f32x2){__builtin_amdgcn_exp2f(w.x), __builtin_amdgcn_exp2f(w.y)} + 1.0f;
    return v * (f32x2){__builtin_amdgcn_rcpf(d.x), __builtin_amdgcn_rcpf(d.y)};
}
DEVI f32x4 gelu4(f32x4 v) { const f32x2 a = gelu2(v.xy), b = gelu2(v.zw); return (f32x4){a.x, a.y, b.x, b.y}; }
DEVI void rowsum16x8(const f32x4 a, const f32x4 b, float& o0, float& o1) {
    float t0, t1, t2, t3;
    asm volatile(
        "s_nop 1\n\t"
        "v_add_f32_dpp %2, %6, %6 row_ror:8 row_mask:0xf bank_mask:0x3\n\t"
        "v_add_f32_dpp %3, %7, %7 row_ror:8 row_mask:0xf bank_mask:0x3\n\t"
        "v_add_f32_dpp %4, %10, %10 row_ror:8 row_mask:0xf bank_mask:0x3\n\t"
        "v_add_f32_dpp %5, %11, %11 row_ror:8 row_mask:0xf bank_mask:0x3\n\t"
        "v_add_f32_dpp %2, %8, %8 row_ror:8 row_mask:0xf bank_mask:0xc\n\t"
        "v_add_f32_dpp %3, %9, %9 row_ror:8 row_mask:0xf bank_mask:0xc\n\t"
        "v_add_f32_dpp %4, %12, %12 row_ror:8 row_mask:0xf bank_mask:0xc\n\t"
        "v_add_f32_dpp %5, %13, %13 row_ror:8 row_mask:0xf bank_mask:0xc\n\t"
        "v_add_f32_dpp %0, %2, %2 row_half_mirror row_mask:0xf bank_mask:0x5\n\t"
        "s_nop 0\n\t"
        "v_add_f32_dpp %1, %4, %4 row_half_mirror row_mask:0xf bank_mask:0x5\n\t"
        "v_add_f32_dpp %0, %3, %3 row_half_mirror row_mask:0xf bank_mask:0xa\n\t"
        "v_add_f32_dpp %1, %5, %5 row_half_mirror row_mask:0xf bank_mask:0xa\n\t"
        "s_nop 0\n\t"
        "v_add_f32_dpp %0, %0, %0 quad_perm:[1,0,3,2] row_mask:0xf bank_mask:0xf\n\t"
        "v_add_f32_dpp %1, %1, %1 quad_perm:[1,0,3,2] row_mask:0xf bank_mask:0xf\n\t"
        "s_nop 0\n\t"
        "v_add_f32_dpp %0, %0, %0 quad_perm:[2,3,0,1] row_mask:0xf bank_mask:0xf\n\t"
        "v_add_f32_dpp %1, %1, %1 quad_perm:[2,3,0,1] row_mask:0xf bank_mask:0xf\n\t"
        : "=&v"(o0), "=&v"(o1), "=&v"(t0), "=&v"(t1), "=&v"(t2), "=&v"(t3)
        : "v"(a[0]), "v"(a[1]), "v"(a[2]), "v"(a[3]), "v"(b[0]), "v"(b[1]), "v"(b[2]), "v"(b[3]));
}

#define K5_PRE 9
#define RH_MAP 0
#define NET_LAYERS 2
constexpr int G_STAGE = 34816;
constexpr int K5_STAGE = 18432, K5_NS = 8;

constexpr int y_load_at(int slot) {
    int next = K5_PRE;
    for (int s = 0;; ++s) { const bool go = next < 18 + 2 * (s / 3); if (s == slot) return go ? next : -1; next += go; }
}

struct GTile { f32x4 a[4][4], e[2]; };
struct GFrag { bf16x8 a[2][4], b[2][5]; const LAS char *pa[2][2], *pb, *p8; int slot; };
DEVI void g_rd(GFrag& f, int kk, int i) {
    if (i < 4) f.a[kk][i] = LDS_FRAG(f.pa[kk][i >> 1] + (i & 1) * 2048);
    else if (i < 8) f.b[kk][i - 4] = LDS_FRAG(f.pb + (kk * 9 + i - 4) * 1024);
    else f.b[kk][4] = LDS_FRAG(f.p8 + kk * 9216);
}
template <int NS, int G, bool ROWS_IN_REGS>
DEVI void g_steps(GTile& c, GFrag& f, int n) {
    LOOP_ALIGN();
#pragma unroll 1
    do {
        int d;
#pragma unroll
        for (int kk = 0; kk < 2; ++kk) {
            WAIT_L(0);
            if (kk) BAR();
            SCHED0();
#pragma unroll
            for (int i = 0; i < 18; ++i) {
                const int nn = i >> 2, m = i & 3;
                f32x4& e = nn < 4 ? c.a[m][nn] : c.e[m & 1];
                e = ROWS_IN_REGS ? MFMA(f.a[kk][m], f.b[kk][nn], e) : MFMA(f.b[kk][nn], f.a[kk][m], e);
                SCHED0();
                if (i < 9) g_rd(f, kk ^ 1, i);
                if (kk == 0) {
                    if (i == 9) d = pin_s(f.slot == NS - 1 ? -(NS - 1) * G : G);
                    if (i >= 10 && i < 14) f.pa[(i - 10) >> 1][i & 1] = opaque(f.pa[(i - 10) >> 1][i & 1] + d);
                    if (i == 14) f.pb = opaque(f.pb + d);
                    if (i == 15) f.p8 = opaque(f.p8 + d);
                } else {
                    if (i == 10) f.slot = pin_s(f.slot == NS - 1 ? 0 : f.slot + 1);
                    if (i == 12) n = pin_s(n - 1);
                }
                SCHED0();
            }
        }
    } while (n);
}
DEVI void keep_s(__amdgpu_buffer_rsrc_t r) { asm volatile("" :: "s"(r)); }
DEVI void keep4(const f32x4 (&c)[4]) { asm volatile("" :: "v"(c[0]), "v"(c[1]), "v"(c[2]), "v"(c[3])); }
template <int D, int DB, bool FIRST, class HOOK>
DEVI void g_step_u(GTile& c, GFrag& f, const f32x4 (&c0)[4], HOOK&& hook) {
    static_for<36>([&](auto ic) {
        constexpr int kk = ic.value / 18, i = ic.value % 18, n = i >> 2, m = i & 3;
        if (i == 0) { WAIT_L(0); if (kk) BAR(); SCHED0(); }
        f32x4& e = n < 4 ? c.a[m][n] : c.e[m & 1];
        if (FIRST && kk == 0) asm volatile("v_mfma_f32_16x16x32_bf16 %0, %1, %2, %3" : "=&v"(e) : "v"(f.b[kk][n]), "v"(f.a[kk][m]), "v"(c0[m]));
        else MFMA_T(f.b[kk][n], f.a[kk][m], e);
        SCHED0();
        if (i < 9) g_rd(f, kk ^ 1, i);
        if (kk == 0) {
            if (i >= 10 && i < 14) f.pa[(i - 10) >> 1][i & 1] = opaque(f.pa[(i - 10) >> 1][i & 1] + D);
            if (i == 14) f.pb = opaque(f.pb + DB);
            if (i == 15) f.p8 = opaque(f.p8 + DB);
        }
        if (FIRST && ic.value == 20) keep4(c0);
        hook(ic);
        SCHED0();
    });
}
#define MF(d, a, b) "v_mfma_f32_16x16x32_bf16 " d ", " a ", " b ", " d "\n\t"
template <int D, int DB>
DEVI void g_step_gelu(GTile& c, GFrag& f, f32x4& z, float r, float c1) {
#pragma unroll
    for (int kk = 0; kk < 2; ++kk) {
        float va = z[2 * kk], vb = z[2 * kk + 1], ta, tb;
        WAIT_L(0); if (kk) BAR(); SCHED0();
#pragma unroll
        for (int i = 0; i < 9; ++i) {
            MFMA_T(f.b[kk][i >> 2], f.a[kk][i & 3], c.a[i & 3][i >> 2]);
            if (i == 5) asm volatile("v_mul_f32 %0, %0, %1" : "+v"(va) : "v"(r));
            if (i == 6) asm volatile("v_mul_f32 %0, %0, %1" : "+v"(vb) : "v"(r));
            if (i == 7) asm volatile("v_mul_f32 %0, %1, %1" : "=v"(ta) : "v"(va));
            if (i == 8) asm volatile("v_mul_f32 %0, %1, %1" : "=v"(tb) : "v"(vb));
            SCHED0();
            g_rd(f, kk ^ 1, i);
            SCHED0();
        }
        asm volatile(MF("%0", "%5", "%6") "v_fmaak_f32 %3, %3, %11, 0xc0135761\n\tv_fmaak_f32 %4, %4, %11, 0xc0135761\n\t"
                     MF("%1", "%5", "%7") "v_mul_f32 %3, %3, %9\n\tv_mul_f32 %4, %4, %10\n\t" MF("%2", "%5", "%8") "v_exp_f32 %3, %3"
                     : "+v"(c.a[1][2]), "+v"(c.a[2][2]), "+v"(c.a[3][2]), "+v"(ta), "+v"(tb)
                     : "v"(f.b[kk][2]), "v"(f.a[kk][1]), "v"(f.a[kk][2]), "v"(f.a[kk][3]), "v"(va), "v"(vb), "v"(c1));
        SCHED0(); f.pa[kk][0] = opaque(f.pa[kk][0] + D); SCHED0();
        asm volatile(MF("%0", "%6", "%7") "v_exp_f32 %5, %5\n\t" MF("%1", "%6", "%8") "v_add_f32 %4, 1.0, %4\n\tv_add_f32 %5, 1.0, %5\n\t"
                     MF("%2", "%6", "%9") "v_rcp_f32 %4, %4\n\t" MF("%3", "%6", "%10") "v_rcp_f32 %5, %5"
                     : "+v"(c.a[0][3]), "+v"(c.a[1][3]), "+v"(c.a[2][3]), "+v"(c.a[3][3]), "+v"(ta), "+v"(tb)
                     : "v"(f.b[kk][3]), "v"(f.a[kk][0]), "v"(f.a[kk][1]), "v"(f.a[kk][2]), "v"(f.a[kk][3]));
        SCHED0(); f.pa[kk][1] = opaque(f.pa[kk][1] + D); SCHED0();
        asm volatile(MF("%0", "%4", "%5") "v_mul_f32 %2, %2, %7\n\tv_mul_f32 %3, %3, %8\n\t" MF("%1", "%4", "%6")
                     : "+v"(c.e[0]), "+v"(c.e[1]), "+v"(va), "+v"(vb) : "v"(f.b[kk][4]), "v"(f.a[kk][0]), "v"(f.a[kk][1]), "v"(ta), "v"(tb));
        SCHED0();
        if (kk == 0) { f.pb = opaque(f.pb + DB); f.p8 = opaque(f.p8 + DB); }
        SCHED0();
        z[2 * kk] = va; z[2 * kk + 1] = vb;
    }
}
DEVI void g_fill(GTile& c, const float (&e)[4]) {
#pragma unroll
    for (int m = 0; m < 4; ++m) {
#pragma unroll
        for (int n = 0; n < 4; ++n) c.a[m][n] = pin((f32x4){e[m], e[m], e[m], e[m]});
        if (m < 2) c.e[m] = pin((f32x4){e[m], e[m], e[m], e[m]});
    }
}

#undef STAMP_SEC
#define STAMP_SEC 1
DEVI void gmlp_body(LAS char* lds, int wm, int wn, int pm, int cg, char* z2t, char* y, const u16* __restrict__ bsp STAMP_PARAM) {
    const int mlo = wm * 4 + wn * 2, mhi = wm * 4 + (1 - wn) * 2;
    const float zero4[4] = {0.f, 0.f, 0.f, 0.f};
    GTile z2, z1, acc;
    GFrag f;
    {
        const int lane = lane_id_fresh();
#pragma unroll
        for (int kk = 0; kk < 2; ++kk) { f.pa[kk][0] = lds + mlo * 2048 + kk * 1024 + lane * 16; f.pa[kk][1] = lds + mhi * 2048 + kk * 1024 + lane * 16; }
        f.pb = lds + 16384 + wn * 4096 + lane * 16; f.p8 = lds + 16384 + 8192 + lane * 16; f.slot = 0;
    }
    g_fill(z2, zero4); g_fill(z1, zero4);
#pragma unroll
    for (int i = 0; i < 4; ++i) f.pa[i >> 1][i & 1] = opaque(f.pa[i >> 1][i & 1]);
    f.pb = opaque(f.pb); f.p8 = opaque(f.p8);
    SCHED0();
    WAIT_V(0); BARX();
    BAR();
    BARX();
    STAMP(2);
#pragma unroll
    for (int i = 0; i < 9; ++i) g_rd(f, 0, i);
    g_steps<4, G_STAGE, true>(z2, f, 9);
    STAMP(3);
    {
        const int lane = lane_id_fresh(), r = lane & 15, q = lane >> 4;
        f32x4 ra[4];
#pragma unroll
        for (int m = 0; m < 4; ++m) ra[m] = *(const LAS f32x4*)(lds + G_TAB + ((m < 2 ? mlo : mhi) + (m & 1)) * 64 + q * 16);
        SCHED0();
#pragma unroll
        for (int m = 0; m < 4; ++m) {
            f32x4 s1, s2;
#pragma unroll
            for (int n = 0; n < 4; ++n) {
                const f32x4 g = gelu4(z2.a[m][n] * ra[m]);
                z2.a[m][n] = g;
                if (n == 0) { s1 = g; s2 = g * g; } else { s1 += g; s2 += g * g; }
            }
            if (m < 2) { const f32x4 g = gelu4(z2.e[m] * ra[m]); z2.e[m] = g; s1 += g; s2 += g * g; }
            float u0, u1;
            rowsum16x8(s1, s2, u0, u1);
            if ((r & 3) == 0) *(LAS f32x2*)(lds + G_PART + wn * 1024 + ((m < 2 ? mlo : mhi) + (m & 1)) * 128 + (q * 4 + (r >> 2)) * 8) = (f32x2){u0, u1};
            SCHED0();
        }
    }
    WAIT_L0(); BAR();
    STAMP(4);
    g_steps<4, G_STAGE, false>(z1, f, 4);
    unsigned b0, b1;
    f32x4 c0[4];
    g_step_u<G_STAGE, G_STAGE, false>(z1, f, c0, [&](auto ic) {
        if (ic.value != 18) return;
        STAMP(5);
        const int lane = lane_id_fresh(), q = lane >> 4;
        b0 = __builtin_amdgcn_raw_buffer_load_b32(RSRC(bsp), (lane & 15) * 4, pm * 256 + mlo * 32, 0);
        b1 = __builtin_amdgcn_raw_buffer_load_b32(RSRC(bsp), (lane & 15) * 4, pm * 256 + mhi * 32, 0);
#pragma unroll
        for (int g = 0; g < 2; ++g) {
            const auto rz2 = RSRC(z2t + (size_t)pm * 589824 + cg * 36864 + ((g ? mhi : mlo) >> 1) * 9216);
            const LAS char* tab = lds + G_TAB + (g ? mhi : mlo) * 64 + q * 16;
            LAS char* own = lds + (wm ? G_OWN : G_STAGE + 16384) + (g ^ wn) * 9216 + lane * 16;
            f32x4 ra[2], rb[2];
#pragma unroll
            for (int T = 0; T < 2; ++T) { ra[T] = *(const LAS f32x4*)(tab + 512 + T * 64); rb[T] = *(const LAS f32x4*)(tab + 1024 + T * 64); }
#pragma unroll
            for (int n = 0; n < (g ? 4 : 5); ++n) {
                const f32x4 v0 = (n < 4 ? z2.a[2 * g][n & 3] : z2.e[0]) * ra[0] + rb[0], v1 = (n < 4 ? z2.a[2 * g + 1][n & 3] : z2.e[1]) * ra[1] + rb[1];
                const u32x4 o = {pack2(v0[0], v1[0]), pack2(v0[1], v1[1]), pack2(v0[2], v1[2]), pack2(v0[3], v1[3])};
                ST16(rz2, lane * 16, n < 4 ? wn * 4096 + n * 1024 : 8192, o, 16);
                *(LAS u32x4*)(own + (n < 4 ? wn * 4096 + n * 1024 : 8192)) = o;
            }
        }
        STAMP(6);
    });
    static_for<3>([&](auto sc) {
        constexpr int D = sc.value == 0 ? G_STAGE : sc.value == 1 ? -3 * G_STAGE : 2 * G_STAGE;
        g_step_u<D, D, false>(z1, f, c0, [&](auto ic) { if (ic.value == 17 && sc.value == 1) WAIT_V(0); });
    });
    float ra[4], c1;
    {
        int lane, da[2];
        g_step_u<-G_STAGE, -G_STAGE, false>(z1, f, c0, [&](auto ic) {
            constexpr int i = ic.value;
            if (i == 9) lane = pin(lane_id_fresh());
            if (i == 14 || i == 15) da[i & 1] = pin((lane & 15) * 128 + (((4 * (i & 1) + (lane >> 4)) ^ (lane & 7)) * 16) - lane * 16 - (i & 1) * 1024);
            if (i == 16 || i == 17) f.pa[0][i & 1] = opaque(f.pa[0][i & 1] + da[0]);
            if (i == 27 || i == 28) f.pa[1][i & 1] = opaque(f.pa[1][i & 1] + da[1]);
            if (i >= 29 && i < 33) {
                constexpr int m = (i - 29) & 3;
                const float e = m & 1 ? bfhi(m < 2 ? b0 : b1) : bflo(m < 2 ? b0 : b1);
                c0[m] = pin((f32x4){e, e, e, e});
            }
            if (i == 33) {
#pragma unroll
                for (int m = 0; m < 4; ++m) ra[m] = *(const LAS float*)(lds + G_TAB + ((m < 2 ? mlo : mhi) + (m & 1)) * 64 + (lane & 15) * 4);
                c1 = pin(-0.10294324f);
            }
        });
    }
    STAMP(7);
    g_step_u<2 * G_STAGE, G_OWN - G_STAGE - 16384, true>(acc, f, c0, [](auto) {});
    static_for<18>([&](auto sc) {
        constexpr int st = sc.value, m = st < 16 ? st >> 2 : st & 1;
        constexpr int D = st == 0 ? -3 * G_STAGE : st % 3 == 1 ? 2 * G_STAGE : -G_STAGE;
        g_step_gelu<D, st == 0 ? 16384 - G_OWN : D>(acc, f, st < 16 ? z1.a[m][st & 3] : z1.e[m], ra[m], c1);
    });
    f.slot = 1;
    STAMP(9);
    g_steps<3, -G_STAGE, false>(acc, f, 12);
    __amdgpu_buffer_rsrc_t ry;
    unsigned vy[4], ve[2];
    int lane;
    u32x4 o = {0u, 0u, 0u, 0u};
    auto y_part = [&](auto jc) {
        constexpr int j = jc.value, m = j / 10, k = j % 10, e = (k & 1) * 2;
        const f32x4 &a = k < 8 ? acc.a[m][k >> 1] : acc.e[m & 1], &z = k < 8 ? z1.a[m][k >> 1] : z1.e[m & 1];
        float t0, t1;
        asm volatile("v_mul_f32 %1, %3, %5\n\tv_mul_f32 %2, %4, %6\n\tv_cvt_pk_bf16_f32 %0, %1, %2" : "=v"(o[k & 3]), "=&v"(t0), "=&v"(t1) : "v"(a[e]), "v"(a[e + 1]), "v"(z[e]), "v"(z[e + 1]));
        if (k == 3 || k == 7) ST16(ry, vy[m] + (k >> 2) * 1024, 0, o, 16);
        if (k == 9) ST8(ry, ve[m & 1], 0, ((u32x2){o[0], o[1]}), 16);
    };
    WAIT_L(0);
    SCHED0();
    static_for<36>([&](auto ic) {
        constexpr int i = ic.value, kk = i < 14 ? 0 : i < 28 ? 1 : i >= 32, t = i < 28 ? i % 14 : 14 + (i & 3), m = t < 10 ? t / 5 : 2 + (t - 10) / 4, n = t < 10 ? t % 5 : (t - 10) & 3;
        if (i == 14) { WAIT_L(0); BAR(); SCHED0(); }
        f32x4& e = n < 4 ? acc.a[m][n & 3] : acc.e[m & 1];
        MFMA_T(f.b[kk][n], f.a[kk][m], e);
        SCHED0();
        if (i < 9) g_rd(f, 1, i);
        if (i == 10) { ry = RSRC(own_s(y) + (size_t)pin_s(pm * 8 * 73728 + cg * 4608)); keep_s(ry); }
        if (i == 12) { lane = pin(lane_id_fresh()); vy[0] = pin((unsigned)(mlo * 73728 + wn * 2048 + lane * 16)); }
        if (i == 14) ve[0] = pin((unsigned)(mlo * 73728 + 4096 + (lane >> 5) * 256 + (lane & 15) * 16 + ((lane >> 4) & 1) * 8));
        if (i == 15) { vy[1] = pin(vy[0] + 73728); ve[1] = pin(ve[0] + 73728); }
        if (i == 16) { vy[2] = pin(vy[0] + (mhi - mlo) * 73728); vy[3] = pin(vy[2] + 73728); asm volatile("s_nop 4"); }
        if constexpr (i >= 17) y_part(IC<i - 17>{});
        SCHED0();
    });
    STAMP(10);
    y_part(IC<19>{});
    SCHED0();
#pragma unroll
    for (int i = 0; i < 4; ++i) {
        const int m = 2 + (i >> 1), p = i & 1;
        if (i == 2) asm volatile("" : "+v"(acc.a[3][0]), "+v"(acc.a[3][1]), "+v"(acc.a[3][2]), "+v"(acc.a[3][3]));
        const f32x4 a = acc.a[m][2 * p] * z1.a[m][2 * p], c = acc.a[m][2 * p + 1] * z1.a[m][2 * p + 1];
        ST16(ry, vy[m] + p * 1024, 0, ((u32x4){pack2(a[0], a[1]), pack2(a[2], a[3]), pack2(c[0], c[1]), pack2(c[2], c[3])}), 16);
        SCHED0();
    }
}

#define NET_IDS \
    int b = __builtin_amdgcn_readfirstlane(blockIdx.x), wid = wid8 & 3; \
    asm volatile("" : "+s"(b), "+s"(wid));     \
    const int xcd = b & 7, idx = b >> 3; \
    const int pa = (xcd >> 1) * 4 + (idx & 3), hp = (xcd & 1) * 8 + (idx >> 2); \
    const int pm = (xcd >> 2) * 8 + (idx & 7), cg = (xcd & 3) * 4 + (idx >> 3); \
    const int rh = RH_MAP ? xcd * 4 + (idx & 3) : (xcd >> 1) * 8 + (idx & 7), c5 = RH_MAP ? idx >> 2 : (xcd & 1) * 4 + (idx >> 3); \
    const int lead = (idx + (idx >> 3)) & 3; \
    auto kord = [&](int t, int n) { \
        const int k = t + ((int)(0x1F000000u << (((t - lead) & 3) * 4)) >> 28); \
        return (unsigned)k < (unsigned)n ? k : t; \
    }; \
    auto kord5 = [&](int t) { \
        const int k = t + ((int)(0x1F000000u << (((t - (RH_MAP ? c5 : lead)) & 3) * 4)) >> 28); \
        return (unsigned)k < 18u ? k : t; \
    };

DEVI u32x4 tr_frag(const LAS char* p, int four_rows) {
    const u32x2 a = __builtin_bit_cast(u32x2, __builtin_amdgcn_ds_read_tr16_b64_v4i16((LAS s16x4*)p));
    const u32x2 b = __builtin_bit_cast(u32x2, __builtin_amdgcn_ds_read_tr16_b64_v4i16((LAS s16x4*)(p + four_rows)));
    return (u32x4){a.x, a.y, b.x, b.y};
}
DEVI float dot2(unsigned v, unsigned w, float acc) { return __builtin_amdgcn_fdot2_f32_bf16(__builtin_bit_cast(bf16x2_t, v), __builtin_bit_cast(bf16x2_t, w), acc, false); }
DEVI float sum2(unsigned v, float acc) { return dot2(v, 0x3f803f80u, acc); }
struct Sel { unsigned lo, hi, nlo, nhi; };
DEVI Sel make_sel() { return {(unsigned)pin_s(0x00003f80), (unsigned)pin_s(0x3f800000), (unsigned)pin_s(0x0000bf80), (unsigned)pin_s((int)0xbf800000u)}; }
DEVI unsigned centre2(unsigned v, f32x2& e, float mean, const Sel& k) {
    const float a = dot2(k.lo, v, e.x), b = dot2(k.hi, v, e.y);
    const unsigned r = pack2(a, b);
    e = (f32x2){dot2(k.nlo, r, a), dot2(k.nhi, r, b)} - mean;
    return r;
}
typedef u32x4 Retile[9];
template <int PITCH>
DEVI void retile_out_make(Retile& o, const LAS char* img, int lane) {
    const int c = lane & 15, q = lane >> 4, p = lane & 3;
    const LAS char* src = img + (q * 8 + (c >> 2)) * PITCH;
#pragma unroll
    for (int i = 0; i < 8; ++i) o[i] = tr_frag(src + p * 16 + (i >> 2) * 144 + ((i >> 1) & 1) * 64 + (i & 1) * 8, 4 * PITCH);
    o[8] = tr_frag(src + 128 + (p >> 1) * 144 + (p & 1) * 8, 4 * PITCH);
}
DEVI void retile_out_store(const Retile& o, char* dst, int nkc, int kc, int w, int lane) {
    const int c = lane & 15, q = lane >> 4;
    const auto rd = RSRC(dst + (size_t)(2 * w * nkc + kc) * 4608);
#pragma unroll
    for (int i = 0; i < 8; ++i) ST16(rd, lane * 16 + (i & 3) * 1024, (i >> 2) * nkc * 4608, o[i], 16);
    ST16(rd, (c >> 3) * nkc * 4608 + 4096 + q * 128 + (c & 7) * 16, 0, o[8], 16);
}

DEVI float dpp_ror(float v, int n) { return n == 8 ? __builtin_amdgcn_update_dpp(0.f, v, 0x128, 0xf, 0xf, true) : n == 4 ? __builtin_amdgcn_update_dpp(0.f, v, 0x124, 0xf, 0xf, true)
                                          : n == 2 ? __builtin_amdgcn_update_dpp(0.f, v, 0x122, 0xf, 0xf, true) : __builtin_amdgcn_update_dpp(0.f, v, 0x121, 0xf, 0xf, true); }
DEVI unsigned long long epoch_of(const char* ws) {
    SCHED0();
    const unsigned long long ep = *(const __attribute__((address_space(4))) unsigned long long*)(ws + pin_s((int)OFF_EP)) + 1ull;
    asm volatile("" :: "s"(ep));
    return ep;
}
DEVI void prologue_done(LAS char* lds, char* ws, unsigned long long ep, int b, int lane) {
    if (lds_arrive(lds + G_WORD + 8) == 7) ST16(RSRC(ws), lane ? 0x80000000u : (unsigned)OFF_FL + b * 16, 0, ((u32x4){(unsigned)ep, (unsigned)(ep >> 32), 0u, 0u}), 16);
}
DEVI void centre_frag(u32x4& f, f32x2 (&er)[4], float mean, const Sel& k) {
#pragma unroll
    for (int e = 0; e < 4; ++e) f[e] = centre2(f[e], er[e], mean, k);
}
template <int N>
DEVI void column_means(LAS char* lds, LAS char* cnt, int me, int lane, float a, float b, float& ma, float& mb) {
    LAS float* red = (LAS float*)(lds + G_PART);
    auto x = __builtin_amdgcn_permlane16_swap(__float_as_uint(a), __float_as_uint(b), false, false);
    red[me * 64 + lane] = __uint_as_float(x[0]) + __uint_as_float(x[1]);
    if (lds_arrive(cnt) < N - 1) lds_count_wait(cnt, N);
    float t = 0.f;
#pragma unroll
    for (int i = 0; i < N; ++i) t += red[i * 64 + lane];
    x = __builtin_amdgcn_permlane32_swap(__float_as_uint(t), __float_as_uint(t), false, false);
    t = (__uint_as_float(x[0]) + __uint_as_float(x[1])) * (1.0f / 576.0f);
    x = __builtin_amdgcn_permlane16_swap(__float_as_uint(t), __float_as_uint(t), false, false);
    ma = __uint_as_float(x[0]); mb = __uint_as_float(x[1]);
}
template <class ARGS>
DEVI unsigned long long prologue(LAS char* lds, char*& ws, const char*& x0, const char*& wqkv, const u16*& wo, const u16*& w_in, int b, int pa, int wid8, ARGS&& args PST_PARAM) {
    PST_DECL;
    const int lane = lane_id_fresh(), c = lane & 15, q = lane >> 4, w = wid8 & 3;
    const int jw = w - 1 + 3 * (((wid8 >> 2) ^ (b >> 3)) & 1);
    const bool wjob = b >= 16 && b < 112 && w;
    const int uo = (wid8 - 1 - (wid8 >> 2)) * 16 + b;
    const bool ojob = b < 16 && w && uo < 72;
    const unsigned l0 = (unsigned)(size_t)lds;
    LAS char* img = lds + 2 * K1_STAGE;
    const int uw = b - 16, head = (uw & 7) * 4 + ((uw >> 3) & 3), part = uw >> 5, hp = head >> 1, odd = head & 1;
    const int c0 = part * 1152 + hp * 72 + odd * 40;
    {
        const int so = pin_s(jw * 96 * 3456 + (c0 & ~15));
        const unsigned v = (lane & 3) < ((head & 3) == 3 ? 2 : 3) ? (lane >> 2) * 3456 + (lane & 3) * 16 : 0x80000000u, d = 16 * 3456 - 1024;
        const unsigned v0 = pin(v), v1 = pin(v + d), v2 = pin(v + 2 * d), v3 = pin(v + 3 * d);
        const int e = ((b - 112) >> 3) * 192 + wid8 * 64 + lane, row = (e * 10923) >> 16, r = e - row * 6;
        const unsigned vp = pin((unsigned)(row * 3456 + (r >> 1) * 1152 + (r & 1) * 128));
        const unsigned la = pin_s(l0 + 2 * K1_STAGE + jw * 6144);
        args(0);
        if (b >= 112 && wid8 < 3) dma1(l0 + G_JUNK, RSRC(wqkv), (b & 7) * 128, vp);
        if (wjob) { dma4<false>(la, RSRC(wqkv), so, v0, v1, v2, v3); dma2<false>(la + 4096, RSRC(wqkv), so + 64 * 3456, v0, v1); }
        args(1);
    }
    if (ojob) {
        const auto rs = RSRC(wo + (uo & 3) * 144);
#pragma unroll
        for (int j = 0; j < 9; ++j) {
            const int i = lane + 64 * j, rl = (i * 3641) >> 16, kp = (uo >> 2) * 32 + rl;
            const int row = kp < 512 ? 18 * (kp >> 4) + (kp & 15) + 2 * ((kp >> 4) & 1) : 36 * ((kp - 512) >> 2) + 16 + (kp & 3);
            dma1(l0 + 2 * K1_STAGE + wid8 * 9216 + j * 1024, rs, 0, (unsigned)(row * 1152 + (i - rl * 18) * 16));
        }
    }
    if (wid8 == 4) { lds_word_set(lds + G_WORD + c * 4, 0u); WAIT_L0(); }
    BAR();
    unsigned long long ep;
    if (wid8 == 4) ep = epoch_of(ws);
    if (wid8 == 4 && b >= 16 && b < 112) lds_count_wait(lds + G_WORD + 12, 12);
    if (wid8 > 4 && b >= 112) {
        __builtin_amdgcn_s_sleep(I_SLEEP);
        const unsigned v = (lane >> 2) * 9216 + (lane & 3) * 16, d = 16 * 9216 - 1024;
#pragma unroll
        for (int i = 0; i < 3; ++i) dma4<false>(l0 + 3 * K1_STAGE + (w - 1) * 12288 + i * 4096, RSRC(w_in + win_strip(b) * 32), (win_third(b, w) * 192 + i * 64) * 9216, v, v + d, v + 2 * d, v + 3 * d);
    }
    if (wid8 == 4) {
        AttnX ax = attn_x(x0, x0, 0, pa, 0, lane);
#pragma unroll
        for (int s = 0; s < 2; ++s)
#pragma unroll
            for (int i = 0; i < 4; ++i) { ax.oA = (pa * 8 + 2 * i) * 18432; attn_issueA(lds, ax, i, s, s); }
    }
    u32x4 xv[9];
    if (wid8 == 0) {
        const unsigned v = (lane >> 3) * 2304 + (lane & 7) * 16;
        const auto rx = RSRC(x0 + (size_t)slot2row(b * 8) * 1152);
#pragma unroll
        for (int i = 0; i < 9; ++i) asm volatile("s_nop 4\n\tbuffer_load_dwordx4 %0, %1, %2, 0 offen offset:%3" : "+v"(xv[i]) : "v"(v), "s"(rx), "i"(i * 128) : "memory");
        const auto rw = RSRC(ws);
        const u32x4 ones = {~0u, ~0u, ~0u, ~0u}, zero = {0u, 0u, 0u, 0u};
        const unsigned vo = lane * 16;
        ST16(rw, vo, (unsigned)OFF_ZST + b * 2048, ones, 16); ST16(rw, vo, (unsigned)OFF_ZST + b * 2048 + 1024, ones, 16);
        ST16(rw, vo, (unsigned)OFF_CNT + b * 2560, zero, 16); ST16(rw, vo, (unsigned)OFF_CNT + b * 2560 + 1024, zero, 16);
        ST16(rw, vo, lane < 32 ? (unsigned)OFF_CNT + b * 2560 + 2048 : (unsigned)(OFF_L0 + L_STB) + b * 512 - 512, zero, 16);
        ST16(rw, vo, (unsigned)(OFF_L0 + L_O) + (b >> 1) * 18432 + 16384 + (b & 1) * 1024, zero, 16);
        ST16(rw, b < 12 ? vo : 0x80000000u, (unsigned)OFF_FL2 + b * 1024, zero, 16);
    }
    PST(1);
    if (wjob) {
        const Sel k = make_sel();
        const LAS char* s0 = opaque(img + jw * 6144 + (q * 8 + (c >> 2)) * 64 + (c0 & 15) + (c & 3) * 8);
        const LAS char* s1 = opaque(img + jw * 6144 + (q * 8 + (c >> 2)) * 64 + (c0 & 15) + 32 + min(c & 3, 2) * 2048);
        const auto rd = RSRC(ws + OFF_TWQKV + (size_t)(hp * 18 + jw * 3) * 6144 + (3 * odd + part) * 1024), rd2 = RSRC(ws + OFF_TWT + (size_t)((hp * 3 + part) * 18 + jw * 3) * 256);
        const unsigned vd2 = pin(c < 12 && !odd ? (unsigned)((c >> 2) * 256 + q * 64 + (c & 3) * 16) : 0x80000000u);
        asm volatile("" :: "s"(rd), "s"(rd2));
        WAIT_V(0);
        PST(2);
        u32x4 f[4];
        float sm[4] = {0.f, 0.f, 0.f, 0.f}, m0, m1;
#pragma unroll
        for (int i = 0; i < 4; ++i) f[i] = tr_frag(i < 3 ? s0 + i * 2048 : s1, 256);
#pragma unroll
        for (int e = 0; e < 16; ++e) sm[(e >= 12) * 2 + (e & 1)] = sum2(f[e >> 2][e & 3], sm[(e >= 12) * 2 + (e & 1)]);
        sm[2] = c < 12 ? sm[2] + sm[3] : 0.f;
        sm[2] += dpp_ror(sm[2], 4); sm[2] += dpp_ror(sm[2], 8);
        column_means<6>(lds, lds + G_WORD + 12, jw, lane, sm[0] + sm[1], sm[2], m0, m1);
        PST(3);
        f32x2 e0[4] = {{-m0, -m0}, {-m0, -m0}, {-m0, -m0}, {-m0, -m0}}, e1[4] = {{-m1, -m1}, {-m1, -m1}, {-m1, -m1}, {-m1, -m1}};
#pragma unroll
        for (int i = 0; i < 3; ++i) { centre_frag(f[i], e0, m0, k); ST16(rd, lane * 16, i * 6144, f[i], 16); }
        centre_frag(f[3], e1, m1, k); ST16(rd2, vd2, 0, f[3], 16);
        PST(4);
        lds_arrive(lds + G_WORD + 12);
    }
    if (wid8 != 4) ep = epoch_of(ws);
    {
        if (wjob) {
            const unsigned fo = pin((unsigned)OFF_FW + ((lane >> 3) * 16 + hp) * 1024 + (3 * odd + part) * 128 + (lane & 7) * 16);
            WAIT_V(0);
            PST(5);
            if (lds_arrive(lds + G_WORD + 4) == 5) {
                const u32x4 e4 = {(unsigned)ep, (unsigned)(ep >> 32), (unsigned)ep, (unsigned)(ep >> 32)};
                ST16(RSRC(ws), fo, 0, e4, 16); ST16(RSRC(ws), fo + 131072, 0, e4, 16);
            }
        }
    }
    if (wid8 == 0) {
        WAIT_V(7);
        asm volatile("" : "+v"(xv[0]), "+v"(xv[1]), "+v"(xv[2]), "+v"(xv[3]), "+v"(xv[4]), "+v"(xv[5]), "+v"(xv[6]), "+v"(xv[7]), "+v"(xv[8]));
        float s = 0.f, q2 = 0.f;
#pragma unroll
        for (int e = 0; e < 36; ++e) { s = sum2(xv[e >> 2][e & 3], s); q2 = dot2(xv[e >> 2][e & 3], xv[e >> 2][e & 3], q2); }
        ST8(RSRC(ws + OFF_L0 + L_STA + (size_t)b * 512), lane * 8, 0, ((u32x2){__float_as_uint(s), __float_as_uint(q2)}), 16);
    }
    if (ojob) {
        WAIT_V(0);
        Retile rt;
        retile_out_make<288>(rt, img + wid8 * 9216, lane); retile_out_store(rt, ws + OFF_TWO, 18, uo >> 2, uo & 3, lane);
    }
    PST(7);
    PST_DUMP(wid8 == 0 || wid8 == 4 || wid8 == 5, wid8 ? wid8 : 2);
    return ep;
}

__global__ __launch_bounds__(512, 2) TRACE_ATTR void k_net(char* ws, const u16* wsp, const u16* bsp, char* out, const char* x0, const char* wqkv, const u16* wo,
                                                             const u16* w_in, const u16* w_out DBG_ARG) {
    extern __shared__ __attribute__((aligned(16))) char smem[];
    LAS char* lds = (LAS char*)smem;
    asm volatile("gm_code_begin:");
    if (blockIdx.x < 16) dma1((unsigned)(size_t)lds + G_JUNK, code_rsrc(), 0, (threadIdx.x * 2 + (blockIdx.x >> 3)) * 128);
    STAMP_DECL;
    const int wid8 = __builtin_amdgcn_readfirstlane(threadIdx.x >> 6);
    unsigned long long ep;
    {
        NET_IDS
        ep = prologue(lds, ws, x0, wqkv, wo, w_in, b, pa, wid8, [&](int part) {
            if (part == 0) { asm volatile("" :: "s"(wqkv), "s"(w_in), "s"(w_out)); return; }
            ws = own_s(ws); wsp = own_s(wsp); bsp = own_s(bsp); out = own_s(out); x0 = own_s(x0); wo = own_s(wo); w_out = own_s(w_out);
        } PST_FWD);
    }
    const char *twin = ws + OFF_TWIN, *twout = ws + OFF_TWOUT;
    if (wid8 >= 4) {
        u32x4 wr[5][9];
#pragma unroll 1
      for (int layer = 0; layer < NET_LAYERS; ++layer) {
        NET_IDS
        TRACE_BEGIN;
        char* wl = ws + OFF_L0 + layer * SZ_L;
        const unsigned fl = layer * F_LAYER;
        const char *xin = wl + L_XB, *st = wl + L_STB, *z2t = wl + (layer ? L_Y : L_Z2T);
        unsigned long long* zst = (unsigned long long*)(ws + OFF_ZST + layer * SZ_ZST);
        const int lane = lane_id_fresh(), l3 = lane >> 3;
        const unsigned vo = lane * 16;
        const unsigned l0 = (unsigned)(size_t)lds, lA = l0 + wid * 4096, lB = l0 + 16384 + wid * 4608;
        const auto rX = RSRC(xin + (size_t)(pm * 8 + 2 * wid) * 18432), rI = RSRC(twin + (size_t)cg * (18 * 18432) + wid * 4608);
        auto issue1B = [&](int g, int slot) { const int k = kord(g < 9 ? g : (g < 13 ? g - 4 : g - 13), 9); dma5<true>(lB + slot * G_STAGE, rI, (g < 9 ? 9 + k : k) * 18432, vo); };
        auto issue1A = [&](int g, int slot) { dma4<true>(lA + slot * G_STAGE, rX, kord(g < 9 ? g : g - 13, 9) * 2048, vo, vo, vo + 16384, vo + 16384); };
        auto issue1 = [&](int g, int slot) { issue1B(g, slot); issue1A(g, slot); };
        const auto rA = RSRC((const char*)wsp + (size_t)(pm * 128 + wid * 32) * 4096), rB = RSRC(z2t + cg * 36864 + wid * 4608);
        const unsigned voA = (unsigned)(2 * l3 * 4096 + ((lane & 7) ^ l3) * 16);
        auto issueB = [&](int k, int slot) { dma5<true>(lB + slot * G_STAGE, rB, (k >> 1) * 589824 + (k & 1) * 18432, vo); };
        const auto rW = RSRC(twout + (size_t)c5 * (72 * 4608));
        auto issue5 = [&](int t) { dma5<true>(l0 + ((t + 6) & 7) * K5_STAGE + wid * 4608, rW, kord5(t) * K5_STAGE + wid * 4608, vo); };
        auto load5 = [&](int p, int i) {
            const int t0 = p ? 2 * p + 6 : 4, ow = kord5(t0 + i) * K5_STAGE + wid * 4096;
            asm volatile("s_nop 4\n\tbuffer_load_dwordx4 %0, %4, %5, %6 offen sc1\n\tbuffer_load_dwordx4 %1, %4, %5, %6 offen offset:1024 sc1\n\t"
                         "buffer_load_dwordx4 %2, %4, %5, %6 offen offset:2048 sc1\n\tbuffer_load_dwordx4 %3, %4, %5, %6 offen offset:3072 sc1"
                         : "+v"(wr[p][4 * i]), "+v"(wr[p][4 * i + 1]), "+v"(wr[p][4 * i + 2]), "+v"(wr[p][4 * i + 3]) : "v"(vo), "s"(rW), "s"(ow) : "memory");
            if (i) {
                const int oe = kord5(t0 + (wid >> 1)) * K5_STAGE + 16384 + (wid & 1) * 1024;
                asm volatile("s_nop 4\n\tbuffer_load_dwordx4 %0, %1, %2, %3 offen sc1" : "+v"(wr[p][8]) : "v"(vo), "s"(rW), "s"(oe) : "memory");
            }
        };
        auto put5 = [&](int w0, int w1) {
#pragma unroll
            for (int w = w0; w < w1; ++w) {
                const int p = w / 9, i = w % 9 == 4 ? -1 : w % 9 - (w % 9 > 4);
                LAS char* sl = lds + (((p ? 2 * p + 6 : 4) + 6) & 7) * K5_STAGE + lane * 16;
                if (i < 0) *(LAS u32x4*)(sl + (wid >> 1) * K5_STAGE + 16384 + (wid & 1) * 1024) = wr[p][8];
                else *(LAS u32x4*)(sl + (i >> 2) * K5_STAGE + wid * 4096 + (i & 3) * 1024) = wr[p][i];
            }
            WAIT_L0();
        };
        const int row = wid * 32 + (lane & 31);
        auto step1 = [&](int t) { BAR(); issue1(t + 4, t & 3); };
        const char* ps;
        unsigned fd, fd2;
        {
            unsigned fo, fi;
            const bool oj = !layer && wid && b < 96, ij = !layer && wid && b >= 112, early = win_strip(b) >= WIN_EARLY;
            const int ojob = b * 3 + wid - 1, oimg = 3072 + (wid - 1) * 9216;
            float m0, m1;
            unsigned fw;
            auto win_store = [&](int h) {
                const int u = win_strip(b) * 2 + h, half = u >= 144, g = u - half * 144, ug = g / 9, j = g - ug * 9, c = lane & 15;
                const auto rd = RSRC(ws + OFF_TWIN + (size_t)ug * (18 * 18432) + (half * 18 + win_third(b, wid) * 6) * 9216);
                const unsigned vd = (lane >> 4) * 256 + (j < 8 ? ((j & ~1) + (c >> 3)) * 1024 + ((j & 1) * 8 + (c & 7)) * 16 : 8192 + c * 16);
#pragma unroll
                for (int i = 0; i < 6; ++i) ST16(rd, vd, i * 9216, wr[1 + h][i], 16);
            };
            auto win_half = [&](int h) {
                const Sel k = make_sel();
                const float m = h ? m1 : m0;
                f32x2 er[4] = {{-m, -m}, {-m, -m}, {-m, -m}, {-m, -m}};
#pragma unroll
                for (int i = 0; i < 6; ++i) centre_frag(wr[1 + h][i], er, m, k);
            };
            attn_producer(lds, ws, x0, wl, fl, ep, layer, pa, hp, wid, [&] { if (b < 16) WAIT_V(0); prologue_done(lds, ws, ep, b, lane); }, [&](int part) {
                if (part == -1 && ij) {
#pragma unroll
                    for (int h = 0; h < 2; ++h) {
                        const LAS char* src = lds + 3 * K1_STAGE + (wid - 1) * 12288 + ((lane >> 4) * 8 + ((lane & 15) >> 2)) * 64 + h * 32
                                              + ((win_strip(b) * 2 + h) % 9 < 8 ? (lane & 1) * 16 + (lane & 2) * 4 : (lane & 3) * 8);
#pragma unroll
                        for (int i = 0; i < 6; ++i) wr[1 + h][i] = tr_frag(src + i * 2048, 256);
                    }
                    WAIT_L(0);
                }
                if (part == 0 && oj) {
                    const auto rs = RSRC(w_out + (size_t)(ojob >> 2) * 32 * 576 + (ojob & 3) * 144);
#pragma unroll
                    for (int j = 0; j < 9; ++j) { const int i = lane + 64 * j; dma1(l0 + oimg + j * 1024, rs, 0, (unsigned)(((i * 3641) >> 16) * (1152 - 288) + i * 16)); }
                }
                if (part == B01_POS && layer) { issue1B(0, 0); issue1B(1, 1); }
                if (part == 1) {
                    fo = flag_out(fl + F_X1, 256, pa, hp, lane); fi = flag_in(fl + F_X1, 256, pm * 16 + cg, 16, lane);
                    ps = pin(st + (size_t)(pm * 128 + wid * 32) * 64 + lane * 16);
                    fd = pin((unsigned)OFF_FL2 + wid * 1024 + vo);
                    fd2 = pin((unsigned)OFF_FL2 + (lane < 32 ? 256 + lane : 288 + ((cg * 9) >> 1) * 3 + min(lane & 15, 14)) * 16);
                    fw = pin((unsigned)OFF_FL2 + (288 + (72 + ((cg * 9) >> 1)) * 3 + min(lane & 15, 14)) * 16);
                    if (ij) {
                        float sm[4] = {0.f, 0.f, 0.f, 0.f};
#pragma unroll
                        for (int e = 0; e < 48; ++e) sm[(e >= 24) * 2 + (e & 1)] = sum2(wr[1 + (e >= 24)][(e % 24) >> 2][e & 3], sm[(e >= 24) * 2 + (e & 1)]);
                        column_means<3>(lds, lds + G_WORD, wid - 1, lane, sm[0] + sm[1], sm[2] + sm[3], m0, m1);
                        win_half(0);
                    }
                }
                if (part == 2 && ij) { if (early) win_store(0); win_half(1); if (early) win_store(1); }
                if (part == 2 && oj) { retile_out_make<288>(wr[1], lds + oimg, lane); WAIT_L(0); }
            });
            if (oj || ij) {
                if (oj || !early) {
                    __builtin_amdgcn_s_sleep(OJOB_SLEEP);
                    if (oj) retile_out_store(wr[1], ws + OFF_TWOUT, 72, ojob >> 2, ojob & 3, lane); else { win_store(0); win_store(1); }
                }
                WAIT_V(0);
                flag_raise(ws, lane ? 0x80000000u : (unsigned)OFF_FL2 + (oj ? ojob : 288 + win_strip(b) * 3 + wid - 1) * 16);
            }
            if (layer == 0) {
                if (ij) __builtin_amdgcn_s_sleep(WIN_LOOK - 12); else if (!oj) __builtin_amdgcn_s_sleep(WIN_LOOK);
                flags_wait<0, 8>(ws, fw);
            }
            BARX();
            if (wid == 0) flag_raise(ws, fo);
            if (layer == 0) { issue1B(0, 0); issue1B(1, 1); }
            issue1B(2, 2); issue1B(3, 3);
            asm volatile("" :: "s"(rX));
            const int o0 = pin_s(kord(0, 9) * 2048);
            const unsigned v2 = pin(vo + 16384);
            if (wid == 0) { if (layer) __builtin_amdgcn_s_sleep(X1_FIRST); flags_wait<0>(ws, fi);
#pragma unroll
                for (int i = 0; i < 4; ++i) dma4<true>(lA + i * 4096, rX, o0 + i * 36864, vo, vo, v2, v2);
            }
            BARX();
        }
        issue1A(1, 1); issue1A(2, 2);
        WAIT_V(8); BAR();
        issue1A(3, 3);
        {
            f32x4 sp[3];
            WAIT_V(8); step1(0);
            WAIT_V(13); step1(1);
            WAIT_V(18); step1(2);
            unsigned got[2];
            if (layer == 0) asm volatile("s_nop 4\n\tbuffer_load_dword %0, %2, %4, 0 offen sc1\n\tbuffer_load_dword %1, %3, %4, 0 offen sc1" : "+v"(got[0]), "+v"(got[1]) : "v"(fd), "v"(fd2), "s"(RSRC(ws)) : "memory");
            WAIT_IF(layer, 18, 20); step1(3);
            WAIT_IF(layer, 18, 20); step1(4);
            WAIT_V(18);
            if (layer == 0) {
                asm volatile("" : "+v"(got[0]), "+v"(got[1]));
                if (__any((got[0] & got[1]) == 0u)) { flags_wait<0>(ws, fd); flags_wait<0>(ws, fd2); }
            }
            BAR(); issue1B(9, 1); side_load(sp, ps, ps); if (layer == 0) load5(3, 0);
            WAIT_IF(layer, 17, 21); BAR(); issue1B(10, 2); if (layer == 0) load5(3, 1);
            WAIT_IF(layer, 5, 14); BARX();
            side_fence(sp);
#pragma unroll
            for (int i = 0; i < 2; ++i) lds_word_set(lds + G_TAB + (wid * 32 + i * 16 + (lane >> 2)) * 4, __float_as_uint(ln_rstd(sp[i])));
            issue1B(11, 3);
            WAIT_L0();
        }
        BAR(); issue1B(12, 0);
        if (layer == 1) {
            const auto rd = RSRC(out + (size_t)b * 9216 + wid * 2304);
            ST16(rd, vo, 0, ((u32x4){0u, 0u, 0u, 0u}), 16); ST16(rd, vo, 1024, ((u32x4){0u, 0u, 0u, 0u}), 16);
            if (lane < 16) ST16(rd, vo, 2048, ((u32x4){0u, 0u, 0u, 0u}), 16);
        }
        if (layer == 0) {
#pragma unroll
            for (int p = 0; p < 6; ++p) load5(p >> 1, p & 1);
        }
        WAIT_V(0);
        __builtin_amdgcn_s_setprio(3);
        const char* src = pin((const char*)zst + (size_t)(pm * 16 + (lane >> 4) * 4) * 1024 + wid * 256 + (lane & 15) * 16);
        const unsigned tw = pin(l0 + G_TAB + 512 + (lane >> 5) * 512 + (wid * 32 + (lane & 15) * 2 + ((lane >> 4) & 1)) * 4);
        const unsigned fz = flag_out(fl + F_Z2, 256, cg, pm, lane), fzi = pin(fl + F_Z2 + (cg * 16 + pm) * 256 + ((lane ^ pm) & 8) * 16 + (lane & 7) * 16);
        const unsigned vA[4] = {pin(voA), pin(voA + 16 * 4096 - 1024), pin(voA + 4096 - 2048), pin(voA + 17 * 4096 - 3072)};
        auto spA = [&](int so, unsigned la) { dma4<false>(la, rA, so, vA[0], vA[1], vA[2], vA[3]); };
        auto issueA = [&](int k, int slot) { spA(k * 128, lA + slot * G_STAGE); };
        {
            const LAS char* pp = opaque(lds + G_PART + row * 8);
            const auto rs = RSRC(zst + (size_t)(pm * 16 + cg) * 128);
            const unsigned vp = pin(lane < 32 ? (unsigned)row * 8u : 0x80000000u);
            asm volatile("" :: "s"(rs));
            BARX();
            const f32x2 a = *(const LAS f32x2*)pp, c = *(const LAS f32x2*)(pp + 1024);
            ST8(rs, vp, 0, ((u32x2){__float_as_uint(add1(a.x, c.x)), min(__float_as_uint(add1(a.y, c.y)), 0xfffffffeu)}), 16);
            SCHED0();
        }
        __builtin_amdgcn_s_setprio(0);
        {
            f32x4 g[4];
            float sm[4];
            auto gather = [&] {
                asm volatile("global_load_dwordx4 %0, %4, off sc1\n\tglobal_load_dwordx4 %1, %4, off offset:1024 sc1\n\t"
                             "global_load_dwordx4 %2, %4, off offset:2048 sc1\n\tglobal_load_dwordx4 %3, %4, off offset:3072 sc1"
                             : "=&v"(g[0]), "=&v"(g[1]), "=&v"(g[2]), "=&v"(g[3]) : "v"(src) : "memory");
            };
            auto total = [&] {
                asm volatile("" : "+v"(g[0]), "+v"(g[1]), "+v"(g[2]), "+v"(g[3]));
#pragma unroll
                for (int i = 0; i < 4; ++i) sm[i] = add1(add1(g[0][i], g[1][i]), add1(g[2][i], g[3][i]));
            };
            step1(9); step1(10);
            BARX(); gather(); issue1(15, 3);
            WAIT_V(22); BARX();
            __builtin_amdgcn_s_setprio(3);
            WAIT_V(9);
            total();
            if (__any(__builtin_isunordered(sm[1], sm[3]))) {
#pragma unroll 1
                for (unsigned spin = 0; spin < 400000u; ++spin) {
                    bool ok = true;
#pragma unroll
                    for (int i = 0; i < 4; ++i) ok &= __float_as_uint(g[i][1]) != 0xffffffffu && __float_as_uint(g[i][3]) != 0xffffffffu;
                    if (__all(ok)) break;
                    gather();
                    WAIT_V(0);
                    total();
                }
            }
            xq4_sum(sm);
            const bool odd = lane & 16;
            float mean;
            const float rs = rstd_of(odd ? sm[2] : sm[0], odd ? sm[3] : sm[1], 1.0f / 2304.0f, mean);
            asm volatile("ds_write_b32 %0, %1" :: "v"(tw), "v"(lane < 32 ? rs : -mean * rs) : "memory");
            __builtin_amdgcn_s_setprio(0);
            issue1B(16, 0);
            WAIT_L0();
        }
        BARX(); issue1A(16, 0); issueA(2 * pm, 1);
        auto kord2 = [&](int t) { const int j = kord(t - 2, 30); return ((j < 2 * (pm & 7) ? j : j + 2) + 2 * (pm & 8)) & 31; };
        unsigned seen;
        auto look = [&] { if (wid == 0) asm volatile("s_nop 4\n\tbuffer_load_dword %0, %1, %2, 0 offen sc1" : "+v"(seen) : "v"(fzi), "s"(RSRC(ws)) : "memory"); };
        auto need = [&](int n) {
            if (wid == 0) {
                asm volatile("" : "+v"(seen));
                if (__ballot(seen == 0u) & ((1ull << n) - 1ull)) flags_wait<0>(ws, lane < n ? fzi : fl + F_Z2 + (cg * 16 + pm) * 256 + pm * 16);
            }
        };
        auto issue = [&](int t, int sl) { const int k = kord2(t); issueB(k, sl); issueA(k, sl); };
        auto steps2 = [&](int t0, int t1) {
            int sl = (2 * t0 + 2) % 3 * G_STAGE, k = kord2(t0 + 3), ob = (k >> 1) * 589824 + (k & 1) * 18432;
            LOOP_ALIGN();
#pragma unroll 1
            for (int t = t0; t < t1; ++t) {
                WAIT_V(9); BARX();
                dma5<true>(lB + sl, rB, ob, vo);
                const int oa = pin_s(k * 128);
                const unsigned la = pin_s(lA + sl);
                k = pin_s(kord2(t + 4)); ob = pin_s((k >> 1) * 589824 + (k & 1) * 18432);
                sl = pin_s(sl == 0 ? 2 * G_STAGE : sl - G_STAGE);
                spA(oa, la);
            }
        };
        WAIT_V(13); BAR(); issue1(17, 2);
        WAIT_V(13); BARX();
        if (wid == 0) flag_raise(ws, fz);
        issueA(2 * pm + 1, 3); if (layer == 0) load5(4, 0);
        if (wid == 0) WAIT_IF(layer, 5, 9); else WAIT_IF(layer, 4, 8);
        BAR(); issueA(kord2(2), 0); if (layer == 0) load5(4, 1);
        BAR(); look(); issueA(kord2(3), 2);
        WAIT_V(4); need(3); BARX(); issueB(kord2(2), 0); issueB(kord2(3), 2); look();
        WAIT_IF(wid == 0, 6, 5); BAR(); issue(4, 1);
        WAIT_V(9); need(8);
        steps2(2, 11); look();
        WAIT_IF(wid == 0, 10, 9); BAR(); issue(14, 0);
        WAIT_V(9); need(16);
        steps2(12, 29);
        WAIT_V(9); BAR(); issue5(0); issue5(1); issue5(2);
        WAIT_V(15); BAR(); issue5(6); issue5(7);
        BAR(); __builtin_amdgcn_s_sleep(W3_SLEEP); issue5(3); put5(0, 9);
        unsigned vb;
        {
            vb = attn_vb(wid, lane);
            const unsigned fo = flag_out(fl + F_Y, 256, pm, cg, lane), fi = flag_in(fl + F_Y, 256, rh * 8 + c5, 16, lane);
            __builtin_amdgcn_s_setprio(3);
            WAIT_V(0); BARX();
            if (wid == 0) { flag_raise(ws, fo); flags_wait<Y_FIRST>(ws, fi); }
            __builtin_amdgcn_s_setprio(0);
            BARX();
        }
        put5(9, 13);
        BAR(); put5(13, 18);
        BAR(); put5(18, 24);
        BAR(); put5(24, 30);
        BAR(); put5(30, 36);
        BAR(); issue5(16); put5(36, 41);
        BAR(); issue5(17); put5(41, 45);
        WAIT_V(5); BAR();
        WAIT_V(0); BAR();
        if (layer == 0) { attn_issueB(lds, ws, hp, wid, vb, 0, 0); attn_issueB(lds, ws, hp, wid, vb, 1, 1); }
        BAR();
        if (layer == 0) {
            __builtin_amdgcn_s_setprio(3);
            const unsigned fo = flag_out(F_X2, 512, (rh >> 1) + wid, wid ? 16 + c5 : (rh & 1) * 8 + c5, wid == 0 || (wid == 1 && (rh & 1) && rh < 31) ? lane : 16);
            BARX();
            if (wid < 2) flag_raise(ws, fo);
        }
        TRACE_DUMP;
      }
        if (blockIdx.x == 0 && wid8 == 4) ST8(RSRC(ws), lane_id_fresh() ? 0x80000000u : (unsigned)OFF_EP, 0, ((u32x2){(unsigned)ep, (unsigned)(ep >> 32)}), 16);
        asm volatile("gm_code_end1:");
        return;
    }
#pragma unroll 1
  for (int layer = 0; layer < NET_LAYERS; ++layer) {
    NET_IDS
    TRACE_BEGIN;
    char* wl = ws + OFF_L0 + layer * SZ_L;
    const unsigned fl = layer * F_LAYER;
    const char* xin = wl + L_XB;
    char* y = wl + (layer ? L_Z2T : L_Y);
#undef STAMP_SEC
#define STAMP_SEC 0
    {
    STAMP_BEGIN;
    attn_consumer(lds, x0, wl, layer, pa, hp, wid, [&] { WAIT_V(0); prologue_done(lds, ws, ep, b, lane_id_fresh()); } STAMP_FWD);
    STAMP_END(1 + 5 * layer);
    }
#undef STAMP_SEC
#define STAMP_SEC 1
    STAMP_BEGIN;
    gmlp_body(lds, wid & 1, wid >> 1, pm, cg, wl + (layer ? L_Y : L_Z2T), y, bsp STAMP_FWD);
    {
        const int lane = lane_id_fresh(), r = lane & 15, q = lane >> 4;
        const int wk = wid >> 1, rt = rh * 4 + (wid & 1) * 2 + wk;
        const ProjOut pr = proj_out((char*)xin, (char*)xin, rt, c5, lane);
        const LAS char *pw[3], *ph[3];
#pragma unroll
        for (int i = 0; i < 3; ++i) {
            pw[i] = opaque(lds + 3 * i * K5_STAGE + wk * 4608 + lane * 16);
            ph[i] = opaque(lds + 3 * i * K5_STAGE + wk * 4608 + 4096 + q * 128 + (r & 7) * 16);
        }
        const auto rY = RSRC(y + (size_t)(rt & ~1) * 73728 + wk * 1024);
        const unsigned vy[2] = {pin((unsigned)(wk * 73728 + lane * 16)), pin((unsigned)((1 - wk) * 73728 + lane * 16))};
        int dk[4];
#pragma unroll
        for (int i = 0; i < 4; ++i) dk[i] = pin_s((kord5(4 + i) - 4 - i) * 4096);
        auto yoff = [&](int t) { return pin_s(t == 0 || t == 17 ? kord5(t) * 4096 : t * 4096 + dk[t & 3]); };
        bf16x8 af[5][4], wf[4][5];
        auto lda = [&](int so, int i) { return __builtin_bit_cast(bf16x8, __builtin_amdgcn_raw_buffer_load_b128(rY, vy[i & 1] + (i >> 1) * 2048, so, 16)); };
        auto rdw = [&](int t, int c, int n) {
            const int sl = (t + 6) & 7, o = (sl % 3) * K5_STAGE + c * 9216;
            wf[2 * (t & 1) + c][n] = n < 4 ? LDS_FRAG(pw[sl / 3] + o + n * 1024) : LDS_FRAG(ph[sl / 3] + o);
        };
        int so[18];
#pragma unroll
        for (int i = 0; i < 5; ++i) so[i] = yoff(i);
        so[17] = yoff(17);
        LAS char* xo = (LAS char*)opaque(lds + wid * 4608 + lane * 16);
        LAS char* xo4 = (LAS char*)opaque(lds + wid * 4608 + 4096 + (lane & 31) * 16);
        const LAS char* xi = opaque(lds + (wid ^ 2) * 4608 + lane * 16);
        const LAS char* xi4 = opaque(lds + (wid ^ 2) * 4608 + 4096 + (lane & 31) * 16);
        const ProjOut po = layer ? proj_out_final(out, rt, c5, lane) : proj_out(wl + SZ_L + L_XA, wl + SZ_L + L_STA, rt, c5, lane);
        asm volatile("" :: "s"(rY));
        SCHED0();
        WAIT_V(0);
        BARX();
        STAMP(11);
        const Res rx = proj_residual_load(pr);
#pragma unroll
        for (int n = 0; n < 10; ++n) rdw(0, n / 5, n % 5);
        f32x4 v[5], u[5];
        proj_residual(v, rx);
#pragma unroll
        for (int n = 0; n < 5; ++n) v[n] = pin(v[n]);
        WAIT_L(0);
        BARX();
        STAMP(12);
#pragma unroll
        for (int i = 0; i < K5_PRE; ++i) { af[i >> 2][i & 3] = lda(so[i >> 2], i & 3); SCHED0(); if (i >= PACE0 && i + 1 < K5_PRE) PACE; SCHED0(); }
        static_for<18>([&](auto tc) {
            constexpr int t = tc.value;
            bf16x8 (&a)[4] = af[t % 5];
#pragma unroll
            for (int h = 0; h < 4; ++h) {
                const int c = t < 17 ? h >> 1 : h & 1, m = t < 17 ? h & 1 : 1 - (h >> 1);
                if (t < 17 ? m == 0 : h < 2) { if (t == 17 && h == 1) WAIT_L(0); else WAIT_L(5); }
#pragma unroll
                for (int n = 0; n < 5; ++n) {
                    f32x4& e = m ? u[n] : v[n];
                    if (t == 0 && h == 1) MFMA_0(wf[0][n], a[1], e); else MFMA_T(wf[2 * (t & 1) + c][n], a[2 * c + m], e);
                    SCHED0();
                    if (t < 17 && m == 0) rdw(t + 1, c, n);
                    if (t < 14 && m == 1 && !(n & 1)) { const int i = y_load_at(6 * t + 3 * c + n / 2); if (i >= 0) af[(i >> 2) % 5][i & 3] = lda(so[i >> 2], i & 3); }
                    if (t < 12 && h == 3 && n == 3) so[t + 5] = yoff(t + 5);
                    if (t == 17 && h * 5 + n >= 13 && h * 5 + n < 18) { const int i = h * 5 + n - 13; *(LAS f32x4*)(i < 4 ? xo + i * 1024 : xo4) = u[i]; }
                    SCHED0();
                }
            }
            if ((t & 1) && t < 17) BAR();
            SCHED0();
        });
        {
            WAIT_L0(); BARX();
#pragma unroll
            for (int n = 0; n < 5; ++n) u[n] = *(const LAS f32x4*)(n < 4 ? xi + n * 1024 : xi4);
            SCHED0();
            asm volatile("s_nop 1" : "+v"(v[0]), "+v"(v[1]), "+v"(v[2]), "+v"(v[3]), "+v"(v[4]));
#pragma unroll
            for (int n = 0; n < 5; ++n) v[n] += u[n];
        }
        STAMP(13);
        if (layer) proj_store<true>(v, po);
        else { proj_store(v, po); WAIT_V(0); BARX(); }
    }
    STAMP_END(3 + 5 * layer);
    TRACE_DUMP;
  }
  asm volatile("gm_code_end2:");
}

}

#define STOP_CHECK(n)

extern "C" void kernel_launch(void* const* d_in, const int* in_sizes, int n_in, void* d_out, int out_size, void* d_ws, size_t ws_size,
                              hipStream_t stream) {
    (void)in_sizes; (void)n_in; (void)out_size; (void)ws_size;
    const u16* x = (const u16*)d_in[0];
    const u16* wqkv = (const u16*)d_in[1];
    const u16* wo = (const u16*)d_in[2];
    const u16* w_in = (const u16*)d_in[4];
    const u16* w_sp = (const u16*)d_in[5];
    const u16* b_sp = (const u16*)d_in[6];
    const u16* w_out = (const u16*)d_in[7];
    char* ws = (char*)d_ws;
    (void)hipFuncSetAttribute((const void*)k_net, hipFuncAttributeMaxDynamicSharedMemorySize, NET_LDS);
    k_net<<<256, 512, NET_LDS, stream>>>(ws, w_sp, b_sp, (char*)d_out, (const char*)x, (const char*)wqkv, wo, w_in, w_out DBG_LAUNCH(0));
}
```

```cpp
#include <hip/hip_runtime.h>
#include <stdint.h>

namespace {

typedef unsigned short u16;
typedef short bf16x8 __attribute__((ext_vector_type(8)));
typedef float f32x4 __attribute__((ext_vector_type(4)));
typedef float f32x2 __attribute__((ext_vector_type(2)));
typedef unsigned u32x4 __attribute__((ext_vector_type(4)));
typedef unsigned u32x2 __attribute__((ext_vector_type(2)));
typedef __bf16 bf16x2_t __attribute__((ext_vector_type(2)));
#define LAS __attribute__((address_space(3)))
#define DEVI __device__ __forceinline__

constexpr int S_ = 2048, D_ = 576, FH = 2304;

constexpr size_t SZ_TWQKV = (size_t)16 * 18 * 6 * 1024;
constexpr size_t SZ_TWT = (size_t)48 * 18 * 256;
constexpr size_t SZ_TWO = (size_t)8 * 18 * 4608;
constexpr size_t SZ_TWIN = (size_t)16 * 18 * 18 * 1024;
constexpr size_t SZ_TWOUT = (size_t)8 * 72 * 4608;
constexpr size_t SZ_X = (size_t)S_ * D_ * 2;
constexpr size_t SZ_Z = (size_t)S_ * FH * 2;
constexpr size_t SZ_ST = (size_t)S_ * 64;
constexpr size_t SZ_ZST = (size_t)16 * 16 * 128 * 8;
constexpr size_t OFF_TWQKV = 0;
constexpr size_t OFF_TWT = OFF_TWQKV + SZ_TWQKV;
constexpr size_t OFF_TWO = OFF_TWT + SZ_TWT;
constexpr size_t OFF_TWIN = OFF_TWO + SZ_TWO;
constexpr size_t OFF_TWOUT = OFF_TWIN + SZ_TWIN;
constexpr size_t OFF_CNT = OFF_TWOUT + SZ_TWOUT;
constexpr size_t SZ_CNT = 10 * 65536;
constexpr size_t OFF_FW = OFF_CNT + SZ_CNT;
constexpr size_t OFF_FL = OFF_FW + 262144;
constexpr size_t OFF_EP = OFF_FL + 4096;
constexpr size_t OFF_FL2 = OFF_EP + 4096;
constexpr size_t OFF_ZST = OFF_FL2 + 12288;
constexpr size_t OFF_L0 = OFF_ZST + 2 * SZ_ZST;
constexpr size_t L_XA = 0, L_STA = L_XA + SZ_X, L_O = L_STA + SZ_ST, L_XB = L_O + SZ_X, L_STB = L_XB + SZ_X, L_Z2T = L_STB + SZ_ST, L_Y = L_Z2T + SZ_Z;
constexpr size_t SZ_L = 0;
static_assert(OFF_L0 + L_Y + SZ_Z <= ((size_t)256 << 20), "workspace");
constexpr unsigned F_O = OFF_CNT, F_X1 = F_O + 65536, F_Z2 = F_O + 131072, F_Y = F_O + 196608, F_LAYER = 262144, F_X2 = F_O + 2 * F_LAYER;

DEVI unsigned pack2(float a, float b) { f32x2 v = {a, b}; bf16x2_t r = __builtin_convertvector(v, bf16x2_t); return __builtin_bit_cast(unsigned, r); }
DEVI float bflo(unsigned u) { return __uint_as_float(u << 16); }
DEVI float bfhi(unsigned u) { return __uint_as_float(u & 0xffff0000u); }
DEVI int slot2row(int sg) { return (sg & ~31) + 2 * (sg & 15) + ((sg >> 4) & 1); }
DEVI float fmax2(float a, float b) { return __builtin_amdgcn_fmed3f(a, b, __builtin_inff()); }
#define XQ4(OP, MID) \
    asm volatile("s_nop 1\n\tv_permlane32_swap_b32 %0, %1\n\tv_permlane32_swap_b32 %2, %3\n\t" OP " %0, %0, %1\n\t" OP " %2, %2, %3\n\t" \
                 "s_nop 1\n\tv_permlane16_swap_b32 %0, %2\n\t" OP " %0, %0, %2\n\t" MID \
                 "v_mov_b32 %2, %0\n\ts_nop 1\n\tv_permlane16_swap_b32 %0, %2\n\tv_mov_b32 %1, %0\n\tv_mov_b32 %3, %2\n\ts_nop 0\n\t" \
                 "v_permlane32_swap_b32 %0, %1\n\tv_permlane32_swap_b32 %2, %3" : "+v"(x[0]), "+v"(x[1]), "+v"(x[2]), "+v"(x[3]))
DEVI void xq4_max(float (&x)[4]) { XQ4("v_max_f32", ""); }
DEVI void xq4_sum(float (&x)[4]) { XQ4("v_add_f32", ""); }
DEVI void xq4_sum_rcp(float (&x)[4]) { XQ4("v_add_f32", "v_rcp_f32 %0, %0\n\ts_nop 0\n\t"); }
template <class T> DEVI T pin(T v) { asm volatile("" : "+v"(v)); return v; }
template <int V> struct IC { static constexpr int value = V; };
template <int N, int I = 0, class F> DEVI void static_for(F&& f) { if constexpr (I < N) { f(IC<I>{}); static_for<N, I + 1>(f); } }
template <class T> DEVI T* own_s(T* p) { T* r; asm volatile("s_mov_b64 %0, %1" : "=&s"(r) : "s"(p)); return r; }
DEVI int pin_s(int v) { asm volatile("" : "+s"(v)); return v; }
DEVI float xq_sum(float v) {
    auto a = __builtin_amdgcn_permlane32_swap(__float_as_uint(v), __float_as_uint(v), false, false);
    v = __uint_as_float(a[0]) + __uint_as_float(a[1]);
    auto b = __builtin_amdgcn_permlane16_swap(__float_as_uint(v), __float_as_uint(v), false, false);
    return __uint_as_float(b[0]) + __uint_as_float(b[1]);
}
#define RSRC(p) __builtin_amdgcn_make_buffer_rsrc((void*)(p), 0, 0x7fffffff, 0x00020000)
#define ST16(rs, vo, so, v, aux) __builtin_amdgcn_raw_buffer_store_b128((v), (rs), (vo) + (so), 0, aux)
#define ST8(rs, vo, so, v, aux) __builtin_amdgcn_raw_buffer_store_b64((v), (rs), (vo), (so), aux)
#define WAIT_V(n) asm volatile("s_waitcnt vmcnt(%0)" ::"i"(n) : "memory")
#define WAIT_L0() asm volatile("s_waitcnt lgkmcnt(0)" ::: "memory")
#define BAR() do { asm volatile("" ::: "memory"); __builtin_amdgcn_s_barrier(); asm volatile("" ::: "memory"); } while (0)
#define BARX() do { __builtin_amdgcn_sched_barrier(0); BAR(); __builtin_amdgcn_sched_barrier(0); } while (0)
#define LOOP_ALIGN() asm volatile(".p2align 6")
#define SCHED0() __builtin_amdgcn_sched_barrier(0)
#define MFMA(a, b, c) __builtin_amdgcn_mfma_f32_16x16x32_bf16((a), (b), (c), 0, 0, 0)
#define MFMA_T(a, b, c) asm volatile("v_mfma_f32_16x16x32_bf16 %0, %1, %2, %0" : "+v"(c) : "v"(a), "v"(b))
#define MFMA_0(a, b, c) asm volatile("v_mfma_f32_16x16x32_bf16 %0, %1, %2, 0" : "=&v"(c) : "v"(a), "v"(b))
#define WAIT_L(n) __builtin_amdgcn_s_waitcnt(0xC07F | ((n) << 8))
#define LDS_FRAG(p) (*(const LAS bf16x8*)(p))
DEVI int lane_id_fresh() { int z = 0; asm volatile("" : "+v"(z)); return __builtin_amdgcn_mbcnt_hi(~0u, __builtin_amdgcn_mbcnt_lo(~0u, z)); }
DEVI const LAS char* opaque(const LAS char* p) { asm volatile("" : "+v"(p)); return p; }
#define DBG_ARG
#define DBG_LAUNCH(n)
#define STAMP(i)
#define STAMP_DECL
#define STAMP_BEGIN
#define STAMP_END(n)
#define STAMP_PARAM
#define STAMP_FWD

#define PST_PARAM
#define PST_FWD
#define PST_DECL
#define PST(i)
#define PST_DUMP(c, k)
#define TRACE_ATTR
#define TRACE_BEGIN
#define TRACE_DUMP

DEVI float add1(float a, float b) { float r; asm("v_add_f32 %0, %1, %2" : "=v"(r) : "v"(a), "v"(b)); return r; }
DEVI float rstd_of(float s, float q, float inv_n, float& mean) {
    float var;
    asm("v_mul_f32 %0, %2, %3\n\tv_mul_f32 %1, %2, %4\n\tv_fma_f32 %1, -%0, %0, %1" : "=&v"(mean), "=&v"(var) : "s"(inv_n), "v"(s), "v"(q));
    return __builtin_amdgcn_rsqf(fmax2(var, 0.f) + 1e-5f);
}
DEVI float ln_rstd(const f32x4 p) {
    float s, q, mean;
    asm("v_add_f32 %0, %2, %4\n\tv_add_f32 %1, %3, %5\n\ts_nop 0\n\t"
        "v_add_f32_dpp %0, %0, %0 quad_perm:[1,0,3,2] row_mask:0xf bank_mask:0xf\n\tv_add_f32_dpp %1, %1, %1 quad_perm:[1,0,3,2] row_mask:0xf bank_mask:0xf\n\ts_nop 0\n\t"
        "v_add_f32_dpp %0, %0, %0 quad_perm:[2,3,0,1] row_mask:0xf bank_mask:0xf\n\tv_add_f32_dpp %1, %1, %1 quad_perm:[2,3,0,1] row_mask:0xf bank_mask:0xf"
        : "=&v"(s), "=&v"(q) : "v"(p[0]), "v"(p[1]), "v"(p[2]), "v"(p[3]));
    return rstd_of(s, q, 1.0f / 576.0f, mean);
}

struct Res { u32x4 a, c; u32x2 e; };
DEVI Res proj_residual_load(const char* xin, int rt, int cg, int lane) {
    const auto rs = RSRC(xin + (size_t)rt * 18432 + cg * 2304);
    Res x;
    x.a = __builtin_amdgcn_raw_buffer_load_b128(rs, lane * 16, 0, 16); x.c = __builtin_amdgcn_raw_buffer_load_b128(rs, lane * 16 + 1024, 0, 16);
    x.e = __builtin_amdgcn_raw_buffer_load_b64(rs, 2048 + (lane & 15) * 16 + ((lane >> 4) & 1) * 8, 0, 16);
    return x;
}
DEVI void proj_residual(f32x4 (&v)[5], const Res& x) {
    v[0] = (f32x4){bflo(x.a.x), bfhi(x.a.x), bflo(x.a.y), bfhi(x.a.y)}; v[1] = (f32x4){bflo(x.a.z), bfhi(x.a.z), bflo(x.a.w), bfhi(x.a.w)};
    v[2] = (f32x4){bflo(x.c.x), bfhi(x.c.x), bflo(x.c.y), bfhi(x.c.y)}; v[3] = (f32x4){bflo(x.c.z), bfhi(x.c.z), bflo(x.c.w), bfhi(x.c.w)};
    v[4] = (f32x4){bflo(x.e.x), bfhi(x.e.x), bflo(x.e.y), bfhi(x.e.y)};
}
struct ProjOut { __amdgpu_buffer_rsrc_t x, st; unsigned v0, v1, v2, vs; };
DEVI ProjOut proj_out(char* xout, char* stout, int rt, int cg, int lane) {
    const int r = lane & 15, q = lane >> 4;
    ProjOut p = {RSRC(xout + (size_t)rt * 18432 + cg * 2304), RSRC(stout + (size_t)rt * 1024 + cg * 8),
                 pin((unsigned)lane * 16u), pin((unsigned)lane * 16u + 1024u), pin((unsigned)(2048 + r * 16 + (q & 1) * 8)), pin((unsigned)(r * 64 + (q >> 1) * 4))};
    asm volatile("" :: "s"(p.x), "s"(p.st));
    return p;
}
DEVI ProjOut proj_out_final(char* out, int rt, int cg, int lane) {
    const int q = lane >> 4;
    const unsigned o = (unsigned)(slot2row(rt * 16 + (lane & 15)) * 1152 + cg * 144);
    ProjOut p = {RSRC(out), RSRC(out), pin(o + q * 16), pin(o + 64 + q * 16), pin(o + 128 + (q & 1) * 8), 0u};
    return p;
}
template <bool FINAL = false>
DEVI void proj_store(f32x4 (&v)[5], const ProjOut& p) {
    ST16(p.x, p.v0, 0, ((u32x4){pack2(v[0][0], v[0][1]), pack2(v[0][2], v[0][3]), pack2(v[1][0], v[1][1]), pack2(v[1][2], v[1][3])}), FINAL ? 0 : 16);
    SCHED0();
    asm volatile("" : "+v"(v[2]), "+v"(v[3]), "+v"(v[4]));
    ST16(p.x, p.v1, 0, ((u32x4){pack2(v[2][0], v[2][1]), pack2(v[2][2], v[2][3]), pack2(v[3][0], v[3][1]), pack2(v[3][2], v[3][3])}), FINAL ? 0 : 16);
    SCHED0();
    ST8(p.x, p.v2, 0, ((u32x2){pack2(v[4][0], v[4][1]), pack2(v[4][2], v[4][3])}), FINAL ? 0 : 16);
    SCHED0();
    if (!FINAL) {
        const f32x4 s4 = ((v[0] + v[1]) + (v[2] + v[3])) + v[4] * 0.5f;
        const f32x4 q4 = ((v[0] * v[0] + v[1] * v[1]) + (v[2] * v[2] + v[3] * v[3])) + v[4] * v[4] * 0.5f;
        auto x = __builtin_amdgcn_permlane32_swap(__float_as_uint((s4[0] + s4[2]) + (s4[1] + s4[3])), __float_as_uint((q4[0] + q4[2]) + (q4[1] + q4[3])), false, false);
        const float t = __uint_as_float(x[0]) + __uint_as_float(x[1]);
        auto y = __builtin_amdgcn_permlane16_swap(__float_as_uint(t), __float_as_uint(t), false, false);
        __builtin_amdgcn_raw_buffer_store_b32(__float_as_uint(__uint_as_float(y[0]) + __uint_as_float(y[1])), p.st, p.vs, 0, 16);
        SCHED0();
    }
}

DEVI void side_load(f32x4 (&d)[3], const char* p, const char* p2) {
    asm volatile("global_load_dwordx4 %0, %3, off sc1\n\tglobal_load_dwordx4 %1, %3, off offset:1024 sc1\n\tglobal_load_dwordx4 %2, %4, off sc1"
                 : "=&v"(d[0]), "=&v"(d[1]), "=&v"(d[2]) : "v"(p), "v"(p2) : "memory");
}
DEVI void side_fence(f32x4 (&d)[3]) { asm volatile("" : "+v"(d[0]), "+v"(d[1]), "+v"(d[2])); }
DEVI void lds_word_set(LAS char* p, unsigned v) { asm volatile("ds_write_b32 %0, %1" :: "v"((unsigned)(size_t)p), "v"(v) : "memory"); }
#define DMA_I(v, off, sc) "buffer_load_dwordx4 " v ", %1, %2 offen offset:" off sc " lds\n\t"
#define DMA5(sc) asm volatile("s_mov_b32 m0, %0\n\ts_mov_b32 exec_hi, 0\n\ts_nop 2\n\t" DMA_I("%3", "0", sc) "s_mov_b32 exec_hi, -1\n\t" \
    DMA_I("%3", "512", sc) DMA_I("%3", "1536", sc) DMA_I("%3", "2560", sc) DMA_I("%3", "3584", sc) :: "s"(la), "s"(rs), "s"(so), "v"(vo) : "memory")
#define DMA4(sc) asm volatile("s_mov_b32 m0, %0\n\ts_nop 3\n\t" DMA_I("%3", "0", sc) DMA_I("%4", "1024", sc) DMA_I("%5", "2048", sc) DMA_I("%6", "3072", sc) \
    :: "s"(la), "s"(rs), "s"(so), "v"(v0), "v"(v1), "v"(v2), "v"(v3) : "memory")
template <bool SC1> DEVI void dma5(unsigned la, __amdgpu_buffer_rsrc_t rs, int so, unsigned vo) { if (SC1) DMA5(" sc1"); else DMA5(""); }
template <bool SC1> DEVI void dma4(unsigned la, __amdgpu_buffer_rsrc_t rs, int so, unsigned v0, unsigned v1, unsigned v2, unsigned v3) { if (SC1) DMA4(" sc1"); else DMA4(""); }
#define DMA2(sc) asm volatile("s_mov_b32 m0, %0\n\ts_nop 3\n\t" DMA_I("%3", "0", sc) DMA_I("%4", "1024", sc) :: "s"(la), "s"(rs), "s"(so), "v"(v0), "v"(v1) : "memory")
template <bool SC1> DEVI void dma2(unsigned la, __amdgpu_buffer_rsrc_t rs, int so, unsigned v0, unsigned v1) { if (SC1) DMA2(" sc1"); else DMA2(""); }
DEVI void dma1(unsigned la, __amdgpu_buffer_rsrc_t rs, int so, unsigned v0) { asm volatile("s_mov_b32 m0, %0\n\ts_nop 3\n\t" DMA_I("%3", "0", "") :: "s"(la), "s"(rs), "s"(so), "v"(v0) : "memory"); }
#define WAIT_IF(c, a, b) do { if (c) WAIT_V(a); else WAIT_V(b); } while (0)
template <int T0, int T1, int P, int S0 = 0, class ISSUE>
DEVI void ring_steps(ISSUE&& issue) {
    int slot = (T0 + S0) % 3;
    LOOP_ALIGN();
#pragma unroll 1
    for (int t = T0; t < T1; ++t) {
        WAIT_V(P); BAR();
        issue(t + 3, slot);
        slot = slot == 2 ? 0 : slot + 1;
    }
}

DEVI unsigned flag_out(unsigned use, int pitch, int group, int writer, int lane) { return pin(lane < 16 ? use + (group * 16 + lane) * pitch + writer * 16 : 0x80000000u); }
DEVI unsigned flag_in(unsigned use, int pitch, int reader, int n, int lane) { return pin(use + reader * pitch + (lane < n ? lane : 0) * 16); }
DEVI void flag_raise(const char* ws, unsigned vo) { ST16(RSRC(ws), vo, 0, ((u32x4){1u, 1u, 1u, 1u}), 16); SCHED0(); }
template <int FIRST>
DEVI void flags_wait(const char* ws, unsigned vo) {
    unsigned v, n = 400000u;
    asm volatile("s_nop 4\n\ts_sleep %4\n"
                 "1:\n\tbuffer_load_dword %0, %2, %3, 0 offen sc1\n\ts_waitcnt vmcnt(0)\n\tv_cmp_eq_u32 vcc, 0, %0\n\ts_cbranch_vccz 2f\n\t"
                 "s_sleep 1\n\ts_sub_u32 %1, %1, 1\n\ts_cbranch_scc0 1b\n2:"
                 : "=&v"(v), "+s"(n) : "v"(vo), "s"(RSRC(ws)), "i"(FIRST) : "vcc", "scc", "memory");
    SCHED0();
}
template <int FIRST>
DEVI void epoch_wait(const char* ws, unsigned vo, unsigned long long ep) {
    unsigned long long v; unsigned n = 400000u;
    asm volatile("s_nop 4\n\ts_sleep %5\n"
                 "1:\n\tbuffer_load_dwordx2 %0, %2, %3, 0 offen sc1\n\ts_waitcnt vmcnt(0)\n\tv_cmp_ne_u64 vcc, %4, %0\n\ts_cbranch_vccz 2f\n\t"
                 "s_sleep 1\n\ts_sub_u32 %1, %1, 1\n\ts_cbranch_scc0 1b\n2:"
                 : "=&v"(v), "+s"(n) : "v"(vo), "s"(RSRC(ws)), "s"(ep), "i"(FIRST) : "vcc", "scc", "memory");
    SCHED0();
}
DEVI unsigned lds_arrive(LAS char* p) {
    unsigned v;
    asm volatile("s_mov_b64 exec, 1\n\tds_add_rtn_u32 %0, %1, %2\n\ts_mov_b64 exec, -1\n\ts_waitcnt lgkmcnt(0)" : "=&v"(v) : "v"((unsigned)(size_t)p), "v"(1u) : "memory");
    return __builtin_amdgcn_readfirstlane(v);
}
DEVI void lds_count_wait(LAS char* p, unsigned n) {
#pragma unroll 1
    for (unsigned spin = 0; spin < 40000000u; ++spin) {
        unsigned v;
        asm volatile("ds_read_b32 %0, %1\n\ts_waitcnt lgkmcnt(0)" : "=v"(v) : "v"((unsigned)(size_t)p) : "memory");
        if (__builtin_amdgcn_readfirstlane(v) >= n) break;
    }
}

constexpr int K1_STAGE = 32768;
constexpr int WO_OFF = 69632;
constexpr int G_OWN = 139264;
constexpr int G_PART = G_OWN + 18432;
constexpr int G_TAB = G_PART + 2048;
constexpr int K1_SCR = G_TAB + 1536;
constexpr int G_WORD = K1_SCR + 576;
constexpr int NET_LDS = G_WORD + 64;

#define WO_PRE 9
#define WO_W 33
#define REL_PRIO 0
#define W_FIRST 2
#define I_SLEEP 30
#define OJOB_SLEEP 20
#define PACE0 1
#define PACE __builtin_amdgcn_s_sleep(1)
#define POLLW 0
#define WO_EV 4
#define WIN_EARLY 0
#define O_FIRST 10
#define X1_FIRST 6
#define X2_FIRST 6
#define Y_FIRST 10
#define Z2_FIRST 10
DEVI int win_strip(int b) { return ((b - 112) & 7) * 18 + ((b - 112) >> 3); }
DEVI int win_third(int b, int w) { return b & 8 ? 3 - w : w - 1; }
DEVI unsigned attn_vb(int wid, int lane) {
    const int c = lane & 15;
    return pin((unsigned)(wid < 3 ? lane * 16 : (c < 8 ? (c >> 1) & 1 : 2) * 4608 + (lane >> 4) * 64 + (c < 8 ? (c >> 2) * 2 + (c & 1) : c & 3) * 16));
}
DEVI void attn_issueB(LAS char* lds, const char* ws, int hp, int wid, unsigned vb, int s, int slot) {
    const unsigned la = (unsigned)(size_t)lds + slot * K1_STAGE + 18432 + wid * 4096;
    if (wid < 3) dma4<true>(la, RSRC(ws), (int)OFF_TWQKV + hp * (18 * 6144) + wid * 4096 + s * 12288, vb, vb, vb, vb);
    else asm volatile("s_mov_b32 m0, %0\n\ts_nop 3\n\tbuffer_load_dwordx4 %4, %1, %2 offen sc1 lds\n\tbuffer_load_dwordx4 %4, %1, %3 offen offset:1024 sc1 lds"
                      :: "s"(la), "s"(RSRC(ws)), "s"((int)OFF_TWT + hp * 13824 + s * 512), "s"((int)OFF_TWT + hp * 13824 + s * 512 + 256 - 1024), "v"(vb) : "memory");
}
struct AttnX { __amdgpu_buffer_rsrc_t rs; unsigned v[4], vh[2]; int oA, oh, step; };
DEVI AttnX attn_x(const char* x0, const char* xa, int layer, int pm, int wid, int lane) {
    const int l3 = lane >> 3, pmh = pm > 0 ? pm : 1;
    const unsigned v = layer ? lane * 16 : 2 * l3 * 1152 + ((lane & 7) ^ l3) * 16 + 4096, d1 = layer ? 0 : 16 * 1152 - 1024, d2 = layer ? 16384 : 1152 - 2048;
    const unsigned vh = layer ? (l3 & 1) * 18432 + (lane >> 4) * 256 + (8 + (lane & 7)) * 16 : v + 16 * 1152;
    return {RSRC(layer ? xa : x0 - 4096), {pin(v), pin(v + d1), pin(v + d2), pin(v + d1 + d2)}, {pin(vh), pin(vh + (layer ? 0 : 1152 - 1024))},
            (pm * 8 + 2 * wid) * 18432, (pmh * 8 - 2) * 18432, layer ? 2048 : 128};
}
DEVI void attn_issueA(LAS char* lds, const AttnX& a, int wid, int s, int slot) {
    const unsigned la = (unsigned)(size_t)lds + slot * K1_STAGE;
    if (wid == 3) dma2<true>(la, a.rs, a.oh + s * a.step, a.vh[0], a.vh[1]);
    dma4<true>(la + 2048 + wid * 4096, a.rs, a.oA + s * a.step, a.v[0], a.v[1], a.v[2], a.v[3]);
}
template <class DONE, class NEXT>
DEVI void attn_producer(LAS char* lds, const char* ws, const char* x0, const char* wl, unsigned fl, unsigned long long ep, int layer, int pm, int hp, int wid, DONE&& done, NEXT&& next) {
    const int lane = lane_id_fresh();
    const int pmh = pm > 0 ? pm : 1;
    const unsigned vo = lane * 16;
    const AttnX ax = attn_x(x0, wl + L_XA, layer, pm, wid, lane);
    const unsigned vb = attn_vb(wid, lane), fd = pin((unsigned)OFF_FL + wid * 1024 + vo);
    auto issueB = [&](int s, int slot) { attn_issueB(lds, ws, hp, wid, vb, s, slot); };
    auto issue = [&](int s, int slot) { attn_issueA(lds, ax, wid, s, slot); issueB(s, slot); };
    const int hr = lane >> 2;
    const char* ps = pin(wl + L_STA + (size_t)(pm * 128 + wid * 32) * 64 + lane * 16);
    const char* psh = pin(wl + L_STA + (size_t)((pmh * 8 - 2 + (hr >> 3)) * 16 + 8 + (hr & 7)) * 64 + (lane & 3) * 16);
    if (layer == 0) {
        const unsigned fi = pin((unsigned)OFF_FW + (pm * 16 + hp) * 1024 + (lane < 6 ? lane : 0) * 128);
        done();
        if (wid == 0) epoch_wait<W_FIRST>(ws, fi, ep);
        BARX();
        issueB(0, 0); issueB(1, 1); issue(2, 2);
        WAIT_IF(wid == 3, 10, 12);
    } else {
        issueB(2, 2);
        const unsigned fi = flag_in(F_X2, 512, pm * 16 + hp, pm > 0 ? 24 : 16, lane);
        if (wid == 0) flags_wait<X2_FIRST>(ws, fi);
        BARX();
#pragma unroll
        for (int i = 0; i < 3; ++i) attn_issueA(lds, ax, wid, i, i);
        WAIT_IF(wid == 3, 12, 8);
    }
    __builtin_amdgcn_s_setprio(0);
    BAR();
    if (layer) WAIT_IF(wid == 3, 6, 4);
    next(-1);
    ring_steps<0, 4, 8>(issue);
    {
        unsigned long long done;
        if (layer == 0) asm volatile("s_nop 4\n\tbuffer_load_dwordx2 %0, %1, %2, 0 offen sc1" : "+v"(done) : "v"(fd), "s"(RSRC(ws)) : "memory");
        WAIT_IF(layer, 8, 9); BAR(); issue(7, 1);
        WAIT_V(8);
        if (layer == 0) { asm volatile("" : "+v"(done)); if (__any(done != ep)) epoch_wait<0>(ws, fd, ep); }
        BAR(); issue(8, 2);
    }
    const auto rW = RSRC(ws + OFF_TWO + (size_t)(hp >> 1) * 82944 + wid * 4096);
    auto issueW = [&](int i) { dma4<true>((unsigned)(size_t)lds + WO_OFF + (i * 4 + wid) * 4096, rW, i * 16384, vo, vo, vo, vo); };
    {
        f32x4 sp[3];
        side_load(sp, ps, psh);
        WAIT_V(11); BAR(); issueW(2); issueW(3);
        WAIT_V(8); BARX();
        side_fence(sp);
#pragma unroll
        for (int i = 0; i < 3; ++i) lds_word_set(lds + K1_SCR + (i < 2 ? 16 + wid * 32 + i * 16 + hr : hr) * 4, __float_as_uint(ln_rstd(sp[i])));
        WAIT_L0();
    }
    BAR();
    next(0);
    issueW(0); issueW(1); issueW(4);
    if (wid == 0) dma2<true>((unsigned)(size_t)lds + WO_OFF + 80896, rW, 80896, vo, vo);
    next(1);
    const unsigned fo = flag_out(fl + F_O, 256, pm, hp, lane), fi = flag_in(fl + F_O, 256, pm * 16 + hp, 16, lane);
    WAIT_V(0);
    if (wid == POLLW) __builtin_amdgcn_s_setprio(3);
    BARX();
    next(2);
    if (wid == POLLW) { flag_raise(ws, fo); flags_wait<O_FIRST>(ws, fi); }
    __builtin_amdgcn_s_setprio(0);
    BARX();
}

#define STAMP_SEC 0
template <class DONE>
DEVI void attn_consumer(LAS char* lds, const char* x0, char* wl, int layer, int pm, int hp, int wid, DONE&& done STAMP_PARAM) {
    const int lane = lane_id_fresh(), r = lane & 15, q = lane >> 4;
    const char* xa = wl + L_XA;
    char* o = wl + L_O;
    const int T = wid & 1, h = wid >> 1;
    const LAS char *ph[2], *pa[2];
#pragma unroll
    for (int kk = 0; kk < 2; ++kk) {
        const int sw = ((4 * kk + q) ^ (r & 7)) * 16;
        ph[kk] = opaque(lds + (layer ? kk * 1024 + q * 256 + (r ^ (T ? 0 : 8)) * 16 : (r ^ (T ? 0 : 8)) * 128 + sw));
        pa[kk] = opaque(lds + T * 2048 + (layer ? kk * 1024 + lane * 16 : r * 128 + sw));
    }
    const LAS char* pb = lds + h * 3072 + lane * 16;
    const LAS char* pt = lds + lane * 16;
    f32x4 acc[5][4];
#pragma unroll
    for (int m = 0; m < 5; ++m)
#pragma unroll
        for (int n = 0; n < 4; ++n) if (m + n) acc[m][n] = pin((f32x4){0.f, 0.f, 0.f, 0.f});
    bf16x8 fa[2][5], fb[2][4];
    auto rd1 = [&](int kk, int i) {
        if (i < 3) fb[kk][i] = LDS_FRAG(pb + 18432 + kk * 6144 + i * 1024);
        else if (i == 3) fb[kk][3] = LDS_FRAG(pt + 30720 + kk * 1024);
        else if (i == 4) fa[kk][0] = LDS_FRAG(ph[kk]);
        else if (i < 9) fa[kk][i - 4] = LDS_FRAG(pa[kk] + 2048 + (i - 5) * 4096);
    };
    f32x4 mb, mb0;
#pragma unroll
    for (int j = 0; j < 4; ++j) {
        const bool ok = ((r - 4 * q - j) & 15) < 8;
        mb[j] = ok ? 0.f : -__builtin_inff();
        mb0[j] = (ok && (pm > 0 || 4 * q + j <= r)) ? 0.f : -__builtin_inff();
    }
    mb = pin(mb); mb0 = pin(mb0);
    const unsigned mp0 = pin((4 * q > r ? 0xffffu : 0u) | (4 * q + 1 > r ? 0xffff0000u : 0u)), mp1 = pin((4 * q + 2 > r ? 0xffffu : 0u) | (4 * q + 3 > r ? 0xffff0000u : 0u));
    const unsigned qtm = pin(q == h ? 0xffffffffu : 0u);
    const unsigned vom = pin((unsigned)(r * 16 + hp * 1024 + (2 * h + (q >> 1)) * 256 + (q & 1) * 8)), vot = pin((unsigned)(r * 16 + 16384 + (hp >> 1) * 256 + (hp & 1) * 8 + h * 4));
    const LAS char* tk0 = opaque(lds + K1_SCR + ((q * 4) ^ (T ? 0 : 8)) * 4);
    const LAS char* tk = opaque(lds + K1_SCR + ((1 + T) * 16 + q * 4) * 4);
    const LAS char* tq = opaque(lds + K1_SCR + ((1 + T) * 16 + r) * 4);
    pb = opaque(pb); pt = opaque(pt);
    STAMP(2);
    const int rt = pm * 8 + (hp & 1) * 4 + wid, cg = hp >> 1;
    const ProjOut pr = layer ? proj_out((char*)xa, (char*)xa, rt, cg, lane) : proj_out_final((char*)x0, rt, cg, lane);
    BAR();
    if (layer == 0) done();
    BARX();
    STAMP(3);
#pragma unroll
    for (int i = 0; i < 9; ++i) rd1(0, i);
    SCHED0();
    const Res rx = {__builtin_amdgcn_raw_buffer_load_b128(pr.x, pr.v0, 0, 16), __builtin_amdgcn_raw_buffer_load_b128(pr.x, pr.v1, 0, 16), __builtin_amdgcn_raw_buffer_load_b64(pr.x, pr.v2, 0, 16)};
    SCHED0();
    int slot = 0, left = 9;
    LOOP_ALIGN();
#pragma unroll 1
    do {
        int d;
#pragma unroll
        for (int kk = 0; kk < 2; ++kk) {
            WAIT_L(0);
            if (kk) BAR();
            SCHED0();
#pragma unroll
            for (int i = 1; i < 20; ++i) {
                const int m = i >> 2, n = i & 3;
                const bf16x8 &A = fa[kk][m], &B = fb[kk][n];
                acc[m][n] = n != 2 ? MFMA(B, A, acc[m][n]) : MFMA(A, B, acc[m][n]);
                SCHED0();
                if (i <= 9) rd1(kk ^ 1, i - 1);
                if (kk == 0) {
                    if (i == 10) d = pin_s(slot == 2 ? -2 * K1_STAGE : K1_STAGE);
                    if (i == 11 || i == 12) ph[i & 1] = opaque(ph[i & 1] + d);
                    if (i == 13 || i == 14) pa[i & 1] = opaque(pa[i & 1] + d);
                    if (i == 15) pb = opaque(pb + d);
                    if (i == 16) pt = opaque(pt + d);
                } else {
                    if (i == 11) slot = pin_s(slot == 2 ? 0 : slot + 1);
                    if (i == 13) left = pin_s(left - 1);
                }
                SCHED0();
            }
        }
    } while (left);
    STAMP(4);
    u32x2 ts[5], tf[5];
    {
        LAS char* xb = lds + wid * 320;
#pragma unroll
        for (int m = 0; m < 5; ++m) ts[m] = (u32x2){pack2(acc[m][3][0], acc[m][3][1]), pack2(acc[m][3][2], acc[m][3][3])};
        if (q == 2) {
#pragma unroll
            for (int m = 0; m < 5; ++m) {
                const unsigned w = h ? ts[m].y : ts[m].x;
                *(LAS u16*)(xb + m * 64 + r * 2) = (u16)w; *(LAS u16*)(xb + m * 64 + 32 + r * 2) = (u16)(w >> 16);
            }
        }
        asm volatile("" ::: "memory");
#pragma unroll
        for (int m = 0; m < 5; ++m) tf[m] = *(const LAS u32x2*)(xb + m * 64 + (r & 1) * 32 + q * 8);
    }
    {
        f32x4 rk[5]; float rq[4];
        rk[0] = *(const LAS f32x4*)(tk0);
#pragma unroll
        for (int g = 0; g < 4; ++g) { rk[g + 1] = *(const LAS f32x4*)(tk + g * 128); rq[g] = *(const LAS float*)(tq + g * 128); }
        const f32x4 zero = {0.f, 0.f, 0.f, 0.f};
        u32x4 kf[5];
#pragma unroll
        for (int m = 0; m < 5; ++m) kf[m] = (u32x4){pack2(acc[m][1][0], acc[m][1][1]), pack2(acc[m][1][2], acc[m][1][3]), ts[m].y, 0u};
        f32x4 sp[4], sc[4];
#pragma unroll
        for (int g = 0; g < 4; ++g) {
            const u32x4 qf = {pack2(acc[g + 1][0][0], acc[g + 1][0][1]), pack2(acc[g + 1][0][2], acc[g + 1][0][3]), ts[g + 1].x & qtm, 0u};
            sp[g] = MFMA(__builtin_bit_cast(bf16x8, kf[g]), __builtin_bit_cast(bf16x8, qf), zero);
            sc[g] = MFMA(__builtin_bit_cast(bf16x8, kf[g + 1]), __builtin_bit_cast(bf16x8, qf), zero);
        }
        u32x2 vf[5];
#pragma unroll
        for (int m = 0; m < 5; ++m) vf[m] = (u32x2){pack2(acc[m][2][0], acc[m][2][1]), pack2(acc[m][2][2], acc[m][2][3])};
        f32x4 rs[4], t[4]; float mx[4], sm[4];
#pragma unroll
        for (int g = 0; g < 4; ++g) {
#pragma unroll
            for (int j = 0; j < 4; ++j) { const bool pv = 4 * q + j > r; rs[g][j] = pv ? rk[g][j] : rk[g + 1][j]; t[g][j] = pv ? sp[g][j] : sc[g][j]; }
            t[g] = (t[g] * rs[g]) * (rq[g] * (0.23570226039551584f * 1.4426950408889634f)) + (g == 0 ? mb0 : mb);
            mx[g] = fmaxf(fmaxf(t[g][0], t[g][1]), fmaxf(t[g][2], t[g][3]));
        }
        xq4_max(mx);
#pragma unroll
        for (int g = 0; g < 4; ++g) {
            t[g] = t[g] - mx[g];
#pragma unroll
            for (int j = 0; j < 4; ++j) t[g][j] = __builtin_amdgcn_exp2f(t[g][j]);
            sm[g] = (t[g][0] + t[g][2]) + (t[g][1] + t[g][3]);
        }
        f32x4 om[4], ot[4];
#pragma unroll
        for (int g = 0; g < 4; ++g) {
            const f32x4 p = t[g] * rs[g];
            const unsigned p01 = pack2(p[0], p[1]), p23 = pack2(p[2], p[3]);
            const u32x4 pf = {p01 & mp0, p23 & mp1, p01 & ~mp0, p23 & ~mp1};
            om[g] = MFMA(__builtin_bit_cast(bf16x8, ((u32x4){vf[g].x, vf[g].y, vf[g + 1].x, vf[g + 1].y})), __builtin_bit_cast(bf16x8, pf), zero);
            ot[g] = MFMA(__builtin_bit_cast(bf16x8, ((u32x4){tf[g].x, tf[g].y, tf[g + 1].x, tf[g + 1].y})), __builtin_bit_cast(bf16x8, pf), zero);
        }
        xq4_sum_rcp(sm);
        const auto ro = RSRC(o + (size_t)(pm * 8 + T) * 18432);
        unsigned tw2[4];
#pragma unroll
        for (int g = 0; g < 4; ++g) {
            const f32x4 v = om[g] * sm[g];
            __builtin_amdgcn_raw_buffer_store_b64((u32x2){pack2(v[0], v[1]), pack2(v[2], v[3])}, ro, vom, g * 36864, 16);
            const f32x2 w = ot[g].xy * sm[g];
            tw2[g] = pack2(w.x, w.y);
        }
        if (q == 2) {
#pragma unroll
            for (int g = 0; g < 4; ++g) __builtin_amdgcn_raw_buffer_store_b32(tw2[g], ro, vot, g * 36864, 16);
        }
    }
    STAMP(5);
    {
        WAIT_V(0); BARX();
        STAMP(6);
        f32x4 v[5];
        proj_residual(v, rx);
        const LAS char* pw = opaque(lds + WO_OFF + lane * 16);
        const LAS char* pw2 = opaque(lds + WO_OFF + 41472 + lane * 16);
        const LAS char* ph2 = opaque(lds + WO_OFF + 4096 + q * 128 + (r & 7) * 16);
        const LAS char* ph3 = opaque(lds + WO_OFF + 41472 + 4096 + q * 128 + (r & 7) * 16);
        bf16x8 wf[90];
        auto rdw = [&](int i) {
            const int kc = i / 5, n = i % 5;
            wf[i] = n < 4 ? LDS_FRAG((kc < 9 ? pw : pw2) + (kc % 9) * 4608 + n * 1024) : LDS_FRAG((kc < 9 ? ph2 : ph3) + (kc % 9) * 4608);
        };
#pragma unroll
        for (int i = 0; i < WO_W; ++i) rdw(i);
#pragma unroll
        for (int n = 0; n < 5; ++n) v[n] = pin(v[n]);
        const auto rO = RSRC(o + (size_t)rt * 18432);
        const ProjOut po = proj_out(wl + L_XB, wl + L_STB, rt, cg, lane);
        asm volatile("" :: "s"(rO));
        WAIT_L(0);
        __builtin_amdgcn_s_setprio(REL_PRIO);
        BARX();
        STAMP(7);
        bf16x8 af[18];
        auto ldo = [&](int kc) { af[kc] = __builtin_bit_cast(bf16x8, __builtin_amdgcn_raw_buffer_load_b128(rO, lane * 16 + (kc & 3) * 1024, (kc >> 2) * 4096, 16)); };
#pragma unroll
        for (int kc = 0; kc < WO_PRE; ++kc) { ldo(kc); SCHED0(); if (kc >= PACE0 && kc + 1 < WO_PRE) PACE; SCHED0(); }
        static_for<90>([&](auto ic) {
            constexpr int i = ic.value, kc = i / 5, n = i % 5, j = WO_W + 2 * (i / 3) + i % 3;
            MFMA_T(wf[i], af[kc], v[n]);
            SCHED0();
            if (i % 3 < 2 && j < 90) rdw(j);
            if (i % WO_EV == 2 && i / WO_EV + WO_PRE < 18) ldo(i / WO_EV + WO_PRE);
            SCHED0();
        });
        asm volatile("s_nop 4" : "+v"(v[0]), "+v"(v[1]));
        STAMP(8);
        proj_store(v, po);
        __builtin_amdgcn_s_setprio(0);
    }
}

DEVI f32x2 gelu2(f32x2 v) {
    const f32x2 w = v * (v * v * -0.10294324f + -2.3022082f);
    const f32x2 d = (f32x2){__builtin_amdgcn_exp2f(w.x), __builtin_amdgcn_exp2f(w.y)} + 1.0f;
    return v * (f32x2){__builtin_amdgcn_rcpf(d.x), __builtin_amdgcn_rcpf(d.y)};
}
DEVI f32x4 gelu4(f32x4 v) { const f32x2 a = gelu2(v.xy), b = gelu2(v.zw); return (f32x4){a.x, a.y, b.x, b.y}; }
DEVI void rowsum16x8(const f32x4 a, const f32x4 b, float& o0, float& o1) {
    float t0, t1, t2, t3;
    asm volatile(
        "s_nop 1\n\t"
        "v_add_f32_dpp %2, %6, %6 row_ror:8 row_mask:0xf bank_mask:0x3\n\t"
        "v_add_f32_dpp %3, %7, %7 row_ror:8 row_mask:0xf bank_mask:0x3\n\t"
        "v_add_f32_dpp %4, %10, %10 row_ror:8 row_mask:0xf bank_mask:0x3\n\t"
        "v_add_f32_dpp %5, %11, %11 row_ror:8 row_mask:0xf bank_mask:0x3\n\t"
        "v_add_f32_dpp %2, %8, %8 row_ror:8 row_mask:0xf bank_mask:0xc\n\t"
        "v_add_f32_dpp %3, %9, %9 row_ror:8 row_mask:0xf bank_mask:0xc\n\t"
        "v_add_f32_dpp %4, %12, %12 row_ror:8 row_mask:0xf bank_mask:0xc\n\t"
        "v_add_f32_dpp %5, %13, %13 row_ror:8 row_mask:0xf bank_mask:0xc\n\t"
        "v_add_f32_dpp %0, %2, %2 row_half_mirror row_mask:0xf bank_mask:0x5\n\t"
        "s_nop 0\n\t"
        "v_add_f32_dpp %1, %4, %4 row_half_mirror row_mask:0xf bank_mask:0x5\n\t"
        "v_add_f32_dpp %0, %3, %3 row_half_mirror row_mask:0xf bank_mask:0xa\n\t"
        "v_add_f32_dpp %1, %5, %5 row_half_mirror row_mask:0xf bank_mask:0xa\n\t"
        "s_nop 0\n\t"
        "v_add_f32_dpp %0, %0, %0 quad_perm:[1,0,3,2] row_mask:0xf bank_mask:0xf\n\t"
        "v_add_f32_dpp %1, %1, %1 quad_perm:[1,0,3,2] row_mask:0xf bank_mask:0xf\n\t"
        "s_nop 0\n\t"
        "v_add_f32_dpp %0, %0, %0 quad_perm:[2,3,0,1] row_mask:0xf bank_mask:0xf\n\t"
        "v_add_f32_dpp %1, %1, %1 quad_perm:[2,3,0,1] row_mask:0xf bank_mask:0xf\n\t"
        : "=&v"(o0), "=&v"(o1), "=&v"(t0), "=&v"(t1), "=&v"(t2), "=&v"(t3)
        : "v"(a[0]), "v"(a[1]), "v"(a[2]), "v"(a[3]), "v"(b[0]), "v"(b[1]), "v"(b[2]), "v"(b[3]));
}

#define K5_PRE 9
#define RH_MAP 0
#define NET_LAYERS 2
constexpr int G_STAGE = 34816;
constexpr int K5_STAGE = 18432, K5_NS = 8;

constexpr int y_load_at(int slot) {
    int next = K5_PRE;
    for (int s = 0;; ++s) { const bool go = next < 18 + 2 * (s / 3); if (s == slot) return go ? next : -1; next += go; }
}

struct GTile { f32x4 a[4][4], e[2]; };
struct GFrag { bf16x8 a[2][4], b[2][5]; const LAS char *pa[2][2], *pb, *p8; int slot; };
DEVI void g_rd(GFrag& f, int kk, int i) {
    if (i < 4) f.a[kk][i] = LDS_FRAG(f.pa[kk][i >> 1] + (i & 1) * 2048);
    else if (i < 8) f.b[kk][i - 4] = LDS_FRAG(f.pb + (kk * 9 + i - 4) * 1024);
    else f.b[kk][4] = LDS_FRAG(f.p8 + kk * 9216);
}
template <int NS, int G, bool ROWS_IN_REGS>
DEVI void g_steps(GTile& c, GFrag& f, int n) {
    LOOP_ALIGN();
#pragma unroll 1
    do {
        int d;
#pragma unroll
        for (int kk = 0; kk < 2; ++kk) {
            WAIT_L(0);
            if (kk) BAR();
            SCHED0();
#pragma unroll
            for (int i = 0; i < 18; ++i) {
                const int nn = i >> 2, m = i & 3;
                f32x4& e = nn < 4 ? c.a[m][nn] : c.e[m & 1];
                e = ROWS_IN_REGS ? MFMA(f.a[kk][m], f.b[kk][nn], e) : MFMA(f.b[kk][nn], f.a[kk][m], e);
                SCHED0();
                if (i < 9) g_rd(f, kk ^ 1, i);
                if (kk == 0) {
                    if (i == 9) d = pin_s(f.slot == NS - 1 ? -(NS - 1) * G : G);
                    if (i >= 10 && i < 14) f.pa[(i - 10) >> 1][i & 1] = opaque(f.pa[(i - 10) >> 1][i & 1] + d);
                    if (i == 14) f.pb = opaque(f.pb + d);
                    if (i == 15) f.p8 = opaque(f.p8 + d);
                } else {
                    if (i == 10) f.slot = pin_s(f.slot == NS - 1 ? 0 : f.slot + 1);
                    if (i == 12) n = pin_s(n - 1);
                }
                SCHED0();
            }
        }
    } while (n);
}
DEVI void keep4(const f32x4 (&c)[4]) { asm volatile("" :: "v"(c[0]), "v"(c[1]), "v"(c[2]), "v"(c[3])); }
template <int D, int DB, bool FIRST, class HOOK>
DEVI void g_step_u(GTile& c, GFrag& f, const f32x4 (&c0)[4], HOOK&& hook) {
    static_for<36>([&](auto ic) {
        constexpr int kk = ic.value / 18, i = ic.value % 18, n = i >> 2, m = i & 3;
        if (i == 0) { WAIT_L(0); if (kk) BAR(); SCHED0(); }
        f32x4& e = n < 4 ? c.a[m][n] : c.e[m & 1];
        if (FIRST && kk == 0) asm volatile("v_mfma_f32_16x16x32_bf16 %0, %1, %2, %3" : "=&v"(e) : "v"(f.b[kk][n]), "v"(f.a[kk][m]), "v"(c0[m]));
        else MFMA_T(f.b[kk][n], f.a[kk][m], e);
        SCHED0();
        if (i < 9) g_rd(f, kk ^ 1, i);
        if (kk == 0) {
            if (i >= 10 && i < 14) f.pa[(i - 10) >> 1][i & 1] = opaque(f.pa[(i - 10) >> 1][i & 1] + D);
            if (i == 14) f.pb = opaque(f.pb + DB);
            if (i == 15) f.p8 = opaque(f.p8 + DB);
        }
        if (FIRST && ic.value == 20) keep4(c0);
        hook(ic);
        SCHED0();
    });
}
#define MF(d, a, b) "v_mfma_f32_16x16x32_bf16 " d ", " a ", " b ", " d "\n\t"
template <int D, int DB>
DEVI void g_step_gelu(GTile& c, GFrag& f, f32x4& z, float r, float c1) {
#pragma unroll
    for (int kk = 0; kk < 2; ++kk) {
        float va = z[2 * kk], vb = z[2 * kk + 1], ta, tb;
        WAIT_L(0); if (kk) BAR(); SCHED0();
#pragma unroll
        for (int i = 0; i < 9; ++i) {
            MFMA_T(f.b[kk][i >> 2], f.a[kk][i & 3], c.a[i & 3][i >> 2]);
            if (i == 5) asm volatile("v_mul_f32 %0, %0, %1" : "+v"(va) : "v"(r));
            if (i == 6) asm volatile("v_mul_f32 %0, %0, %1" : "+v"(vb) : "v"(r));
            if (i == 7) asm volatile("v_mul_f32 %0, %1, %1" : "=v"(ta) : "v"(va));
            if (i == 8) asm volatile("v_mul_f32 %0, %1, %1" : "=v"(tb) : "v"(vb));
            SCHED0();
            g_rd(f, kk ^ 1, i);
            SCHED0();
        }
        asm volatile(MF("%0", "%5", "%6") "v_fmaak_f32 %3, %3, %11, 0xc0135761\n\tv_fmaak_f32 %4, %4, %11, 0xc0135761\n\t"
                     MF("%1", "%5", "%7") "v_mul_f32 %3, %3, %9\n\tv_mul_f32 %4, %4, %10\n\t" MF("%2", "%5", "%8") "v_exp_f32 %3, %3"
                     : "+v"(c.a[1][2]), "+v"(c.a[2][2]), "+v"(c.a[3][2]), "+v"(ta), "+v"(tb)
                     : "v"(f.b[kk][2]), "v"(f.a[kk][1]), "v"(f.a[kk][2]), "v"(f.a[kk][3]), "v"(va), "v"(vb), "v"(c1));
        SCHED0(); f.pa[kk][0] = opaque(f.pa[kk][0] + D); SCHED0();
        asm volatile(MF("%0", "%6", "%7") "v_exp_f32 %5, %5\n\t" MF("%1", "%6", "%8") "v_add_f32 %4, 1.0, %4\n\tv_add_f32 %5, 1.0, %5\n\t"
                     MF("%2", "%6", "%9") "v_rcp_f32 %4, %4\n\t" MF("%3", "%6", "%10") "v_rcp_f32 %5, %5"
                     : "+v"(c.a[0][3]), "+v"(c.a[1][3]), "+v"(c.a[2][3]), "+v"(c.a[3][3]), "+v"(ta), "+v"(tb)
                     : "v"(f.b[kk][3]), "v"(f.a[kk][0]), "v"(f.a[kk][1]), "v"(f.a[kk][2]), "v"(f.a[kk][3]));
        SCHED0(); f.pa[kk][1] = opaque(f.pa[kk][1] + D); SCHED0();
        asm volatile(MF("%0", "%4", "%5") "v_mul_f32 %2, %2, %7\n\tv_mul_f32 %3, %3, %8\n\t" MF("%1", "%4", "%6")
                     : "+v"(c.e[0]), "+v"(c.e[1]), "+v"(va), "+v"(vb) : "v"(f.b[kk][4]), "v"(f.a[kk][0]), "v"(f.a[kk][1]), "v"(ta), "v"(tb));
        SCHED0();
        if (kk == 0) { f.pb = opaque(f.pb + DB); f.p8 = opaque(f.p8 + DB); }
        SCHED0();
        z[2 * kk] = va; z[2 * kk + 1] = vb;
    }
}
DEVI void g_fill(GTile& c, const float (&e)[4]) {
#pragma unroll
    for (int m = 0; m < 4; ++m) {
#pragma unroll
        for (int n = 0; n < 4; ++n) c.a[m][n] = pin((f32x4){e[m], e[m], e[m], e[m]});
        if (m < 2) c.e[m] = pin((f32x4){e[m], e[m], e[m], e[m]});
    }
}

#undef STAMP_SEC
#define STAMP_SEC 1
DEVI void gmlp_body(LAS char* lds, int wm, int wn, int pm, int cg, char* z2t, char* y, const u16* __restrict__ bsp STAMP_PARAM) {
    const int mlo = wm * 4 + wn * 2, mhi = wm * 4 + (1 - wn) * 2;
    const float zero4[4] = {0.f, 0.f, 0.f, 0.f};
    GTile z2, z1, acc;
    GFrag f;
    {
        const int lane = lane_id_fresh();
#pragma unroll
        for (int kk = 0; kk < 2; ++kk) { f.pa[kk][0] = lds + mlo * 2048 + kk * 1024 + lane * 16; f.pa[kk][1] = lds + mhi * 2048 + kk * 1024 + lane * 16; }
        f.pb = lds + 16384 + wn * 4096 + lane * 16; f.p8 = lds + 16384 + 8192 + lane * 16; f.slot = 0;
    }
    g_fill(z2, zero4); g_fill(z1, zero4);
#pragma unroll
    for (int i = 0; i < 4; ++i) f.pa[i >> 1][i & 1] = opaque(f.pa[i >> 1][i & 1]);
    f.pb = opaque(f.pb); f.p8 = opaque(f.p8);
    SCHED0();
    WAIT_V(0); BARX();
    BAR();
    BARX();
    STAMP(2);
#pragma unroll
    for (int i = 0; i < 9; ++i) g_rd(f, 0, i);
    g_steps<4, G_STAGE, true>(z2, f, 9);
    STAMP(3);
    {
        const int lane = lane_id_fresh(), r = lane & 15, q = lane >> 4;
        f32x4 ra[4];
#pragma unroll
        for (int m = 0; m < 4; ++m) ra[m] = *(const LAS f32x4*)(lds + G_TAB + ((m < 2 ? mlo : mhi) + (m & 1)) * 64 + q * 16);
        SCHED0();
#pragma unroll
        for (int m = 0; m < 4; ++m) {
            f32x4 s1, s2;
#pragma unroll
            for (int n = 0; n < 4; ++n) {
                const f32x4 g = gelu4(z2.a[m][n] * ra[m]);
                z2.a[m][n] = g;
                if (n == 0) { s1 = g; s2 = g * g; } else { s1 += g; s2 += g * g; }
            }
            if (m < 2) { const f32x4 g = gelu4(z2.e[m] * ra[m]); z2.e[m] = g; s1 += g; s2 += g * g; }
            float u0, u1;
            rowsum16x8(s1, s2, u0, u1);
            if ((r & 3) == 0) *(LAS f32x2*)(lds + G_PART + wn * 1024 + ((m < 2 ? mlo : mhi) + (m & 1)) * 128 + (q * 4 + (r >> 2)) * 8) = (f32x2){u0, u1};
            SCHED0();
        }
    }
    WAIT_L0(); BAR();
    STAMP(4);
    g_steps<4, G_STAGE, false>(z1, f, 4);
    unsigned b0, b1;
    f32x4 c0[4];
    g_step_u<G_STAGE, G_STAGE, false>(z1, f, c0, [&](auto ic) {
        if (ic.value != 18) return;
        STAMP(5);
        const int lane = lane_id_fresh(), q = lane >> 4;
        b0 = __builtin_amdgcn_raw_buffer_load_b32(RSRC(bsp), (lane & 15) * 4, pm * 256 + mlo * 32, 0);
        b1 = __builtin_amdgcn_raw_buffer_load_b32(RSRC(bsp), (lane & 15) * 4, pm * 256 + mhi * 32, 0);
#pragma unroll
        for (int g = 0; g < 2; ++g) {
            const auto rz2 = RSRC(z2t + (size_t)pm * 589824 + cg * 36864 + ((g ? mhi : mlo) >> 1) * 9216);
            const LAS char* tab = lds + G_TAB + (g ? mhi : mlo) * 64 + q * 16;
            LAS char* own = lds + (wm ? G_OWN : G_STAGE + 16384) + (g ^ wn) * 9216 + lane * 16;
            f32x4 ra[2], rb[2];
#pragma unroll
            for (int T = 0; T < 2; ++T) { ra[T] = *(const LAS f32x4*)(tab + 512 + T * 64); rb[T] = *(const LAS f32x4*)(tab + 1024 + T * 64); }
#pragma unroll
            for (int n = 0; n < (g ? 4 : 5); ++n) {
                const f32x4 v0 = (n < 4 ? z2.a[2 * g][n & 3] : z2.e[0]) * ra[0] + rb[0], v1 = (n < 4 ? z2.a[2 * g + 1][n & 3] : z2.e[1]) * ra[1] + rb[1];
                const u32x4 o = {pack2(v0[0], v1[0]), pack2(v0[1], v1[1]), pack2(v0[2], v1[2]), pack2(v0[3], v1[3])};
                ST16(rz2, lane * 16, n < 4 ? wn * 4096 + n * 1024 : 8192, o, 16);
                *(LAS u32x4*)(own + (n < 4 ? wn * 4096 + n * 1024 : 8192)) = o;
            }
        }
        STAMP(6);
    });
    static_for<3>([&](auto sc) {
        constexpr int D = sc.value == 0 ? G_STAGE : sc.value == 1 ? -3 * G_STAGE : 2 * G_STAGE;
        g_step_u<D, D, false>(z1, f, c0, [&](auto ic) { if (ic.value == 17 && sc.value == 1) WAIT_V(0); });
    });
    float ra[4], c1;
    {
        int lane, da[2];
        g_step_u<-G_STAGE, -G_STAGE, false>(z1, f, c0, [&](auto ic) {
            constexpr int i = ic.value;
            if (i == 9) lane = pin(lane_id_fresh());
            if (i == 14 || i == 15) da[i & 1] = pin((lane & 15) * 128 + (((4 * (i & 1) + (lane >> 4)) ^ (lane & 7)) * 16) - lane * 16 - (i & 1) * 1024);
            if (i == 16 || i == 17) f.pa[0][i & 1] = opaque(f.pa[0][i & 1] + da[0]);
            if (i == 27 || i == 28) f.pa[1][i & 1] = opaque(f.pa[1][i & 1] + da[1]);
            if (i >= 29 && i < 33) {
                constexpr int m = (i - 29) & 3;
                const float e = m & 1 ? bfhi(m < 2 ? b0 : b1) : bflo(m < 2 ? b0 : b1);
                c0[m] = pin((f32x4){e, e, e, e});
            }
            if (i == 33) {
#pragma unroll
                for (int m = 0; m < 4; ++m) ra[m] = *(const LAS float*)(lds + G_TAB + ((m < 2 ? mlo : mhi) + (m & 1)) * 64 + (lane & 15) * 4);
                c1 = pin(-0.10294324f);
            }
        });
    }
    STAMP(7);
    g_step_u<2 * G_STAGE, G_OWN - G_STAGE - 16384, true>(acc, f, c0, [](auto) {});
    static_for<18>([&](auto sc) {
        constexpr int st = sc.value, m = st < 16 ? st >> 2 : st & 1;
        constexpr int D = st == 0 ? -3 * G_STAGE : st % 3 == 1 ? 2 * G_STAGE : -G_STAGE;
        g_step_gelu<D, st == 0 ? 16384 - G_OWN : D>(acc, f, st < 16 ? z1.a[m][st & 3] : z1.e[m], ra[m], c1);
    });
    f.slot = 1;
    STAMP(9);
    g_steps<3, -G_STAGE, false>(acc, f, 13);
    STAMP(10);
    const int lane = lane_id_fresh();
#pragma unroll
    for (int g = 0; g < 2; ++g) {
        const auto ry = RSRC(y + (size_t)(pm * 8 + (g ? mhi : mlo)) * 73728 + cg * 4608);
#pragma unroll
        for (int T = 0; T < 2; ++T) {
            const int m = 2 * g + T;
#pragma unroll
            for (int p = 0; p < 2; ++p) {
                const f32x4 a = acc.a[m][2 * p] * z1.a[m][2 * p], c = acc.a[m][2 * p + 1] * z1.a[m][2 * p + 1];
                ST16(ry, lane * 16 + wn * 2048 + p * 1024, T * 73728, ((u32x4){pack2(a[0], a[1]), pack2(a[2], a[3]), pack2(c[0], c[1]), pack2(c[2], c[3])}), 16);
            }
            if (g == 0) {
                const f32x4 a = acc.e[T] * z1.e[T];
                ST8(ry, (lane >> 5) * 256 + (lane & 15) * 16 + ((lane >> 4) & 1) * 8, T * 73728 + 4096, ((u32x2){pack2(a[0], a[1]), pack2(a[2], a[3])}), 16);
            }
        }
    }
}

#define NET_IDS \
    int b = __builtin_amdgcn_readfirstlane(blockIdx.x), wid = wid8 & 3; \
    asm volatile("" : "+s"(b), "+s"(wid));     \
    const int xcd = b & 7, idx = b >> 3; \
    const int pa = (xcd >> 1) * 4 + (idx & 3), hp = (xcd & 1) * 8 + (idx >> 2); \
    const int pm = (xcd >> 2) * 8 + (idx & 7), cg = (xcd & 3) * 4 + (idx >> 3); \
    const int rh = RH_MAP ? xcd * 4 + (idx & 3) : (xcd >> 1) * 8 + (idx & 7), c5 = RH_MAP ? idx >> 2 : (xcd & 1) * 4 + (idx >> 3); \
    const int lead = (idx + (idx >> 3)) & 3; \
    auto kord = [&](int t, int n) { \
        const int k = t + ((int)(0x1F000000u << (((t - lead) & 3) * 4)) >> 28); \
        return (unsigned)k < (unsigned)n ? k : t; \
    }; \
    auto kord5 = [&](int t) { \
        const int k = t + ((int)(0x1F000000u << (((t - (RH_MAP ? c5 : lead)) & 3) * 4)) >> 28); \
        return (unsigned)k < 18u ? k : t; \
    };

typedef short s16x4 __attribute__((ext_vector_type(4)));
DEVI u32x4 tr_frag(const LAS char* p, int four_rows) {
    const u32x2 a = __builtin_bit_cast(u32x2, __builtin_amdgcn_ds_read_tr16_b64_v4i16((LAS s16x4*)p));
    const u32x2 b = __builtin_bit_cast(u32x2, __builtin_amdgcn_ds_read_tr16_b64_v4i16((LAS s16x4*)(p + four_rows)));
    return (u32x4){a.x, a.y, b.x, b.y};
}
DEVI float dot2(unsigned v, unsigned w, float acc) { return __builtin_amdgcn_fdot2_f32_bf16(__builtin_bit_cast(bf16x2_t, v), __builtin_bit_cast(bf16x2_t, w), acc, false); }
DEVI float sum2(unsigned v, float acc) { return dot2(v, 0x3f803f80u, acc); }
struct Sel { unsigned lo, hi, nlo, nhi; };
DEVI Sel make_sel() { return {(unsigned)pin_s(0x00003f80), (unsigned)pin_s(0x3f800000), (unsigned)pin_s(0x0000bf80), (unsigned)pin_s((int)0xbf800000u)}; }
DEVI unsigned centre2(unsigned v, f32x2& e, float mean, const Sel& k) {
    const float a = dot2(k.lo, v, e.x), b = dot2(k.hi, v, e.y);
    const unsigned r = pack2(a, b);
    e = (f32x2){dot2(k.nlo, r, a), dot2(k.nhi, r, b)} - mean;
    return r;
}
typedef u32x4 Retile[9];
template <int PITCH>
DEVI void retile_out_make(Retile& o, const LAS char* img, int lane) {
    const int c = lane & 15, q = lane >> 4, p = lane & 3;
    const LAS char* src = img + (q * 8 + (c >> 2)) * PITCH;
#pragma unroll
    for (int i = 0; i < 8; ++i) o[i] = tr_frag(src + p * 16 + (i >> 2) * 144 + ((i >> 1) & 1) * 64 + (i & 1) * 8, 4 * PITCH);
    o[8] = tr_frag(src + 128 + (p >> 1) * 144 + (p & 1) * 8, 4 * PITCH);
}
DEVI void retile_out_store(const Retile& o, char* dst, int nkc, int kc, int w, int lane) {
    const int c = lane & 15, q = lane >> 4;
    const auto rd = RSRC(dst + (size_t)(2 * w * nkc + kc) * 4608);
#pragma unroll
    for (int i = 0; i < 8; ++i) ST16(rd, lane * 16 + (i & 3) * 1024, (i >> 2) * nkc * 4608, o[i], 16);
    ST16(rd, (c >> 3) * nkc * 4608 + 4096 + q * 128 + (c & 7) * 16, 0, o[8], 16);
}

DEVI float dpp_ror(float v, int n) { return n == 8 ? __builtin_amdgcn_update_dpp(0.f, v, 0x128, 0xf, 0xf, true) : n == 4 ? __builtin_amdgcn_update_dpp(0.f, v, 0x124, 0xf, 0xf, true)
                                          : n == 2 ? __builtin_amdgcn_update_dpp(0.f, v, 0x122, 0xf, 0xf, true) : __builtin_amdgcn_update_dpp(0.f, v, 0x121, 0xf, 0xf, true); }
DEVI unsigned long long epoch_of(const char* ws) {
    SCHED0();
    const unsigned long long ep = *(const __attribute__((address_space(4))) unsigned long long*)(ws + pin_s((int)OFF_EP)) + 1ull;
    asm volatile("" :: "s"(ep));
    return ep;
}
DEVI void prologue_done(LAS char* lds, char* ws, unsigned long long ep, int b, int lane) {
    if (lds_arrive(lds + G_WORD + 8) == 7) ST16(RSRC(ws), lane ? 0x80000000u : (unsigned)OFF_FL + b * 16, 0, ((u32x4){(unsigned)ep, (unsigned)(ep >> 32), 0u, 0u}), 16);
}
DEVI void centre_frag(u32x4& f, f32x2 (&er)[4], float mean, const Sel& k) {
#pragma unroll
    for (int e = 0; e < 4; ++e) f[e] = centre2(f[e], er[e], mean, k);
}
template <int N>
DEVI void column_means(LAS char* lds, LAS char* cnt, int me, int lane, float a, float b, float& ma, float& mb) {
    LAS float* red = (LAS float*)(lds + G_PART);
    auto x = __builtin_amdgcn_permlane16_swap(__float_as_uint(a), __float_as_uint(b), false, false);
    red[me * 64 + lane] = __uint_as_float(x[0]) + __uint_as_float(x[1]);
    if (lds_arrive(cnt) < N - 1) lds_count_wait(cnt, N);
    float t = 0.f;
#pragma unroll
    for (int i = 0; i < N; ++i) t += red[i * 64 + lane];
    x = __builtin_amdgcn_permlane32_swap(__float_as_uint(t), __float_as_uint(t), false, false);
    t = (__uint_as_float(x[0]) + __uint_as_float(x[1])) * (1.0f / 576.0f);
    x = __builtin_amdgcn_permlane16_swap(__float_as_uint(t), __float_as_uint(t), false, false);
    ma = __uint_as_float(x[0]); mb = __uint_as_float(x[1]);
}
template <class ARGS>
DEVI unsigned long long prologue(LAS char* lds, char*& ws, const char*& x0, const char*& wqkv, const u16*& wo, const u16*& w_in, int b, int pa, int wid8, ARGS&& args PST_PARAM) {
    PST_DECL;
    const int lane = lane_id_fresh(), c = lane & 15, q = lane >> 4, w = wid8 & 3;
    const int jw = w - 1 + 3 * (((wid8 >> 2) ^ (b >> 3)) & 1);
    const bool wjob = b >= 16 && b < 112 && w;
    const int uo = (wid8 - 1 - (wid8 >> 2)) * 16 + b;
    const bool ojob = b < 16 && w && uo < 72;
    const unsigned l0 = (unsigned)(size_t)lds;
    LAS char* img = lds + 2 * K1_STAGE;
    const int uw = b - 16, head = (uw & 7) * 4 + ((uw >> 3) & 3), part = uw >> 5, hp = head >> 1, odd = head & 1;
    const int c0 = part * 1152 + hp * 72 + odd * 40;
    {
        const int so = pin_s(jw * 96 * 3456 + (c0 & ~15));
        const unsigned v = (lane & 3) < ((head & 3) == 3 ? 2 : 3) ? (lane >> 2) * 3456 + (lane & 3) * 16 : 0x80000000u, d = 16 * 3456 - 1024;
        const unsigned v0 = pin(v), v1 = pin(v + d), v2 = pin(v + 2 * d), v3 = pin(v + 3 * d);
        const int e = ((b - 112) >> 3) * 192 + wid8 * 64 + lane, row = (e * 10923) >> 16, r = e - row * 6;
        const unsigned vp = pin((unsigned)(row * 3456 + (r >> 1) * 1152 + (r & 1) * 128));
        const unsigned la = pin_s(l0 + 2 * K1_STAGE + jw * 6144);
        args(0);
        if (b >= 112 && wid8 < 3) dma1(l0 + G_OWN, RSRC(wqkv), (b & 7) * 128, vp);
        if (wjob) { dma4<false>(la, RSRC(wqkv), so, v0, v1, v2, v3); dma2<false>(la + 4096, RSRC(wqkv), so + 64 * 3456, v0, v1); }
        args(1);
    }
    if (ojob) {
        const auto rs = RSRC(wo + (uo & 3) * 144);
#pragma unroll
        for (int j = 0; j < 9; ++j) {
            const int i = lane + 64 * j, rl = (i * 3641) >> 16, kp = (uo >> 2) * 32 + rl;
            const int row = kp < 512 ? 18 * (kp >> 4) + (kp & 15) + 2 * ((kp >> 4) & 1) : 36 * ((kp - 512) >> 2) + 16 + (kp & 3);
            dma1(l0 + 2 * K1_STAGE + wid8 * 9216 + j * 1024, rs, 0, (unsigned)(row * 1152 + (i - rl * 18) * 16));
        }
    }
    if (wid8 == 4) { lds_word_set(lds + G_WORD + c * 4, 0u); WAIT_L0(); }
    BAR();
    unsigned long long ep;
    if (wid8 == 4) ep = epoch_of(ws);
    if (wid8 == 4 && b >= 16 && b < 112) lds_count_wait(lds + G_WORD + 12, 12);
    if (wid8 > 4 && b >= 112) {
        __builtin_amdgcn_s_sleep(I_SLEEP);
        const unsigned v = (lane >> 2) * 9216 + (lane & 3) * 16, d = 16 * 9216 - 1024;
#pragma unroll
        for (int i = 0; i < 3; ++i) dma4<false>(l0 + 3 * K1_STAGE + (w - 1) * 12288 + i * 4096, RSRC(w_in + win_strip(b) * 32), (win_third(b, w) * 192 + i * 64) * 9216, v, v + d, v + 2 * d, v + 3 * d);
    }
    if (wid8 == 4) {
        AttnX ax = attn_x(x0, x0, 0, pa, 0, lane);
#pragma unroll
        for (int s = 0; s < 2; ++s)
#pragma unroll
            for (int i = 0; i < 4; ++i) { ax.oA = (pa * 8 + 2 * i) * 18432; attn_issueA(lds, ax, i, s, s); }
    }
    u32x4 xv[9];
    if (wid8 == 0) {
        const unsigned v = (lane >> 3) * 2304 + (lane & 7) * 16;
        const auto rx = RSRC(x0 + (size_t)slot2row(b * 8) * 1152);
#pragma unroll
        for (int i = 0; i < 9; ++i) asm volatile("s_nop 4\n\tbuffer_load_dwordx4 %0, %1, %2, 0 offen offset:%3" : "+v"(xv[i]) : "v"(v), "s"(rx), "i"(i * 128) : "memory");
        const auto rw = RSRC(ws);
        const u32x4 ones = {~0u, ~0u, ~0u, ~0u}, zero = {0u, 0u, 0u, 0u};
        const unsigned vo = lane * 16;
        ST16(rw, vo, (unsigned)OFF_ZST + b * 2048, ones, 16); ST16(rw, vo, (unsigned)OFF_ZST + b * 2048 + 1024, ones, 16);
        ST16(rw, vo, (unsigned)OFF_CNT + b * 2560, zero, 16); ST16(rw, vo, (unsigned)OFF_CNT + b * 2560 + 1024, zero, 16);
        ST16(rw, vo, lane < 32 ? (unsigned)OFF_CNT + b * 2560 + 2048 : (unsigned)(OFF_L0 + L_STB) + b * 512 - 512, zero, 16);
        ST16(rw, vo, (unsigned)(OFF_L0 + L_O) + (b >> 1) * 18432 + 16384 + (b & 1) * 1024, zero, 16);
        ST16(rw, b < 12 ? vo : 0x80000000u, (unsigned)OFF_FL2 + b * 1024, zero, 16);
    }
    PST(1);
    if (wjob) {
        const Sel k = make_sel();
        const LAS char* s0 = opaque(img + jw * 6144 + (q * 8 + (c >> 2)) * 64 + (c0 & 15) + (c & 3) * 8);
        const LAS char* s1 = opaque(img + jw * 6144 + (q * 8 + (c >> 2)) * 64 + (c0 & 15) + 32 + min(c & 3, 2) * 2048);
        const auto rd = RSRC(ws + OFF_TWQKV + (size_t)(hp * 18 + jw * 3) * 6144 + (3 * odd + part) * 1024), rd2 = RSRC(ws + OFF_TWT + (size_t)((hp * 3 + part) * 18 + jw * 3) * 256);
        const unsigned vd2 = pin(c < 12 && !odd ? (unsigned)((c >> 2) * 256 + q * 64 + (c & 3) * 16) : 0x80000000u);
        asm volatile("" :: "s"(rd), "s"(rd2));
        WAIT_V(0);
        PST(2);
        u32x4 f[4];
        float sm[4] = {0.f, 0.f, 0.f, 0.f}, m0, m1;
#pragma unroll
        for (int i = 0; i < 4; ++i) f[i] = tr_frag(i < 3 ? s0 + i * 2048 : s1, 256);
#pragma unroll
        for (int e = 0; e < 16; ++e) sm[(e >= 12) * 2 + (e & 1)] = sum2(f[e >> 2][e & 3], sm[(e >= 12) * 2 + (e & 1)]);
        sm[2] = c < 12 ? sm[2] + sm[3] : 0.f;
        sm[2] += dpp_ror(sm[2], 4); sm[2] += dpp_ror(sm[2], 8);
        column_means<6>(lds, lds + G_WORD + 12, jw, lane, sm[0] + sm[1], sm[2], m0, m1);
        PST(3);
        f32x2 e0[4] = {{-m0, -m0}, {-m0, -m0}, {-m0, -m0}, {-m0, -m0}}, e1[4] = {{-m1, -m1}, {-m1, -m1}, {-m1, -m1}, {-m1, -m1}};
#pragma unroll
        for (int i = 0; i < 3; ++i) { centre_frag(f[i], e0, m0, k); ST16(rd, lane * 16, i * 6144, f[i], 16); }
        centre_frag(f[3], e1, m1, k); ST16(rd2, vd2, 0, f[3], 16);
        PST(4);
        lds_arrive(lds + G_WORD + 12);
    }
    if (wid8 != 4) ep = epoch_of(ws);
    {
        if (wjob) {
            const unsigned fo = pin((unsigned)OFF_FW + ((lane >> 3) * 16 + hp) * 1024 + (3 * odd + part) * 128 + (lane & 7) * 16);
            WAIT_V(0);
            PST(5);
            if (lds_arrive(lds + G_WORD + 4) == 5) {
                const u32x4 e4 = {(unsigned)ep, (unsigned)(ep >> 32), (unsigned)ep, (unsigned)(ep >> 32)};
                ST16(RSRC(ws), fo, 0, e4, 16); ST16(RSRC(ws), fo + 131072, 0, e4, 16);
            }
        }
    }
    if (wid8 == 0) {
        WAIT_V(7);
        asm volatile("" : "+v"(xv[0]), "+v"(xv[1]), "+v"(xv[2]), "+v"(xv[3]), "+v"(xv[4]), "+v"(xv[5]), "+v"(xv[6]), "+v"(xv[7]), "+v"(xv[8]));
        float s = 0.f, q2 = 0.f;
#pragma unroll
        for (int e = 0; e < 36; ++e) { s = sum2(xv[e >> 2][e & 3], s); q2 = dot2(xv[e >> 2][e & 3], xv[e >> 2][e & 3], q2); }
        ST8(RSRC(ws + OFF_L0 + L_STA + (size_t)b * 512), lane * 8, 0, ((u32x2){__float_as_uint(s), __float_as_uint(q2)}), 16);
    }
    if (ojob) {
        WAIT_V(0);
        Retile rt;
        retile_out_make<288>(rt, img + wid8 * 9216, lane); retile_out_store(rt, ws + OFF_TWO, 18, uo >> 2, uo & 3, lane);
    }
    PST(7);
    PST_DUMP(wid8 == 0 || wid8 == 4 || wid8 == 5, wid8 ? wid8 : 2);
    return ep;
}

__global__ __launch_bounds__(512, 2) TRACE_ATTR void k_net(char* ws, const u16* wsp, const u16* bsp, char* out, const char* x0, const char* wqkv, const u16* wo,
                                                             const u16* w_in, const u16* w_out DBG_ARG) {
    extern __shared__ __attribute__((aligned(16))) char smem[];
    LAS char* lds = (LAS char*)smem;
    asm volatile("gm_code_begin:");
    if (blockIdx.x < 16) {
        unsigned long long pc; int d0, len;
        asm volatile("s_getpc_b64 %0\n1:\n\ts_mov_b32 %1, gm_code_begin-1b\n\ts_mov_b32 %2, gm_code_end1-gm_code_begin\n\ts_max_i32 %2, %2, gm_code_end2-gm_code_begin"
                     : "=s"(pc), "=s"(d0), "=s"(len) :: "scc");
        const auto rs = __builtin_amdgcn_make_buffer_rsrc((void*)((pc + d0) & ~15ull), 0, len, 0x00020000);
        dma1((unsigned)(size_t)lds + G_OWN, rs, 0, (threadIdx.x * 2 + (blockIdx.x >> 3)) * 128);
    }
    STAMP_DECL;
    const int wid8 = __builtin_amdgcn_readfirstlane(threadIdx.x >> 6);
    unsigned long long ep;
    {
        NET_IDS
        ep = prologue(lds, ws, x0, wqkv, wo, w_in, b, pa, wid8, [&](int part) {
            if (part == 0) { asm volatile("" :: "s"(wqkv), "s"(w_in), "s"(w_out)); return; }
            ws = own_s(ws); wsp = own_s(wsp); bsp = own_s(bsp); out = own_s(out); x0 = own_s(x0); wo = own_s(wo); w_out = own_s(w_out);
        } PST_FWD);
    }
    const char *twin = ws + OFF_TWIN, *twout = ws + OFF_TWOUT;
    if (wid8 >= 4) {
        u32x4 wr[5][9];
#pragma unroll 1
      for (int layer = 0; layer < NET_LAYERS; ++layer) {
        NET_IDS
        TRACE_BEGIN;
        char* wl = ws + OFF_L0 + layer * SZ_L;
        const unsigned fl = layer * F_LAYER;
        const char *xin = wl + L_XB, *st = wl + L_STB, *z2t = wl + (layer ? L_Y : L_Z2T);
        unsigned long long* zst = (unsigned long long*)(ws + OFF_ZST + layer * SZ_ZST);
        const int lane = lane_id_fresh(), l3 = lane >> 3;
        const unsigned vo = lane * 16;
        const unsigned l0 = (unsigned)(size_t)lds, lA = l0 + wid * 4096, lB = l0 + 16384 + wid * 4608;
        const auto rX = RSRC(xin + (size_t)(pm * 8 + 2 * wid) * 18432), rI = RSRC(twin + (size_t)cg * (18 * 18432) + wid * 4608);
        auto issue1B = [&](int g, int slot) { const int k = kord(g < 9 ? g : (g < 13 ? g - 4 : g - 13), 9); dma5<true>(lB + slot * G_STAGE, rI, (g < 9 ? 9 + k : k) * 18432, vo); };
        auto issue1A = [&](int g, int slot) { dma4<true>(lA + slot * G_STAGE, rX, kord(g < 9 ? g : g - 13, 9) * 2048, vo, vo, vo + 16384, vo + 16384); };
        auto issue1 = [&](int g, int slot) { issue1B(g, slot); issue1A(g, slot); };
        const auto rA = RSRC((const char*)wsp + (size_t)(pm * 128 + wid * 32) * 4096), rB = RSRC(z2t + cg * 36864 + wid * 4608);
        const unsigned voA = (unsigned)(2 * l3 * 4096 + ((lane & 7) ^ l3) * 16);
        auto issueB = [&](int k, int slot) { dma5<true>(lB + slot * G_STAGE, rB, (k >> 1) * 589824 + (k & 1) * 18432, vo); };
        const auto rW = RSRC(twout + (size_t)c5 * (72 * 4608));
        auto issue5 = [&](int t) { dma5<true>(l0 + ((t + 6) & 7) * K5_STAGE + wid * 4608, rW, kord5(t) * K5_STAGE + wid * 4608, vo); };
        auto load5 = [&](int p, int i) {
            const int t0 = p ? 2 * p + 6 : 4, ow = kord5(t0 + i) * K5_STAGE + wid * 4096;
            asm volatile("s_nop 4\n\tbuffer_load_dwordx4 %0, %4, %5, %6 offen sc1\n\tbuffer_load_dwordx4 %1, %4, %5, %6 offen offset:1024 sc1\n\t"
                         "buffer_load_dwordx4 %2, %4, %5, %6 offen offset:2048 sc1\n\tbuffer_load_dwordx4 %3, %4, %5, %6 offen offset:3072 sc1"
                         : "+v"(wr[p][4 * i]), "+v"(wr[p][4 * i + 1]), "+v"(wr[p][4 * i + 2]), "+v"(wr[p][4 * i + 3]) : "v"(vo), "s"(rW), "s"(ow) : "memory");
            if (i) {
                const int oe = kord5(t0 + (wid >> 1)) * K5_STAGE + 16384 + (wid & 1) * 1024;
                asm volatile("s_nop 4\n\tbuffer_load_dwordx4 %0, %1, %2, %3 offen sc1" : "+v"(wr[p][8]) : "v"(vo), "s"(rW), "s"(oe) : "memory");
            }
        };
        auto put5 = [&](int w0, int w1) {
#pragma unroll
            for (int w = w0; w < w1; ++w) {
                const int p = w / 9, i = w % 9 == 4 ? -1 : w % 9 - (w % 9 > 4);
                LAS char* sl = lds + (((p ? 2 * p + 6 : 4) + 6) & 7) * K5_STAGE + lane * 16;
                if (i < 0) *(LAS u32x4*)(sl + (wid >> 1) * K5_STAGE + 16384 + (wid & 1) * 1024) = wr[p][8];
                else *(LAS u32x4*)(sl + (i >> 2) * K5_STAGE + wid * 4096 + (i & 3) * 1024) = wr[p][i];
            }
            WAIT_L0();
        };
        const int row = wid * 32 + (lane & 31);
        auto step1 = [&](int t) { BAR(); issue1(t + 4, t & 3); };
        const char* ps;
        unsigned fd, fd2;
        {
            unsigned fo, fi;
            const bool oj = !layer && wid && b < 96, ij = !layer && wid && b >= 112, early = win_strip(b) >= WIN_EARLY;
            const int ojob = b * 3 + wid - 1, oimg = 2048 + (wid - 1) * 9216;
            float m0, m1;
            unsigned fw;
            auto win_store = [&](int h) {
                const int u = win_strip(b) * 2 + h, half = u >= 144, g = u - half * 144, ug = g / 9, j = g - ug * 9, c = lane & 15;
                const auto rd = RSRC(ws + OFF_TWIN + (size_t)ug * (18 * 18432) + (half * 18 + win_third(b, wid) * 6) * 9216);
                const unsigned vd = (lane >> 4) * 256 + (j < 8 ? ((j & ~1) + (c >> 3)) * 1024 + ((j & 1) * 8 + (c & 7)) * 16 : 8192 + c * 16);
#pragma unroll
                for (int i = 0; i < 6; ++i) ST16(rd, vd, i * 9216, wr[1 + h][i], 16);
            };
            auto win_half = [&](int h) {
                const Sel k = make_sel();
                const float m = h ? m1 : m0;
                f32x2 er[4] = {{-m, -m}, {-m, -m}, {-m, -m}, {-m, -m}};
#pragma unroll
                for (int i = 0; i < 6; ++i) centre_frag(wr[1 + h][i], er, m, k);
            };
            attn_producer(lds, ws, x0, wl, fl, ep, layer, pa, hp, wid, [&] { if (b < 16) WAIT_V(0); prologue_done(lds, ws, ep, b, lane); }, [&](int part) {
                if (part == -1 && ij) {
#pragma unroll
                    for (int h = 0; h < 2; ++h) {
                        const LAS char* src = lds + 3 * K1_STAGE + (wid - 1) * 12288 + ((lane >> 4) * 8 + ((lane & 15) >> 2)) * 64 + h * 32
                                              + ((win_strip(b) * 2 + h) % 9 < 8 ? (lane & 1) * 16 + (lane & 2) * 4 : (lane & 3) * 8);
#pragma unroll
                        for (int i = 0; i < 6; ++i) wr[1 + h][i] = tr_frag(src + i * 2048, 256);
                    }
                    WAIT_L(0);
                }
                if (part == 0 && oj) {
                    const auto rs = RSRC(w_out + (size_t)(ojob >> 2) * 32 * 576 + (ojob & 3) * 144);
#pragma unroll
                    for (int j = 0; j < 9; ++j) { const int i = lane + 64 * j; dma1(l0 + oimg + j * 1024, rs, 0, (unsigned)(((i * 3641) >> 16) * (1152 - 288) + i * 16)); }
                }
                if (part == 1) {
                    if (layer) { issue1B(0, 0); issue1B(1, 1); }
                    fo = flag_out(fl + F_X1, 256, pa, hp, lane); fi = flag_in(fl + F_X1, 256, pm * 16 + cg, 16, lane);
                    ps = pin(st + (size_t)(pm * 128 + wid * 32) * 64 + lane * 16);
                    fd = pin((unsigned)OFF_FL2 + wid * 1024 + vo);
                    fd2 = pin((unsigned)OFF_FL2 + (lane < 32 ? 256 + lane : 288 + ((cg * 9) >> 1) * 3 + min(lane & 15, 14)) * 16);
                    fw = pin((unsigned)OFF_FL2 + (288 + (72 + ((cg * 9) >> 1)) * 3 + min(lane & 15, 14)) * 16);
                    if (ij) {
                        float sm[4] = {0.f, 0.f, 0.f, 0.f};
#pragma unroll
                        for (int e = 0; e < 48; ++e) sm[(e >= 24) * 2 + (e & 1)] = sum2(wr[1 + (e >= 24)][(e % 24) >> 2][e & 3], sm[(e >= 24) * 2 + (e & 1)]);
                        column_means<3>(lds, lds + G_WORD, wid - 1, lane, sm[0] + sm[1], sm[2] + sm[3], m0, m1);
                        win_half(0);
                    }
                }
                if (part == 2 && ij) { if (early) win_store(0); win_half(1); if (early) win_store(1); }
                if (part == 2 && oj) { retile_out_make<288>(wr[1], lds + oimg, lane); WAIT_L(0); }
            });
            if (oj || ij) {
                if (oj || !early) {
                    __builtin_amdgcn_s_sleep(OJOB_SLEEP);
                    if (oj) retile_out_store(wr[1], ws + OFF_TWOUT, 72, ojob >> 2, ojob & 3, lane); else { win_store(0); win_store(1); }
                }
                WAIT_V(0);
                flag_raise(ws, lane ? 0x80000000u : (unsigned)OFF_FL2 + (oj ? ojob : 288 + win_strip(b) * 3 + wid - 1) * 16);
            }
            BARX();
            if (wid == 0) flag_raise(ws, fo);
            if (layer == 0) { flags_wait<0>(ws, fw); issue1B(0, 0); issue1B(1, 1); }
            issue1B(2, 2); issue1B(3, 3);
            asm volatile("" :: "s"(rX));
            if (wid == 0) flags_wait<X1_FIRST>(ws, fi);
            BARX();
        }
        issue1A(0, 0); issue1A(1, 1); issue1A(2, 2);
        WAIT_V(8); BAR();
        issue1A(3, 3);
        {
            f32x4 sp[3];
            WAIT_V(8); step1(0);
            WAIT_V(13); step1(1);
            WAIT_V(18); step1(2);
            unsigned got[2];
            if (layer == 0) asm volatile("s_nop 4\n\tbuffer_load_dword %0, %2, %4, 0 offen sc1\n\tbuffer_load_dword %1, %3, %4, 0 offen sc1" : "+v"(got[0]), "+v"(got[1]) : "v"(fd), "v"(fd2), "s"(RSRC(ws)) : "memory");
            WAIT_IF(layer, 18, 20); step1(3);
            WAIT_IF(layer, 18, 20); step1(4);
            WAIT_V(18);
            if (layer == 0) {
                asm volatile("" : "+v"(got[0]), "+v"(got[1]));
                if (__any((got[0] & got[1]) == 0u)) { flags_wait<0>(ws, fd); flags_wait<0>(ws, fd2); }
            }
            BAR(); issue1B(9, 1); side_load(sp, ps, ps); if (layer == 0) load5(3, 0);
            WAIT_IF(layer, 17, 21); BAR(); issue1B(10, 2); if (layer == 0) load5(3, 1);
            WAIT_IF(layer, 5, 14); BARX();
            side_fence(sp);
#pragma unroll
            for (int i = 0; i < 2; ++i) lds_word_set(lds + G_TAB + (wid * 32 + i * 16 + (lane >> 2)) * 4, __float_as_uint(ln_rstd(sp[i])));
            issue1B(11, 3);
            WAIT_L0();
        }
        BAR(); issue1B(12, 0);
        if (layer == 1) {
            const auto rd = RSRC(out + (size_t)b * 9216 + wid * 2304);
            ST16(rd, vo, 0, ((u32x4){0u, 0u, 0u, 0u}), 16); ST16(rd, vo, 1024, ((u32x4){0u, 0u, 0u, 0u}), 16);
            if (lane < 16) ST16(rd, vo, 2048, ((u32x4){0u, 0u, 0u, 0u}), 16);
        }
        if (layer == 0) {
#pragma unroll
            for (int p = 0; p < 6; ++p) load5(p >> 1, p & 1);
        }
        WAIT_V(0);
        __builtin_amdgcn_s_setprio(3);
        const char* src = pin((const char*)zst + (size_t)(pm * 16 + (lane >> 4) * 4) * 1024 + wid * 256 + (lane & 15) * 16);
        const unsigned tw = pin(l0 + G_TAB + 512 + (lane >> 5) * 512 + (wid * 32 + (lane & 15) * 2 + ((lane >> 4) & 1)) * 4);
        const unsigned fz = flag_out(fl + F_Z2, 256, cg, pm, lane), fzi = pin(fl + F_Z2 + (cg * 16 + pm) * 256 + ((lane ^ pm) & 8) * 16 + (lane & 7) * 16);
        const unsigned vA[4] = {pin(voA), pin(voA + 16 * 4096 - 1024), pin(voA + 4096 - 2048), pin(voA + 17 * 4096 - 3072)};
        auto spA = [&](int so, unsigned la) { dma4<false>(la, rA, so, vA[0], vA[1], vA[2], vA[3]); };
        auto issueA = [&](int k, int slot) { spA(k * 128, lA + slot * G_STAGE); };
        {
            const LAS char* pp = opaque(lds + G_PART + row * 8);
            const auto rs = RSRC(zst + (size_t)(pm * 16 + cg) * 128);
            const unsigned vp = pin(lane < 32 ? (unsigned)row * 8u : 0x80000000u);
            asm volatile("" :: "s"(rs));
            BARX();
            const f32x2 a = *(const LAS f32x2*)pp, c = *(const LAS f32x2*)(pp + 1024);
            ST8(rs, vp, 0, ((u32x2){__float_as_uint(add1(a.x, c.x)), min(__float_as_uint(add1(a.y, c.y)), 0xfffffffeu)}), 16);
            SCHED0();
        }
        __builtin_amdgcn_s_setprio(0);
        {
            f32x4 g[4];
            float sm[4];
            auto gather = [&] {
                asm volatile("global_load_dwordx4 %0, %4, off sc1\n\tglobal_load_dwordx4 %1, %4, off offset:1024 sc1\n\t"
                             "global_load_dwordx4 %2, %4, off offset:2048 sc1\n\tglobal_load_dwordx4 %3, %4, off offset:3072 sc1"
                             : "=&v"(g[0]), "=&v"(g[1]), "=&v"(g[2]), "=&v"(g[3]) : "v"(src) : "memory");
            };
            auto total = [&] {
                asm volatile("" : "+v"(g[0]), "+v"(g[1]), "+v"(g[2]), "+v"(g[3]));
#pragma unroll
                for (int i = 0; i < 4; ++i) sm[i] = add1(add1(g[0][i], g[1][i]), add1(g[2][i], g[3][i]));
            };
            step1(9); step1(10);
            BARX(); gather(); issue1(15, 3);
            WAIT_V(22); BARX();
            __builtin_amdgcn_s_setprio(3);
            WAIT_V(9);
            total();
            if (__any(__builtin_isunordered(sm[1], sm[3]))) {
#pragma unroll 1
                for (unsigned spin = 0; spin < 400000u; ++spin) {
                    bool ok = true;
#pragma unroll
                    for (int i = 0; i < 4; ++i) ok &= __float_as_uint(g[i][1]) != 0xffffffffu && __float_as_uint(g[i][3]) != 0xffffffffu;
                    if (__all(ok)) break;
                    gather();
                    WAIT_V(0);
                    total();
                }
            }
            xq4_sum(sm);
            const bool odd = lane & 16;
            float mean;
            const float rs = rstd_of(odd ? sm[2] : sm[0], odd ? sm[3] : sm[1], 1.0f / 2304.0f, mean);
            asm volatile("ds_write_b32 %0, %1" :: "v"(tw), "v"(lane < 32 ? rs : -mean * rs) : "memory");
            __builtin_amdgcn_s_setprio(0);
            issue1B(16, 0);
            WAIT_L0();
        }
        BARX(); issue1A(16, 0); issueA(2 * pm, 1);
        auto kord2 = [&](int t) { const int j = kord(t - 2, 30); return ((j < 2 * (pm & 7) ? j : j + 2) + 2 * (pm & 8)) & 31; };
        unsigned seen;
        auto look = [&] { if (wid == 0) asm volatile("s_nop 4\n\tbuffer_load_dword %0, %1, %2, 0 offen sc1" : "+v"(seen) : "v"(fzi), "s"(RSRC(ws)) : "memory"); };
        auto need = [&](int n) {
            if (wid == 0) {
                asm volatile("" : "+v"(seen));
                if (__ballot(seen == 0u) & ((1ull << n) - 1ull)) flags_wait<0>(ws, lane < n ? fzi : fl + F_Z2 + (cg * 16 + pm) * 256 + pm * 16);
            }
        };
        auto issue = [&](int t, int sl) { const int k = kord2(t); issueB(k, sl); issueA(k, sl); };
        auto steps2 = [&](int t0, int t1) {
            int sl = (2 * t0 + 2) % 3 * G_STAGE, k = kord2(t0 + 3), ob = (k >> 1) * 589824 + (k & 1) * 18432;
            LOOP_ALIGN();
#pragma unroll 1
            for (int t = t0; t < t1; ++t) {
                WAIT_V(9); BARX();
                dma5<true>(lB + sl, rB, ob, vo);
                const int oa = pin_s(k * 128);
                const unsigned la = pin_s(lA + sl);
                k = pin_s(kord2(t + 4)); ob = pin_s((k >> 1) * 589824 + (k & 1) * 18432);
                sl = pin_s(sl == 0 ? 2 * G_STAGE : sl - G_STAGE);
                spA(oa, la);
            }
        };
        WAIT_V(13); BAR(); issue1(17, 2);
        WAIT_V(13); BARX();
        if (wid == 0) flag_raise(ws, fz);
        issueA(2 * pm + 1, 3); if (layer == 0) load5(4, 0);
        if (wid == 0) WAIT_IF(layer, 5, 9); else WAIT_IF(layer, 4, 8);
        BAR(); issueA(kord2(2), 0); if (layer == 0) load5(4, 1);
        BAR(); look(); issueA(kord2(3), 2);
        WAIT_V(4); need(3); BARX(); issueB(kord2(2), 0); issueB(kord2(3), 2); look();
        WAIT_IF(wid == 0, 6, 5); BAR(); issue(4, 1);
        WAIT_V(9); need(8);
        steps2(2, 11); look();
        WAIT_IF(wid == 0, 10, 9); BAR(); issue(14, 0);
        WAIT_V(9); need(16);
        steps2(12, 29);
        WAIT_V(9); BAR(); issue5(0); issue5(1); issue5(2);
        WAIT_V(15); BAR(); issue5(6); issue5(7);
        BAR(); issue5(3); put5(0, 9);
        unsigned vb;
        {
            vb = attn_vb(wid, lane);
            const unsigned fo = flag_out(fl + F_Y, 256, pm, cg, lane), fi = flag_in(fl + F_Y, 256, rh * 8 + c5, 16, lane);
            __builtin_amdgcn_s_setprio(3);
            WAIT_V(0); BARX();
            if (wid == 0) { flag_raise(ws, fo); flags_wait<Y_FIRST>(ws, fi); }
            __builtin_amdgcn_s_setprio(0);
            BARX();
        }
        put5(9, 13);
        BAR(); put5(13, 18);
        BAR(); put5(18, 24);
        BAR(); put5(24, 30);
        BAR(); put5(30, 36);
        BAR(); issue5(16); put5(36, 41);
        BAR(); issue5(17); put5(41, 45);
        WAIT_V(5); BAR();
        WAIT_V(0); BAR();
        if (layer == 0) { attn_issueB(lds, ws, hp, wid, vb, 0, 0); attn_issueB(lds, ws, hp, wid, vb, 1, 1); }
        BAR();
        if (layer == 0) {
            __builtin_amdgcn_s_setprio(3);
            const unsigned fo = flag_out(F_X2, 512, (rh >> 1) + wid, wid ? 16 + c5 : (rh & 1) * 8 + c5, wid == 0 || (wid == 1 && (rh & 1) && rh < 31) ? lane : 16);
            BARX();
            if (wid < 2) flag_raise(ws, fo);
        }
        TRACE_DUMP;
      }
        if (blockIdx.x == 0 && wid8 == 4) ST8(RSRC(ws), lane_id_fresh() ? 0x80000000u : (unsigned)OFF_EP, 0, ((u32x2){(unsigned)ep, (unsigned)(ep >> 32)}), 16);
        asm volatile("gm_code_end1:");
        return;
    }
#pragma unroll 1
  for (int layer = 0; layer < NET_LAYERS; ++layer) {
    NET_IDS
    TRACE_BEGIN;
    char* wl = ws + OFF_L0 + layer * SZ_L;
    const unsigned fl = layer * F_LAYER;
    const char* xin = wl + L_XB;
    char* y = wl + (layer ? L_Z2T : L_Y);
#undef STAMP_SEC
#define STAMP_SEC 0
    {
    STAMP_BEGIN;
    attn_consumer(lds, x0, wl, layer, pa, hp, wid, [&] { WAIT_V(0); prologue_done(lds, ws, ep, b, lane_id_fresh()); } STAMP_FWD);
    STAMP_END(1 + 5 * layer);
    }
#undef STAMP_SEC
#define STAMP_SEC 1
    STAMP_BEGIN;
    gmlp_body(lds, wid & 1, wid >> 1, pm, cg, wl + (layer ? L_Y : L_Z2T), y, bsp STAMP_FWD);
    {
        WAIT_V(0);
        BARX();
        STAMP(11);
        const int lane = lane_id_fresh(), r = lane & 15, q = lane >> 4;
        const int wk = wid >> 1, rt = rh * 4 + (wid & 1) * 2 + wk;
        const Res rx = proj_residual_load(xin, rt, c5, lane);
        const LAS char *pw[3], *ph[3];
#pragma unroll
        for (int i = 0; i < 3; ++i) {
            pw[i] = opaque(lds + 3 * i * K5_STAGE + wk * 4608 + lane * 16);
            ph[i] = opaque(lds + 3 * i * K5_STAGE + wk * 4608 + 4096 + q * 128 + (r & 7) * 16);
        }
        const auto rY = RSRC(y + (size_t)(rt & ~1) * 73728 + wk * 1024);
        const unsigned vy[2] = {pin((unsigned)(wk * 73728 + lane * 16)), pin((unsigned)((1 - wk) * 73728 + lane * 16))};
        int dk[4];
#pragma unroll
        for (int i = 0; i < 4; ++i) dk[i] = pin_s((kord5(4 + i) - 4 - i) * 4096);
        auto yoff = [&](int t) { return pin_s(t == 0 || t == 17 ? kord5(t) * 4096 : t * 4096 + dk[t & 3]); };
        bf16x8 af[5][4], wf[4][5];
        auto lda = [&](int so, int i) { return __builtin_bit_cast(bf16x8, __builtin_amdgcn_raw_buffer_load_b128(rY, vy[i & 1] + (i >> 1) * 2048, so, 16)); };
        auto rdw = [&](int t, int c, int n) {
            const int sl = (t + 6) & 7, o = (sl % 3) * K5_STAGE + c * 9216;
            wf[2 * (t & 1) + c][n] = n < 4 ? LDS_FRAG(pw[sl / 3] + o + n * 1024) : LDS_FRAG(ph[sl / 3] + o);
        };
#pragma unroll
        for (int n = 0; n < 10; ++n) rdw(0, n / 5, n % 5);
        int so[18];
#pragma unroll
        for (int i = 0; i < 5; ++i) so[i] = yoff(i);
        so[17] = yoff(17);
        f32x4 v[5], u[5];
        proj_residual(v, rx);
#pragma unroll
        for (int n = 0; n < 5; ++n) v[n] = pin(v[n]);
        LAS char* xo = (LAS char*)opaque(lds + wid * 4608 + lane * 16);
        LAS char* xo4 = (LAS char*)opaque(lds + wid * 4608 + 4096 + (lane & 31) * 16);
        const LAS char* xi = opaque(lds + (wid ^ 2) * 4608 + lane * 16);
        const LAS char* xi4 = opaque(lds + (wid ^ 2) * 4608 + 4096 + (lane & 31) * 16);
        const ProjOut po = layer ? proj_out_final(out, rt, c5, lane) : proj_out(wl + SZ_L + L_XA, wl + SZ_L + L_STA, rt, c5, lane);
        asm volatile("" :: "s"(rY));
        WAIT_L(0);
        BARX();
        STAMP(12);
#pragma unroll
        for (int i = 0; i < K5_PRE; ++i) { af[i >> 2][i & 3] = lda(so[i >> 2], i & 3); SCHED0(); if (i >= PACE0 && i + 1 < K5_PRE) PACE; SCHED0(); }
        static_for<18>([&](auto tc) {
            constexpr int t = tc.value;
            bf16x8 (&a)[4] = af[t % 5];
#pragma unroll
            for (int h = 0; h < 4; ++h) {
                const int c = t < 17 ? h >> 1 : h & 1, m = t < 17 ? h & 1 : 1 - (h >> 1);
                if (t < 17 ? m == 0 : h < 2) { if (t == 17 && h == 1) WAIT_L(0); else WAIT_L(5); }
#pragma unroll
                for (int n = 0; n < 5; ++n) {
                    f32x4& e = m ? u[n] : v[n];
                    if (t == 0 && h == 1) MFMA_0(wf[0][n], a[1], e); else MFMA_T(wf[2 * (t & 1) + c][n], a[2 * c + m], e);
                    SCHED0();
                    if (t < 17 && m == 0) rdw(t + 1, c, n);
                    if (t < 14 && m == 1 && !(n & 1)) { const int i = y_load_at(6 * t + 3 * c + n / 2); if (i >= 0) af[(i >> 2) % 5][i & 3] = lda(so[i >> 2], i & 3); }
                    if (t < 12 && h == 3 && n == 3) so[t + 5] = yoff(t + 5);
                    if (t == 17 && h * 5 + n >= 13 && h * 5 + n < 18) { const int i = h * 5 + n - 13; *(LAS f32x4*)(i < 4 ? xo + i * 1024 : xo4) = u[i]; }
                    SCHED0();
                }
            }
            if ((t & 1) && t < 17) BAR();
            SCHED0();
        });
        {
            WAIT_L0(); BARX();
#pragma unroll
            for (int n = 0; n < 5; ++n) u[n] = *(const LAS f32x4*)(n < 4 ? xi + n * 1024 : xi4);
            SCHED0();
            asm volatile("s_nop 1" : "+v"(v[0]), "+v"(v[1]), "+v"(v[2]), "+v"(v[3]), "+v"(v[4]));
#pragma unroll
            for (int n = 0; n < 5; ++n) v[n] += u[n];
        }
        STAMP(13);
        if (layer) proj_store<true>(v, po);
        else { proj_store(v, po); WAIT_V(0); BARX(); }
    }
    STAMP_END(3 + 5 * layer);
    TRACE_DUMP;
  }
  asm volatile("gm_code_end2:");
}

}

#define STOP_CHECK(n)

extern "C" void kernel_launch(void* const* d_in, const int* in_sizes, int n_in, void* d_out, int out_size, void* d_ws, size_t ws_size,
                              hipStream_t stream) {
    (void)in_sizes; (void)n_in; (void)out_size; (void)ws_size;
    const u16* x = (const u16*)d_in[0];
    const u16* wqkv = (const u16*)d_in[1];
    const u16* wo = (const u16*)d_in[2];
    const u16* w_in = (const u16*)d_in[4];
    const u16* w_sp = (const u16*)d_in[5];
    const u16* b_sp = (const u16*)d_in[6];
    const u16* w_out = (const u16*)d_in[7];
    char* ws = (char*)d_ws;
    (void)hipFuncSetAttribute((const void*)k_net, hipFuncAttributeMaxDynamicSharedMemorySize, NET_LDS);
    k_net<<<256, 512, NET_LDS, stream>>>(ws, w_sp, b_sp, (char*)d_out, (const char*)x, (const char*)wqkv, wo, w_in, w_out DBG_LAUNCH(0));
}
```

```cpp
#include <hip/hip_runtime.h>
#include <stdint.h>

namespace {

typedef unsigned short u16;
typedef short bf16x8 __attribute__((ext_vector_type(8)));
typedef float f32x4 __attribute__((ext_vector_type(4)));
typedef float f32x2 __attribute__((ext_vector_type(2)));
typedef unsigned u32x4 __attribute__((ext_vector_type(4)));
typedef unsigned u32x2 __attribute__((ext_vector_type(2)));
typedef short s16x4 __attribute__((ext_vector_type(4)));
typedef __bf16 bf16x2_t __attribute__((ext_vector_type(2)));
#define LAS __attribute__((address_space(3)))
#define DEVI __device__ __forceinline__

constexpr int S_ = 2048, D_ = 576, FH = 2304;

constexpr size_t SZ_TWQKV = (size_t)16 * 18 * 6 * 1024;
constexpr size_t SZ_TWT = (size_t)48 * 18 * 256;
constexpr size_t SZ_TWO = (size_t)8 * 18 * 4608;
constexpr size_t SZ_TWIN = (size_t)16 * 18 * 18 * 1024;
constexpr size_t SZ_TWOUT = (size_t)8 * 72 * 4608;
constexpr size_t SZ_X = (size_t)S_ * D_ * 2;
constexpr size_t SZ_Z = (size_t)S_ * FH * 2;
constexpr size_t SZ_ST = (size_t)S_ * 64;
constexpr size_t SZ_ZST = (size_t)16 * 16 * 128 * 8;
constexpr size_t OFF_TWQKV = 0;
constexpr size_t OFF_TWT = OFF_TWQKV + SZ_TWQKV;
constexpr size_t OFF_TWO = OFF_TWT + SZ_TWT;
constexpr size_t OFF_TWIN = OFF_TWO + SZ_TWO;
constexpr size_t OFF_TWOUT = OFF_TWIN + SZ_TWIN;
constexpr size_t OFF_CNT = OFF_TWOUT + SZ_TWOUT;
constexpr size_t SZ_CNT = 10 * 65536;
constexpr size_t OFF_FW = OFF_CNT + SZ_CNT;
constexpr size_t OFF_FL = OFF_FW + 262144;
constexpr size_t OFF_EP = OFF_FL + 4096;
constexpr size_t OFF_FL2 = OFF_EP + 4096;
constexpr size_t OFF_ZST = OFF_FL2 + 12288;
constexpr size_t OFF_L0 = OFF_ZST + 2 * SZ_ZST;
constexpr size_t L_XA = 0, L_STA = L_XA + SZ_X, L_O = L_STA + SZ_ST, L_XB = L_O + SZ_X, L_STB = L_XB + SZ_X, L_Z2T = L_STB + SZ_ST, L_Y = L_Z2T + SZ_Z;
constexpr size_t SZ_L = 0;
static_assert(OFF_L0 + L_Y + SZ_Z <= ((size_t)256 << 20), "workspace");
constexpr unsigned F_O = OFF_CNT, F_X1 = F_O + 65536, F_Z2 = F_O + 131072, F_Y = F_O + 196608, F_LAYER = 262144, F_X2 = F_O + 2 * F_LAYER;

DEVI unsigned pack2(float a, float b) { f32x2 v = {a, b}; bf16x2_t r = __builtin_convertvector(v, bf16x2_t); return __builtin_bit_cast(unsigned, r); }
DEVI float bflo(unsigned u) { return __uint_as_float(u << 16); }
DEVI float bfhi(unsigned u) { return __uint_as_float(u & 0xffff0000u); }
DEVI int slot2row(int sg) { return (sg & ~31) + 2 * (sg & 15) + ((sg >> 4) & 1); }
DEVI float fmax2(float a, float b) { return __builtin_amdgcn_fmed3f(a, b, __builtin_inff()); }
#define XQ4(OP, MID) \
    asm volatile("s_nop 1\n\tv_permlane32_swap_b32 %0, %1\n\tv_permlane32_swap_b32 %2, %3\n\t" OP " %0, %0, %1\n\t" OP " %2, %2, %3\n\t" \
                 "s_nop 1\n\tv_permlane16_swap_b32 %0, %2\n\t" OP " %0, %0, %2\n\t" MID \
                 "v_mov_b32 %2, %0\n\ts_nop 1\n\tv_permlane16_swap_b32 %0, %2\n\tv_mov_b32 %1, %0\n\tv_mov_b32 %3, %2\n\ts_nop 0\n\t" \
                 "v_permlane32_swap_b32 %0, %1\n\tv_permlane32_swap_b32 %2, %3" : "+v"(x[0]), "+v"(x[1]), "+v"(x[2]), "+v"(x[3]))
DEVI void xq4_max(float (&x)[4]) { XQ4("v_max_f32", ""); }
DEVI void xq4_sum(float (&x)[4]) { XQ4("v_add_f32", ""); }
DEVI void xq4_sum_rcp(float (&x)[4]) { XQ4("v_add_f32", "v_rcp_f32 %0, %0\n\ts_nop 0\n\t"); }
template <class T> DEVI T pin(T v) { asm volatile("" : "+v"(v)); return v; }
template <int V> struct IC { static constexpr int value = V; };
template <int N, int I = 0, class F> DEVI void static_for(F&& f) { if constexpr (I < N) { f(IC<I>{}); static_for<N, I + 1>(f); } }
template <class T> DEVI T* own_s(T* p) { T* r; asm volatile("s_mov_b64 %0, %1" : "=&s"(r) : "s"(p)); return r; }
DEVI int pin_s(int v) { asm volatile("" : "+s"(v)); return v; }
DEVI float xq_sum(float v) {
    auto a = __builtin_amdgcn_permlane32_swap(__float_as_uint(v), __float_as_uint(v), false, false);
    v = __uint_as_float(a[0]) + __uint_as_float(a[1]);
    auto b = __builtin_amdgcn_permlane16_swap(__float_as_uint(v), __float_as_uint(v), false, false);
    return __uint_as_float(b[0]) + __uint_as_float(b[1]);
}
#define RSRC(p) __builtin_amdgcn_make_buffer_rsrc((void*)(p), 0, 0x7fffffff, 0x00020000)
#define ST16(rs, vo, so, v, aux) __builtin_amdgcn_raw_buffer_store_b128((v), (rs), (vo) + (so), 0, aux)
#define ST8(rs, vo, so, v, aux) __builtin_amdgcn_raw_buffer_store_b64((v), (rs), (vo), (so), aux)
#define WAIT_V(n) asm volatile("s_waitcnt vmcnt(%0)" ::"i"(n) : "memory")
#define WAIT_L0() asm volatile("s_waitcnt lgkmcnt(0)" ::: "memory")
#define BAR() do { asm volatile("" ::: "memory"); __builtin_amdgcn_s_barrier(); asm volatile("" ::: "memory"); } while (0)
#define BARX() do { __builtin_amdgcn_sched_barrier(0); BAR(); __builtin_amdgcn_sched_barrier(0); } while (0)
#define LOOP_ALIGN() asm volatile(".p2align 6")
#define SCHED0() __builtin_amdgcn_sched_barrier(0)
#define MFMA(a, b, c) __builtin_amdgcn_mfma_f32_16x16x32_bf16((a), (b), (c), 0, 0, 0)
#define MFMA_T(a, b, c) asm volatile("v_mfma_f32_16x16x32_bf16 %0, %1, %2, %0" : "+v"(c) : "v"(a), "v"(b))
#define MFMA_0(a, b, c) asm volatile("v_mfma_f32_16x16x32_bf16 %0, %1, %2, 0" : "=&v"(c) : "v"(a), "v"(b))
#define WAIT_L(n) __builtin_amdgcn_s_waitcnt(0xC07F | ((n) << 8))
#define LDS_FRAG(p) (*(const LAS bf16x8*)(p))
DEVI int lane_id_fresh() { int z = 0; asm volatile("" : "+v"(z)); return __builtin_amdgcn_mbcnt_hi(~0u, __builtin_amdgcn_mbcnt_lo(~0u, z)); }
DEVI const LAS char* opaque(const LAS char* p) { asm volatile("" : "+v"(p)); return p; }
#define DBG_ARG
#define DBG_LAUNCH(n)
#define STAMP(i)
#define STAMP_DECL
#define STAMP_BEGIN
#define STAMP_END(n)
#define STAMP_PARAM
#define STAMP_FWD

#define PST_PARAM
#define PST_FWD
#define PST_DECL
#define PST(i)
#define PST_DUMP(c, k)
#define TRACE_ATTR
#define TRACE_BEGIN
#define TRACE_DUMP

DEVI float add1(float a, float b) { float r; asm("v_add_f32 %0, %1, %2" : "=v"(r) : "v"(a), "v"(b)); return r; }
DEVI float mul1(float a, float b) { float r; asm("v_mul_f32 %0, %1, %2" : "=v"(r) : "v"(a), "v"(b)); return r; }
DEVI float rstd_of(float s, float q, float inv_n, float& mean) {
    float var;
    asm("v_mul_f32 %0, %2, %3\n\tv_mul_f32 %1, %2, %4\n\tv_fma_f32 %1, -%0, %0, %1" : "=&v"(mean), "=&v"(var) : "s"(inv_n), "v"(s), "v"(q));
    return __builtin_amdgcn_rsqf(fmax2(var, 0.f) + 1e-5f);
}
DEVI float ln_rstd(const f32x4 p) {
    float s, q, mean;
    asm("v_add_f32 %0, %2, %4\n\tv_add_f32 %1, %3, %5\n\ts_nop 0\n\t"
        "v_add_f32_dpp %0, %0, %0 quad_perm:[1,0,3,2] row_mask:0xf bank_mask:0xf\n\tv_add_f32_dpp %1, %1, %1 quad_perm:[1,0,3,2] row_mask:0xf bank_mask:0xf\n\ts_nop 0\n\t"
        "v_add_f32_dpp %0, %0, %0 quad_perm:[2,3,0,1] row_mask:0xf bank_mask:0xf\n\tv_add_f32_dpp %1, %1, %1 quad_perm:[2,3,0,1] row_mask:0xf bank_mask:0xf"
        : "=&v"(s), "=&v"(q) : "v"(p[0]), "v"(p[1]), "v"(p[2]), "v"(p[3]));
    return rstd_of(s, q, 1.0f / 576.0f, mean);
}

struct Res { u32x4 a, c; u32x2 e; };
DEVI void proj_residual(f32x4 (&v)[5], const Res& x) {
    v[0] = (f32x4){bflo(x.a.x), bfhi(x.a.x), bflo(x.a.y), bfhi(x.a.y)}; v[1] = (f32x4){bflo(x.a.z), bfhi(x.a.z), bflo(x.a.w), bfhi(x.a.w)};
    v[2] = (f32x4){bflo(x.c.x), bfhi(x.c.x), bflo(x.c.y), bfhi(x.c.y)}; v[3] = (f32x4){bflo(x.c.z), bfhi(x.c.z), bflo(x.c.w), bfhi(x.c.w)};
    v[4] = (f32x4){bflo(x.e.x), bfhi(x.e.x), bflo(x.e.y), bfhi(x.e.y)};
}
struct ProjOut { __amdgpu_buffer_rsrc_t x, st; unsigned v0, v1, v2, vs; };
DEVI ProjOut proj_out(char* xout, char* stout, int rt, int cg, int lane) {
    const int r = lane & 15, q = lane >> 4;
    ProjOut p = {RSRC(xout + (size_t)rt * 18432 + cg * 2304), RSRC(stout + (size_t)rt * 1024 + cg * 8),
                 pin((unsigned)lane * 16u), pin((unsigned)lane * 16u + 1024u), pin((unsigned)(2048 + r * 16 + (q & 1) * 8)), pin((unsigned)(r * 64 + (q >> 1) * 4))};
    asm volatile("" :: "s"(p.x), "s"(p.st));
    return p;
}
DEVI ProjOut proj_out_final(char* out, int rt, int cg, int lane) {
    const int q = lane >> 4;
    const unsigned o = (unsigned)(slot2row(rt * 16 + (lane & 15)) * 1152 + cg * 144);
    ProjOut p = {RSRC(out), RSRC(out), pin(o + q * 16), pin(o + 64 + q * 16), pin(o + 128 + (q & 1) * 8), 0u};
    return p;
}
DEVI Res proj_residual_load(const ProjOut& p) {
    return {__builtin_amdgcn_raw_buffer_load_b128(p.x, p.v0, 0, 16), __builtin_amdgcn_raw_buffer_load_b128(p.x, p.v1, 0, 16), __builtin_amdgcn_raw_buffer_load_b64(p.x, p.v2, 0, 16)};
}
template <bool FINAL = false>
DEVI void proj_store(f32x4 (&v)[5], const ProjOut& p) {
    ST16(p.x, p.v0, 0, ((u32x4){pack2(v[0][0], v[0][1]), pack2(v[0][2], v[0][3]), pack2(v[1][0], v[1][1]), pack2(v[1][2], v[1][3])}), FINAL ? 0 : 16);
    SCHED0();
    asm volatile("" : "+v"(v[2]), "+v"(v[3]), "+v"(v[4]));
    ST16(p.x, p.v1, 0, ((u32x4){pack2(v[2][0], v[2][1]), pack2(v[2][2], v[2][3]), pack2(v[3][0], v[3][1]), pack2(v[3][2], v[3][3])}), FINAL ? 0 : 16);
    SCHED0();
    ST8(p.x, p.v2, 0, ((u32x2){pack2(v[4][0], v[4][1]), pack2(v[4][2], v[4][3])}), FINAL ? 0 : 16);
    SCHED0();
    if (!FINAL) {
        const f32x4 s4 = ((v[0] + v[1]) + (v[2] + v[3])) + v[4] * 0.5f;
        const f32x4 q4 = ((v[0] * v[0] + v[1] * v[1]) + (v[2] * v[2] + v[3] * v[3])) + v[4] * v[4] * 0.5f;
        auto x = __builtin_amdgcn_permlane32_swap(__float_as_uint((s4[0] + s4[2]) + (s4[1] + s4[3])), __float_as_uint((q4[0] + q4[2]) + (q4[1] + q4[3])), false, false);
        const float t = __uint_as_float(x[0]) + __uint_as_float(x[1]);
        auto y = __builtin_amdgcn_permlane16_swap(__float_as_uint(t), __float_as_uint(t), false, false);
        __builtin_amdgcn_raw_buffer_store_b32(__float_as_uint(__uint_as_float(y[0]) + __uint_as_float(y[1])), p.st, p.vs, 0, 16);
        SCHED0();
    }
}

DEVI void side_load(f32x4 (&d)[3], const char* p, const char* p2) {
    asm volatile("global_load_dwordx4 %0, %3, off sc1\n\tglobal_load_dwordx4 %1, %3, off offset:1024 sc1\n\tglobal_load_dwordx4 %2, %4, off sc1"
                 : "=&v"(d[0]), "=&v"(d[1]), "=&v"(d[2]) : "v"(p), "v"(p2) : "memory");
}
DEVI void side_fence(f32x4 (&d)[3]) { asm volatile("" : "+v"(d[0]), "+v"(d[1]), "+v"(d[2])); }
DEVI void lds_word_set(LAS char* p, unsigned v) { asm volatile("ds_write_b32 %0, %1" :: "v"((unsigned)(size_t)p), "v"(v) : "memory"); }
#define DMA_I(v, off, sc) "buffer_load_dwordx4 " v ", %1, %2 offen offset:" off sc " lds\n\t"
#define DMA5(sc) asm volatile("s_mov_b32 m0, %0\n\ts_mov_b32 exec_hi, 0\n\ts_nop 2\n\t" DMA_I("%3", "0", sc) "s_mov_b32 exec_hi, -1\n\t" \
    DMA_I("%3", "512", sc) DMA_I("%3", "1536", sc) DMA_I("%3", "2560", sc) DMA_I("%3", "3584", sc) :: "s"(la), "s"(rs), "s"(so), "v"(vo) : "memory")
#define DMA4(sc) asm volatile("s_mov_b32 m0, %0\n\ts_nop 3\n\t" DMA_I("%3", "0", sc) DMA_I("%4", "1024", sc) DMA_I("%5", "2048", sc) DMA_I("%6", "3072", sc) \
    :: "s"(la), "s"(rs), "s"(so), "v"(v0), "v"(v1), "v"(v2), "v"(v3) : "memory")
template <bool SC1> DEVI void dma5(unsigned la, __amdgpu_buffer_rsrc_t rs, int so, unsigned vo) { if (SC1) DMA5(" sc1"); else DMA5(""); }
template <bool SC1> DEVI void dma4(unsigned la, __amdgpu_buffer_rsrc_t rs, int so, unsigned v0, unsigned v1, unsigned v2, unsigned v3) { if (SC1) DMA4(" sc1"); else DMA4(""); }
#define DMA2(sc) asm volatile("s_mov_b32 m0, %0\n\ts_nop 3\n\t" DMA_I("%3", "0", sc) DMA_I("%4", "1024", sc) :: "s"(la), "s"(rs), "s"(so), "v"(v0), "v"(v1) : "memory")
template <bool SC1> DEVI void dma2(unsigned la, __amdgpu_buffer_rsrc_t rs, int so, unsigned v0, unsigned v1) { if (SC1) DMA2(" sc1"); else DMA2(""); }
DEVI void dma1(unsigned la, __amdgpu_buffer_rsrc_t rs, int so, unsigned v0) { asm volatile("s_mov_b32 m0, %0\n\ts_nop 3\n\t" DMA_I("%3", "0", "") :: "s"(la), "s"(rs), "s"(so), "v"(v0) : "memory"); }
#define WAIT_IF(c, a, b) do { if (c) WAIT_V(a); else WAIT_V(b); } while (0)
template <int T0, int T1, int P, int S0 = 0, class ISSUE>
DEVI void ring_steps(ISSUE&& issue) {
    int slot = (T0 + S0) % 3;
    LOOP_ALIGN();
#pragma unroll 1
    for (int t = T0; t < T1; ++t) {
        WAIT_V(P); BAR();
        issue(t + 3, slot);
        slot = slot == 2 ? 0 : slot + 1;
    }
}

DEVI unsigned flag_out(unsigned use, int pitch, int group, int writer, int lane) { return pin(lane < 16 ? use + (group * 16 + lane) * pitch + writer * 16 : 0x80000000u); }
DEVI unsigned flag_in(unsigned use, int pitch, int reader, int n, int lane) { return pin(use + reader * pitch + (lane < n ? lane : 0) * 16); }
DEVI void flag_raise(const char* ws, unsigned vo) { ST16(RSRC(ws), vo, 0, ((u32x4){1u, 1u, 1u, 1u}), 16); SCHED0(); }
template <int FIRST, int AGAIN = 1>
DEVI void flags_wait(const char* ws, unsigned vo) {
    unsigned v, n = 400000u;
    asm volatile("s_nop 4\n\ts_sleep %4\n"
                 "1:\n\tbuffer_load_dword %0, %2, %3, 0 offen sc1\n\ts_waitcnt vmcnt(0)\n\tv_cmp_eq_u32 vcc, 0, %0\n\ts_cbranch_vccz 2f\n\t"
                 "s_sleep %5\n\ts_sub_u32 %1, %1, 1\n\ts_cbranch_scc0 1b\n2:"
                 : "=&v"(v), "+s"(n) : "v"(vo), "s"(RSRC(ws)), "i"(FIRST), "i"(AGAIN) : "vcc", "scc", "memory");
    SCHED0();
}
template <int FIRST>
DEVI void epoch_wait(const char* ws, unsigned vo, unsigned long long ep) {
    unsigned long long v; unsigned n = 400000u;
    asm volatile("s_nop 4\n\ts_sleep %5\n"
                 "1:\n\tbuffer_load_dwordx2 %0, %2, %3, 0 offen sc1\n\ts_waitcnt vmcnt(0)\n\tv_cmp_ne_u64 vcc, %4, %0\n\ts_cbranch_vccz 2f\n\t"
                 "s_sleep 1\n\ts_sub_u32 %1, %1, 1\n\ts_cbranch_scc0 1b\n2:"
                 : "=&v"(v), "+s"(n) : "v"(vo), "s"(RSRC(ws)), "s"(ep), "i"(FIRST) : "vcc", "scc", "memory");
    SCHED0();
}
DEVI unsigned lds_arrive(LAS char* p) {
    unsigned v;
    asm volatile("s_mov_b64 exec, 1\n\tds_add_rtn_u32 %0, %1, %2\n\ts_mov_b64 exec, -1\n\ts_waitcnt lgkmcnt(0)" : "=&v"(v) : "v"((unsigned)(size_t)p), "v"(1u) : "memory");
    return __builtin_amdgcn_readfirstlane(v);
}
DEVI void lds_count_wait(LAS char* p, unsigned n) {
#pragma unroll 1
    for (unsigned spin = 0; spin < 40000000u; ++spin) {
        unsigned v;
        asm volatile("ds_read_b32 %0, %1\n\ts_waitcnt lgkmcnt(0)" : "=v"(v) : "v"((unsigned)(size_t)p) : "memory");
        if (__builtin_amdgcn_readfirstlane(v) >= n) break;
    }
}

constexpr int K1_STAGE = 32768;
constexpr int WO_OFF = 69632;
constexpr int P_IMG = 102400;
constexpr int G_OWN = 139264;
constexpr int G_PART = G_OWN + 18432;
constexpr int G_TAB = G_PART + 2048;
constexpr int K1_SCR = G_TAB + 1536;
constexpr int G_WORD = K1_SCR + 576;
constexpr int G_JUNK = G_WORD + 64;
constexpr int NET_LDS = G_JUNK + 1024;
DEVI __amdgpu_buffer_rsrc_t code_rsrc() {
    unsigned long long pc; int d0, len;
    asm volatile("s_getpc_b64 %0\n1:\n\ts_mov_b32 %1, gm_code_begin-1b\n\ts_mov_b32 %2, gm_code_end1-gm_code_begin\n\ts_max_i32 %2, %2, gm_code_end2-gm_code_begin"
                 : "=s"(pc), "=s"(d0), "=s"(len) :: "scc");
    return __builtin_amdgcn_make_buffer_rsrc((void*)((pc + d0) & ~4095ull), 0, len + ((unsigned)(pc + d0) & 4095), 0x00020000);
}

#define WO_PRE 9
#define WO_W 33
#define REL_PRIO 0
#define W_FIRST 2
#define I_SLEEP 12
#define PACE0 1
#define PACE __builtin_amdgcn_s_sleep(1)
#define POLLW 0
#define WO_EV 4
#define WIN_LOOK 32
#define W3_SLEEP 0
#define RSTD_PRIO 3
#define WIN_PRE 1
#define O_FIRST 8
#define X1_FIRST 4
#define X2_FIRST 4
#define Y_FIRST 14
#define Z2_FIRST 10
DEVI int win_strip(int b) { return ((b - 112) & 7) * 18 + ((b - 112) >> 3); }
DEVI int win_third(int b, int w) { return b & 8 ? 3 - w : w - 1; }
DEVI unsigned attn_vb(int wid, int lane) {
    const int c = lane & 15;
    return pin((unsigned)(wid < 3 ? lane * 16 : (c < 8 ? (c >> 1) & 1 : 2) * 4608 + (lane >> 4) * 64 + (c < 8 ? (c >> 2) * 2 + (c & 1) : c & 3) * 16));
}
DEVI void attn_issueB(LAS char* lds, const char* ws, int hp, int wid, unsigned vb, int s, int slot) {
    const unsigned la = (unsigned)(size_t)lds + slot * K1_STAGE + 18432 + wid * 4096;
    if (wid < 3) dma4<true>(la, RSRC(ws), (int)OFF_TWQKV + hp * (18 * 6144) + wid * 4096 + s * 12288, vb, vb, vb, vb);
    else asm volatile("s_mov_b32 m0, %0\n\ts_nop 3\n\tbuffer_load_dwordx4 %4, %1, %2 offen sc1 lds\n\tbuffer_load_dwordx4 %4, %1, %3 offen offset:1024 sc1 lds"
                      :: "s"(la), "s"(RSRC(ws)), "s"((int)OFF_TWT + hp * 13824 + s * 512), "s"((int)OFF_TWT + hp * 13824 + s * 512 + 256 - 1024), "v"(vb) : "memory");
}
struct AttnX { __amdgpu_buffer_rsrc_t rs; unsigned v[4], vh[2]; int oA, oh, step; };
DEVI AttnX attn_x(const char* x0, const char* xa, int layer, int pm, int wid, int lane) {
    const int l3 = lane >> 3, pmh = pm > 0 ? pm : 1;
    const unsigned v = layer ? lane * 16 : 2 * l3 * 1152 + ((lane & 7) ^ l3) * 16 + 4096, d1 = layer ? 0 : 16 * 1152 - 1024, d2 = layer ? 16384 : 1152 - 2048;
    const unsigned vh = layer ? (l3 & 1) * 18432 + (lane >> 4) * 256 + (8 + (lane & 7)) * 16 : v + 16 * 1152;
    return {RSRC(layer ? xa : x0 - 4096), {pin(v), pin(v + d1), pin(v + d2), pin(v + d1 + d2)}, {pin(vh), pin(vh + (layer ? 0 : 1152 - 1024))},
            (pm * 8 + 2 * wid) * 18432, (pmh * 8 - 2) * 18432, layer ? 2048 : 128};
}
DEVI void attn_issueA(LAS char* lds, const AttnX& a, int wid, int s, int slot) {
    const unsigned la = (unsigned)(size_t)lds + slot * K1_STAGE;
    if (wid == 3) dma2<true>(la, a.rs, a.oh + s * a.step, a.vh[0], a.vh[1]);
    dma4<true>(la + 2048 + wid * 4096, a.rs, a.oA + s * a.step, a.v[0], a.v[1], a.v[2], a.v[3]);
}
template <class DONE, class NEXT>
DEVI void attn_producer(LAS char* lds, const char* ws, const char* x0, const char* wl, unsigned fl, unsigned long long ep, int layer, int pm, int hp, int wid, DONE&& done, NEXT&& next) {
    const int lane = lane_id_fresh();
    const int pmh = pm > 0 ? pm : 1;
    const unsigned vo = lane * 16;
    const AttnX ax = attn_x(x0, wl + L_XA, layer, pm, wid, lane);
    const unsigned vb = attn_vb(wid, lane), fd = pin((unsigned)OFF_FL + wid * 1024 + vo);
    auto issueB = [&](int s, int slot) { attn_issueB(lds, ws, hp, wid, vb, s, slot); };
    auto issue = [&](int s, int slot) { attn_issueA(lds, ax, wid, s, slot); issueB(s, slot); };
    const int hr = lane >> 2;
    const char* ps = pin(wl + L_STA + (size_t)(pm * 128 + wid * 32) * 64 + lane * 16);
    const char* psh = pin(wl + L_STA + (size_t)((pmh * 8 - 2 + (hr >> 3)) * 16 + 8 + (hr & 7)) * 64 + (lane & 3) * 16);
    LAS char *tr = (LAS char*)opaque(lds + K1_SCR + (16 + wid * 32 + hr) * 4), *th = (LAS char*)opaque(lds + K1_SCR + hr * 4);
    if (layer == 0) {
        const unsigned fi = pin((unsigned)OFF_FW + (pm * 16 + hp) * 1024 + (lane < 6 ? lane : 0) * 128);
        done();
        next(-2);
        if (wid == 0) { epoch_wait<W_FIRST>(ws, fi, ep); issueB(0, 0); }
        BARX();
        if (wid) issueB(0, 0);
        issueB(1, 1); issue(2, 2);
        WAIT_IF(wid == 3, 10, 12);
    } else {
        issueB(2, 2);
        const unsigned fi = flag_in(F_X2, 512, pm * 16 + hp, pm > 0 ? 24 : 16, lane);
        if (wid == 0) {
            flags_wait<X2_FIRST>(ws, fi);
            AttnX a0 = ax;
#pragma unroll
            for (int i = 0; i < 4; ++i) { attn_issueA(lds, a0, i, 0, 0); a0.oA += 36864; }
        }
        BARX();
        attn_issueA(lds, ax, wid, 1, 1); attn_issueA(lds, ax, wid, 2, 2);
        WAIT_IF(wid == 3, 12, 8);
    }
    __builtin_amdgcn_s_setprio(0);
    BAR();
    if (layer) WAIT_IF(wid == 3, 6, 4);
    next(-1);
    ring_steps<0, 4, 8>(issue);
    {
        unsigned long long done;
        if (layer == 0) asm volatile("s_nop 4\n\tbuffer_load_dwordx2 %0, %1, %2, 0 offen sc1" : "+v"(done) : "v"(fd), "s"(RSRC(ws)) : "memory");
        WAIT_IF(layer, 8, 9); BAR(); issue(7, 1);
        WAIT_V(8);
        if (layer == 0) { asm volatile("" : "+v"(done)); if (__any(done != ep)) epoch_wait<0>(ws, fd, ep); }
        BAR(); issue(8, 2);
    }
    const auto rW = RSRC(ws + OFF_TWO + (size_t)(hp >> 1) * 82944 + wid * 4096);
    auto issueW = [&](int i) { dma4<true>((unsigned)(size_t)lds + WO_OFF + (i * 4 + wid) * 4096, rW, i * 16384, vo, vo, vo, vo); };
    {
        f32x4 sp[3];
        side_load(sp, ps, psh);
        WAIT_V(11); BAR(); issueW(2); issueW(3);
        WAIT_V(8); BARX();
        __builtin_amdgcn_s_setprio(RSTD_PRIO);
        side_fence(sp);
#pragma unroll
        for (int i = 0; i < 3; ++i) lds_word_set(i < 2 ? tr + i * 64 : th, __float_as_uint(ln_rstd(sp[i])));
        __builtin_amdgcn_s_setprio(0);
        issueW(4);
        if (wid == 0) dma2<true>((unsigned)(size_t)lds + WO_OFF + 80896, rW, 80896, vo, vo);
        WAIT_L0();
    }
    BAR();
    issueW(0); issueW(1);
    next(1);
    const unsigned fo = flag_out(fl + F_O, 256, pm, hp, lane), fi = flag_in(fl + F_O, 256, pm * 16 + hp, 16, lane);
    WAIT_V(0);
    if (wid == POLLW) __builtin_amdgcn_s_setprio(3);
    BARX();
    if (wid == POLLW) flag_raise(ws, fo);
    next(2);
    if (wid == POLLW) flags_wait<O_FIRST>(ws, fi);
    __builtin_amdgcn_s_setprio(0);
    BARX();
}

#define STAMP_SEC 0
template <class DONE>
DEVI void attn_consumer(LAS char* lds, const char* x0, char* wl, int layer, int pm, int hp, int wid, DONE&& done STAMP_PARAM) {
    const int lane = lane_id_fresh(), r = lane & 15, q = lane >> 4;
    const char* xa = wl + L_XA;
    char* o = wl + L_O;
    const int T = wid & 1, h = wid >> 1;
    const LAS char *ph[2], *pa[2];
#pragma unroll
    for (int kk = 0; kk < 2; ++kk) {
        const int sw = ((4 * kk + q) ^ (r & 7)) * 16;
        ph[kk] = opaque(lds + (layer ? kk * 1024 + q * 256 + (r ^ (T ? 0 : 8)) * 16 : (r ^ (T ? 0 : 8)) * 128 + sw));
        pa[kk] = opaque(lds + T * 2048 + (layer ? kk * 1024 + lane * 16 : r * 128 + sw));
    }
    const LAS char* pb = lds + h * 3072 + lane * 16;
    const LAS char* pt = lds + q * 256 + (r ^ (r < 8 ? 4 * h : 2 * h)) * 16;
    f32x4 acc[5][4];
#pragma unroll
    for (int m = 0; m < 5; ++m)
#pragma unroll
        for (int n = 0; n < 4; ++n) if (m + n) acc[m][n] = pin((f32x4){0.f, 0.f, 0.f, 0.f});
    bf16x8 fa[2][5], fb[2][4];
    auto rd1 = [&](int kk, int i) {
        if (i < 3) fb[kk][i] = LDS_FRAG(pb + 18432 + kk * 6144 + i * 1024);
        else if (i == 3) fb[kk][3] = LDS_FRAG(pt + 30720 + kk * 1024);
        else if (i == 4) fa[kk][0] = LDS_FRAG(ph[kk]);
        else if (i < 9) fa[kk][i - 4] = LDS_FRAG(pa[kk] + 2048 + (i - 5) * 4096);
    };
    f32x4 mb, mb0;
#pragma unroll
    for (int j = 0; j < 4; ++j) {
        const bool ok = ((r - 4 * q - j) & 15) < 8;
        mb[j] = ok ? 0.f : -__builtin_inff();
        mb0[j] = (ok && (pm > 0 || 4 * q + j <= r)) ? 0.f : -__builtin_inff();
    }
    mb = pin(mb); mb0 = pin(mb0);
    const unsigned mp0 = pin((4 * q > r ? 0xffffu : 0u) | (4 * q + 1 > r ? 0xffff0000u : 0u)), mp1 = pin((4 * q + 2 > r ? 0xffffu : 0u) | (4 * q + 3 > r ? 0xffff0000u : 0u));
    const unsigned qtm = pin(q == 0 ? 0xffffffffu : 0u);
    const unsigned vom = pin((unsigned)(r * 16 + hp * 1024 + (2 * h + (q >> 1)) * 256 + (q & 1) * 36864));
    const unsigned vot = pin((unsigned)(r * 16 + 16384 + (hp >> 1) * 256 + (hp & 1) * 8 + h * 4 + q * 36864));
    LAS char* xw = (LAS char*)opaque(lds + (q == 2 ? wid * 640 : G_JUNK) + r * 8);
    const LAS char* xr = opaque(lds + wid * 640 + (q * 4 + (r >> 2)) * 8);
    const LAS char* tk0 = opaque(lds + K1_SCR + ((q * 4) ^ (T ? 0 : 8)) * 4);
    const LAS char* tk = opaque(lds + K1_SCR + ((1 + T) * 16 + q * 4) * 4);
    const LAS char* tq = opaque(lds + K1_SCR + ((1 + T) * 16 + r) * 4);
    pb = opaque(pb); pt = opaque(pt);
    STAMP(2);
    const int rt = pm * 8 + (hp & 1) * 4 + wid, cg = hp >> 1;
    const ProjOut pr = layer ? proj_out((char*)xa, (char*)xa, rt, cg, lane) : proj_out_final((char*)x0, rt, cg, lane);
    BAR();
    if (layer == 0) done();
    BARX();
    STAMP(3);
#pragma unroll
    for (int i = 0; i < 9; ++i) rd1(0, i);
    SCHED0();
    const Res rx = proj_residual_load(pr);
    SCHED0();
    int slot = 0, left = 8;
    LOOP_ALIGN();
#pragma unroll 1
    do {
        int d;
#pragma unroll
        for (int kk = 0; kk < 2; ++kk) {
            WAIT_L(0);
            if (kk) BAR();
            SCHED0();
#pragma unroll
            for (int i = 1; i < 20; ++i) {
                const int m = i >> 2, n = i & 3;
                const bf16x8 &A = fa[kk][m], &B = fb[kk][n];
                acc[m][n] = n != 2 ? MFMA(B, A, acc[m][n]) : MFMA(A, B, acc[m][n]);
                SCHED0();
                if (i <= 9) rd1(kk ^ 1, i - 1);
                if (kk == 0) {
                    if (i == 10) d = pin_s(slot == 2 ? -2 * K1_STAGE : K1_STAGE);
                    if (i == 11 || i == 12) ph[i & 1] = opaque(ph[i & 1] + d);
                    if (i == 13 || i == 14) pa[i & 1] = opaque(pa[i & 1] + d);
                    if (i == 15) pb = opaque(pb + d);
                    if (i == 16) pt = opaque(pt + d);
                } else {
                    if (i == 11) slot = pin_s(slot == 2 ? 0 : slot + 1);
                    if (i == 13) left = pin_s(left - 1);
                }
                SCHED0();
            }
        }
    } while (left);
    {
        typedef unsigned u32x10 __attribute__((ext_vector_type(10)));
        f32x4 rk[5], rs[4], ks[4]; float rq[4];
        u32x2 ts[5];
        u32x4 kf[5], qf[4];
        u32x10 tf, vf;
        WAIT_L(0);
        SCHED0();
        static_for<38>([&](auto ic) {
            constexpr int i = ic.value, kk = i < 14 ? 0 : i < 28 ? 1 : i >= 33, t = i % 14, n = i >= 28 ? 2 : t < 5 ? 3 : t < 10 ? 1 : 0, m = i >= 28 ? (i - 28) % 5 : t < 10 ? t % 5 : t - 9;
            if (i == 14) { WAIT_L(0); BAR(); SCHED0(); }
            acc[m][n] = n != 2 ? MFMA(fb[kk][n], fa[kk][m], acc[m][n]) : MFMA(fa[kk][m], fb[kk][n], acc[m][n]);
            SCHED0();
            if (i < 9) rd1(1, i);
            if (i == 14) { rk[0] = *(const LAS f32x4*)(tk0); rk[1] = *(const LAS f32x4*)(tk); }
            if (i == 15) { rk[2] = *(const LAS f32x4*)(tk + 128); rk[3] = *(const LAS f32x4*)(tk + 256); }
            if (i == 16) {
                rk[4] = *(const LAS f32x4*)(tk + 384);
#pragma unroll
                for (int g = 0; g < 4; ++g) rq[g] = *(const LAS float*)(tq + g * 128);
            }
            if constexpr (i >= 19 && i < 24) {
                ts[i - 19] = (u32x2){pack2(acc[i - 19][3][0], acc[i - 19][3][1]), pack2(acc[i - 19][3][2], acc[i - 19][3][3])};
                *(LAS u32x2*)(xw + (i - 19) * 128) = ts[i - 19];
            }
            if constexpr (i >= 24 && i < 29) kf[i - 24] = pin((u32x4){pack2(acc[i - 24][1][0], acc[i - 24][1][1]), pack2(acc[i - 24][1][2], acc[i - 24][1][3]), ts[i - 24].x, ts[i - 24].y});
            if constexpr (i >= 29 && i < 33) qf[i - 29] = pin((u32x4){pack2(acc[i - 28][0][0], acc[i - 28][0][1]), pack2(acc[i - 28][0][2], acc[i - 28][0][3]), pin(0u), kf[i - 28].z & qtm});
            if (i == 33) {
#pragma unroll
                for (int e = 0; e < 5; ++e) {
                    const u32x2 w = __builtin_bit_cast(u32x2, __builtin_amdgcn_ds_read_tr16_b64_v4i16((LAS s16x4*)(xr + e * 128)));
                    tf[2 * e] = w.x; tf[2 * e + 1] = w.y;
                }
            }
            if constexpr (i >= 34) {
#pragma unroll
                for (int j = 0; j < 4; ++j) rs[i - 34][j] = 4 * q + j > r ? rk[i - 34][j] : rk[i - 33][j];
                rs[i - 34] = pin(rs[i - 34]);
            }
            SCHED0();
        });
        STAMP(4);
        const f32x4 zero = {0.f, 0.f, 0.f, 0.f};
        f32x4 sp[4], sc[4];
#pragma unroll
        for (int g = 0; g < 4; ++g) {
            sp[g] = MFMA(__builtin_bit_cast(bf16x8, kf[g]), __builtin_bit_cast(bf16x8, qf[g]), zero);
            sc[g] = MFMA(__builtin_bit_cast(bf16x8, kf[g + 1]), __builtin_bit_cast(bf16x8, qf[g]), zero);
            const float c = rq[g] * (0.23570226039551584f * 1.4426950408889634f);
#pragma unroll
            for (int j = 0; j < 4; ++j) ks[g][j] = mul1(rs[g][j], c);
            SCHED0();
        }
#pragma unroll
        for (int m = 0; m < 5; ++m) { vf[2 * m] = pack2(acc[m][2][0], acc[m][2][1]); vf[2 * m + 1] = pack2(acc[m][2][2], acc[m][2][3]); }
        vf = pin(vf); tf = pin(tf);
        f32x4 t[4]; float mx[4], sm[4];
#pragma unroll
        for (int g = 0; g < 4; ++g) {
#pragma unroll
            for (int j = 0; j < 4; ++j) t[g][j] = 4 * q + j > r ? sp[g][j] : sc[g][j];
            t[g] = t[g] * ks[g] + (g == 0 ? mb0 : mb);
            mx[g] = fmaxf(fmaxf(t[g][0], t[g][1]), fmaxf(t[g][2], t[g][3]));
        }
        xq4_max(mx);
#pragma unroll
        for (int g = 0; g < 4; ++g) {
            t[g] = t[g] - mx[g];
#pragma unroll
            for (int j = 0; j < 4; ++j) t[g][j] = __builtin_amdgcn_exp2f(t[g][j]);
            sm[g] = (t[g][0] + t[g][2]) + (t[g][1] + t[g][3]);
        }
        f32x4 om[4], ot[4];
        static_for<4>([&](auto gc) {
            constexpr int g = gc.value;
            const f32x4 p = t[g] * rs[g];
            const unsigned p01 = pack2(p[0], p[1]), p23 = pack2(p[2], p[3]);
            const u32x4 pf = {p01 & mp0, p23 & mp1, p01 & ~mp0, p23 & ~mp1};
            om[g] = MFMA(__builtin_bit_cast(bf16x8, (u32x4)__builtin_shufflevector(vf, vf, 2 * g, 2 * g + 1, 2 * g + 2, 2 * g + 3)), __builtin_bit_cast(bf16x8, pf), zero);
            ot[g] = MFMA(__builtin_bit_cast(bf16x8, (u32x4)__builtin_shufflevector(tf, tf, 2 * g, 2 * g + 1, 2 * g + 2, 2 * g + 3)), __builtin_bit_cast(bf16x8, pf), zero);
        });
        xq4_sum_rcp(sm);
        const auto ro = RSRC(o + (size_t)(pm * 8 + T) * 18432);
#pragma unroll
        for (int g = 0; g < 4; g += 2) {
            const f32x4 a = om[g] * sm[g], c = om[g + 1] * sm[g + 1];
            const auto x = __builtin_amdgcn_permlane16_swap(pack2(a[0], a[1]), pack2(c[0], c[1]), false, false);
            const auto y = __builtin_amdgcn_permlane16_swap(pack2(a[2], a[3]), pack2(c[2], c[3]), false, false);
            ST16(ro, vom, g * 36864, ((u32x4){x[0], y[0], x[1], y[1]}), 16);
        }
        unsigned tw[4];
#pragma unroll
        for (int g = 0; g < 4; ++g) { const f32x2 w = ot[g].xy * sm[g]; tw[g] = pack2(w.x, w.y); }
        __builtin_amdgcn_raw_buffer_store_b32(q < 2 ? (q ? tw[1] : tw[0]) : (q == 3 ? tw[3] : tw[2]), ro, vot, 0, 16);
    }
    STAMP(5);
    {
        WAIT_V(0); BARX();
        STAMP(6);
        f32x4 v[5];
        proj_residual(v, rx);
        const LAS char* pw = opaque(lds + WO_OFF + lane * 16);
        const LAS char* pw2 = opaque(lds + WO_OFF + 41472 + lane * 16);
        const LAS char* ph2 = opaque(lds + WO_OFF + 4096 + q * 128 + (r & 7) * 16);
        const LAS char* ph3 = opaque(lds + WO_OFF + 41472 + 4096 + q * 128 + (r & 7) * 16);
        bf16x8 wf[90];
        auto rdw = [&](int i) {
            const int kc = i / 5, n = i % 5;
            wf[i] = n < 4 ? LDS_FRAG((kc < 9 ? pw : pw2) + (kc % 9) * 4608 + n * 1024) : LDS_FRAG((kc < 9 ? ph2 : ph3) + (kc % 9) * 4608);
        };
#pragma unroll
        for (int i = 0; i < WO_W; ++i) rdw(i);
#pragma unroll
        for (int n = 0; n < 5; ++n) v[n] = pin(v[n]);
        const auto rO = RSRC(o + (size_t)rt * 18432);
        const ProjOut po = proj_out(wl + L_XB, wl + L_STB, rt, cg, lane);
        asm volatile("" :: "s"(rO));
        WAIT_L(0);
        __builtin_amdgcn_s_setprio(REL_PRIO);
        BARX();
        STAMP(7);
        bf16x8 af[18];
        auto ldo = [&](int kc) { af[kc] = __builtin_bit_cast(bf16x8, __builtin_amdgcn_raw_buffer_load_b128(rO, lane * 16 + (kc & 3) * 1024, (kc >> 2) * 4096, 16)); };
#pragma unroll
        for (int kc = 0; kc < WO_PRE; ++kc) { ldo(kc); SCHED0(); if (kc >= PACE0 && kc + 1 < WO_PRE) PACE; SCHED0(); }
        static_for<90>([&](auto ic) {
            constexpr int i = ic.value, kc = i / 5, n = i % 5, j = WO_W + 2 * (i / 3) + i % 3;
            MFMA_T(wf[i], af[kc], v[n]);
            SCHED0();
            if (i % 3 < 2 && j < 90) rdw(j);
            if (i % WO_EV == 2 && i / WO_EV + WO_PRE < 18) ldo(i / WO_EV + WO_PRE);
            SCHED0();
        });
        asm volatile("s_nop 4" : "+v"(v[0]), "+v"(v[1]));
        STAMP(8);
        proj_store(v, po);
        __builtin_amdgcn_s_setprio(0);
    }
}

DEVI f32x2 gelu2(f32x2 v) {
    const f32x2 w = v * (v * v * -0.10294324f + -2.3022082f);
    const f32x2 d = (f32x2){__builtin_amdgcn_exp2f(w.x), __builtin_amdgcn_exp2f(w.y)} + 1.0f;
    return v * (f32x2){__builtin_amdgcn_rcpf(d.x), __builtin_amdgcn_rcpf(d.y)};
}
DEVI f32x4 gelu4(f32x4 v) { const f32x2 a = gelu2(v.xy), b = gelu2(v.zw); return (f32x4){a.x, a.y, b.x, b.y}; }
DEVI void rowsum16x8(const f32x4 a, const f32x4 b, float& o0, float& o1) {
    float t0, t1, t2, t3;
    asm volatile(
        "s_nop 1\n\t"
        "v_add_f32_dpp %2, %6, %6 row_ror:8 row_mask:0xf bank_mask:0x3\n\t"
        "v_add_f32_dpp %3, %7, %7 row_ror:8 row_mask:0xf bank_mask:0x3\n\t"
        "v_add_f32_dpp %4, %10, %10 row_ror:8 row_mask:0xf bank_mask:0x3\n\t"
        "v_add_f32_dpp %5, %11, %11 row_ror:8 row_mask:0xf bank_mask:0x3\n\t"
        "v_add_f32_dpp %2, %8, %8 row_ror:8 row_mask:0xf bank_mask:0xc\n\t"
        "v_add_f32_dpp %3, %9, %9 row_ror:8 row_mask:0xf bank_mask:0xc\n\t"
        "v_add_f32_dpp %4, %12, %12 row_ror:8 row_mask:0xf bank_mask:0xc\n\t"
        "v_add_f32_dpp %5, %13, %13 row_ror:8 row_mask:0xf bank_mask:0xc\n\t"
        "v_add_f32_dpp %0, %2, %2 row_half_mirror row_mask:0xf bank_mask:0x5\n\t"
        "s_nop 0\n\t"
        "v_add_f32_dpp %1, %4, %4 row_half_mirror row_mask:0xf bank_mask:0x5\n\t"
        "v_add_f32_dpp %0, %3, %3 row_half_mirror row_mask:0xf bank_mask:0xa\n\t"
        "v_add_f32_dpp %1, %5, %5 row_half_mirror row_mask:0xf bank_mask:0xa\n\t"
        "s_nop 0\n\t"
        "v_add_f32_dpp %0, %0, %0 quad_perm:[1,0,3,2] row_mask:0xf bank_mask:0xf\n\t"
        "v_add_f32_dpp %1, %1, %1 quad_perm:[1,0,3,2] row_mask:0xf bank_mask:0xf\n\t"
        "s_nop 0\n\t"
        "v_add_f32_dpp %0, %0, %0 quad_perm:[2,3,0,1] row_mask:0xf bank_mask:0xf\n\t"
        "v_add_f32_dpp %1, %1, %1 quad_perm:[2,3,0,1] row_mask:0xf bank_mask:0xf\n\t"
        : "=&v"(o0), "=&v"(o1), "=&v"(t0), "=&v"(t1), "=&v"(t2), "=&v"(t3)
        : "v"(a[0]), "v"(a[1]), "v"(a[2]), "v"(a[3]), "v"(b[0]), "v"(b[1]), "v"(b[2]), "v"(b[3]));
}

#define K5_PRE 9
#define RH_MAP 0
#define NET_LAYERS 2
constexpr int G_STAGE = 34816;
constexpr int K5_STAGE = 18432, K5_NS = 8;

constexpr int y_load_at(int slot) {
    int next = K5_PRE;
    for (int s = 0;; ++s) { const bool go = next < 18 + 2 * (s / 3); if (s == slot) return go ? next : -1; next += go; }
}

struct GTile { f32x4 a[4][4], e[2]; };
struct GFrag { bf16x8 a[2][4], b[2][5]; const LAS char *pa[2][2], *pb, *p8; int slot; };
DEVI void g_rd(GFrag& f, int kk, int i) {
    if (i < 4) f.a[kk][i] = LDS_FRAG(f.pa[kk][i >> 1] + (i & 1) * 2048);
    else if (i < 8) f.b[kk][i - 4] = LDS_FRAG(f.pb + (kk * 9 + i - 4) * 1024);
    else f.b[kk][4] = LDS_FRAG(f.p8 + kk * 9216);
}
template <int NS, int G, bool ROWS_IN_REGS>
DEVI void g_steps(GTile& c, GFrag& f, int n) {
    LOOP_ALIGN();
#pragma unroll 1
    do {
        int d;
#pragma unroll
        for (int kk = 0; kk < 2; ++kk) {
            WAIT_L(0);
            if (kk) BAR();
            SCHED0();
#pragma unroll
            for (int i = 0; i < 18; ++i) {
                const int nn = i >> 2, m = i & 3;
                f32x4& e = nn < 4 ? c.a[m][nn] : c.e[m & 1];
                e = ROWS_IN_REGS ? MFMA(f.a[kk][m], f.b[kk][nn], e) : MFMA(f.b[kk][nn], f.a[kk][m], e);
                SCHED0();
                if (i < 9) g_rd(f, kk ^ 1, i);
                if (kk == 0) {
                    if (i == 9) d = pin_s(f.slot == NS - 1 ? -(NS - 1) * G : G);
                    if (i >= 10 && i < 14) f.pa[(i - 10) >> 1][i & 1] = opaque(f.pa[(i - 10) >> 1][i & 1] + d);
                    if (i == 14) f.pb = opaque(f.pb + d);
                    if (i == 15) f.p8 = opaque(f.p8 + d);
                } else {
                    if (i == 10) f.slot = pin_s(f.slot == NS - 1 ? 0 : f.slot + 1);
                    if (i == 12) n = pin_s(n - 1);
                }
                SCHED0();
            }
        }
    } while (n);
}
DEVI void keep_s(__amdgpu_buffer_rsrc_t r) { asm volatile("" :: "s"(r)); }
DEVI void keep4(const f32x4 (&c)[4]) { asm volatile("" :: "v"(c[0]), "v"(c[1]), "v"(c[2]), "v"(c[3])); }
template <int D, int DB, bool FIRST, class HOOK>
DEVI void g_step_u(GTile& c, GFrag& f, const f32x4 (&c0)[4], HOOK&& hook) {
    static_for<36>([&](auto ic) {
        constexpr int kk = ic.value / 18, i = ic.value % 18, n = i >> 2, m = i & 3;
        if (i == 0) { WAIT_L(0); if (kk) BAR(); SCHED0(); }
        f32x4& e = n < 4 ? c.a[m][n] : c.e[m & 1];
        if (FIRST && kk == 0) asm volatile("v_mfma_f32_16x16x32_bf16 %0, %1, %2, %3" : "=&v"(e) : "v"(f.b[kk][n]), "v"(f.a[kk][m]), "v"(c0[m]));
        else MFMA_T(f.b[kk][n], f.a[kk][m], e);
        SCHED0();
        if (i < 9) g_rd(f, kk ^ 1, i);
        if (kk == 0) {
            if (i >= 10 && i < 14) f.pa[(i - 10) >> 1][i & 1] = opaque(f.pa[(i - 10) >> 1][i & 1] + D);
            if (i == 14) f.pb = opaque(f.pb + DB);
            if (i == 15) f.p8 = opaque(f.p8 + DB);
        }
        if (FIRST && ic.value == 20) keep4(c0);
        hook(ic);
        SCHED0();
    });
}
#define MF(d, a, b) "v_mfma_f32_16x16x32_bf16 " d ", " a ", " b ", " d "\n\t"
template <int D, int DB>
DEVI void g_step_gelu(GTile& c, GFrag& f, f32x4& z, float r, float c1) {
#pragma unroll
    for (int kk = 0; kk < 2; ++kk) {
        float va = z[2 * kk], vb = z[2 * kk + 1], ta, tb;
        WAIT_L(0); if (kk) BAR(); SCHED0();
#pragma unroll
        for (int i = 0; i < 9; ++i) {
            MFMA_T(f.b[kk][i >> 2], f.a[kk][i & 3], c.a[i & 3][i >> 2]);
            if (i == 5) asm volatile("v_mul_f32 %0, %0, %1" : "+v"(va) : "v"(r));
            if (i == 6) asm volatile("v_mul_f32 %0, %0, %1" : "+v"(vb) : "v"(r));
            if (i == 7) asm volatile("v_mul_f32 %0, %1, %1" : "=v"(ta) : "v"(va));
            if (i == 8) asm volatile("v_mul_f32 %0, %1, %1" : "=v"(tb) : "v"(vb));
            SCHED0();
            g_rd(f, kk ^ 1, i);
            SCHED0();
        }
        asm volatile(MF("%0", "%5", "%6") "v_fmaak_f32 %3, %3, %11, 0xc0135761\n\tv_fmaak_f32 %4, %4, %11, 0xc0135761\n\t"
                     MF("%1", "%5", "%7") "v_mul_f32 %3, %3, %9\n\tv_mul_f32 %4, %4, %10\n\t" MF("%2", "%5", "%8") "v_exp_f32 %3, %3"
                     : "+v"(c.a[1][2]), "+v"(c.a[2][2]), "+v"(c.a[3][2]), "+v"(ta), "+v"(tb)
                     : "v"(f.b[kk][2]), "v"(f.a[kk][1]), "v"(f.a[kk][2]), "v"(f.a[kk][3]), "v"(va), "v"(vb), "v"(c1));
        SCHED0(); f.pa[kk][0] = opaque(f.pa[kk][0] + D); SCHED0();
        asm volatile(MF("%0", "%6", "%7") "v_exp_f32 %5, %5\n\t" MF("%1", "%6", "%8") "v_add_f32 %4, 1.0, %4\n\tv_add_f32 %5, 1.0, %5\n\t"
                     MF("%2", "%6", "%9") "v_rcp_f32 %4, %4\n\t" MF("%3", "%6", "%10") "v_rcp_f32 %5, %5"
                     : "+v"(c.a[0][3]), "+v"(c.a[1][3]), "+v"(c.a[2][3]), "+v"(c.a[3][3]), "+v"(ta), "+v"(tb)
                     : "v"(f.b[kk][3]), "v"(f.a[kk][0]), "v"(f.a[kk][1]), "v"(f.a[kk][2]), "v"(f.a[kk][3]));
        SCHED0(); f.pa[kk][1] = opaque(f.pa[kk][1] + D); SCHED0();
        asm volatile(MF("%0", "%4", "%5") "v_mul_f32 %2, %2, %7\n\tv_mul_f32 %3, %3, %8\n\t" MF("%1", "%4", "%6")
                     : "+v"(c.e[0]), "+v"(c.e[1]), "+v"(va), "+v"(vb) : "v"(f.b[kk][4]), "v"(f.a[kk][0]), "v"(f.a[kk][1]), "v"(ta), "v"(tb));
        SCHED0();
        if (kk == 0) { f.pb = opaque(f.pb + DB); f.p8 = opaque(f.p8 + DB); }
        SCHED0();
        z[2 * kk] = va; z[2 * kk + 1] = vb;
    }
}
DEVI void g_fill(GTile& c, const float (&e)[4]) {
#pragma unroll
    for (int m = 0; m < 4; ++m) {
#pragma unroll
        for (int n = 0; n < 4; ++n) c.a[m][n] = pin((f32x4){e[m], e[m], e[m], e[m]});
        if (m < 2) c.e[m] = pin((f32x4){e[m], e[m], e[m], e[m]});
    }
}

#undef STAMP_SEC
#define STAMP_SEC 1
DEVI void gmlp_body(LAS char* lds, int wm, int wn, int pm, int cg, char* z2t, char* y, const u16* __restrict__ bsp STAMP_PARAM) {
    const int mlo = wm * 4 + wn * 2, mhi = wm * 4 + (1 - wn) * 2;
    const float zero4[4] = {0.f, 0.f, 0.f, 0.f};
    GTile z2, z1, acc;
    GFrag f;
    {
        const int lane = lane_id_fresh();
#pragma unroll
        for (int kk = 0; kk < 2; ++kk) { f.pa[kk][0] = lds + mlo * 2048 + kk * 1024 + lane * 16; f.pa[kk][1] = lds + mhi * 2048 + kk * 1024 + lane * 16; }
        f.pb = lds + 16384 + wn * 4096 + lane * 16; f.p8 = lds + 16384 + 8192 + lane * 16; f.slot = 0;
    }
    g_fill(z2, zero4); g_fill(z1, zero4);
#pragma unroll
    for (int i = 0; i < 4; ++i) f.pa[i >> 1][i & 1] = opaque(f.pa[i >> 1][i & 1]);
    f.pb = opaque(f.pb); f.p8 = opaque(f.p8);
    SCHED0();
    WAIT_V(0); BARX();
    BAR();
    BARX();
    STAMP(2);
#pragma unroll
    for (int i = 0; i < 9; ++i) g_rd(f, 0, i);
    g_steps<4, G_STAGE, true>(z2, f, 9);
    STAMP(3);
    {
        const int lane = lane_id_fresh(), r = lane & 15, q = lane >> 4;
        f32x4 ra[4];
#pragma unroll
        for (int m = 0; m < 4; ++m) ra[m] = *(const LAS f32x4*)(lds + G_TAB + ((m < 2 ? mlo : mhi) + (m & 1)) * 64 + q * 16);
        SCHED0();
#pragma unroll
        for (int m = 0; m < 4; ++m) {
            f32x4 s1, s2;
#pragma unroll
            for (int n = 0; n < 4; ++n) {
                const f32x4 g = gelu4(z2.a[m][n] * ra[m]);
                z2.a[m][n] = g;
                if (n == 0) { s1 = g; s2 = g * g; } else { s1 += g; s2 += g * g; }
            }
            if (m < 2) { const f32x4 g = gelu4(z2.e[m] * ra[m]); z2.e[m] = g; s1 += g; s2 += g * g; }
            float u0, u1;
            rowsum16x8(s1, s2, u0, u1);
            if ((r & 3) == 0) *(LAS f32x2*)(lds + G_PART + wn * 1024 + ((m < 2 ? mlo : mhi) + (m & 1)) * 128 + (q * 4 + (r >> 2)) * 8) = (f32x2){u0, u1};
            SCHED0();
        }
    }
    WAIT_L0(); BAR();
    STAMP(4);
    g_steps<4, G_STAGE, false>(z1, f, 4);
    unsigned b0, b1;
    f32x4 c0[4];
    g_step_u<G_STAGE, G_STAGE, false>(z1, f, c0, [&](auto ic) {
        if (ic.value != 18) return;
        STAMP(5);
        const int lane = lane_id_fresh(), q = lane >> 4;
        b0 = __builtin_amdgcn_raw_buffer_load_b32(RSRC(bsp), (lane & 15) * 4, pm * 256 + mlo * 32, 0);
        b1 = __builtin_amdgcn_raw_buffer_load_b32(RSRC(bsp), (lane & 15) * 4, pm * 256 + mhi * 32, 0);
#pragma unroll
        for (int g = 0; g < 2; ++g) {
            const auto rz2 = RSRC(z2t + (size_t)pm * 589824 + cg * 36864 + ((g ? mhi : mlo) >> 1) * 9216);
            const LAS char* tab = lds + G_TAB + (g ? mhi : mlo) * 64 + q * 16;
            LAS char* own = lds + (wm ? G_OWN : G_STAGE + 16384) + (g ^ wn) * 9216 + lane * 16;
            f32x4 ra[2], rb[2];
#pragma unroll
            for (int T = 0; T < 2; ++T) { ra[T] = *(const LAS f32x4*)(tab + 512 + T * 64); rb[T] = *(const LAS f32x4*)(tab + 1024 + T * 64); }
#pragma unroll
            for (int n = 0; n < (g ? 4 : 5); ++n) {
                const f32x4 v0 = (n < 4 ? z2.a[2 * g][n & 3] : z2.e[0]) * ra[0] + rb[0], v1 = (n < 4 ? z2.a[2 * g + 1][n & 3] : z2.e[1]) * ra[1] + rb[1];
                const u32x4 o = {pack2(v0[0], v1[0]), pack2(v0[1], v1[1]), pack2(v0[2], v1[2]), pack2(v0[3], v1[3])};
                ST16(rz2, lane * 16, n < 4 ? wn * 4096 + n * 1024 : 8192, o, 16);
                *(LAS u32x4*)(own + (n < 4 ? wn * 4096 + n * 1024 : 8192)) = o;
            }
        }
        STAMP(6);
    });
    static_for<3>([&](auto sc) {
        constexpr int D = sc.value == 0 ? G_STAGE : sc.value == 1 ? -3 * G_STAGE : 2 * G_STAGE;
        g_step_u<D, D, false>(z1, f, c0, [&](auto ic) { if (ic.value == 17 && sc.value == 1) WAIT_V(0); });
    });
    float ra[4], c1;
    {
        int lane, da[2];
        g_step_u<-G_STAGE, -G_STAGE, false>(z1, f, c0, [&](auto ic) {
            constexpr int i = ic.value;
            if (i == 9) lane = pin(lane_id_fresh());
            if (i == 14 || i == 15) da[i & 1] = pin((lane & 15) * 128 + (((4 * (i & 1) + (lane >> 4)) ^ (lane & 7)) * 16) - lane * 16 - (i & 1) * 1024);
            if (i == 16 || i == 17) f.pa[0][i & 1] = opaque(f.pa[0][i & 1] + da[0]);
            if (i == 27 || i == 28) f.pa[1][i & 1] = opaque(f.pa[1][i & 1] + da[1]);
            if (i >= 29 && i < 33) {
                constexpr int m = (i - 29) & 3;
                const float e = m & 1 ? bfhi(m < 2 ? b0 : b1) : bflo(m < 2 ? b0 : b1);
                c0[m] = pin((f32x4){e, e, e, e});
            }
            if (i == 33) {
#pragma unroll
                for (int m = 0; m < 4; ++m) ra[m] = *(const LAS float*)(lds + G_TAB + ((m < 2 ? mlo : mhi) + (m & 1)) * 64 + (lane & 15) * 4);
                c1 = pin(-0.10294324f);
            }
        });
    }
    STAMP(7);
    g_step_u<2 * G_STAGE, G_OWN - G_STAGE - 16384, true>(acc, f, c0, [](auto) {});
    static_for<18>([&](auto sc) {
        constexpr int st = sc.value, m = st < 16 ? st >> 2 : st & 1;
        constexpr int D = st == 0 ? -3 * G_STAGE : st % 3 == 1 ? 2 * G_STAGE : -G_STAGE;
        g_step_gelu<D, st == 0 ? 16384 - G_OWN : D>(acc, f, st < 16 ? z1.a[m][st & 3] : z1.e[m], ra[m], c1);
    });
    f.slot = 1;
    STAMP(9);
    g_steps<3, -G_STAGE, false>(acc, f, 12);
    __amdgpu_buffer_rsrc_t ry;
    unsigned vy[4], ve[2];
    int lane;
    u32x4 o = {0u, 0u, 0u, 0u};
    auto y_part = [&](auto jc) {
        constexpr int j = jc.value, m = j / 10, k = j % 10, e = (k & 1) * 2;
        const f32x4 &a = k < 8 ? acc.a[m][k >> 1] : acc.e[m & 1], &z = k < 8 ? z1.a[m][k >> 1] : z1.e[m & 1];
        float t0, t1;
        asm volatile("v_mul_f32 %1, %3, %5\n\tv_mul_f32 %2, %4, %6\n\tv_cvt_pk_bf16_f32 %0, %1, %2" : "=v"(o[k & 3]), "=&v"(t0), "=&v"(t1) : "v"(a[e]), "v"(a[e + 1]), "v"(z[e]), "v"(z[e + 1]));
        if (k == 3 || k == 7) ST16(ry, vy[m] + (k >> 2) * 1024, 0, o, 16);
        if (k == 9) ST8(ry, ve[m & 1], 0, ((u32x2){o[0], o[1]}), 16);
    };
    WAIT_L(0);
    SCHED0();
    static_for<36>([&](auto ic) {
        constexpr int i = ic.value, kk = i < 14 ? 0 : i < 28 ? 1 : i >= 32, t = i < 28 ? i % 14 : 14 + (i & 3), m = t < 10 ? t / 5 : 2 + (t - 10) / 4, n = t < 10 ? t % 5 : (t - 10) & 3;
        if (i == 14) { WAIT_L(0); BAR(); SCHED0(); }
        f32x4& e = n < 4 ? acc.a[m][n & 3] : acc.e[m & 1];
        MFMA_T(f.b[kk][n], f.a[kk][m], e);
        SCHED0();
        if (i < 9) g_rd(f, 1, i);
        if (i == 10) { ry = RSRC(own_s(y) + (size_t)pin_s(pm * 8 * 73728 + cg * 4608)); keep_s(ry); }
        if (i == 12) { lane = pin(lane_id_fresh()); vy[0] = pin((unsigned)(mlo * 73728 + wn * 2048 + lane * 16)); }
        if (i == 14) ve[0] = pin((unsigned)(mlo * 73728 + 4096 + (lane >> 5) * 256 + (lane & 15) * 16 + ((lane >> 4) & 1) * 8));
        if (i == 15) { vy[1] = pin(vy[0] + 73728); ve[1] = pin(ve[0] + 73728); }
        if (i == 16) { vy[2] = pin(vy[0] + (mhi - mlo) * 73728); vy[3] = pin(vy[2] + 73728); asm volatile("s_nop 4"); }
        if constexpr (i >= 17) y_part(IC<i - 17>{});
        SCHED0();
    });
    STAMP(10);
    y_part(IC<19>{});
    SCHED0();
#pragma unroll
    for (int i = 0; i < 4; ++i) {
        const int m = 2 + (i >> 1), p = i & 1;
        if (i == 2) asm volatile("" : "+v"(acc.a[3][0]), "+v"(acc.a[3][1]), "+v"(acc.a[3][2]), "+v"(acc.a[3][3]));
        const f32x4 a = acc.a[m][2 * p] * z1.a[m][2 * p], c = acc.a[m][2 * p + 1] * z1.a[m][2 * p + 1];
        ST16(ry, vy[m] + p * 1024, 0, ((u32x4){pack2(a[0], a[1]), pack2(a[2], a[3]), pack2(c[0], c[1]), pack2(c[2], c[3])}), 16);
        SCHED0();
    }
}

#define NET_IDS \
    int b = __builtin_amdgcn_readfirstlane(blockIdx.x), wid = wid8 & 3; \
    asm volatile("" : "+s"(b), "+s"(wid));     \
    const int xcd = b & 7, idx = b >> 3; \
    const int pa = (xcd >> 1) * 4 + (idx & 3), hp = (xcd & 1) * 8 + (idx >> 2); \
    const int pm = (xcd >> 2) * 8 + (idx & 7), cg = (xcd & 3) * 4 + (idx >> 3); \
    const int rh = RH_MAP ? xcd * 4 + (idx & 3) : (xcd >> 1) * 8 + (idx & 7), c5 = RH_MAP ? idx >> 2 : (xcd & 1) * 4 + (idx >> 3); \
    const int lead = (idx + (idx >> 3)) & 3; \
    auto kord = [&](int t, int n) { \
        const int k = t + ((int)(0x1F000000u << (((t - lead) & 3) * 4)) >> 28); \
        return (unsigned)k < (unsigned)n ? k : t; \
    }; \
    auto kord5 = [&](int t) { \
        const int k = t + ((int)(0x1F000000u << (((t - (RH_MAP ? c5 : lead)) & 3) * 4)) >> 28); \
        return (unsigned)k < 18u ? k : t; \
    };

DEVI u32x4 tr_frag(const LAS char* p, int four_rows) {
    const u32x2 a = __builtin_bit_cast(u32x2, __builtin_amdgcn_ds_read_tr16_b64_v4i16((LAS s16x4*)p));
    const u32x2 b = __builtin_bit_cast(u32x2, __builtin_amdgcn_ds_read_tr16_b64_v4i16((LAS s16x4*)(p + four_rows)));
    return (u32x4){a.x, a.y, b.x, b.y};
}
DEVI float dot2(unsigned v, unsigned w, float acc) { return __builtin_amdgcn_fdot2_f32_bf16(__builtin_bit_cast(bf16x2_t, v), __builtin_bit_cast(bf16x2_t, w), acc, false); }
DEVI float sum2(unsigned v, float acc) { return dot2(v, 0x3f803f80u, acc); }
struct Sel { unsigned lo, hi, nlo, nhi; };
DEVI Sel make_sel() { return {(unsigned)pin_s(0x00003f80), (unsigned)pin_s(0x3f800000), (unsigned)pin_s(0x0000bf80), (unsigned)pin_s((int)0xbf800000u)}; }
DEVI unsigned centre2(unsigned v, f32x2& e, float mean, const Sel& k) {
    const float a = dot2(k.lo, v, e.x), b = dot2(k.hi, v, e.y);
    const unsigned r = pack2(a, b);
    e = (f32x2){dot2(k.nlo, r, a), dot2(k.nhi, r, b)} - mean;
    return r;
}
typedef u32x4 Retile[9];
template <int PITCH>
DEVI void retile_out_make(Retile& o, const LAS char* img, int lane) {
    const int c = lane & 15, q = lane >> 4, p = lane & 3;
    const LAS char* src = img + (q * 8 + (c >> 2)) * PITCH;
#pragma unroll
    for (int i = 0; i < 8; ++i) o[i] = tr_frag(src + p * 16 + (i >> 2) * 144 + ((i >> 1) & 1) * 64 + (i & 1) * 8, 4 * PITCH);
    o[8] = tr_frag(src + 128 + (p >> 1) * 144 + (p & 1) * 8, 4 * PITCH);
}
DEVI void retile_out_store(const Retile& o, char* dst, int nkc, int kc, int w, int lane) {
    const int c = lane & 15, q = lane >> 4;
    const auto rd = RSRC(dst + (size_t)(2 * w * nkc + kc) * 4608);
#pragma unroll
    for (int i = 0; i < 8; ++i) ST16(rd, lane * 16 + (i & 3) * 1024, (i >> 2) * nkc * 4608, o[i], 16);
    ST16(rd, (c >> 3) * nkc * 4608 + 4096 + q * 128 + (c & 7) * 16, 0, o[8], 16);
}

DEVI float dpp_ror(float v, int n) { return n == 8 ? __builtin_amdgcn_update_dpp(0.f, v, 0x128, 0xf, 0xf, true) : n == 4 ? __builtin_amdgcn_update_dpp(0.f, v, 0x124, 0xf, 0xf, true)
                                          : n == 2 ? __builtin_amdgcn_update_dpp(0.f, v, 0x122, 0xf, 0xf, true) : __builtin_amdgcn_update_dpp(0.f, v, 0x121, 0xf, 0xf, true); }
DEVI unsigned long long epoch_of(const char* ws) {
    SCHED0();
    const unsigned long long ep = *(const __attribute__((address_space(4))) unsigned long long*)(ws + pin_s((int)OFF_EP)) + 1ull;
    asm volatile("" :: "s"(ep));
    return ep;
}
DEVI void prologue_done(LAS char* lds, char* ws, unsigned long long ep, int b, int lane) {
    if (lds_arrive(lds + G_WORD + 8) == 7) ST16(RSRC(ws), lane ? 0x80000000u : (unsigned)OFF_FL + b * 16, 0, ((u32x4){(unsigned)ep, (unsigned)(ep >> 32), 0u, 0u}), 16);
}
DEVI void centre_frag(u32x4& f, f32x2 (&er)[4], float mean, const Sel& k) {
#pragma unroll
    for (int e = 0; e < 4; ++e) f[e] = centre2(f[e], er[e], mean, k);
}
template <int N>
DEVI void column_means(LAS char* lds, LAS char* cnt, int me, int lane, float a, float b, float& ma, float& mb) {
    LAS float* red = (LAS float*)(lds + G_PART);
    auto x = __builtin_amdgcn_permlane16_swap(__float_as_uint(a), __float_as_uint(b), false, false);
    red[me * 64 + lane] = __uint_as_float(x[0]) + __uint_as_float(x[1]);
    if (lds_arrive(cnt) < N - 1) lds_count_wait(cnt, N);
    float t = 0.f;
#pragma unroll
    for (int i = 0; i < N; ++i) t += red[i * 64 + lane];
    x = __builtin_amdgcn_permlane32_swap(__float_as_uint(t), __float_as_uint(t), false, false);
    t = (__uint_as_float(x[0]) + __uint_as_float(x[1])) * (1.0f / 576.0f);
    x = __builtin_amdgcn_permlane16_swap(__float_as_uint(t), __float_as_uint(t), false, false);
    ma = __uint_as_float(x[0]); mb = __uint_as_float(x[1]);
}
template <class ARGS>
DEVI unsigned long long prologue(LAS char* lds, char*& ws, const char*& x0, const char*& wqkv, const u16*& wo, const u16*& w_in, const u16*& w_out, int b, int pa, int wid8, ARGS&& args PST_PARAM) {
    PST_DECL;
    const int lane = lane_id_fresh(), c = lane & 15, q = lane >> 4, w = wid8 & 3;
    const int jw = w - 1 + 3 * (((wid8 >> 2) ^ (b >> 3)) & 1);
    const bool wjob = b >= 16 && b < 112 && w;
    const int uo = (wid8 - 1 - (wid8 >> 2)) * 16 + b;
    const bool ojob = b < 16 && w && uo < 72;
    const unsigned l0 = (unsigned)(size_t)lds;
    LAS char* img = lds + 2 * K1_STAGE;
    const int uw = b - 16, head = (uw & 7) * 4 + ((uw >> 3) & 3), part = uw >> 5, hp = head >> 1, odd = head & 1;
    const int c0 = part * 1152 + hp * 72 + odd * 40;
    {
        const int so = pin_s(jw * 96 * 3456 + (c0 & ~15));
        const unsigned v = (lane & 3) < ((head & 3) == 3 ? 2 : 3) ? (lane >> 2) * 3456 + (lane & 3) * 16 : 0x80000000u, d = 16 * 3456 - 1024;
        const unsigned v0 = pin(v), v1 = pin(v + d), v2 = pin(v + 2 * d), v3 = pin(v + 3 * d);
        const int e = ((b - 112) >> 3) * 192 + wid8 * 64 + lane, row = (e * 10923) >> 16, r = e - row * 6;
        const unsigned vp = pin((unsigned)(row * 3456 + (r >> 1) * 1152 + (r & 1) * 128));
        const unsigned la = pin_s(l0 + 2 * K1_STAGE + jw * 6144);
        args(0);
        if (b >= 112 && wid8 < 3) dma1(l0 + G_JUNK, RSRC(wqkv), (b & 7) * 128, vp);
        if (wjob) { dma4<false>(la, RSRC(wqkv), so, v0, v1, v2, v3); dma2<false>(la + 4096, RSRC(wqkv), so + 64 * 3456, v0, v1); }
        args(1);
    }
    if (ojob) {
        const auto rs = RSRC(wo + (uo & 3) * 144);
#pragma unroll
        for (int j = 0; j < 9; ++j) {
            const int i = lane + 64 * j, rl = (i * 3641) >> 16, kp = (uo >> 2) * 32 + rl;
            const int row = kp < 512 ? 18 * (kp >> 4) + (kp & 15) + 2 * ((kp >> 4) & 1) : 36 * ((kp - 512) >> 2) + 16 + (kp & 3);
            dma1(l0 + 2 * K1_STAGE + wid8 * 9216 + j * 1024, rs, 0, (unsigned)(row * 1152 + (i - rl * 18) * 16));
        }
    }
    if (wid8 == 4) { lds_word_set(lds + G_WORD + c * 4, 0u); WAIT_L0(); }
    BAR();
    unsigned long long ep;
    if (wid8 == 4) ep = epoch_of(ws);
    if (wid8 == 4 && b >= 16 && b < 112) lds_count_wait(lds + G_WORD + 12, 12);
    if (wid8 > 4 && b >= 112) {
        __builtin_amdgcn_s_sleep(I_SLEEP);
        const unsigned v = (lane >> 2) * 9216 + (lane & 3) * 16, d = 16 * 9216 - 1024;
#pragma unroll
        for (int i = 0; i < 3; ++i) dma4<false>(l0 + P_IMG + (w - 1) * 12288 + i * 4096, RSRC(w_in + win_strip(b) * 32), (win_third(b, w) * 192 + i * 64) * 9216, v, v + d, v + 2 * d, v + 3 * d);
    }
    if (wid8 == 4) {
        AttnX ax = attn_x(x0, x0, 0, pa, 0, lane);
#pragma unroll
        for (int s = 0; s < 2; ++s)
#pragma unroll
            for (int i = 0; i < 4; ++i) { ax.oA = (pa * 8 + 2 * i) * 18432; attn_issueA(lds, ax, i, s, s); }
    }
    u32x4 xv[9];
    if (wid8 == 0) {
        const unsigned v = (lane >> 3) * 2304 + (lane & 7) * 16;
        const auto rx = RSRC(x0 + (size_t)slot2row(b * 8) * 1152);
#pragma unroll
        for (int i = 0; i < 9; ++i) asm volatile("s_nop 4\n\tbuffer_load_dwordx4 %0, %1, %2, 0 offen offset:%3" : "+v"(xv[i]) : "v"(v), "s"(rx), "i"(i * 128) : "memory");
        const auto rw = RSRC(ws);
        const u32x4 ones = {~0u, ~0u, ~0u, ~0u}, zero = {0u, 0u, 0u, 0u};
        const unsigned vo = lane * 16;
        ST16(rw, vo, (unsigned)OFF_ZST + b * 2048, ones, 16); ST16(rw, vo, (unsigned)OFF_ZST + b * 2048 + 1024, ones, 16);
        ST16(rw, vo, (unsigned)OFF_CNT + b * 2560, zero, 16); ST16(rw, vo, (unsigned)OFF_CNT + b * 2560 + 1024, zero, 16);
        ST16(rw, vo, lane < 32 ? (unsigned)OFF_CNT + b * 2560 + 2048 : (unsigned)(OFF_L0 + L_STB) + b * 512 - 512, zero, 16);
        ST16(rw, vo, (unsigned)(OFF_L0 + L_O) + (b >> 1) * 18432 + 16384 + (b & 1) * 1024, zero, 16);
        ST16(rw, b < 12 ? vo : 0x80000000u, (unsigned)OFF_FL2 + b * 1024, zero, 16);
    }
    PST(1);
    if (wjob) {
        const Sel k = make_sel();
        const LAS char* s0 = opaque(img + jw * 6144 + (q * 8 + (c >> 2)) * 64 + (c0 & 15) + (c & 3) * 8);
        const LAS char* s1 = opaque(img + jw * 6144 + (q * 8 + (c >> 2)) * 64 + (c0 & 15) + 32 + min(c & 3, 2) * 2048);
        const auto rd = RSRC(ws + OFF_TWQKV + (size_t)(hp * 18 + jw * 3) * 6144 + (3 * odd + part) * 1024), rd2 = RSRC(ws + OFF_TWT + (size_t)((hp * 3 + part) * 18 + jw * 3) * 256);
        const unsigned vd2 = pin(c < 12 && !odd ? (unsigned)((c >> 2) * 256 + q * 64 + (c & 3) * 16) : 0x80000000u);
        asm volatile("" :: "s"(rd), "s"(rd2));
        WAIT_V(0);
        PST(2);
        u32x4 f[4];
        float sm[4] = {0.f, 0.f, 0.f, 0.f}, m0, m1;
#pragma unroll
        for (int i = 0; i < 4; ++i) f[i] = tr_frag(i < 3 ? s0 + i * 2048 : s1, 256);
#pragma unroll
        for (int e = 0; e < 16; ++e) sm[(e >= 12) * 2 + (e & 1)] = sum2(f[e >> 2][e & 3], sm[(e >= 12) * 2 + (e & 1)]);
        sm[2] = c < 12 ? sm[2] + sm[3] : 0.f;
        sm[2] += dpp_ror(sm[2], 4); sm[2] += dpp_ror(sm[2], 8);
        column_means<6>(lds, lds + G_WORD + 12, jw, lane, sm[0] + sm[1], sm[2], m0, m1);
        PST(3);
        f32x2 e0[4] = {{-m0, -m0}, {-m0, -m0}, {-m0, -m0}, {-m0, -m0}}, e1[4] = {{-m1, -m1}, {-m1, -m1}, {-m1, -m1}, {-m1, -m1}};
#pragma unroll
        for (int i = 0; i < 3; ++i) { centre_frag(f[i], e0, m0, k); ST16(rd, lane * 16, i * 6144, f[i], 16); }
        centre_frag(f[3], e1, m1, k); ST16(rd2, vd2, 0, f[3], 16);
        PST(4);
        lds_arrive(lds + G_WORD + 12);
    }
    if (wid8 != 4) ep = epoch_of(ws);
    {
        if (wjob) {
            const unsigned fo = pin((unsigned)OFF_FW + ((lane >> 3) * 16 + hp) * 1024 + (3 * odd + part) * 128 + (lane & 7) * 16);
            WAIT_V(0);
            PST(5);
            if (lds_arrive(lds + G_WORD + 4) == 5) {
                const u32x4 e4 = {(unsigned)ep, (unsigned)(ep >> 32), (unsigned)ep, (unsigned)(ep >> 32)};
                ST16(RSRC(ws), fo, 0, e4, 16); ST16(RSRC(ws), fo + 131072, 0, e4, 16);
            }
            if (wid8 > 4) {
                const int oj = uw * 3 + w - 1;
                const auto rs = RSRC(w_out + (size_t)(oj >> 2) * 32 * 576 + (oj & 3) * 144);
#pragma unroll
                for (int j = 0; j < 9; ++j) { const int i = lane + 64 * j; dma1(l0 + P_IMG + (w - 1) * 9216 + j * 1024, rs, 0, (unsigned)(((i * 3641) >> 16) * (1152 - 288) + i * 16)); }
            }
        }
    }
    if (wid8 == 0) {
        WAIT_V(7);
        asm volatile("" : "+v"(xv[0]), "+v"(xv[1]), "+v"(xv[2]), "+v"(xv[3]), "+v"(xv[4]), "+v"(xv[5]), "+v"(xv[6]), "+v"(xv[7]), "+v"(xv[8]));
        float s = 0.f, q2 = 0.f;
#pragma unroll
        for (int e = 0; e < 36; ++e) { s = sum2(xv[e >> 2][e & 3], s); q2 = dot2(xv[e >> 2][e & 3], xv[e >> 2][e & 3], q2); }
        ST8(RSRC(ws + OFF_L0 + L_STA + (size_t)b * 512), lane * 8, 0, ((u32x2){__float_as_uint(s), __float_as_uint(q2)}), 16);
    }
    if (ojob) {
        WAIT_V(0);
        Retile rt;
        retile_out_make<288>(rt, img + wid8 * 9216, lane); retile_out_store(rt, ws + OFF_TWO, 18, uo >> 2, uo & 3, lane);
    }
    PST(7);
    PST_DUMP(wid8 == 0 || wid8 == 4 || wid8 == 5, wid8 ? wid8 : 2);
    return ep;
}

__global__ __launch_bounds__(512, 2) TRACE_ATTR void k_net(char* ws, const u16* wsp, const u16* bsp, char* out, const char* x0, const char* wqkv, const u16* wo,
                                                             const u16* w_in, const u16* w_out DBG_ARG) {
    extern __shared__ __attribute__((aligned(16))) char smem[];
    LAS char* lds = (LAS char*)smem;
    asm volatile("gm_code_begin:");
    if (blockIdx.x < 16) dma1((unsigned)(size_t)lds + G_JUNK, code_rsrc(), 0, (threadIdx.x * 2 + (blockIdx.x >> 3)) * 128);
    STAMP_DECL;
    const int wid8 = __builtin_amdgcn_readfirstlane(threadIdx.x >> 6);
    unsigned long long ep;
    {
        NET_IDS
        ep = prologue(lds, ws, x0, wqkv, wo, w_in, w_out, b, pa, wid8, [&](int part) {
            if (part == 0) { asm volatile("" :: "s"(wqkv), "s"(w_in), "s"(w_out)); return; }
            ws = own_s(ws); wsp = own_s(wsp); bsp = own_s(bsp); out = own_s(out); x0 = own_s(x0); wo = own_s(wo); w_out = own_s(w_out);
        } PST_FWD);
    }
    const char *twin = ws + OFF_TWIN, *twout = ws + OFF_TWOUT;
    if (wid8 >= 4) {
        u32x4 wr[5][9];
#pragma unroll 1
      for (int layer = 0; layer < NET_LAYERS; ++layer) {
        NET_IDS
        TRACE_BEGIN;
        char* wl = ws + OFF_L0 + layer * SZ_L;
        const unsigned fl = layer * F_LAYER;
        const char *xin = wl + L_XB, *st = wl + L_STB, *z2t = wl + (layer ? L_Y : L_Z2T);
        unsigned long long* zst = (unsigned long long*)(ws + OFF_ZST + layer * SZ_ZST);
        const int lane = lane_id_fresh(), l3 = lane >> 3;
        const unsigned vo = lane * 16;
        const unsigned l0 = (unsigned)(size_t)lds, lA = l0 + wid * 4096, lB = l0 + 16384 + wid * 4608;
        const auto rX = RSRC(xin + (size_t)(pm * 8 + 2 * wid) * 18432), rI = RSRC(twin + (size_t)cg * (18 * 18432) + wid * 4608);
        auto issue1B = [&](int g, int slot) { const int k = kord(g < 9 ? g : (g < 13 ? g - 4 : g - 13), 9); dma5<true>(lB + slot * G_STAGE, rI, (g < 9 ? 9 + k : k) * 18432, vo); };
        auto issue1A = [&](int g, int slot) { dma4<true>(lA + slot * G_STAGE, rX, kord(g < 9 ? g : g - 13, 9) * 2048, vo, vo, vo + 16384, vo + 16384); };
        auto issue1 = [&](int g, int slot) { issue1B(g, slot); issue1A(g, slot); };
        const auto rA = RSRC((const char*)wsp + (size_t)(pm * 128 + wid * 32) * 4096), rB = RSRC(z2t + cg * 36864 + wid * 4608);
        const unsigned voA = (unsigned)(2 * l3 * 4096 + ((lane & 7) ^ l3) * 16);
        auto issueB = [&](int k, int slot) { dma5<true>(lB + slot * G_STAGE, rB, (k >> 1) * 589824 + (k & 1) * 18432, vo); };
        const auto rW = RSRC(twout + (size_t)c5 * (72 * 4608));
        auto issue5 = [&](int t) { dma5<true>(l0 + ((t + 6) & 7) * K5_STAGE + wid * 4608, rW, kord5(t) * K5_STAGE + wid * 4608, vo); };
        auto load5 = [&](int p, int i) {
            const int t0 = p ? 2 * p + 6 : 4, ow = kord5(t0 + i) * K5_STAGE + wid * 4096;
            asm volatile("s_nop 4\n\tbuffer_load_dwordx4 %0, %4, %5, %6 offen sc1\n\tbuffer_load_dwordx4 %1, %4, %5, %6 offen offset:1024 sc1\n\t"
                         "buffer_load_dwordx4 %2, %4, %5, %6 offen offset:2048 sc1\n\tbuffer_load_dwordx4 %3, %4, %5, %6 offen offset:3072 sc1"
                         : "+v"(wr[p][4 * i]), "+v"(wr[p][4 * i + 1]), "+v"(wr[p][4 * i + 2]), "+v"(wr[p][4 * i + 3]) : "v"(vo), "s"(rW), "s"(ow) : "memory");
            if (i) {
                const int oe = kord5(t0 + (wid >> 1)) * K5_STAGE + 16384 + (wid & 1) * 1024;
                asm volatile("s_nop 4\n\tbuffer_load_dwordx4 %0, %1, %2, %3 offen sc1" : "+v"(wr[p][8]) : "v"(vo), "s"(rW), "s"(oe) : "memory");
            }
        };
        auto put5 = [&](int w0, int w1) {
#pragma unroll
            for (int w = w0; w < w1; ++w) {
                const int p = w / 9, i = w % 9 == 4 ? -1 : w % 9 - (w % 9 > 4);
                LAS char* sl = lds + (((p ? 2 * p + 6 : 4) + 6) & 7) * K5_STAGE + lane * 16;
                if (i < 0) *(LAS u32x4*)(sl + (wid >> 1) * K5_STAGE + 16384 + (wid & 1) * 1024) = wr[p][8];
                else *(LAS u32x4*)(sl + (i >> 2) * K5_STAGE + wid * 4096 + (i & 3) * 1024) = wr[p][i];
            }
            WAIT_L0();
        };
        const int row = wid * 32 + (lane & 31);
        auto step1 = [&](int t) { BAR(); issue1(t + 4, t & 3); };
        const char* ps;
        unsigned fd, fd2;
        {
            unsigned fo, fi;
            const bool oj = !layer && wid && b >= 16 && b < 112, ij = !layer && wid && b >= 112;
            const int ojob = (b - 16) * 3 + wid - 1;
            float mw[2];
            unsigned fw;
            auto win_half = [&](int h, bool make, bool store) {
                const int u = win_strip(b) * 2 + h, half = u >= 144, g = u - half * 144, ug = g / 9, j = g - ug * 9, c = lane & 15;
                const auto rd = RSRC(ws + OFF_TWIN + (size_t)ug * (18 * 18432) + (half * 18 + win_third(b, wid) * 6) * 9216);
                const unsigned vd = (lane >> 4) * 256 + (j < 8 ? ((j & ~1) + (c >> 3)) * 1024 + ((j & 1) * 8 + (c & 7)) * 16 : 8192 + c * 16);
                const Sel k = make_sel();
                const float m = mw[h];
                f32x2 er[4] = {{-m, -m}, {-m, -m}, {-m, -m}, {-m, -m}};
#pragma unroll
                for (int i = 0; i < 6; ++i) { if (make) centre_frag(wr[1 + h][i], er, m, k); if (store) ST16(rd, vd, i * 9216, wr[1 + h][i], 16); }
            };
            attn_producer(lds, ws, x0, wl, fl, ep, layer, pa, hp, wid, [&] { if (b < 16) WAIT_V(0); prologue_done(lds, ws, ep, b, lane); }, [&](int part) {
                if (part == -2 && ij) {
                    WAIT_V(0);
#pragma unroll
                    for (int h = 0; h < 2; ++h) {
                        const LAS char* src = lds + P_IMG + (wid - 1) * 12288 + ((lane >> 4) * 8 + ((lane & 15) >> 2)) * 64 + h * 32
                                              + ((win_strip(b) * 2 + h) % 9 < 8 ? (lane & 1) * 16 + (lane & 2) * 4 : (lane & 3) * 8);
#pragma unroll
                        for (int i = 0; i < 6; ++i) wr[1 + h][i] = tr_frag(src + i * 2048, 256);
                    }
                    float sm[4] = {0.f, 0.f, 0.f, 0.f};
#pragma unroll
                    for (int e = 0; e < 48; ++e) sm[(e >= 24) * 2 + (e & 1)] = sum2(wr[1 + (e >= 24)][(e % 24) >> 2][e & 3], sm[(e >= 24) * 2 + (e & 1)]);
                    column_means<3>(lds, lds + G_WORD, wid - 1, lane, sm[0] + sm[1], sm[2] + sm[3], mw[0], mw[1]);
                    if (WIN_PRE) win_half(0, true, false);
                }
                if (part == -1 && oj) { retile_out_make<288>(wr[1], lds + P_IMG + (wid - 1) * 9216, lane); WAIT_L(0); }
                if (part == 1) {
                    if (layer) { issue1B(0, 0); issue1B(1, 1); }
                    if (oj) retile_out_store(wr[1], ws + OFF_TWOUT, 72, ojob >> 2, ojob & 3, lane);
                    if (ij) { win_half(0, !WIN_PRE, true); win_half(1, true, WIN_PRE); }
                    fo = flag_out(fl + F_X1, 256, pa, hp, lane); fi = flag_in(fl + F_X1, 256, pm * 16 + cg, 16, lane);
                    ps = pin(st + (size_t)(pm * 128 + wid * 32) * 64 + lane * 16);
                    fd = pin((unsigned)OFF_FL2 + wid * 1024 + vo);
                    fd2 = pin((unsigned)OFF_FL2 + (lane < 32 ? 256 + lane : 288 + ((cg * 9) >> 1) * 3 + min(lane & 15, 14)) * 16);
                    fw = pin((unsigned)OFF_FL2 + (288 + (72 + ((cg * 9) >> 1)) * 3 + min(lane & 15, 14)) * 16);
                }
                if (part == 2 && ij && !WIN_PRE) win_half(1, false, true);
            });
            if (oj || ij) {
                WAIT_V(0);
                flag_raise(ws, lane ? 0x80000000u : (unsigned)OFF_FL2 + (oj ? ojob : 288 + win_strip(b) * 3 + wid - 1) * 16);
            }
            if (layer == 0) {
                __builtin_amdgcn_s_sleep(WIN_LOOK);
                flags_wait<0, 8>(ws, fw);
            }
            BARX();
            if (wid == 0) flag_raise(ws, fo);
            if (layer == 0) { issue1B(0, 0); issue1B(1, 1); }
            issue1B(2, 2); issue1B(3, 3);
            asm volatile("" :: "s"(rX));
            const int o0 = pin_s(kord(0, 9) * 2048);
            const unsigned v2 = pin(vo + 16384);
            if (wid == 0) { if (layer) __builtin_amdgcn_s_sleep(X1_FIRST); flags_wait<0>(ws, fi);
#pragma unroll
                for (int i = 0; i < 4; ++i) dma4<true>(lA + i * 4096, rX, o0 + i * 36864, vo, vo, v2, v2);
            }
            BARX();
        }
        issue1A(1, 1); issue1A(2, 2);
        WAIT_V(8); BAR();
        issue1A(3, 3);
        {
            f32x4 sp[3];
            WAIT_V(8); step1(0);
            WAIT_V(13); step1(1);
            WAIT_V(18); step1(2);
            unsigned got[2];
            if (layer == 0) asm volatile("s_nop 4\n\tbuffer_load_dword %0, %2, %4, 0 offen sc1\n\tbuffer_load_dword %1, %3, %4, 0 offen sc1" : "+v"(got[0]), "+v"(got[1]) : "v"(fd), "v"(fd2), "s"(RSRC(ws)) : "memory");
            WAIT_IF(layer, 18, 20); step1(3);
            WAIT_IF(layer, 18, 20); step1(4);
            WAIT_V(18);
            if (layer == 0) {
                asm volatile("" : "+v"(got[0]), "+v"(got[1]));
                if (__any((got[0] & got[1]) == 0u)) { flags_wait<0>(ws, fd); flags_wait<0>(ws, fd2); }
            }
            BAR(); issue1B(9, 1); side_load(sp, ps, ps); if (layer == 0) load5(3, 0);
            WAIT_IF(layer, 17, 21); BAR(); issue1B(10, 2); if (layer == 0) load5(3, 1);
            WAIT_IF(layer, 5, 14); BARX();
            side_fence(sp);
#pragma unroll
            for (int i = 0; i < 2; ++i) lds_word_set(lds + G_TAB + (wid * 32 + i * 16 + (lane >> 2)) * 4, __float_as_uint(ln_rstd(sp[i])));
            issue1B(11, 3);
            WAIT_L0();
        }
        BAR(); issue1B(12, 0);
        if (layer == 1) {
            const auto rd = RSRC(out + (size_t)b * 9216 + wid * 2304);
            ST16(rd, vo, 0, ((u32x4){0u, 0u, 0u, 0u}), 16); ST16(rd, vo, 1024, ((u32x4){0u, 0u, 0u, 0u}), 16);
            if (lane < 16) ST16(rd, vo, 2048, ((u32x4){0u, 0u, 0u, 0u}), 16);
        }
        if (layer == 0) {
#pragma unroll
            for (int p = 0; p < 6; ++p) load5(p >> 1, p & 1);
        }
        WAIT_V(0);
        __builtin_amdgcn_s_setprio(3);
        const char* src = pin((const char*)zst + (size_t)(pm * 16 + (lane >> 4) * 4) * 1024 + wid * 256 + (lane & 15) * 16);
        const unsigned tw = pin(l0 + G_TAB + 512 + (lane >> 5) * 512 + (wid * 32 + (lane & 15) * 2 + ((lane >> 4) & 1)) * 4);
        const unsigned fz = flag_out(fl + F_Z2, 256, cg, pm, lane), fzi = pin(fl + F_Z2 + (cg * 16 + pm) * 256 + ((lane ^ pm) & 8) * 16 + (lane & 7) * 16);
        const unsigned vA[4] = {pin(voA), pin(voA + 16 * 4096 - 1024), pin(voA + 4096 - 2048), pin(voA + 17 * 4096 - 3072)};
        auto spA = [&](int so, unsigned la) { dma4<false>(la, rA, so, vA[0], vA[1], vA[2], vA[3]); };
        auto issueA = [&](int k, int slot) { spA(k * 128, lA + slot * G_STAGE); };
        {
            const LAS char* pp = opaque(lds + G_PART + row * 8);
            const auto rs = RSRC(zst + (size_t)(pm * 16 + cg) * 128);
            const unsigned vp = pin(lane < 32 ? (unsigned)row * 8u : 0x80000000u);
            asm volatile("" :: "s"(rs));
            BARX();
            const f32x2 a = *(const LAS f32x2*)pp, c = *(const LAS f32x2*)(pp + 1024);
            ST8(rs, vp, 0, ((u32x2){__float_as_uint(add1(a.x, c.x)), min(__float_as_uint(add1(a.y, c.y)), 0xfffffffeu)}), 16);
            SCHED0();
        }
        __builtin_amdgcn_s_setprio(0);
        {
            f32x4 g[4];
            float sm[4];
            auto gather = [&] {
                asm volatile("global_load_dwordx4 %0, %4, off sc1\n\tglobal_load_dwordx4 %1, %4, off offset:1024 sc1\n\t"
                             "global_load_dwordx4 %2, %4, off offset:2048 sc1\n\tglobal_load_dwordx4 %3, %4, off offset:3072 sc1"
                             : "=&v"(g[0]), "=&v"(g[1]), "=&v"(g[2]), "=&v"(g[3]) : "v"(src) : "memory");
            };
            auto total = [&] {
                asm volatile("" : "+v"(g[0]), "+v"(g[1]), "+v"(g[2]), "+v"(g[3]));
#pragma unroll
                for (int i = 0; i < 4; ++i) sm[i] = add1(add1(g[0][i], g[1][i]), add1(g[2][i], g[3][i]));
            };
            step1(9); step1(10);
            BARX(); gather(); issue1(15, 3);
            WAIT_V(22); BARX();
            __builtin_amdgcn_s_setprio(3);
            WAIT_V(9);
            total();
            if (__any(__builtin_isunordered(sm[1], sm[3]))) {
#pragma unroll 1
                for (unsigned spin = 0; spin < 400000u; ++spin) {
                    bool ok = true;
#pragma unroll
                    for (int i = 0; i < 4; ++i) ok &= __float_as_uint(g[i][1]) != 0xffffffffu && __float_as_uint(g[i][3]) != 0xffffffffu;
                    if (__all(ok)) break;
                    gather();
                    WAIT_V(0);
                    total();
                }
            }
            xq4_sum(sm);
            const bool odd = lane & 16;
            float mean;
            const float rs = rstd_of(odd ? sm[2] : sm[0], odd ? sm[3] : sm[1], 1.0f / 2304.0f, mean);
            asm volatile("ds_write_b32 %0, %1" :: "v"(tw), "v"(lane < 32 ? rs : -mean * rs) : "memory");
            __builtin_amdgcn_s_setprio(0);
            issue1B(16, 0);
            WAIT_L0();
        }
        BARX(); issue1A(16, 0); issueA(2 * pm, 1);
        auto kord2 = [&](int t) { const int j = kord(t - 2, 30); return ((j < 2 * (pm & 7) ? j : j + 2) + 2 * (pm & 8)) & 31; };
        unsigned seen;
        auto look = [&] { if (wid == 0) asm volatile("s_nop 4\n\tbuffer_load_dword %0, %1, %2, 0 offen sc1" : "+v"(seen) : "v"(fzi), "s"(RSRC(ws)) : "memory"); };
        auto need = [&](int n) {
            if (wid == 0) {
                asm volatile("" : "+v"(seen));
                if (__ballot(seen == 0u) & ((1ull << n) - 1ull)) flags_wait<0>(ws, lane < n ? fzi : fl + F_Z2 + (cg * 16 + pm) * 256 + pm * 16);
            }
        };
        auto issue = [&](int t, int sl) { const int k = kord2(t); issueB(k, sl); issueA(k, sl); };
        auto steps2 = [&](int t0, int t1) {
            int sl = (2 * t0 + 2) % 3 * G_STAGE, k = kord2(t0 + 3), ob = (k >> 1) * 589824 + (k & 1) * 18432;
            LOOP_ALIGN();
#pragma unroll 1
            for (int t = t0; t < t1; ++t) {
                WAIT_V(9); BARX();
                dma5<true>(lB + sl, rB, ob, vo);
                const int oa = pin_s(k * 128);
                const unsigned la = pin_s(lA + sl);
                k = pin_s(kord2(t + 4)); ob = pin_s((k >> 1) * 589824 + (k & 1) * 18432);
                sl = pin_s(sl == 0 ? 2 * G_STAGE : sl - G_STAGE);
                spA(oa, la);
            }
        };
        WAIT_V(13); BAR(); issue1(17, 2);
        WAIT_V(13); BARX();
        if (wid == 0) flag_raise(ws, fz);
        issueA(2 * pm + 1, 3); if (layer == 0) load5(4, 0);
        if (wid == 0) WAIT_IF(layer, 5, 9); else WAIT_IF(layer, 4, 8);
        BAR(); issueA(kord2(2), 0); if (layer == 0) load5(4, 1);
        BAR(); look(); issueA(kord2(3), 2);
        WAIT_V(4); need(3); BARX(); issueB(kord2(2), 0); issueB(kord2(3), 2); look();
        WAIT_IF(wid == 0, 6, 5); BAR(); issue(4, 1);
        WAIT_V(9); need(8);
        steps2(2, 11); look();
        WAIT_IF(wid == 0, 10, 9); BAR(); issue(14, 0);
        WAIT_V(9); need(16);
        steps2(12, 29);
        WAIT_V(9); BAR(); issue5(0); issue5(1); issue5(2);
        WAIT_V(15); BAR(); issue5(6); issue5(7);
        BAR(); __builtin_amdgcn_s_sleep(W3_SLEEP); issue5(3); put5(0, 9);
        unsigned vb;
        {
            vb = attn_vb(wid, lane);
            const unsigned fo = flag_out(fl + F_Y, 256, pm, cg, lane), fi = flag_in(fl + F_Y, 256, rh * 8 + c5, 16, lane);
            __builtin_amdgcn_s_setprio(3);
            WAIT_V(0); BARX();
            if (wid == 0) { flag_raise(ws, fo); flags_wait<Y_FIRST>(ws, fi); }
            __builtin_amdgcn_s_setprio(0);
            BARX();
        }
        put5(9, 13);
        BAR(); put5(13, 18);
        BAR(); put5(18, 24);
        BAR(); put5(24, 30);
        BAR(); put5(30, 36);
        BAR(); issue5(16); put5(36, 41);
        BAR(); issue5(17); put5(41, 45);
        WAIT_V(5); BAR();
        WAIT_V(0); BAR();
        if (layer == 0) { attn_issueB(lds, ws, hp, wid, vb, 0, 0); attn_issueB(lds, ws, hp, wid, vb, 1, 1); }
        BAR();
        if (layer == 0) {
            __builtin_amdgcn_s_setprio(3);
            const unsigned fo = flag_out(F_X2, 512, (rh >> 1) + wid, wid ? 16 + c5 : (rh & 1) * 8 + c5, wid == 0 || (wid == 1 && (rh & 1) && rh < 31) ? lane : 16);
            BARX();
            if (wid < 2) flag_raise(ws, fo);
        }
        TRACE_DUMP;
      }
        if (blockIdx.x == 0 && wid8 == 4) ST8(RSRC(ws), lane_id_fresh() ? 0x80000000u : (unsigned)OFF_EP, 0, ((u32x2){(unsigned)ep, (unsigned)(ep >> 32)}), 16);
        asm volatile("gm_code_end1:");
        return;
    }
#pragma unroll 1
  for (int layer = 0; layer < NET_LAYERS; ++layer) {
    NET_IDS
    TRACE_BEGIN;
    char* wl = ws + OFF_L0 + layer * SZ_L;
    const unsigned fl = layer * F_LAYER;
    const char* xin = wl + L_XB;
    char* y = wl + (layer ? L_Z2T : L_Y);
#undef STAMP_SEC
#define STAMP_SEC 0
    {
    STAMP_BEGIN;
    attn_consumer(lds, x0, wl, layer, pa, hp, wid, [&] { WAIT_V(0); prologue_done(lds, ws, ep, b, lane_id_fresh()); } STAMP_FWD);
    STAMP_END(1 + 5 * layer);
    }
#undef STAMP_SEC
#define STAMP_SEC 1
    STAMP_BEGIN;
    gmlp_body(lds, wid & 1, wid >> 1, pm, cg, wl + (layer ? L_Y : L_Z2T), y, bsp STAMP_FWD);
    {
        const int lane = lane_id_fresh(), r = lane & 15, q = lane >> 4;
        const int wk = wid >> 1, rt = rh * 4 + (wid & 1) * 2 + wk;
        const ProjOut pr = proj_out((char*)xin, (char*)xin, rt, c5, lane);
        const LAS char *pw[3], *ph[3];
#pragma unroll
        for (int i = 0; i < 3; ++i) {
            pw[i] = opaque(lds + 3 * i * K5_STAGE + wk * 4608 + lane * 16);
            ph[i] = opaque(lds + 3 * i * K5_STAGE + wk * 4608 + 4096 + q * 128 + (r & 7) * 16);
        }
        const auto rY = RSRC(y + (size_t)(rt & ~1) * 73728 + wk * 1024);
        const unsigned vy[2] = {pin((unsigned)(wk * 73728 + lane * 16)), pin((unsigned)((1 - wk) * 73728 + lane * 16))};
        int dk[4];
#pragma unroll
        for (int i = 0; i < 4; ++i) dk[i] = pin_s((kord5(4 + i) - 4 - i) * 4096);
        auto yoff = [&](int t) { return pin_s(t == 0 || t == 17 ? kord5(t) * 4096 : t * 4096 + dk[t & 3]); };
        bf16x8 af[5][4], wf[4][5];
        auto lda = [&](int so, int i) { return __builtin_bit_cast(bf16x8, __builtin_amdgcn_raw_buffer_load_b128(rY, vy[i & 1] + (i >> 1) * 2048, so, 16)); };
        auto rdw = [&](int t, int c, int n) {
            const int sl = (t + 6) & 7, o = (sl % 3) * K5_STAGE + c * 9216;
            wf[2 * (t & 1) + c][n] = n < 4 ? LDS_FRAG(pw[sl / 3] + o + n * 1024) : LDS_FRAG(ph[sl / 3] + o);
        };
        int so[18];
#pragma unroll
        for (int i = 0; i < 5; ++i) so[i] = yoff(i);
        so[17] = yoff(17);
        LAS char* xo = (LAS char*)opaque(lds + wid * 4608 + lane * 16);
        LAS char* xo4 = (LAS char*)opaque(lds + wid * 4608 + 4096 + (lane & 31) * 16);
        const LAS char* xi = opaque(lds + (wid ^ 2) * 4608 + lane * 16);
        const LAS char* xi4 = opaque(lds + (wid ^ 2) * 4608 + 4096 + (lane & 31) * 16);
        const ProjOut po = layer ? proj_out_final(out, rt, c5, lane) : proj_out(wl + SZ_L + L_XA, wl + SZ_L + L_STA, rt, c5, lane);
        asm volatile("" :: "s"(rY));
        SCHED0();
        WAIT_V(0);
        BARX();
        STAMP(11);
        const Res rx = proj_residual_load(pr);
#pragma unroll
        for (int n = 0; n < 10; ++n) rdw(0, n / 5, n % 5);
        f32x4 v[5], u[5];
        proj_residual(v, rx);
#pragma unroll
        for (int n = 0; n < 5; ++n) v[n] = pin(v[n]);
        WAIT_L(0);
        BARX();
        STAMP(12);
#pragma unroll
        for (int i = 0; i < K5_PRE; ++i) { af[i >> 2][i & 3] = lda(so[i >> 2], i & 3); SCHED0(); if (i >= PACE0 && i + 1 < K5_PRE) PACE; SCHED0(); }
        static_for<18>([&](auto tc) {
            constexpr int t = tc.value;
            bf16x8 (&a)[4] = af[t % 5];
#pragma unroll
            for (int h = 0; h < 4; ++h) {
                const int c = t < 17 ? h >> 1 : h & 1, m = t < 17 ? h & 1 : 1 - (h >> 1);
                if (t < 17 ? m == 0 : h < 2) { if (t == 17 && h == 1) WAIT_L(0); else WAIT_L(5); }
#pragma unroll
                for (int n = 0; n < 5; ++n) {
                    f32x4& e = m ? u[n] : v[n];
                    if (t == 0 && h == 1) MFMA_0(wf[0][n], a[1], e); else MFMA_T(wf[2 * (t & 1) + c][n], a[2 * c + m], e);
                    SCHED0();
                    if (t < 17 && m == 0) rdw(t + 1, c, n);
                    if (t < 14 && m == 1 && !(n & 1)) { const int i = y_load_at(6 * t + 3 * c + n / 2); if (i >= 0) af[(i >> 2) % 5][i & 3] = lda(so[i >> 2], i & 3); }
                    if (t < 12 && h == 3 && n == 3) so[t + 5] = yoff(t + 5);
                    if (t == 17 && h * 5 + n >= 13 && h * 5 + n < 18) { const int i = h * 5 + n - 13; *(LAS f32x4*)(i < 4 ? xo + i * 1024 : xo4) = u[i]; }
                    SCHED0();
                }
            }
            if ((t & 1) && t < 17) BAR();
            SCHED0();
        });
        {
            WAIT_L0(); BARX();
#pragma unroll
            for (int n = 0; n < 5; ++n) u[n] = *(const LAS f32x4*)(n < 4 ? xi + n * 1024 : xi4);
            SCHED0();
            asm volatile("s_nop 1" : "+v"(v[0]), "+v"(v[1]), "+v"(v[2]), "+v"(v[3]), "+v"(v[4]));
#pragma unroll
            for (int n = 0; n < 5; ++n) v[n] += u[n];
        }
        STAMP(13);
        if (layer) proj_store<true>(v, po);
        else { proj_store(v, po); WAIT_V(0); BARX(); }
    }
    STAMP_END(3 + 5 * layer);
    TRACE_DUMP;
  }
  asm volatile("gm_code_end2:");
}

}

#define STOP_CHECK(n)

extern "C" void kernel_launch(void* const* d_in, const int* in_sizes, int n_in, void* d_out, int out_size, void* d_ws, size_t ws_size,
                              hipStream_t stream) {
    (void)in_sizes; (void)n_in; (void)out_size; (void)ws_size;
    const u16* x = (const u16*)d_in[0];
    const u16* wqkv = (const u16*)d_in[1];
    const u16* wo = (const u16*)d_in[2];
    const u16* w_in = (const u16*)d_in[4];
    const u16* w_sp = (const u16*)d_in[5];
    const u16* b_sp = (const u16*)d_in[6];
    const u16* w_out = (const u16*)d_in[7];
    char* ws = (char*)d_ws;
    (void)hipFuncSetAttribute((const void*)k_net, hipFuncAttributeMaxDynamicSharedMemorySize, NET_LDS);
    k_net<<<256, 512, NET_LDS, stream>>>(ws, w_sp, b_sp, (char*)d_out, (const char*)x, (const char*)wqkv, wo, w_in, w_out DBG_LAUNCH(0));
}
```

```cpp
#include <hip/hip_runtime.h>
#include <stdint.h>

namespace {

typedef unsigned short u16;
typedef short bf16x8 __attribute__((ext_vector_type(8)));
typedef float f32x4 __attribute__((ext_vector_type(4)));
typedef float f32x2 __attribute__((ext_vector_type(2)));
typedef unsigned u32x4 __attribute__((ext_vector_type(4)));
typedef unsigned u32x2 __attribute__((ext_vector_type(2)));
typedef short s16x4 __attribute__((ext_vector_type(4)));
typedef __bf16 bf16x2_t __attribute__((ext_vector_type(2)));
#define LAS __attribute__((address_space(3)))
#define DEVI __device__ __forceinline__

constexpr int S_ = 2048, D_ = 576, FH = 2304;

constexpr size_t SZ_TWQKV = (size_t)16 * 18 * 6 * 1024;
constexpr size_t SZ_TWT = (size_t)48 * 18 * 256;
constexpr size_t SZ_TWO = (size_t)8 * 18 * 4608;
constexpr size_t SZ_TWIN = (size_t)16 * 18 * 18 * 1024;
constexpr size_t SZ_TWOUT = (size_t)8 * 72 * 4608;
constexpr size_t SZ_X = (size_t)S_ * D_ * 2;
constexpr size_t SZ_Z = (size_t)S_ * FH * 2;
constexpr size_t SZ_ST = (size_t)S_ * 64;
constexpr size_t SZ_ZST = (size_t)16 * 16 * 128 * 8;
constexpr size_t OFF_TWQKV = 0;
constexpr size_t OFF_TWT = OFF_TWQKV + SZ_TWQKV;
constexpr size_t OFF_TWO = OFF_TWT + SZ_TWT;
constexpr size_t OFF_TWIN = OFF_TWO + SZ_TWO;
constexpr size_t OFF_TWOUT = OFF_TWIN + SZ_TWIN;
constexpr size_t OFF_CNT = OFF_TWOUT + SZ_TWOUT;
constexpr size_t SZ_CNT = 10 * 65536;
constexpr size_t OFF_FW = OFF_CNT + SZ_CNT;
constexpr size_t OFF_FL = OFF_FW + 262144;
constexpr size_t OFF_EP = OFF_FL + 4096;
constexpr size_t OFF_FL2 = OFF_EP + 4096;
constexpr size_t OFF_ZST = OFF_FL2 + 12288;
constexpr size_t OFF_L0 = OFF_ZST + 2 * SZ_ZST;
constexpr size_t L_XA = 0, L_STA = L_XA + SZ_X, L_O = L_STA + SZ_ST, L_XB = L_O + SZ_X, L_STB = L_XB + SZ_X, L_Z2T = L_STB + SZ_ST, L_Y = L_Z2T + SZ_Z;
constexpr size_t SZ_L = 0;
static_assert(OFF_L0 + L_Y + SZ_Z <= ((size_t)256 << 20), "workspace");
constexpr unsigned F_O = OFF_CNT, F_X1 = F_O + 65536, F_Z2 = F_O + 131072, F_Y = F_O + 196608, F_LAYER = 262144, F_X2 = F_O + 2 * F_LAYER;

DEVI unsigned pack2(float a, float b) { f32x2 v = {a, b}; bf16x2_t r = __builtin_convertvector(v, bf16x2_t); return __builtin_bit_cast(unsigned, r); }
DEVI float bflo(unsigned u) { return __uint_as_float(u << 16); }
DEVI float bfhi(unsigned u) { return __uint_as_float(u & 0xffff0000u); }
DEVI int slot2row(int sg) { return (sg & ~31) + 2 * (sg & 15) + ((sg >> 4) & 1); }
DEVI float fmax2(float a, float b) { return __builtin_amdgcn_fmed3f(a, b, __builtin_inff()); }
#define XQ4(OP, MID) \
    asm volatile("s_nop 1\n\tv_permlane32_swap_b32 %0, %1\n\tv_permlane32_swap_b32 %2, %3\n\t" OP " %0, %0, %1\n\t" OP " %2, %2, %3\n\t" \
                 "s_nop 1\n\tv_permlane16_swap_b32 %0, %2\n\t" OP " %0, %0, %2\n\t" MID \
                 "v_mov_b32 %2, %0\n\ts_nop 1\n\tv_permlane16_swap_b32 %0, %2\n\tv_mov_b32 %1, %0\n\tv_mov_b32 %3, %2\n\ts_nop 0\n\t" \
                 "v_permlane32_swap_b32 %0, %1\n\tv_permlane32_swap_b32 %2, %3" : "+v"(x[0]), "+v"(x[1]), "+v"(x[2]), "+v"(x[3]))
DEVI void xq4_max(float (&x)[4]) { XQ4("v_max_f32", ""); }
DEVI void xq4_sum(float (&x)[4]) { XQ4("v_add_f32", ""); }
DEVI void xq4_sum_rcp(float (&x)[4]) { XQ4("v_add_f32", "v_rcp_f32 %0, %0\n\ts_nop 0\n\t"); }
template <class T> DEVI T pin(T v) { asm volatile("" : "+v"(v)); return v; }
template <int V> struct IC { static constexpr int value = V; };
template <int N, int I = 0, class F> DEVI void static_for(F&& f) { if constexpr (I < N) { f(IC<I>{}); static_for<N, I + 1>(f); } }
template <class T> DEVI T* own_s(T* p) { T* r; asm volatile("s_mov_b64 %0, %1" : "=&s"(r) : "s"(p)); return r; }
DEVI int pin_s(int v) { asm volatile("" : "+s"(v)); return v; }
DEVI float xq_sum(float v) {
    auto a = __builtin_amdgcn_permlane32_swap(__float_as_uint(v), __float_as_uint(v), false, false);
    v = __uint_as_float(a[0]) + __uint_as_float(a[1]);
    auto b = __builtin_amdgcn_permlane16_swap(__float_as_uint(v), __float_as_uint(v), false, false);
    return __uint_as_float(b[0]) + __uint_as_float(b[1]);
}
#define RSRC(p) __builtin_amdgcn_make_buffer_rsrc((void*)(p), 0, 0x7fffffff, 0x00020000)
#define ST16(rs, vo, so, v, aux) __builtin_amdgcn_raw_buffer_store_b128((v), (rs), (vo) + (so), 0, aux)
#define ST8(rs, vo, so, v, aux) __builtin_amdgcn_raw_buffer_store_b64((v), (rs), (vo), (so), aux)
#define WAIT_V(n) asm volatile("s_waitcnt vmcnt(%0)" ::"i"(n) : "memory")
#define WAIT_L0() asm volatile("s_waitcnt lgkmcnt(0)" ::: "memory")
#define BAR() do { asm volatile("" ::: "memory"); __builtin_amdgcn_s_barrier(); asm volatile("" ::: "memory"); } while (0)
#define BARX() do { __builtin_amdgcn_sched_barrier(0); BAR(); __builtin_amdgcn_sched_barrier(0); } while (0)
#define LOOP_ALIGN() asm volatile(".p2align 6")
#define SCHED0() __builtin_amdgcn_sched_barrier(0)
#define MFMA(a, b, c) __builtin_amdgcn_mfma_f32_16x16x32_bf16((a), (b), (c), 0, 0, 0)
#define MFMA_T(a, b, c) asm volatile("v_mfma_f32_16x16x32_bf16 %0, %1, %2, %0" : "+v"(c) : "v"(a), "v"(b))
#define MFMA_0(a, b, c) asm volatile("v_mfma_f32_16x16x32_bf16 %0, %1, %2, 0" : "=&v"(c) : "v"(a), "v"(b))
#define WAIT_L(n) __builtin_amdgcn_s_waitcnt(0xC07F | ((n) << 8))
#define LDS_FRAG(p) (*(const LAS bf16x8*)(p))
DEVI int lane_id_fresh() { int z = 0; asm volatile("" : "+v"(z)); return __builtin_amdgcn_mbcnt_hi(~0u, __builtin_amdgcn_mbcnt_lo(~0u, z)); }
DEVI const LAS char* opaque(const LAS char* p) { asm volatile("" : "+v"(p)); return p; }
#define DBG_ARG
#define DBG_LAUNCH(n)
#define STAMP(i)
#define STAMP_DECL
#define STAMP_BEGIN
#define STAMP_END(n)
#define STAMP_PARAM
#define STAMP_FWD

#define PST_PARAM
#define PST_FWD
#define PST_DECL
#define PST(i)
#define PST_DUMP(c, k)
#define TRACE_ATTR
#define TRACE_BEGIN
#define TRACE_DUMP

DEVI float add1(float a, float b) { float r; asm("v_add_f32 %0, %1, %2" : "=v"(r) : "v"(a), "v"(b)); return r; }
DEVI float mul1(float a, float b) { float r; asm("v_mul_f32 %0, %1, %2" : "=v"(r) : "v"(a), "v"(b)); return r; }
DEVI float rstd_of(float s, float q, float inv_n, float& mean) {
    float var;
    asm("v_mul_f32 %0, %2, %3\n\tv_mul_f32 %1, %2, %4\n\tv_fma_f32 %1, -%0, %0, %1" : "=&v"(mean), "=&v"(var) : "s"(inv_n), "v"(s), "v"(q));
    return __builtin_amdgcn_rsqf(fmax2(var, 0.f) + 1e-5f);
}
DEVI float ln_rstd(const f32x4 p) {
    float s, q, mean;
    asm("v_add_f32 %0, %2, %4\n\tv_add_f32 %1, %3, %5\n\ts_nop 0\n\t"
        "v_add_f32_dpp %0, %0, %0 quad_perm:[1,0,3,2] row_mask:0xf bank_mask:0xf\n\tv_add_f32_dpp %1, %1, %1 quad_perm:[1,0,3,2] row_mask:0xf bank_mask:0xf\n\ts_nop 0\n\t"
        "v_add_f32_dpp %0, %0, %0 quad_perm:[2,3,0,1] row_mask:0xf bank_mask:0xf\n\tv_add_f32_dpp %1, %1, %1 quad_perm:[2,3,0,1] row_mask:0xf bank_mask:0xf"
        : "=&v"(s), "=&v"(q) : "v"(p[0]), "v"(p[1]), "v"(p[2]), "v"(p[3]));
    return rstd_of(s, q, 1.0f / 576.0f, mean);
}

struct Res { u32x4 a, c; u32x2 e; };
DEVI void proj_residual(f32x4 (&v)[5], const Res& x) {
    v[0] = (f32x4){bflo(x.a.x), bfhi(x.a.x), bflo(x.a.y), bfhi(x.a.y)}; v[1] = (f32x4){bflo(x.a.z), bfhi(x.a.z), bflo(x.a.w), bfhi(x.a.w)};
    v[2] = (f32x4){bflo(x.c.x), bfhi(x.c.x), bflo(x.c.y), bfhi(x.c.y)}; v[3] = (f32x4){bflo(x.c.z), bfhi(x.c.z), bflo(x.c.w), bfhi(x.c.w)};
    v[4] = (f32x4){bflo(x.e.x), bfhi(x.e.x), bflo(x.e.y), bfhi(x.e.y)};
}
struct ProjOut { __amdgpu_buffer_rsrc_t x, st; unsigned v0, v1, v2, vs; };
DEVI ProjOut proj_out(char* xout, char* stout, int rt, int cg, int lane) {
    const int r = lane & 15, q = lane >> 4;
    ProjOut p = {RSRC(xout + (size_t)rt * 18432 + cg * 2304), RSRC(stout + (size_t)rt * 1024 + cg * 8),
                 pin((unsigned)lane * 16u), pin((unsigned)lane * 16u + 1024u), pin((unsigned)(2048 + r * 16 + (q & 1) * 8)), pin((unsigned)(r * 64 + (q >> 1) * 4))};
    asm volatile("" :: "s"(p.x), "s"(p.st));
    return p;
}
DEVI ProjOut proj_out_final(char* out, int rt, int cg, int lane) {
    const int q = lane >> 4;
    const unsigned o = (unsigned)(slot2row(rt * 16 + (lane & 15)) * 1152 + cg * 144);
    ProjOut p = {RSRC(out), RSRC(out), pin(o + q * 16), pin(o + 64 + q * 16), pin(o + 128 + (q & 1) * 8), 0u};
    return p;
}
DEVI Res proj_residual_load(const ProjOut& p) {
    return {__builtin_amdgcn_raw_buffer_load_b128(p.x, p.v0, 0, 16), __builtin_amdgcn_raw_buffer_load_b128(p.x, p.v1, 0, 16), __builtin_amdgcn_raw_buffer_load_b64(p.x, p.v2, 0, 16)};
}
template <bool FINAL = false>
DEVI void proj_store(f32x4 (&v)[5], const ProjOut& p) {
    ST16(p.x, p.v0, 0, ((u32x4){pack2(v[0][0], v[0][1]), pack2(v[0][2], v[0][3]), pack2(v[1][0], v[1][1]), pack2(v[1][2], v[1][3])}), FINAL ? 0 : 16);
    SCHED0();
    asm volatile("" : "+v"(v[2]), "+v"(v[3]), "+v"(v[4]));
    ST16(p.x, p.v1, 0, ((u32x4){pack2(v[2][0], v[2][1]), pack2(v[2][2], v[2][3]), pack2(v[3][0], v[3][1]), pack2(v[3][2], v[3][3])}), FINAL ? 0 : 16);
    SCHED0();
    ST8(p.x, p.v2, 0, ((u32x2){pack2(v[4][0], v[4][1]), pack2(v[4][2], v[4][3])}), FINAL ? 0 : 16);
    SCHED0();
    if (!FINAL) {
        const f32x4 s4 = ((v[0] + v[1]) + (v[2] + v[3])) + v[4] * 0.5f;
        const f32x4 q4 = ((v[0] * v[0] + v[1] * v[1]) + (v[2] * v[2] + v[3] * v[3])) + v[4] * v[4] * 0.5f;
        auto x = __builtin_amdgcn_permlane32_swap(__float_as_uint((s4[0] + s4[2]) + (s4[1] + s4[3])), __float_as_uint((q4[0] + q4[2]) + (q4[1] + q4[3])), false, false);
        const float t = __uint_as_float(x[0]) + __uint_as_float(x[1]);
        auto y = __builtin_amdgcn_permlane16_swap(__float_as_uint(t), __float_as_uint(t), false, false);
        __builtin_amdgcn_raw_buffer_store_b32(__float_as_uint(__uint_as_float(y[0]) + __uint_as_float(y[1])), p.st, p.vs, 0, 16);
        SCHED0();
    }
}

DEVI void side_load(f32x4 (&d)[3], const char* p, const char* p2) {
    asm volatile("global_load_dwordx4 %0, %3, off sc1\n\tglobal_load_dwordx4 %1, %3, off offset:1024 sc1\n\tglobal_load_dwordx4 %2, %4, off sc1"
                 : "=&v"(d[0]), "=&v"(d[1]), "=&v"(d[2]) : "v"(p), "v"(p2) : "memory");
}
DEVI void side_fence(f32x4 (&d)[3]) { asm volatile("" : "+v"(d[0]), "+v"(d[1]), "+v"(d[2])); }
DEVI void lds_word_set(LAS char* p, unsigned v) { asm volatile("ds_write_b32 %0, %1" :: "v"((unsigned)(size_t)p), "v"(v) : "memory"); }
#define DMA_I(v, off, sc) "buffer_load_dwordx4 " v ", %1, %2 offen offset:" off sc " lds\n\t"
#define DMA5(sc) asm volatile("s_mov_b32 m0, %0\n\ts_mov_b32 exec_hi, 0\n\ts_nop 2\n\t" DMA_I("%3", "0", sc) "s_mov_b32 exec_hi, -1\n\t" \
    DMA_I("%3", "512", sc) DMA_I("%3", "1536", sc) DMA_I("%3", "2560", sc) DMA_I("%3", "3584", sc) :: "s"(la), "s"(rs), "s"(so), "v"(vo) : "memory")
#define DMA4(sc) asm volatile("s_mov_b32 m0, %0\n\ts_nop 3\n\t" DMA_I("%3", "0", sc) DMA_I("%4", "1024", sc) DMA_I("%5", "2048", sc) DMA_I("%6", "3072", sc) \
    :: "s"(la), "s"(rs), "s"(so), "v"(v0), "v"(v1), "v"(v2), "v"(v3) : "memory")
template <bool SC1> DEVI void dma5(unsigned la, __amdgpu_buffer_rsrc_t rs, int so, unsigned vo) { if (SC1) DMA5(" sc1"); else DMA5(""); }
template <bool SC1> DEVI void dma4(unsigned la, __amdgpu_buffer_rsrc_t rs, int so, unsigned v0, unsigned v1, unsigned v2, unsigned v3) { if (SC1) DMA4(" sc1"); else DMA4(""); }
#define DMA2(sc) asm volatile("s_mov_b32 m0, %0\n\ts_nop 3\n\t" DMA_I("%3", "0", sc) DMA_I("%4", "1024", sc) :: "s"(la), "s"(rs), "s"(so), "v"(v0), "v"(v1) : "memory")
template <bool SC1> DEVI void dma2(unsigned la, __amdgpu_buffer_rsrc_t rs, int so, unsigned v0, unsigned v1) { if (SC1) DMA2(" sc1"); else DMA2(""); }
DEVI void dma1(unsigned la, __amdgpu_buffer_rsrc_t rs, int so, unsigned v0) { asm volatile("s_mov_b32 m0, %0\n\ts_nop 3\n\t" DMA_I("%3", "0", "") :: "s"(la), "s"(rs), "s"(so), "v"(v0) : "memory"); }
#define WAIT_IF(c, a, b) do { if (c) WAIT_V(a); else WAIT_V(b); } while (0)
template <int T0, int T1, int P, int S0 = 0, class ISSUE>
DEVI void ring_steps(ISSUE&& issue) {
    int slot = (T0 + S0) % 3;
    LOOP_ALIGN();
#pragma unroll 1
    for (int t = T0; t < T1; ++t) {
        WAIT_V(P); BAR();
        issue(t + 3, slot);
        slot = slot == 2 ? 0 : slot + 1;
    }
}

DEVI unsigned flag_out(unsigned use, int pitch, int group, int writer, int lane) { return pin(lane < 16 ? use + (group * 16 + lane) * pitch + writer * 16 : 0x80000000u); }
DEVI unsigned flag_in(unsigned use, int pitch, int reader, int n, int lane) { return pin(use + reader * pitch + (lane < n ? lane : 0) * 16); }
DEVI void flag_raise(const char* ws, unsigned vo) { ST16(RSRC(ws), vo, 0, ((u32x4){1u, 1u, 1u, 1u}), 16); SCHED0(); }
template <int FIRST, int AGAIN = 1>
DEVI void flags_wait(const char* ws, unsigned vo) {
    unsigned v, n = 400000u;
    asm volatile("s_nop 4\n\ts_sleep %4\n"
                 "1:\n\tbuffer_load_dword %0, %2, %3, 0 offen sc1\n\ts_waitcnt vmcnt(0)\n\tv_cmp_eq_u32 vcc, 0, %0\n\ts_cbranch_vccz 2f\n\t"
                 "s_sleep %5\n\ts_sub_u32 %1, %1, 1\n\ts_cbranch_scc0 1b\n2:"
                 : "=&v"(v), "+s"(n) : "v"(vo), "s"(RSRC(ws)), "i"(FIRST), "i"(AGAIN) : "vcc", "scc", "memory");
    SCHED0();
}
template <int FIRST>
DEVI void epoch_wait(const char* ws, unsigned vo, unsigned long long ep) {
    unsigned long long v; unsigned n = 400000u;
    asm volatile("s_nop 4\n\ts_sleep %5\n"
                 "1:\n\tbuffer_load_dwordx2 %0, %2, %3, 0 offen sc1\n\ts_waitcnt vmcnt(0)\n\tv_cmp_ne_u64 vcc, %4, %0\n\ts_cbranch_vccz 2f\n\t"
                 "s_sleep 1\n\ts_sub_u32 %1, %1, 1\n\ts_cbranch_scc0 1b\n2:"
                 : "=&v"(v), "+s"(n) : "v"(vo), "s"(RSRC(ws)), "s"(ep), "i"(FIRST) : "vcc", "scc", "memory");
    SCHED0();
}
DEVI unsigned lds_arrive(LAS char* p) {
    unsigned v;
    asm volatile("s_mov_b64 exec, 1\n\tds_add_rtn_u32 %0, %1, %2\n\ts_mov_b64 exec, -1\n\ts_waitcnt lgkmcnt(0)" : "=&v"(v) : "v"((unsigned)(size_t)p), "v"(1u) : "memory");
    return __builtin_amdgcn_readfirstlane(v);
}
DEVI void lds_count_wait(LAS char* p, unsigned n) {
#pragma unroll 1
    for (unsigned spin = 0; spin < 40000000u; ++spin) {
        unsigned v;
        asm volatile("ds_read_b32 %0, %1\n\ts_waitcnt lgkmcnt(0)" : "=v"(v) : "v"((unsigned)(size_t)p) : "memory");
        if (__builtin_amdgcn_readfirstlane(v) >= n) break;
    }
}

constexpr int K1_STAGE = 32768;
constexpr int WO_OFF = 69632;
constexpr int P_IMG = 102400;
constexpr int G_OWN = 139264;
constexpr int G_PART = G_OWN + 18432;
constexpr int G_TAB = G_PART + 2048;
constexpr int K1_SCR = G_TAB + 1536;
constexpr int G_WORD = K1_SCR + 576;
constexpr int G_JUNK = G_WORD + 64;
constexpr int NET_LDS = G_JUNK + 1024;
DEVI __amdgpu_buffer_rsrc_t code_rsrc() {
    unsigned long long pc; int d0, len;
    asm volatile("s_getpc_b64 %0\n1:\n\ts_mov_b32 %1, gm_code_begin-1b\n\ts_mov_b32 %2, gm_code_end1-gm_code_begin\n\ts_max_i32 %2, %2, gm_code_end2-gm_code_begin"
                 : "=s"(pc), "=s"(d0), "=s"(len) :: "scc");
    return __builtin_amdgcn_make_buffer_rsrc((void*)((pc + d0) & ~4095ull), 0, len + ((unsigned)(pc + d0) & 4095), 0x00020000);
}

#define WO_PRE 9
#define WO_W 33
#define REL_PRIO 0
#define W_FIRST 2
#define I_SLEEP 12
#define PACE0 1
#define PACE __builtin_amdgcn_s_sleep(1)
#define POLLW 0
#define WO_EV 4
#define WIN_LOOK 20
#define W3_SLEEP 0
#define RSTD_PRIO 3
#define WIN_B 2
#define SIDE_LEFT 3
#define O_FIRST 8
#define X1_FIRST 4
#define X2_FIRST 4
#define Y_FIRST 14
#define Z2_FIRST 10
DEVI int win_strip(int b) { return ((b - 112) & 7) * 18 + ((b - 112) >> 3); }
DEVI int win_third(int b, int w) { return b & 8 ? 3 - w : w - 1; }
DEVI unsigned attn_vb(int wid, int lane) {
    const int c = lane & 15;
    return pin((unsigned)(wid < 3 ? lane * 16 : (c < 8 ? (c >> 1) & 1 : 2) * 4608 + (lane >> 4) * 64 + (c < 8 ? (c >> 2) * 2 + (c & 1) : c & 3) * 16));
}
DEVI void attn_issueB(LAS char* lds, const char* ws, int hp, int wid, unsigned vb, int s, int slot) {
    const unsigned la = (unsigned)(size_t)lds + slot * K1_STAGE + 18432 + wid * 4096;
    if (wid < 3) dma4<true>(la, RSRC(ws), (int)OFF_TWQKV + hp * (18 * 6144) + wid * 4096 + s * 12288, vb, vb, vb, vb);
    else asm volatile("s_mov_b32 m0, %0\n\ts_nop 3\n\tbuffer_load_dwordx4 %4, %1, %2 offen sc1 lds\n\tbuffer_load_dwordx4 %4, %1, %3 offen offset:1024 sc1 lds"
                      :: "s"(la), "s"(RSRC(ws)), "s"((int)OFF_TWT + hp * 13824 + s * 512), "s"((int)OFF_TWT + hp * 13824 + s * 512 + 256 - 1024), "v"(vb) : "memory");
}
struct AttnX { __amdgpu_buffer_rsrc_t rs; unsigned v[4], vh[2]; int oA, oh, step; };
DEVI AttnX attn_x(const char* x0, const char* xa, int layer, int pm, int wid, int lane) {
    const int l3 = lane >> 3, pmh = pm > 0 ? pm : 1;
    const unsigned v = layer ? lane * 16 : 2 * l3 * 1152 + ((lane & 7) ^ l3) * 16 + 4096, d1 = layer ? 0 : 16 * 1152 - 1024, d2 = layer ? 16384 : 1152 - 2048;
    const unsigned vh = layer ? (l3 & 1) * 18432 + (lane >> 4) * 256 + (8 + (lane & 7)) * 16 : v + 16 * 1152;
    return {RSRC(layer ? xa : x0 - 4096), {pin(v), pin(v + d1), pin(v + d2), pin(v + d1 + d2)}, {pin(vh), pin(vh + (layer ? 0 : 1152 - 1024))},
            (pm * 8 + 2 * wid) * 18432, (pmh * 8 - 2) * 18432, layer ? 2048 : 128};
}
DEVI void attn_issueA(LAS char* lds, const AttnX& a, int wid, int s, int slot) {
    const unsigned la = (unsigned)(size_t)lds + slot * K1_STAGE;
    if (wid == 3) dma2<true>(la, a.rs, a.oh + s * a.step, a.vh[0], a.vh[1]);
    dma4<true>(la + 2048 + wid * 4096, a.rs, a.oA + s * a.step, a.v[0], a.v[1], a.v[2], a.v[3]);
}
template <class DONE, class NEXT>
DEVI void attn_producer(LAS char* lds, const char* ws, const char* x0, const char* wl, unsigned fl, unsigned long long ep, int layer, int pm, int hp, int wid, bool side, DONE&& done, NEXT&& next) {
    const int lane = lane_id_fresh();
    const int pmh = pm > 0 ? pm : 1;
    const unsigned vo = lane * 16;
    const AttnX ax = attn_x(x0, wl + L_XA, layer, pm, wid, lane);
    const unsigned vb = attn_vb(wid, lane), fd = pin((unsigned)OFF_FL + wid * 1024 + vo);
    auto issueB = [&](int s, int slot) { attn_issueB(lds, ws, hp, wid, vb, s, slot); };
    auto issue = [&](int s, int slot) { attn_issueA(lds, ax, wid, s, slot); issueB(s, slot); };
    const int hr = lane >> 2;
    const char* ps = pin(wl + L_STA + (size_t)(pm * 128 + wid * 32) * 64 + lane * 16);
    const char* psh = pin(wl + L_STA + (size_t)((pmh * 8 - 2 + (hr >> 3)) * 16 + 8 + (hr & 7)) * 64 + (lane & 3) * 16);
    LAS char *tr = (LAS char*)opaque(lds + K1_SCR + (16 + wid * 32 + hr) * 4), *th = (LAS char*)opaque(lds + K1_SCR + hr * 4);
    if (layer == 0) {
        const unsigned fi = pin((unsigned)OFF_FW + (pm * 16 + hp) * 1024 + (lane < 6 ? lane : 0) * 128);
        done();
        next(-2);
        if (wid == 0) { epoch_wait<W_FIRST>(ws, fi, ep); issueB(0, 0); }
        BARX();
        if (wid) issueB(0, 0);
        issueB(1, 1); issue(2, 2);
        WAIT_IF(wid == 3, 10, 12);
    } else {
        issueB(2, 2);
        const unsigned fi = flag_in(F_X2, 512, pm * 16 + hp, pm > 0 ? 24 : 16, lane);
        if (wid == 0) {
            flags_wait<X2_FIRST>(ws, fi);
            AttnX a0 = ax;
#pragma unroll
            for (int i = 0; i < 4; ++i) { attn_issueA(lds, a0, i, 0, 0); a0.oA += 36864; }
        }
        BARX();
        attn_issueA(lds, ax, wid, 1, 1); attn_issueA(lds, ax, wid, 2, 2);
        WAIT_IF(wid == 3, 12, 8);
    }
    __builtin_amdgcn_s_setprio(0);
    BAR();
    if (layer) WAIT_IF(wid == 3, 6, 4);
    next(-1);
    ring_steps<0, 4, 8>(issue);
    {
        unsigned long long done;
        if (layer == 0) asm volatile("s_nop 4\n\tbuffer_load_dwordx2 %0, %1, %2, 0 offen sc1" : "+v"(done) : "v"(fd), "s"(RSRC(ws)) : "memory");
        WAIT_IF(layer, 8, 9); BAR(); issue(7, 1);
        WAIT_V(8);
        if (layer == 0) { asm volatile("" : "+v"(done)); if (__any(done != ep)) epoch_wait<0>(ws, fd, ep); }
        BAR(); issue(8, 2);
    }
    const auto rW = RSRC(ws + OFF_TWO + (size_t)(hp >> 1) * 82944 + wid * 4096);
    auto issueW = [&](int i) { dma4<true>((unsigned)(size_t)lds + WO_OFF + (i * 4 + wid) * 4096, rW, i * 16384, vo, vo, vo, vo); };
    {
        f32x4 sp[3];
        side_load(sp, ps, psh);
        WAIT_V(11); BAR(); issueW(2); issueW(3);
        if (side) next(0);
        WAIT_IF(side, 14, 8); BARX();
        __builtin_amdgcn_s_setprio(RSTD_PRIO);
        side_fence(sp);
#pragma unroll
        for (int i = 0; i < 3; ++i) lds_word_set(i < 2 ? tr + i * 64 : th, __float_as_uint(ln_rstd(sp[i])));
        __builtin_amdgcn_s_setprio(0);
        issueW(4);
        if (wid == 0) dma2<true>((unsigned)(size_t)lds + WO_OFF + 80896, rW, 80896, vo, vo);
        WAIT_L0();
    }
    BAR();
    issueW(0); issueW(1);
    next(1);
    const unsigned fo = flag_out(fl + F_O, 256, pm, hp, lane), fi = flag_in(fl + F_O, 256, pm * 16 + hp, 16, lane);
    WAIT_IF(side, SIDE_LEFT, 0);
    if (wid == POLLW) __builtin_amdgcn_s_setprio(3);
    BARX();
    if (wid == POLLW) flag_raise(ws, fo);
    next(2);
    if (wid == POLLW) flags_wait<O_FIRST>(ws, fi);
    __builtin_amdgcn_s_setprio(0);
    BARX();
}

#define STAMP_SEC 0
template <class DONE>
DEVI void attn_consumer(LAS char* lds, const char* x0, char* wl, int layer, int pm, int hp, int wid, DONE&& done STAMP_PARAM) {
    const int lane = lane_id_fresh(), r = lane & 15, q = lane >> 4;
    const char* xa = wl + L_XA;
    char* o = wl + L_O;
    const int T = wid & 1, h = wid >> 1;
    const LAS char *ph[2], *pa[2];
#pragma unroll
    for (int kk = 0; kk < 2; ++kk) {
        const int sw = ((4 * kk + q) ^ (r & 7)) * 16;
        ph[kk] = opaque(lds + (layer ? kk * 1024 + q * 256 + (r ^ (T ? 0 : 8)) * 16 : (r ^ (T ? 0 : 8)) * 128 + sw));
        pa[kk] = opaque(lds + T * 2048 + (layer ? kk * 1024 + lane * 16 : r * 128 + sw));
    }
    const LAS char* pb = lds + h * 3072 + lane * 16;
    const LAS char* pt = lds + q * 256 + (r ^ (r < 8 ? 4 * h : 2 * h)) * 16;
    f32x4 acc[5][4];
#pragma unroll
    for (int m = 0; m < 5; ++m)
#pragma unroll
        for (int n = 0; n < 4; ++n) if (m + n) acc[m][n] = pin((f32x4){0.f, 0.f, 0.f, 0.f});
    bf16x8 fa[2][5], fb[2][4];
    auto rd1 = [&](int kk, int i) {
        if (i < 3) fb[kk][i] = LDS_FRAG(pb + 18432 + kk * 6144 + i * 1024);
        else if (i == 3) fb[kk][3] = LDS_FRAG(pt + 30720 + kk * 1024);
        else if (i == 4) fa[kk][0] = LDS_FRAG(ph[kk]);
        else if (i < 9) fa[kk][i - 4] = LDS_FRAG(pa[kk] + 2048 + (i - 5) * 4096);
    };
    f32x4 mb, mb0;
#pragma unroll
    for (int j = 0; j < 4; ++j) {
        const bool ok = ((r - 4 * q - j) & 15) < 8;
        mb[j] = ok ? 0.f : -__builtin_inff();
        mb0[j] = (ok && (pm > 0 || 4 * q + j <= r)) ? 0.f : -__builtin_inff();
    }
    mb = pin(mb); mb0 = pin(mb0);
    const unsigned mp0 = pin((4 * q > r ? 0xffffu : 0u) | (4 * q + 1 > r ? 0xffff0000u : 0u)), mp1 = pin((4 * q + 2 > r ? 0xffffu : 0u) | (4 * q + 3 > r ? 0xffff0000u : 0u));
    const unsigned qtm = pin(q == 0 ? 0xffffffffu : 0u);
    const unsigned vom = pin((unsigned)(r * 16 + hp * 1024 + (2 * h + (q >> 1)) * 256 + (q & 1) * 36864));
    const unsigned vot = pin((unsigned)(r * 16 + 16384 + (hp >> 1) * 256 + (hp & 1) * 8 + h * 4 + q * 36864));
    LAS char* xw = (LAS char*)opaque(lds + (q == 2 ? wid * 640 : G_JUNK) + r * 8);
    const LAS char* xr = opaque(lds + wid * 640 + (q * 4 + (r >> 2)) * 8);
    const LAS char* tk0 = opaque(lds + K1_SCR + ((q * 4) ^ (T ? 0 : 8)) * 4);
    const LAS char* tk = opaque(lds + K1_SCR + ((1 + T) * 16 + q * 4) * 4);
    const LAS char* tq = opaque(lds + K1_SCR + ((1 + T) * 16 + r) * 4);
    pb = opaque(pb); pt = opaque(pt);
    STAMP(2);
    const int rt = pm * 8 + (hp & 1) * 4 + wid, cg = hp >> 1;
    const ProjOut pr = layer ? proj_out((char*)xa, (char*)xa, rt, cg, lane) : proj_out_final((char*)x0, rt, cg, lane);
    BAR();
    if (layer == 0) done();
    BARX();
    STAMP(3);
#pragma unroll
    for (int i = 0; i < 9; ++i) rd1(0, i);
    SCHED0();
    const Res rx = proj_residual_load(pr);
    SCHED0();
    int slot = 0, left = 8;
    LOOP_ALIGN();
#pragma unroll 1
    do {
        int d;
#pragma unroll
        for (int kk = 0; kk < 2; ++kk) {
            WAIT_L(0);
            if (kk) BAR();
            SCHED0();
#pragma unroll
            for (int i = 1; i < 20; ++i) {
                const int m = i >> 2, n = i & 3;
                const bf16x8 &A = fa[kk][m], &B = fb[kk][n];
                acc[m][n] = n != 2 ? MFMA(B, A, acc[m][n]) : MFMA(A, B, acc[m][n]);
                SCHED0();
                if (i <= 9) rd1(kk ^ 1, i - 1);
                if (kk == 0) {
                    if (i == 10) d = pin_s(slot == 2 ? -2 * K1_STAGE : K1_STAGE);
                    if (i == 11 || i == 12) ph[i & 1] = opaque(ph[i & 1] + d);
                    if (i == 13 || i == 14) pa[i & 1] = opaque(pa[i & 1] + d);
                    if (i == 15) pb = opaque(pb + d);
                    if (i == 16) pt = opaque(pt + d);
                } else {
                    if (i == 11) slot = pin_s(slot == 2 ? 0 : slot + 1);
                    if (i == 13) left = pin_s(left - 1);
                }
                SCHED0();
            }
        }
    } while (left);
    {
        typedef unsigned u32x10 __attribute__((ext_vector_type(10)));
        f32x4 rk[5], rs[4], ks[4]; float rq[4];
        u32x2 ts[5];
        u32x4 kf[5], qf[4];
        u32x10 tf, vf;
        WAIT_L(0);
        SCHED0();
        static_for<38>([&](auto ic) {
            constexpr int i = ic.value, kk = i < 14 ? 0 : i < 28 ? 1 : i >= 33, t = i % 14, n = i >= 28 ? 2 : t < 5 ? 3 : t < 10 ? 1 : 0, m = i >= 28 ? (i - 28) % 5 : t < 10 ? t % 5 : t - 9;
            if (i == 14) { WAIT_L(0); BAR(); SCHED0(); }
            acc[m][n] = n != 2 ? MFMA(fb[kk][n], fa[kk][m], acc[m][n]) : MFMA(fa[kk][m], fb[kk][n], acc[m][n]);
            SCHED0();
            if (i < 9) rd1(1, i);
            if (i == 14) { rk[0] = *(const LAS f32x4*)(tk0); rk[1] = *(const LAS f32x4*)(tk); }
            if (i == 15) { rk[2] = *(const LAS f32x4*)(tk + 128); rk[3] = *(const LAS f32x4*)(tk + 256); }
            if (i == 16) {
                rk[4] = *(const LAS f32x4*)(tk + 384);
#pragma unroll
                for (int g = 0; g < 4; ++g) rq[g] = *(const LAS float*)(tq + g * 128);
            }
            if constexpr (i >= 19 && i < 24) {
                ts[i - 19] = (u32x2){pack2(acc[i - 19][3][0], acc[i - 19][3][1]), pack2(acc[i - 19][3][2], acc[i - 19][3][3])};
                *(LAS u32x2*)(xw + (i - 19) * 128) = ts[i - 19];
            }
            if constexpr (i >= 24 && i < 29) kf[i - 24] = pin((u32x4){pack2(acc[i - 24][1][0], acc[i - 24][1][1]), pack2(acc[i - 24][1][2], acc[i - 24][1][3]), ts[i - 24].x, ts[i - 24].y});
            if constexpr (i >= 29 && i < 33) qf[i - 29] = pin((u32x4){pack2(acc[i - 28][0][0], acc[i - 28][0][1]), pack2(acc[i - 28][0][2], acc[i - 28][0][3]), pin(0u), kf[i - 28].z & qtm});
            if (i == 33) {
#pragma unroll
                for (int e = 0; e < 5; ++e) {
                    const u32x2 w = __builtin_bit_cast(u32x2, __builtin_amdgcn_ds_read_tr16_b64_v4i16((LAS s16x4*)(xr + e * 128)));
                    tf[2 * e] = w.x; tf[2 * e + 1] = w.y;
                }
            }
            if constexpr (i >= 34) {
#pragma unroll
                for (int j = 0; j < 4; ++j) rs[i - 34][j] = 4 * q + j > r ? rk[i - 34][j] : rk[i - 33][j];
                rs[i - 34] = pin(rs[i - 34]);
            }
            SCHED0();
        });
        STAMP(4);
        const f32x4 zero = {0.f, 0.f, 0.f, 0.f};
        f32x4 sp[4], sc[4];
#pragma unroll
        for (int g = 0; g < 4; ++g) {
            sp[g] = MFMA(__builtin_bit_cast(bf16x8, kf[g]), __builtin_bit_cast(bf16x8, qf[g]), zero);
            sc[g] = MFMA(__builtin_bit_cast(bf16x8, kf[g + 1]), __builtin_bit_cast(bf16x8, qf[g]), zero);
            const float c = rq[g] * (0.23570226039551584f * 1.4426950408889634f);
#pragma unroll
            for (int j = 0; j < 4; ++j) ks[g][j] = mul1(rs[g][j], c);
            SCHED0();
        }
#pragma unroll
        for (int m = 0; m < 5; ++m) { vf[2 * m] = pack2(acc[m][2][0], acc[m][2][1]); vf[2 * m + 1] = pack2(acc[m][2][2], acc[m][2][3]); }
        vf = pin(vf); tf = pin(tf);
        f32x4 t[4]; float mx[4], sm[4];
#pragma unroll
        for (int g = 0; g < 4; ++g) {
#pragma unroll
            for (int j = 0; j < 4; ++j) t[g][j] = 4 * q + j > r ? sp[g][j] : sc[g][j];
            t[g] = t[g] * ks[g] + (g == 0 ? mb0 : mb);
            mx[g] = fmaxf(fmaxf(t[g][0], t[g][1]), fmaxf(t[g][2], t[g][3]));
        }
        xq4_max(mx);
#pragma unroll
        for (int g = 0; g < 4; ++g) {
            t[g] = t[g] - mx[g];
#pragma unroll
            for (int j = 0; j < 4; ++j) t[g][j] = __builtin_amdgcn_exp2f(t[g][j]);
            sm[g] = (t[g][0] + t[g][2]) + (t[g][1] + t[g][3]);
        }
        f32x4 om[4], ot[4];
        static_for<4>([&](auto gc) {
            constexpr int g = gc.value;
            const f32x4 p = t[g] * rs[g];
            const unsigned p01 = pack2(p[0], p[1]), p23 = pack2(p[2], p[3]);
            const u32x4 pf = {p01 & mp0, p23 & mp1, p01 & ~mp0, p23 & ~mp1};
            om[g] = MFMA(__builtin_bit_cast(bf16x8, (u32x4)__builtin_shufflevector(vf, vf, 2 * g, 2 * g + 1, 2 * g + 2, 2 * g + 3)), __builtin_bit_cast(bf16x8, pf), zero);
            ot[g] = MFMA(__builtin_bit_cast(bf16x8, (u32x4)__builtin_shufflevector(tf, tf, 2 * g, 2 * g + 1, 2 * g + 2, 2 * g + 3)), __builtin_bit_cast(bf16x8, pf), zero);
        });
        xq4_sum_rcp(sm);
        const auto ro = RSRC(o + (size_t)(pm * 8 + T) * 18432);
#pragma unroll
        for (int g = 0; g < 4; g += 2) {
            const f32x4 a = om[g] * sm[g], c = om[g + 1] * sm[g + 1];
            const auto x = __builtin_amdgcn_permlane16_swap(pack2(a[0], a[1]), pack2(c[0], c[1]), false, false);
            const auto y = __builtin_amdgcn_permlane16_swap(pack2(a[2], a[3]), pack2(c[2], c[3]), false, false);
            ST16(ro, vom, g * 36864, ((u32x4){x[0], y[0], x[1], y[1]}), 16);
        }
        unsigned tw[4];
#pragma unroll
        for (int g = 0; g < 4; ++g) { const f32x2 w = ot[g].xy * sm[g]; tw[g] = pack2(w.x, w.y); }
        __builtin_amdgcn_raw_buffer_store_b32(q < 2 ? (q ? tw[1] : tw[0]) : (q == 3 ? tw[3] : tw[2]), ro, vot, 0, 16);
    }
    STAMP(5);
    {
        WAIT_V(0); BARX();
        STAMP(6);
        f32x4 v[5];
        proj_residual(v, rx);
        const LAS char* pw = opaque(lds + WO_OFF + lane * 16);
        const LAS char* pw2 = opaque(lds + WO_OFF + 41472 + lane * 16);
        const LAS char* ph2 = opaque(lds + WO_OFF + 4096 + q * 128 + (r & 7) * 16);
        const LAS char* ph3 = opaque(lds + WO_OFF + 41472 + 4096 + q * 128 + (r & 7) * 16);
        bf16x8 wf[90];
        auto rdw = [&](int i) {
            const int kc = i / 5, n = i % 5;
            wf[i] = n < 4 ? LDS_FRAG((kc < 9 ? pw : pw2) + (kc % 9) * 4608 + n * 1024) : LDS_FRAG((kc < 9 ? ph2 : ph3) + (kc % 9) * 4608);
        };
#pragma unroll
        for (int i = 0; i < WO_W; ++i) rdw(i);
#pragma unroll
        for (int n = 0; n < 5; ++n) v[n] = pin(v[n]);
        const auto rO = RSRC(o + (size_t)rt * 18432);
        const ProjOut po = proj_out(wl + L_XB, wl + L_STB, rt, cg, lane);
        asm volatile("" :: "s"(rO));
        WAIT_L(0);
        __builtin_amdgcn_s_setprio(REL_PRIO);
        BARX();
        STAMP(7);
        bf16x8 af[18];
        auto ldo = [&](int kc) { af[kc] = __builtin_bit_cast(bf16x8, __builtin_amdgcn_raw_buffer_load_b128(rO, lane * 16 + (kc & 3) * 1024, (kc >> 2) * 4096, 16)); };
#pragma unroll
        for (int kc = 0; kc < WO_PRE; ++kc) { ldo(kc); SCHED0(); if (kc >= PACE0 && kc + 1 < WO_PRE) PACE; SCHED0(); }
        static_for<90>([&](auto ic) {
            constexpr int i = ic.value, kc = i / 5, n = i % 5, j = WO_W + 2 * (i / 3) + i % 3;
            MFMA_T(wf[i], af[kc], v[n]);
            SCHED0();
            if (i % 3 < 2 && j < 90) rdw(j);
            if (i % WO_EV == 2 && i / WO_EV + WO_PRE < 18) ldo(i / WO_EV + WO_PRE);
            SCHED0();
        });
        asm volatile("s_nop 4" : "+v"(v[0]), "+v"(v[1]));
        STAMP(8);
        proj_store(v, po);
        __builtin_amdgcn_s_setprio(0);
    }
}

DEVI f32x2 gelu2(f32x2 v) {
    const f32x2 w = v * (v * v * -0.10294324f + -2.3022082f);
    const f32x2 d = (f32x2){__builtin_amdgcn_exp2f(w.x), __builtin_amdgcn_exp2f(w.y)} + 1.0f;
    return v * (f32x2){__builtin_amdgcn_rcpf(d.x), __builtin_amdgcn_rcpf(d.y)};
}
DEVI f32x4 gelu4(f32x4 v) { const f32x2 a = gelu2(v.xy), b = gelu2(v.zw); return (f32x4){a.x, a.y, b.x, b.y}; }
DEVI void rowsum16x8(const f32x4 a, const f32x4 b, float& o0, float& o1) {
    float t0, t1, t2, t3;
    asm volatile(
        "s_nop 1\n\t"
        "v_add_f32_dpp %2, %6, %6 row_ror:8 row_mask:0xf bank_mask:0x3\n\t"
        "v_add_f32_dpp %3, %7, %7 row_ror:8 row_mask:0xf bank_mask:0x3\n\t"
        "v_add_f32_dpp %4, %10, %10 row_ror:8 row_mask:0xf bank_mask:0x3\n\t"
        "v_add_f32_dpp %5, %11, %11 row_ror:8 row_mask:0xf bank_mask:0x3\n\t"
        "v_add_f32_dpp %2, %8, %8 row_ror:8 row_mask:0xf bank_mask:0xc\n\t"
        "v_add_f32_dpp %3, %9, %9 row_ror:8 row_mask:0xf bank_mask:0xc\n\t"
        "v_add_f32_dpp %4, %12, %12 row_ror:8 row_mask:0xf bank_mask:0xc\n\t"
        "v_add_f32_dpp %5, %13, %13 row_ror:8 row_mask:0xf bank_mask:0xc\n\t"
        "v_add_f32_dpp %0, %2, %2 row_half_mirror row_mask:0xf bank_mask:0x5\n\t"
        "s_nop 0\n\t"
        "v_add_f32_dpp %1, %4, %4 row_half_mirror row_mask:0xf bank_mask:0x5\n\t"
        "v_add_f32_dpp %0, %3, %3 row_half_mirror row_mask:0xf bank_mask:0xa\n\t"
        "v_add_f32_dpp %1, %5, %5 row_half_mirror row_mask:0xf bank_mask:0xa\n\t"
        "s_nop 0\n\t"
        "v_add_f32_dpp %0, %0, %0 quad_perm:[1,0,3,2] row_mask:0xf bank_mask:0xf\n\t"
        "v_add_f32_dpp %1, %1, %1 quad_perm:[1,0,3,2] row_mask:0xf bank_mask:0xf\n\t"
        "s_nop 0\n\t"
        "v_add_f32_dpp %0, %0, %0 quad_perm:[2,3,0,1] row_mask:0xf bank_mask:0xf\n\t"
        "v_add_f32_dpp %1, %1, %1 quad_perm:[2,3,0,1] row_mask:0xf bank_mask:0xf\n\t"
        : "=&v"(o0), "=&v"(o1), "=&v"(t0), "=&v"(t1), "=&v"(t2), "=&v"(t3)
        : "v"(a[0]), "v"(a[1]), "v"(a[2]), "v"(a[3]), "v"(b[0]), "v"(b[1]), "v"(b[2]), "v"(b[3]));
}

#define K5_PRE 9
#define RH_MAP 0
#define NET_LAYERS 2
constexpr int G_STAGE = 34816;
constexpr int K5_STAGE = 18432, K5_NS = 8;

constexpr int y_load_at(int slot) {
    int next = K5_PRE;
    for (int s = 0;; ++s) { const bool go = next < 18 + 2 * (s / 3); if (s == slot) return go ? next : -1; next += go; }
}

struct GTile { f32x4 a[4][4], e[2]; };
struct GFrag { bf16x8 a[2][4], b[2][5]; const LAS char *pa[2][2], *pb, *p8; int slot; };
DEVI void g_rd(GFrag& f, int kk, int i) {
    if (i < 4) f.a[kk][i] = LDS_FRAG(f.pa[kk][i >> 1] + (i & 1) * 2048);
    else if (i < 8) f.b[kk][i - 4] = LDS_FRAG(f.pb + (kk * 9 + i - 4) * 1024);
    else f.b[kk][4] = LDS_FRAG(f.p8 + kk * 9216);
}
template <int NS, int G, bool ROWS_IN_REGS>
DEVI void g_steps(GTile& c, GFrag& f, int n) {
    LOOP_ALIGN();
#pragma unroll 1
    do {
        int d;
#pragma unroll
        for (int kk = 0; kk < 2; ++kk) {
            WAIT_L(0);
            if (kk) BAR();
            SCHED0();
#pragma unroll
            for (int i = 0; i < 18; ++i) {
                const int nn = i >> 2, m = i & 3;
                f32x4& e = nn < 4 ? c.a[m][nn] : c.e[m & 1];
                e = ROWS_IN_REGS ? MFMA(f.a[kk][m], f.b[kk][nn], e) : MFMA(f.b[kk][nn], f.a[kk][m], e);
                SCHED0();
                if (i < 9) g_rd(f, kk ^ 1, i);
                if (kk == 0) {
                    if (i == 9) d = pin_s(f.slot == NS - 1 ? -(NS - 1) * G : G);
                    if (i >= 10 && i < 14) f.pa[(i - 10) >> 1][i & 1] = opaque(f.pa[(i - 10) >> 1][i & 1] + d);
                    if (i == 14) f.pb = opaque(f.pb + d);
                    if (i == 15) f.p8 = opaque(f.p8 + d);
                } else {
                    if (i == 10) f.slot = pin_s(f.slot == NS - 1 ? 0 : f.slot + 1);
                    if (i == 12) n = pin_s(n - 1);
                }
                SCHED0();
            }
        }
    } while (n);
}
DEVI void keep_s(__amdgpu_buffer_rsrc_t r) { asm volatile("" :: "s"(r)); }
DEVI void keep4(const f32x4 (&c)[4]) { asm volatile("" :: "v"(c[0]), "v"(c[1]), "v"(c[2]), "v"(c[3])); }
template <int D, int DB, bool FIRST, class HOOK>
DEVI void g_step_u(GTile& c, GFrag& f, const f32x4 (&c0)[4], HOOK&& hook) {
    static_for<36>([&](auto ic) {
        constexpr int kk = ic.value / 18, i = ic.value % 18, n = i >> 2, m = i & 3;
        if (i == 0) { WAIT_L(0); if (kk) BAR(); SCHED0(); }
        f32x4& e = n < 4 ? c.a[m][n] : c.e[m & 1];
        if (FIRST && kk == 0) asm volatile("v_mfma_f32_16x16x32_bf16 %0, %1, %2, %3" : "=&v"(e) : "v"(f.b[kk][n]), "v"(f.a[kk][m]), "v"(c0[m]));
        else MFMA_T(f.b[kk][n], f.a[kk][m], e);
        SCHED0();
        if (i < 9) g_rd(f, kk ^ 1, i);
        if (kk == 0) {
            if (i >= 10 && i < 14) f.pa[(i - 10) >> 1][i & 1] = opaque(f.pa[(i - 10) >> 1][i & 1] + D);
            if (i == 14) f.pb = opaque(f.pb + DB);
            if (i == 15) f.p8 = opaque(f.p8 + DB);
        }
        if (FIRST && ic.value == 20) keep4(c0);
        hook(ic);
        SCHED0();
    });
}
#define MF(d, a, b) "v_mfma_f32_16x16x32_bf16 " d ", " a ", " b ", " d "\n\t"
template <int D, int DB>
DEVI void g_step_gelu(GTile& c, GFrag& f, f32x4& z, float r, float c1) {
#pragma unroll
    for (int kk = 0; kk < 2; ++kk) {
        float va = z[2 * kk], vb = z[2 * kk + 1], ta, tb;
        WAIT_L(0); if (kk) BAR(); SCHED0();
#pragma unroll
        for (int i = 0; i < 9; ++i) {
            MFMA_T(f.b[kk][i >> 2], f.a[kk][i & 3], c.a[i & 3][i >> 2]);
            if (i == 5) asm volatile("v_mul_f32 %0, %0, %1" : "+v"(va) : "v"(r));
            if (i == 6) asm volatile("v_mul_f32 %0, %0, %1" : "+v"(vb) : "v"(r));
            if (i == 7) asm volatile("v_mul_f32 %0, %1, %1" : "=v"(ta) : "v"(va));
            if (i == 8) asm volatile("v_mul_f32 %0, %1, %1" : "=v"(tb) : "v"(vb));
            SCHED0();
            g_rd(f, kk ^ 1, i);
            SCHED0();
        }
        asm volatile(MF("%0", "%5", "%6") "v_fmaak_f32 %3, %3, %11, 0xc0135761\n\tv_fmaak_f32 %4, %4, %11, 0xc0135761\n\t"
                     MF("%1", "%5", "%7") "v_mul_f32 %3, %3, %9\n\tv_mul_f32 %4, %4, %10\n\t" MF("%2", "%5", "%8") "v_exp_f32 %3, %3"
                     : "+v"(c.a[1][2]), "+v"(c.a[2][2]), "+v"(c.a[3][2]), "+v"(ta), "+v"(tb)
                     : "v"(f.b[kk][2]), "v"(f.a[kk][1]), "v"(f.a[kk][2]), "v"(f.a[kk][3]), "v"(va), "v"(vb), "v"(c1));
        SCHED0(); f.pa[kk][0] = opaque(f.pa[kk][0] + D); SCHED0();
        asm volatile(MF("%0", "%6", "%7") "v_exp_f32 %5, %5\n\t" MF("%1", "%6", "%8") "v_add_f32 %4, 1.0, %4\n\tv_add_f32 %5, 1.0, %5\n\t"
                     MF("%2", "%6", "%9") "v_rcp_f32 %4, %4\n\t" MF("%3", "%6", "%10") "v_rcp_f32 %5, %5"
                     : "+v"(c.a[0][3]), "+v"(c.a[1][3]), "+v"(c.a[2][3]), "+v"(c.a[3][3]), "+v"(ta), "+v"(tb)
                     : "v"(f.b[kk][3]), "v"(f.a[kk][0]), "v"(f.a[kk][1]), "v"(f.a[kk][2]), "v"(f.a[kk][3]));
        SCHED0(); f.pa[kk][1] = opaque(f.pa[kk][1] + D); SCHED0();
        asm volatile(MF("%0", "%4", "%5") "v_mul_f32 %2, %2, %7\n\tv_mul_f32 %3, %3, %8\n\t" MF("%1", "%4", "%6")
                     : "+v"(c.e[0]), "+v"(c.e[1]), "+v"(va), "+v"(vb) : "v"(f.b[kk][4]), "v"(f.a[kk][0]), "v"(f.a[kk][1]), "v"(ta), "v"(tb));
        SCHED0();
        if (kk == 0) { f.pb = opaque(f.pb + DB); f.p8 = opaque(f.p8 + DB); }
        SCHED0();
        z[2 * kk] = va; z[2 * kk + 1] = vb;
    }
}
DEVI void g_fill(GTile& c, const float (&e)[4]) {
#pragma unroll
    for (int m = 0; m < 4; ++m) {
#pragma unroll
        for (int n = 0; n < 4; ++n) c.a[m][n] = pin((f32x4){e[m], e[m], e[m], e[m]});
        if (m < 2) c.e[m] = pin((f32x4){e[m], e[m], e[m], e[m]});
    }
}

#undef STAMP_SEC
#define STAMP_SEC 1
DEVI void gmlp_body(LAS char* lds, int wm, int wn, int pm, int cg, char* z2t, char* y, const u16* __restrict__ bsp STAMP_PARAM) {
    const int mlo = wm * 4 + wn * 2, mhi = wm * 4 + (1 - wn) * 2;
    const float zero4[4] = {0.f, 0.f, 0.f, 0.f};
    GTile z2, z1, acc;
    GFrag f;
    {
        const int lane = lane_id_fresh();
#pragma unroll
        for (int kk = 0; kk < 2; ++kk) { f.pa[kk][0] = lds + mlo * 2048 + kk * 1024 + lane * 16; f.pa[kk][1] = lds + mhi * 2048 + kk * 1024 + lane * 16; }
        f.pb = lds + 16384 + wn * 4096 + lane * 16; f.p8 = lds + 16384 + 8192 + lane * 16; f.slot = 0;
    }
    g_fill(z2, zero4); g_fill(z1, zero4);
#pragma unroll
    for (int i = 0; i < 4; ++i) f.pa[i >> 1][i & 1] = opaque(f.pa[i >> 1][i & 1]);
    f.pb = opaque(f.pb); f.p8 = opaque(f.p8);
    SCHED0();
    WAIT_V(0); BARX();
    BAR();
    BARX();
    STAMP(2);
#pragma unroll
    for (int i = 0; i < 9; ++i) g_rd(f, 0, i);
    g_steps<4, G_STAGE, true>(z2, f, 9);
    STAMP(3);
    {
        const int lane = lane_id_fresh(), r = lane & 15, q = lane >> 4;
        f32x4 ra[4];
#pragma unroll
        for (int m = 0; m < 4; ++m) ra[m] = *(const LAS f32x4*)(lds + G_TAB + ((m < 2 ? mlo : mhi) + (m & 1)) * 64 + q * 16);
        SCHED0();
#pragma unroll
        for (int m = 0; m < 4; ++m) {
            f32x4 s1, s2;
#pragma unroll
            for (int n = 0; n < 4; ++n) {
                const f32x4 g = gelu4(z2.a[m][n] * ra[m]);
                z2.a[m][n] = g;
                if (n == 0) { s1 = g; s2 = g * g; } else { s1 += g; s2 += g * g; }
            }
            if (m < 2) { const f32x4 g = gelu4(z2.e[m] * ra[m]); z2.e[m] = g; s1 += g; s2 += g * g; }
            float u0, u1;
            rowsum16x8(s1, s2, u0, u1);
            if ((r & 3) == 0) *(LAS f32x2*)(lds + G_PART + wn * 1024 + ((m < 2 ? mlo : mhi) + (m & 1)) * 128 + (q * 4 + (r >> 2)) * 8) = (f32x2){u0, u1};
            SCHED0();
        }
    }
    WAIT_L0(); BAR();
    STAMP(4);
    g_steps<4, G_STAGE, false>(z1, f, 4);
    unsigned b0, b1;
    f32x4 c0[4];
    g_step_u<G_STAGE, G_STAGE, false>(z1, f, c0, [&](auto ic) {
        if (ic.value != 18) return;
        STAMP(5);
        const int lane = lane_id_fresh(), q = lane >> 4;
        b0 = __builtin_amdgcn_raw_buffer_load_b32(RSRC(bsp), (lane & 15) * 4, pm * 256 + mlo * 32, 0);
        b1 = __builtin_amdgcn_raw_buffer_load_b32(RSRC(bsp), (lane & 15) * 4, pm * 256 + mhi * 32, 0);
#pragma unroll
        for (int g = 0; g < 2; ++g) {
            const auto rz2 = RSRC(z2t + (size_t)pm * 589824 + cg * 36864 + ((g ? mhi : mlo) >> 1) * 9216);
            const LAS char* tab = lds + G_TAB + (g ? mhi : mlo) * 64 + q * 16;
            LAS char* own = lds + (wm ? G_OWN : G_STAGE + 16384) + (g ^ wn) * 9216 + lane * 16;
            f32x4 ra[2], rb[2];
#pragma unroll
            for (int T = 0; T < 2; ++T) { ra[T] = *(const LAS f32x4*)(tab + 512 + T * 64); rb[T] = *(const LAS f32x4*)(tab + 1024 + T * 64); }
#pragma unroll
            for (int n = 0; n < (g ? 4 : 5); ++n) {
                const f32x4 v0 = (n < 4 ? z2.a[2 * g][n & 3] : z2.e[0]) * ra[0] + rb[0], v1 = (n < 4 ? z2.a[2 * g + 1][n & 3] : z2.e[1]) * ra[1] + rb[1];
                const u32x4 o = {pack2(v0[0], v1[0]), pack2(v0[1], v1[1]), pack2(v0[2], v1[2]), pack2(v0[3], v1[3])};
                ST16(rz2, lane * 16, n < 4 ? wn * 4096 + n * 1024 : 8192, o, 16);
                *(LAS u32x4*)(own + (n < 4 ? wn * 4096 + n * 1024 : 8192)) = o;
            }
        }
        STAMP(6);
    });
    static_for<3>([&](auto sc) {
        constexpr int D = sc.value == 0 ? G_STAGE : sc.value == 1 ? -3 * G_STAGE : 2 * G_STAGE;
        g_step_u<D, D, false>(z1, f, c0, [&](auto ic) { if (ic.value == 17 && sc.value == 1) WAIT_V(0); });
    });
    float ra[4], c1;
    {
        int lane, da[2];
        g_step_u<-G_STAGE, -G_STAGE, false>(z1, f, c0, [&](auto ic) {
            constexpr int i = ic.value;
            if (i == 9) lane = pin(lane_id_fresh());
            if (i == 14 || i == 15) da[i & 1] = pin((lane & 15) * 128 + (((4 * (i & 1) + (lane >> 4)) ^ (lane & 7)) * 16) - lane * 16 - (i & 1) * 1024);
            if (i == 16 || i == 17) f.pa[0][i & 1] = opaque(f.pa[0][i & 1] + da[0]);
            if (i == 27 || i == 28) f.pa[1][i & 1] = opaque(f.pa[1][i & 1] + da[1]);
            if (i >= 29 && i < 33) {
                constexpr int m = (i - 29) & 3;
                const float e = m & 1 ? bfhi(m < 2 ? b0 : b1) : bflo(m < 2 ? b0 : b1);
                c0[m] = pin((f32x4){e, e, e, e});
            }
            if (i == 33) {
#pragma unroll
                for (int m = 0; m < 4; ++m) ra[m] = *(const LAS float*)(lds + G_TAB + ((m < 2 ? mlo : mhi) + (m & 1)) * 64 + (lane & 15) * 4);
                c1 = pin(-0.10294324f);
            }
        });
    }
    STAMP(7);
    g_step_u<2 * G_STAGE, G_OWN - G_STAGE - 16384, true>(acc, f, c0, [](auto) {});
    static_for<18>([&](auto sc) {
        constexpr int st = sc.value, m = st < 16 ? st >> 2 : st & 1;
        constexpr int D = st == 0 ? -3 * G_STAGE : st % 3 == 1 ? 2 * G_STAGE : -G_STAGE;
        g_step_gelu<D, st == 0 ? 16384 - G_OWN : D>(acc, f, st < 16 ? z1.a[m][st & 3] : z1.e[m], ra[m], c1);
    });
    f.slot = 1;
    STAMP(9);
    g_steps<3, -G_STAGE, false>(acc, f, 12);
    __amdgpu_buffer_rsrc_t ry;
    unsigned vy[4], ve[2];
    int lane;
    u32x4 o = {0u, 0u, 0u, 0u};
    auto y_part = [&](auto jc) {
        constexpr int j = jc.value, m = j / 10, k = j % 10, e = (k & 1) * 2;
        const f32x4 &a = k < 8 ? acc.a[m][k >> 1] : acc.e[m & 1], &z = k < 8 ? z1.a[m][k >> 1] : z1.e[m & 1];
        float t0, t1;
        asm volatile("v_mul_f32 %1, %3, %5\n\tv_mul_f32 %2, %4, %6\n\tv_cvt_pk_bf16_f32 %0, %1, %2" : "=v"(o[k & 3]), "=&v"(t0), "=&v"(t1) : "v"(a[e]), "v"(a[e + 1]), "v"(z[e]), "v"(z[e + 1]));
        if (k == 3 || k == 7) ST16(ry, vy[m] + (k >> 2) * 1024, 0, o, 16);
        if (k == 9) ST8(ry, ve[m & 1], 0, ((u32x2){o[0], o[1]}), 16);
    };
    WAIT_L(0);
    SCHED0();
    static_for<36>([&](auto ic) {
        constexpr int i = ic.value, kk = i < 14 ? 0 : i < 28 ? 1 : i >= 32, t = i < 28 ? i % 14 : 14 + (i & 3), m = t < 10 ? t / 5 : 2 + (t - 10) / 4, n = t < 10 ? t % 5 : (t - 10) & 3;
        if (i == 14) { WAIT_L(0); BAR(); SCHED0(); }
        f32x4& e = n < 4 ? acc.a[m][n & 3] : acc.e[m & 1];
        MFMA_T(f.b[kk][n], f.a[kk][m], e);
        SCHED0();
        if (i < 9) g_rd(f, 1, i);
        if (i == 10) { ry = RSRC(own_s(y) + (size_t)pin_s(pm * 8 * 73728 + cg * 4608)); keep_s(ry); }
        if (i == 12) { lane = pin(lane_id_fresh()); vy[0] = pin((unsigned)(mlo * 73728 + wn * 2048 + lane * 16)); }
        if (i == 14) ve[0] = pin((unsigned)(mlo * 73728 + 4096 + (lane >> 5) * 256 + (lane & 15) * 16 + ((lane >> 4) & 1) * 8));
        if (i == 15) { vy[1] = pin(vy[0] + 73728); ve[1] = pin(ve[0] + 73728); }
        if (i == 16) { vy[2] = pin(vy[0] + (mhi - mlo) * 73728); vy[3] = pin(vy[2] + 73728); asm volatile("s_nop 4"); }
        if constexpr (i >= 17) y_part(IC<i - 17>{});
        SCHED0();
    });
    STAMP(10);
    y_part(IC<19>{});
    SCHED0();
#pragma unroll
    for (int i = 0; i < 4; ++i) {
        const int m = 2 + (i >> 1), p = i & 1;
        if (i == 2) asm volatile("" : "+v"(acc.a[3][0]), "+v"(acc.a[3][1]), "+v"(acc.a[3][2]), "+v"(acc.a[3][3]));
        const f32x4 a = acc.a[m][2 * p] * z1.a[m][2 * p], c = acc.a[m][2 * p + 1] * z1.a[m][2 * p + 1];
        ST16(ry, vy[m] + p * 1024, 0, ((u32x4){pack2(a[0], a[1]), pack2(a[2], a[3]), pack2(c[0], c[1]), pack2(c[2], c[3])}), 16);
        SCHED0();
    }
}

#define NET_IDS \
    int b = __builtin_amdgcn_readfirstlane(blockIdx.x), wid = wid8 & 3; \
    asm volatile("" : "+s"(b), "+s"(wid));     \
    const int xcd = b & 7, idx = b >> 3; \
    const int pa = (xcd >> 1) * 4 + (idx & 3), hp = (xcd & 1) * 8 + (idx >> 2); \
    const int pm = (xcd >> 2) * 8 + (idx & 7), cg = (xcd & 3) * 4 + (idx >> 3); \
    const int rh = RH_MAP ? xcd * 4 + (idx & 3) : (xcd >> 1) * 8 + (idx & 7), c5 = RH_MAP ? idx >> 2 : (xcd & 1) * 4 + (idx >> 3); \
    const int lead = (idx + (idx >> 3)) & 3; \
    auto kord = [&](int t, int n) { \
        const int k = t + ((int)(0x1F000000u << (((t - lead) & 3) * 4)) >> 28); \
        return (unsigned)k < (unsigned)n ? k : t; \
    }; \
    auto kord5 = [&](int t) { \
        const int k = t + ((int)(0x1F000000u << (((t - (RH_MAP ? c5 : lead)) & 3) * 4)) >> 28); \
        return (unsigned)k < 18u ? k : t; \
    };

DEVI u32x4 tr_frag(const LAS char* p, int four_rows) {
    const u32x2 a = __builtin_bit_cast(u32x2, __builtin_amdgcn_ds_read_tr16_b64_v4i16((LAS s16x4*)p));
    const u32x2 b = __builtin_bit_cast(u32x2, __builtin_amdgcn_ds_read_tr16_b64_v4i16((LAS s16x4*)(p + four_rows)));
    return (u32x4){a.x, a.y, b.x, b.y};
}
DEVI float dot2(unsigned v, unsigned w, float acc) { return __builtin_amdgcn_fdot2_f32_bf16(__builtin_bit_cast(bf16x2_t, v), __builtin_bit_cast(bf16x2_t, w), acc, false); }
DEVI float sum2(unsigned v, float acc) { return dot2(v, 0x3f803f80u, acc); }
struct Sel { unsigned lo, hi, nlo, nhi; };
DEVI Sel make_sel() { return {(unsigned)pin_s(0x00003f80), (unsigned)pin_s(0x3f800000), (unsigned)pin_s(0x0000bf80), (unsigned)pin_s((int)0xbf800000u)}; }
DEVI unsigned centre2(unsigned v, f32x2& e, float mean, const Sel& k) {
    const float a = dot2(k.lo, v, e.x), b = dot2(k.hi, v, e.y);
    const unsigned r = pack2(a, b);
    e = (f32x2){dot2(k.nlo, r, a), dot2(k.nhi, r, b)} - mean;
    return r;
}
typedef u32x4 Retile[9];
template <int PITCH>
DEVI void retile_out_make(Retile& o, const LAS char* img, int lane) {
    const int c = lane & 15, q = lane >> 4, p = lane & 3;
    const LAS char* src = img + (q * 8 + (c >> 2)) * PITCH;
#pragma unroll
    for (int i = 0; i < 8; ++i) o[i] = tr_frag(src + p * 16 + (i >> 2) * 144 + ((i >> 1) & 1) * 64 + (i & 1) * 8, 4 * PITCH);
    o[8] = tr_frag(src + 128 + (p >> 1) * 144 + (p & 1) * 8, 4 * PITCH);
}
template <int I0 = 0, int I1 = 9>
DEVI void retile_out_store(const Retile& o, char* dst, int nkc, int kc, int w, int lane) {
    const int c = lane & 15, q = lane >> 4;
    const auto rd = RSRC(dst + (size_t)(2 * w * nkc + kc) * 4608);
#pragma unroll
    for (int i = I0; i < 8 && i < I1; ++i) ST16(rd, lane * 16 + (i & 3) * 1024, (i >> 2) * nkc * 4608, o[i], 16);
    if (I1 == 9) ST16(rd, (c >> 3) * nkc * 4608 + 4096 + q * 128 + (c & 7) * 16, 0, o[8], 16);
}

DEVI float dpp_ror(float v, int n) { return n == 8 ? __builtin_amdgcn_update_dpp(0.f, v, 0x128, 0xf, 0xf, true) : n == 4 ? __builtin_amdgcn_update_dpp(0.f, v, 0x124, 0xf, 0xf, true)
                                          : n == 2 ? __builtin_amdgcn_update_dpp(0.f, v, 0x122, 0xf, 0xf, true) : __builtin_amdgcn_update_dpp(0.f, v, 0x121, 0xf, 0xf, true); }
DEVI unsigned long long epoch_of(const char* ws) {
    SCHED0();
    const unsigned long long ep = *(const __attribute__((address_space(4))) unsigned long long*)(ws + pin_s((int)OFF_EP)) + 1ull;
    asm volatile("" :: "s"(ep));
    return ep;
}
DEVI void prologue_done(LAS char* lds, char* ws, unsigned long long ep, int b, int lane) {
    if (lds_arrive(lds + G_WORD + 8) == 7) ST16(RSRC(ws), lane ? 0x80000000u : (unsigned)OFF_FL + b * 16, 0, ((u32x4){(unsigned)ep, (unsigned)(ep >> 32), 0u, 0u}), 16);
}
DEVI void centre_frag(u32x4& f, f32x2 (&er)[4], float mean, const Sel& k) {
#pragma unroll
    for (int e = 0; e < 4; ++e) f[e] = centre2(f[e], er[e], mean, k);
}
template <int N>
DEVI void column_means(LAS char* lds, LAS char* cnt, int me, int lane, float a, float b, float& ma, float& mb) {
    LAS float* red = (LAS float*)(lds + G_PART);
    auto x = __builtin_amdgcn_permlane16_swap(__float_as_uint(a), __float_as_uint(b), false, false);
    red[me * 64 + lane] = __uint_as_float(x[0]) + __uint_as_float(x[1]);
    if (lds_arrive(cnt) < N - 1) lds_count_wait(cnt, N);
    float t = 0.f;
#pragma unroll
    for (int i = 0; i < N; ++i) t += red[i * 64 + lane];
    x = __builtin_amdgcn_permlane32_swap(__float_as_uint(t), __float_as_uint(t), false, false);
    t = (__uint_as_float(x[0]) + __uint_as_float(x[1])) * (1.0f / 576.0f);
    x = __builtin_amdgcn_permlane16_swap(__float_as_uint(t), __float_as_uint(t), false, false);
    ma = __uint_as_float(x[0]); mb = __uint_as_float(x[1]);
}
template <class ARGS>
DEVI unsigned long long prologue(LAS char* lds, char*& ws, const char*& x0, const char*& wqkv, const u16*& wo, const u16*& w_in, const u16*& w_out, int b, int pa, int wid8, ARGS&& args PST_PARAM) {
    PST_DECL;
    const int lane = lane_id_fresh(), c = lane & 15, q = lane >> 4, w = wid8 & 3;
    const int jw = w - 1 + 3 * (((wid8 >> 2) ^ (b >> 3)) & 1);
    const bool wjob = b >= 16 && b < 112 && w;
    const int uo = (wid8 - 1 - (wid8 >> 2)) * 16 + b;
    const bool ojob = b < 16 && w && uo < 72;
    const unsigned l0 = (unsigned)(size_t)lds;
    LAS char* img = lds + 2 * K1_STAGE;
    const int uw = b - 16, head = (uw & 7) * 4 + ((uw >> 3) & 3), part = uw >> 5, hp = head >> 1, odd = head & 1;
    const int c0 = part * 1152 + hp * 72 + odd * 40;
    {
        const int so = pin_s(jw * 96 * 3456 + (c0 & ~15));
        const unsigned v = (lane & 3) < ((head & 3) == 3 ? 2 : 3) ? (lane >> 2) * 3456 + (lane & 3) * 16 : 0x80000000u, d = 16 * 3456 - 1024;
        const unsigned v0 = pin(v), v1 = pin(v + d), v2 = pin(v + 2 * d), v3 = pin(v + 3 * d);
        const int e = ((b - 112) >> 3) * 192 + wid8 * 64 + lane, row = (e * 10923) >> 16, r = e - row * 6;
        const unsigned vp = pin((unsigned)(row * 3456 + (r >> 1) * 1152 + (r & 1) * 128));
        const unsigned la = pin_s(l0 + 2 * K1_STAGE + jw * 6144);
        args(0);
        if (b >= 112 && wid8 < 3) dma1(l0 + G_JUNK, RSRC(wqkv), (b & 7) * 128, vp);
        if (wjob) { dma4<false>(la, RSRC(wqkv), so, v0, v1, v2, v3); dma2<false>(la + 4096, RSRC(wqkv), so + 64 * 3456, v0, v1); }
        args(1);
    }
    if (ojob) {
        const auto rs = RSRC(wo + (uo & 3) * 144);
#pragma unroll
        for (int j = 0; j < 9; ++j) {
            const int i = lane + 64 * j, rl = (i * 3641) >> 16, kp = (uo >> 2) * 32 + rl;
            const int row = kp < 512 ? 18 * (kp >> 4) + (kp & 15) + 2 * ((kp >> 4) & 1) : 36 * ((kp - 512) >> 2) + 16 + (kp & 3);
            dma1(l0 + 2 * K1_STAGE + wid8 * 9216 + j * 1024, rs, 0, (unsigned)(row * 1152 + (i - rl * 18) * 16));
        }
    }
    if (wid8 == 4) { lds_word_set(lds + G_WORD + c * 4, 0u); WAIT_L0(); }
    BAR();
    unsigned long long ep;
    if (wid8 == 4) ep = epoch_of(ws);
    if (wid8 == 4 && b >= 16 && b < 112) lds_count_wait(lds + G_WORD + 12, 12);
    if (wid8 > 4 && b >= 112) {
        __builtin_amdgcn_s_sleep(I_SLEEP);
        const unsigned v = (lane >> 2) * 9216 + (lane & 3) * 16, d = 16 * 9216 - 1024;
#pragma unroll
        for (int i = 0; i < 3; ++i) dma4<false>(l0 + P_IMG + (w - 1) * 12288 + i * 4096, RSRC(w_in + win_strip(b) * 32), (win_third(b, w) * 192 + i * 64) * 9216, v, v + d, v + 2 * d, v + 3 * d);
    }
    if (wid8 == 4) {
        AttnX ax = attn_x(x0, x0, 0, pa, 0, lane);
#pragma unroll
        for (int s = 0; s < 2; ++s)
#pragma unroll
            for (int i = 0; i < 4; ++i) { ax.oA = (pa * 8 + 2 * i) * 18432; attn_issueA(lds, ax, i, s, s); }
    }
    u32x4 xv[9];
    if (wid8 == 0) {
        const unsigned v = (lane >> 3) * 2304 + (lane & 7) * 16;
        const auto rx = RSRC(x0 + (size_t)slot2row(b * 8) * 1152);
#pragma unroll
        for (int i = 0; i < 9; ++i) asm volatile("s_nop 4\n\tbuffer_load_dwordx4 %0, %1, %2, 0 offen offset:%3" : "+v"(xv[i]) : "v"(v), "s"(rx), "i"(i * 128) : "memory");
        const auto rw = RSRC(ws);
        const u32x4 ones = {~0u, ~0u, ~0u, ~0u}, zero = {0u, 0u, 0u, 0u};
        const unsigned vo = lane * 16;
        ST16(rw, vo, (unsigned)OFF_ZST + b * 2048, ones, 16); ST16(rw, vo, (unsigned)OFF_ZST + b * 2048 + 1024, ones, 16);
        ST16(rw, vo, (unsigned)OFF_CNT + b * 2560, zero, 16); ST16(rw, vo, (unsigned)OFF_CNT + b * 2560 + 1024, zero, 16);
        ST16(rw, vo, lane < 32 ? (unsigned)OFF_CNT + b * 2560 + 2048 : (unsigned)(OFF_L0 + L_STB) + b * 512 - 512, zero, 16);
        ST16(rw, vo, (unsigned)(OFF_L0 + L_O) + (b >> 1) * 18432 + 16384 + (b & 1) * 1024, zero, 16);
        ST16(rw, b < 12 ? vo : 0x80000000u, (unsigned)OFF_FL2 + b * 1024, zero, 16);
    }
    PST(1);
    if (wjob) {
        const Sel k = make_sel();
        const LAS char* s0 = opaque(img + jw * 6144 + (q * 8 + (c >> 2)) * 64 + (c0 & 15) + (c & 3) * 8);
        const LAS char* s1 = opaque(img + jw * 6144 + (q * 8 + (c >> 2)) * 64 + (c0 & 15) + 32 + min(c & 3, 2) * 2048);
        const auto rd = RSRC(ws + OFF_TWQKV + (size_t)(hp * 18 + jw * 3) * 6144 + (3 * odd + part) * 1024), rd2 = RSRC(ws + OFF_TWT + (size_t)((hp * 3 + part) * 18 + jw * 3) * 256);
        const unsigned vd2 = pin(c < 12 && !odd ? (unsigned)((c >> 2) * 256 + q * 64 + (c & 3) * 16) : 0x80000000u);
        asm volatile("" :: "s"(rd), "s"(rd2));
        WAIT_V(0);
        PST(2);
        u32x4 f[4];
        float sm[4] = {0.f, 0.f, 0.f, 0.f}, m0, m1;
#pragma unroll
        for (int i = 0; i < 4; ++i) f[i] = tr_frag(i < 3 ? s0 + i * 2048 : s1, 256);
#pragma unroll
        for (int e = 0; e < 16; ++e) sm[(e >= 12) * 2 + (e & 1)] = sum2(f[e >> 2][e & 3], sm[(e >= 12) * 2 + (e & 1)]);
        sm[2] = c < 12 ? sm[2] + sm[3] : 0.f;
        sm[2] += dpp_ror(sm[2], 4); sm[2] += dpp_ror(sm[2], 8);
        column_means<6>(lds, lds + G_WORD + 12, jw, lane, sm[0] + sm[1], sm[2], m0, m1);
        PST(3);
        f32x2 e0[4] = {{-m0, -m0}, {-m0, -m0}, {-m0, -m0}, {-m0, -m0}}, e1[4] = {{-m1, -m1}, {-m1, -m1}, {-m1, -m1}, {-m1, -m1}};
#pragma unroll
        for (int i = 0; i < 3; ++i) { centre_frag(f[i], e0, m0, k); ST16(rd, lane * 16, i * 6144, f[i], 16); }
        centre_frag(f[3], e1, m1, k); ST16(rd2, vd2, 0, f[3], 16);
        PST(4);
        lds_arrive(lds + G_WORD + 12);
    }
    if (wid8 != 4) ep = epoch_of(ws);
    {
        if (wjob) {
            const unsigned fo = pin((unsigned)OFF_FW + ((lane >> 3) * 16 + hp) * 1024 + (3 * odd + part) * 128 + (lane & 7) * 16);
            WAIT_V(0);
            PST(5);
            if (lds_arrive(lds + G_WORD + 4) == 5) {
                const u32x4 e4 = {(unsigned)ep, (unsigned)(ep >> 32), (unsigned)ep, (unsigned)(ep >> 32)};
                ST16(RSRC(ws), fo, 0, e4, 16); ST16(RSRC(ws), fo + 131072, 0, e4, 16);
            }
            if (wid8 > 4) {
                const int oj = uw * 3 + w - 1;
                const auto rs = RSRC(w_out + (size_t)(oj >> 2) * 32 * 576 + (oj & 3) * 144);
#pragma unroll
                for (int j = 0; j < 9; ++j) { const int i = lane + 64 * j; dma1(l0 + P_IMG + (w - 1) * 9216 + j * 1024, rs, 0, (unsigned)(((i * 3641) >> 16) * (1152 - 288) + i * 16)); }
            }
        }
    }
    if (wid8 == 0) {
        WAIT_V(7);
        asm volatile("" : "+v"(xv[0]), "+v"(xv[1]), "+v"(xv[2]), "+v"(xv[3]), "+v"(xv[4]), "+v"(xv[5]), "+v"(xv[6]), "+v"(xv[7]), "+v"(xv[8]));
        float s = 0.f, q2 = 0.f;
#pragma unroll
        for (int e = 0; e < 36; ++e) { s = sum2(xv[e >> 2][e & 3], s); q2 = dot2(xv[e >> 2][e & 3], xv[e >> 2][e & 3], q2); }
        ST8(RSRC(ws + OFF_L0 + L_STA + (size_t)b * 512), lane * 8, 0, ((u32x2){__float_as_uint(s), __float_as_uint(q2)}), 16);
    }
    if (ojob) {
        WAIT_V(0);
        Retile rt;
        retile_out_make<288>(rt, img + wid8 * 9216, lane); retile_out_store(rt, ws + OFF_TWO, 18, uo >> 2, uo & 3, lane);
    }
    PST(7);
    PST_DUMP(wid8 == 0 || wid8 == 4 || wid8 == 5, wid8 ? wid8 : 2);
    return ep;
}

__global__ __launch_bounds__(512, 2) TRACE_ATTR void k_net(char* ws, const u16* wsp, const u16* bsp, char* out, const char* x0, const char* wqkv, const u16* wo,
                                                             const u16* w_in, const u16* w_out DBG_ARG) {
    extern __shared__ __attribute__((aligned(16))) char smem[];
    LAS char* lds = (LAS char*)smem;
    asm volatile("gm_code_begin:");
    if (blockIdx.x < 16) dma1((unsigned)(size_t)lds + G_JUNK, code_rsrc(), 0, (threadIdx.x * 2 + (blockIdx.x >> 3)) * 128);
    STAMP_DECL;
    const int wid8 = __builtin_amdgcn_readfirstlane(threadIdx.x >> 6);
    unsigned long long ep;
    {
        NET_IDS
        ep = prologue(lds, ws, x0, wqkv, wo, w_in, w_out, b, pa, wid8, [&](int part) {
            if (part == 0) { asm volatile("" :: "s"(wqkv), "s"(w_in), "s"(w_out)); return; }
            ws = own_s(ws); wsp = own_s(wsp); bsp = own_s(bsp); out = own_s(out); x0 = own_s(x0); wo = own_s(wo); w_out = own_s(w_out);
        } PST_FWD);
    }
    const char *twin = ws + OFF_TWIN, *twout = ws + OFF_TWOUT;
    if (wid8 >= 4) {
        u32x4 wr[5][9];
#pragma unroll 1
      for (int layer = 0; layer < NET_LAYERS; ++layer) {
        NET_IDS
        TRACE_BEGIN;
        char* wl = ws + OFF_L0 + layer * SZ_L;
        const unsigned fl = layer * F_LAYER;
        const char *xin = wl + L_XB, *st = wl + L_STB, *z2t = wl + (layer ? L_Y : L_Z2T);
        unsigned long long* zst = (unsigned long long*)(ws + OFF_ZST + layer * SZ_ZST);
        const int lane = lane_id_fresh(), l3 = lane >> 3;
        const unsigned vo = lane * 16;
        const unsigned l0 = (unsigned)(size_t)lds, lA = l0 + wid * 4096, lB = l0 + 16384 + wid * 4608;
        const auto rX = RSRC(xin + (size_t)(pm * 8 + 2 * wid) * 18432), rI = RSRC(twin + (size_t)cg * (18 * 18432) + wid * 4608);
        auto issue1B = [&](int g, int slot) { const int k = kord(g < 9 ? g : (g < 13 ? g - 4 : g - 13), 9); dma5<true>(lB + slot * G_STAGE, rI, (g < 9 ? 9 + k : k) * 18432, vo); };
        auto issue1A = [&](int g, int slot) { dma4<true>(lA + slot * G_STAGE, rX, kord(g < 9 ? g : g - 13, 9) * 2048, vo, vo, vo + 16384, vo + 16384); };
        auto issue1 = [&](int g, int slot) { issue1B(g, slot); issue1A(g, slot); };
        const auto rA = RSRC((const char*)wsp + (size_t)(pm * 128 + wid * 32) * 4096), rB = RSRC(z2t + cg * 36864 + wid * 4608);
        const unsigned voA = (unsigned)(2 * l3 * 4096 + ((lane & 7) ^ l3) * 16);
        auto issueB = [&](int k, int slot) { dma5<true>(lB + slot * G_STAGE, rB, (k >> 1) * 589824 + (k & 1) * 18432, vo); };
        const auto rW = RSRC(twout + (size_t)c5 * (72 * 4608));
        auto issue5 = [&](int t) { dma5<true>(l0 + ((t + 6) & 7) * K5_STAGE + wid * 4608, rW, kord5(t) * K5_STAGE + wid * 4608, vo); };
        auto load5 = [&](int p, int i) {
            const int t0 = p ? 2 * p + 6 : 4, ow = kord5(t0 + i) * K5_STAGE + wid * 4096;
            asm volatile("s_nop 4\n\tbuffer_load_dwordx4 %0, %4, %5, %6 offen sc1\n\tbuffer_load_dwordx4 %1, %4, %5, %6 offen offset:1024 sc1\n\t"
                         "buffer_load_dwordx4 %2, %4, %5, %6 offen offset:2048 sc1\n\tbuffer_load_dwordx4 %3, %4, %5, %6 offen offset:3072 sc1"
                         : "+v"(wr[p][4 * i]), "+v"(wr[p][4 * i + 1]), "+v"(wr[p][4 * i + 2]), "+v"(wr[p][4 * i + 3]) : "v"(vo), "s"(rW), "s"(ow) : "memory");
            if (i) {
                const int oe = kord5(t0 + (wid >> 1)) * K5_STAGE + 16384 + (wid & 1) * 1024;
                asm volatile("s_nop 4\n\tbuffer_load_dwordx4 %0, %1, %2, %3 offen sc1" : "+v"(wr[p][8]) : "v"(vo), "s"(rW), "s"(oe) : "memory");
            }
        };
        auto put5 = [&](int w0, int w1) {
#pragma unroll
            for (int w = w0; w < w1; ++w) {
                const int p = w / 9, i = w % 9 == 4 ? -1 : w % 9 - (w % 9 > 4);
                LAS char* sl = lds + (((p ? 2 * p + 6 : 4) + 6) & 7) * K5_STAGE + lane * 16;
                if (i < 0) *(LAS u32x4*)(sl + (wid >> 1) * K5_STAGE + 16384 + (wid & 1) * 1024) = wr[p][8];
                else *(LAS u32x4*)(sl + (i >> 2) * K5_STAGE + wid * 4096 + (i & 3) * 1024) = wr[p][i];
            }
            WAIT_L0();
        };
        const int row = wid * 32 + (lane & 31);
        auto step1 = [&](int t) { BAR(); issue1(t + 4, t & 3); };
        const char* ps;
        unsigned fd, fd2;
        {
            unsigned fo, fi;
            const bool oj = !layer && wid && b >= 16 && b < 112, ij = !layer && wid && b >= 112;
            const int ojob = (b - 16) * 3 + wid - 1;
            float mw[2];
            unsigned fw;
            auto win_half = [&](int h, bool make, bool store) {
                const int u = win_strip(b) * 2 + h, half = u >= 144, g = u - half * 144, ug = g / 9, j = g - ug * 9, c = lane & 15;
                const auto rd = RSRC(ws + OFF_TWIN + (size_t)ug * (18 * 18432) + (half * 18 + win_third(b, wid) * 6) * 9216);
                const unsigned vd = (lane >> 4) * 256 + (j < 8 ? ((j & ~1) + (c >> 3)) * 1024 + ((j & 1) * 8 + (c & 7)) * 16 : 8192 + c * 16);
                const Sel k = make_sel();
                const float m = mw[h];
                f32x2 er[4] = {{-m, -m}, {-m, -m}, {-m, -m}, {-m, -m}};
#pragma unroll
                for (int i = 0; i < 6; ++i) { if (make) centre_frag(wr[1 + h][i], er, m, k); if (store) ST16(rd, vd, i * 9216, wr[1 + h][i], 16); }
            };
            attn_producer(lds, ws, x0, wl, fl, ep, layer, pa, hp, wid, oj || ij, [&] { if (b < 16) WAIT_V(0); prologue_done(lds, ws, ep, b, lane); }, [&](int part) {
                if (part == -2 && ij) {
                    WAIT_V(0);
#pragma unroll
                    for (int h = 0; h < 2; ++h) {
                        const LAS char* src = lds + P_IMG + (wid - 1) * 12288 + ((lane >> 4) * 8 + ((lane & 15) >> 2)) * 64 + h * 32
                                              + ((win_strip(b) * 2 + h) % 9 < 8 ? (lane & 1) * 16 + (lane & 2) * 4 : (lane & 3) * 8);
#pragma unroll
                        for (int i = 0; i < 6; ++i) wr[1 + h][i] = tr_frag(src + i * 2048, 256);
                    }
                    float sm[4] = {0.f, 0.f, 0.f, 0.f};
#pragma unroll
                    for (int e = 0; e < 48; ++e) sm[(e >= 24) * 2 + (e & 1)] = sum2(wr[1 + (e >= 24)][(e % 24) >> 2][e & 3], sm[(e >= 24) * 2 + (e & 1)]);
                    column_means<3>(lds, lds + G_WORD, wid - 1, lane, sm[0] + sm[1], sm[2] + sm[3], mw[0], mw[1]);
                    win_half(0, true, false);
                }
                if (part == -1 && oj) { retile_out_make<288>(wr[1], lds + P_IMG + (wid - 1) * 9216, lane); WAIT_L(0); }
                if (part == 0) { if (ij) win_half(0, false, true); else retile_out_store<0, 6>(wr[1], ws + OFF_TWOUT, 72, ojob >> 2, ojob & 3, lane); }
                if (part == 1) {
                    if (layer) { issue1B(0, 0); issue1B(1, 1); }
                    if (oj) retile_out_store<6, 9>(wr[1], ws + OFF_TWOUT, 72, ojob >> 2, ojob & 3, lane);
                    if (ij) win_half(1, true, true);
                    fo = flag_out(fl + F_X1, 256, pa, hp, lane); fi = flag_in(fl + F_X1, 256, pm * 16 + cg, 16, lane);
                    ps = pin(st + (size_t)(pm * 128 + wid * 32) * 64 + lane * 16);
                    fd = pin((unsigned)OFF_FL2 + wid * 1024 + vo);
                    fd2 = pin((unsigned)OFF_FL2 + (lane < 32 ? 256 + lane : 288 + ((cg * 9) >> 1) * 3 + min(lane & 15, 14)) * 16);
                    fw = pin((unsigned)OFF_FL2 + (288 + (72 + ((cg * 9) >> 1)) * 3 + min(lane & 15, 14)) * 16);
                }
                if (part == 2 && (oj || ij)) {
                    WAIT_V(0);
                    flag_raise(ws, lane ? 0x80000000u : (unsigned)OFF_FL2 + (oj ? ojob : 288 + win_strip(b) * 3 + wid - 1) * 16);
                }
            });
            if (layer == 0) {
                __builtin_amdgcn_s_sleep(WIN_LOOK);
                flags_wait<0, 8>(ws, fw);
                if (WIN_B) { issue1B(0, 0); if (WIN_B > 1) issue1B(1, 1); }
            }
            BARX();
            if (wid == 0) flag_raise(ws, fo);
            if (layer == 0) { if (WIN_B < 1) issue1B(0, 0); if (WIN_B < 2) issue1B(1, 1); }
            issue1B(2, 2); issue1B(3, 3);
            asm volatile("" :: "s"(rX));
            const int o0 = pin_s(kord(0, 9) * 2048);
            const unsigned v2 = pin(vo + 16384);
            if (wid == 0) { if (layer) __builtin_amdgcn_s_sleep(X1_FIRST); flags_wait<0>(ws, fi);
#pragma unroll
                for (int i = 0; i < 4; ++i) dma4<true>(lA + i * 4096, rX, o0 + i * 36864, vo, vo, v2, v2);
            }
            BARX();
        }
        issue1A(1, 1); issue1A(2, 2);
        WAIT_V(8); BAR();
        issue1A(3, 3);
        {
            f32x4 sp[3];
            WAIT_V(8); step1(0);
            WAIT_V(13); step1(1);
            WAIT_V(18); step1(2);
            unsigned got[2];
            if (layer == 0) asm volatile("s_nop 4\n\tbuffer_load_dword %0, %2, %4, 0 offen sc1\n\tbuffer_load_dword %1, %3, %4, 0 offen sc1" : "+v"(got[0]), "+v"(got[1]) : "v"(fd), "v"(fd2), "s"(RSRC(ws)) : "memory");
            WAIT_IF(layer, 18, 20); step1(3);
            WAIT_IF(layer, 18, 20); step1(4);
            WAIT_V(18);
            if (layer == 0) {
                asm volatile("" : "+v"(got[0]), "+v"(got[1]));
                if (__any((got[0] & got[1]) == 0u)) { flags_wait<0>(ws, fd); flags_wait<0>(ws, fd2); }
            }
            BAR(); issue1B(9, 1); side_load(sp, ps, ps); if (layer == 0) load5(3, 0);
            WAIT_IF(layer, 17, 21); BAR(); issue1B(10, 2); if (layer == 0) load5(3, 1);
            WAIT_IF(layer, 5, 14); BARX();
            side_fence(sp);
#pragma unroll
            for (int i = 0; i < 2; ++i) lds_word_set(lds + G_TAB + (wid * 32 + i * 16 + (lane >> 2)) * 4, __float_as_uint(ln_rstd(sp[i])));
            issue1B(11, 3);
            WAIT_L0();
        }
        BAR(); issue1B(12, 0);
        if (layer == 1) {
            const auto rd = RSRC(out + (size_t)b * 9216 + wid * 2304);
            ST16(rd, vo, 0, ((u32x4){0u, 0u, 0u, 0u}), 16); ST16(rd, vo, 1024, ((u32x4){0u, 0u, 0u, 0u}), 16);
            if (lane < 16) ST16(rd, vo, 2048, ((u32x4){0u, 0u, 0u, 0u}), 16);
        }
        if (layer == 0) {
#pragma unroll
            for (int p = 0; p < 6; ++p) load5(p >> 1, p & 1);
        }
        WAIT_V(0);
        __builtin_amdgcn_s_setprio(3);
        const char* src = pin((const char*)zst + (size_t)(pm * 16 + (lane >> 4) * 4) * 1024 + wid * 256 + (lane & 15) * 16);
        const unsigned tw = pin(l0 + G_TAB + 512 + (lane >> 5) * 512 + (wid * 32 + (lane & 15) * 2 + ((lane >> 4) & 1)) * 4);
        const unsigned fz = flag_out(fl + F_Z2, 256, cg, pm, lane), fzi = pin(fl + F_Z2 + (cg * 16 + pm) * 256 + ((lane ^ pm) & 8) * 16 + (lane & 7) * 16);
        const unsigned vA[4] = {pin(voA), pin(voA + 16 * 4096 - 1024), pin(voA + 4096 - 2048), pin(voA + 17 * 4096 - 3072)};
        auto spA = [&](int so, unsigned la) { dma4<false>(la, rA, so, vA[0], vA[1], vA[2], vA[3]); };
        auto issueA = [&](int k, int slot) { spA(k * 128, lA + slot * G_STAGE); };
        {
            const LAS char* pp = opaque(lds + G_PART + row * 8);
            const auto rs = RSRC(zst + (size_t)(pm * 16 + cg) * 128);
            const unsigned vp = pin(lane < 32 ? (unsigned)row * 8u : 0x80000000u);
            asm volatile("" :: "s"(rs));
            BARX();
            const f32x2 a = *(const LAS f32x2*)pp, c = *(const LAS f32x2*)(pp + 1024);
            ST8(rs, vp, 0, ((u32x2){__float_as_uint(add1(a.x, c.x)), min(__float_as_uint(add1(a.y, c.y)), 0xfffffffeu)}), 16);
            SCHED0();
        }
        __builtin_amdgcn_s_setprio(0);
        {
            f32x4 g[4];
            float sm[4];
            auto gather = [&] {
                asm volatile("global_load_dwordx4 %0, %4, off sc1\n\tglobal_load_dwordx4 %1, %4, off offset:1024 sc1\n\t"
                             "global_load_dwordx4 %2, %4, off offset:2048 sc1\n\tglobal_load_dwordx4 %3, %4, off offset:3072 sc1"
                             : "=&v"(g[0]), "=&v"(g[1]), "=&v"(g[2]), "=&v"(g[3]) : "v"(src) : "memory");
            };
            auto total = [&] {
                asm volatile("" : "+v"(g[0]), "+v"(g[1]), "+v"(g[2]), "+v"(g[3]));
#pragma unroll
                for (int i = 0; i < 4; ++i) sm[i] = add1(add1(g[0][i], g[1][i]), add1(g[2][i], g[3][i]));
            };
            step1(9); step1(10);
            BARX(); gather(); issue1(15, 3);
            WAIT_V(22); BARX();
            __builtin_amdgcn_s_setprio(3);
            WAIT_V(9);
            total();
            if (__any(__builtin_isunordered(sm[1], sm[3]))) {
#pragma unroll 1
                for (unsigned spin = 0; spin < 400000u; ++spin) {
                    bool ok = true;
#pragma unroll
                    for (int i = 0; i < 4; ++i) ok &= __float_as_uint(g[i][1]) != 0xffffffffu && __float_as_uint(g[i][3]) != 0xffffffffu;
                    if (__all(ok)) break;
                    gather();
                    WAIT_V(0);
                    total();
                }
            }
            xq4_sum(sm);
            const bool odd = lane & 16;
            float mean;
            const float rs = rstd_of(odd ? sm[2] : sm[0], odd ? sm[3] : sm[1], 1.0f / 2304.0f, mean);
            asm volatile("ds_write_b32 %0, %1" :: "v"(tw), "v"(lane < 32 ? rs : -mean * rs) : "memory");
            __builtin_amdgcn_s_setprio(0);
            issue1B(16, 0);
            WAIT_L0();
        }
        BARX(); issue1A(16, 0); issueA(2 * pm, 1);
        auto kord2 = [&](int t) { const int j = kord(t - 2, 30); return ((j < 2 * (pm & 7) ? j : j + 2) + 2 * (pm & 8)) & 31; };
        unsigned seen;
        auto look = [&] { if (wid == 0) asm volatile("s_nop 4\n\tbuffer_load_dword %0, %1, %2, 0 offen sc1" : "+v"(seen) : "v"(fzi), "s"(RSRC(ws)) : "memory"); };
        auto need = [&](int n) {
            if (wid == 0) {
                asm volatile("" : "+v"(seen));
                if (__ballot(seen == 0u) & ((1ull << n) - 1ull)) flags_wait<0>(ws, lane < n ? fzi : fl + F_Z2 + (cg * 16 + pm) * 256 + pm * 16);
            }
        };
        auto issue = [&](int t, int sl) { const int k = kord2(t); issueB(k, sl); issueA(k, sl); };
        auto steps2 = [&](int t0, int t1) {
            int sl = (2 * t0 + 2) % 3 * G_STAGE, k = kord2(t0 + 3), ob = (k >> 1) * 589824 + (k & 1) * 18432;
            LOOP_ALIGN();
#pragma unroll 1
            for (int t = t0; t < t1; ++t) {
                WAIT_V(9); BARX();
                dma5<true>(lB + sl, rB, ob, vo);
                const int oa = pin_s(k * 128);
                const unsigned la = pin_s(lA + sl);
                k = pin_s(kord2(t + 4)); ob = pin_s((k >> 1) * 589824 + (k & 1) * 18432);
                sl = pin_s(sl == 0 ? 2 * G_STAGE : sl - G_STAGE);
                spA(oa, la);
            }
        };
        WAIT_V(13); BAR(); issue1(17, 2);
        WAIT_V(13); BARX();
        if (wid == 0) flag_raise(ws, fz);
        issueA(2 * pm + 1, 3); if (layer == 0) load5(4, 0);
        if (wid == 0) WAIT_IF(layer, 5, 9); else WAIT_IF(layer, 4, 8);
        BAR(); issueA(kord2(2), 0); if (layer == 0) load5(4, 1);
        BAR(); look(); issueA(kord2(3), 2);
        WAIT_V(4); need(3); BARX(); issueB(kord2(2), 0); issueB(kord2(3), 2); look();
        WAIT_IF(wid == 0, 6, 5); BAR(); issue(4, 1);
        WAIT_V(9); need(8);
        steps2(2, 11); look();
        WAIT_IF(wid == 0, 10, 9); BAR(); issue(14, 0);
        WAIT_V(9); need(16);
        steps2(12, 29);
        WAIT_V(9); BAR(); issue5(0); issue5(1); issue5(2);
        WAIT_V(15); BAR(); issue5(6); issue5(7);
        BAR(); __builtin_amdgcn_s_sleep(W3_SLEEP); issue5(3); put5(0, 9);
        unsigned vb;
        {
            vb = attn_vb(wid, lane);
            const unsigned fo = flag_out(fl + F_Y, 256, pm, cg, lane), fi = flag_in(fl + F_Y, 256, rh * 8 + c5, 16, lane);
            __builtin_amdgcn_s_setprio(3);
            WAIT_V(0); BARX();
            if (wid == 0) { flag_raise(ws, fo); flags_wait<Y_FIRST>(ws, fi); }
            __builtin_amdgcn_s_setprio(0);
            BARX();
        }
        put5(9, 13);
        BAR(); put5(13, 18);
        BAR(); put5(18, 24);
        BAR(); put5(24, 30);
        BAR(); put5(30, 36);
        BAR(); issue5(16); put5(36, 41);
        BAR(); issue5(17); put5(41, 45);
        WAIT_V(5); BAR();
        WAIT_V(0); BAR();
        if (layer == 0) { attn_issueB(lds, ws, hp, wid, vb, 0, 0); attn_issueB(lds, ws, hp, wid, vb, 1, 1); }
        BAR();
        if (layer == 0) {
            __builtin_amdgcn_s_setprio(3);
            const unsigned fo = flag_out(F_X2, 512, (rh >> 1) + wid, wid ? 16 + c5 : (rh & 1) * 8 + c5, wid == 0 || (wid == 1 && (rh & 1) && rh < 31) ? lane : 16);
            BARX();
            if (wid < 2) flag_raise(ws, fo);
        }
        TRACE_DUMP;
      }
        if (blockIdx.x == 0 && wid8 == 4) ST8(RSRC(ws), lane_id_fresh() ? 0x80000000u : (unsigned)OFF_EP, 0, ((u32x2){(unsigned)ep, (unsigned)(ep >> 32)}), 16);
        asm volatile("gm_code_end1:");
        return;
    }
#pragma unroll 1
  for (int layer = 0; layer < NET_LAYERS; ++layer) {
    NET_IDS
    TRACE_BEGIN;
    char* wl = ws + OFF_L0 + layer * SZ_L;
    const unsigned fl = layer * F_LAYER;
    const char* xin = wl + L_XB;
    char* y = wl + (layer ? L_Z2T : L_Y);
#undef STAMP_SEC
#define STAMP_SEC 0
    {
    STAMP_BEGIN;
    attn_consumer(lds, x0, wl, layer, pa, hp, wid, [&] { WAIT_V(0); prologue_done(lds, ws, ep, b, lane_id_fresh()); } STAMP_FWD);
    STAMP_END(1 + 5 * layer);
    }
#undef STAMP_SEC
#define STAMP_SEC 1
    STAMP_BEGIN;
    gmlp_body(lds, wid & 1, wid >> 1, pm, cg, wl + (layer ? L_Y : L_Z2T), y, bsp STAMP_FWD);
    {
        const int lane = lane_id_fresh(), r = lane & 15, q = lane >> 4;
        const int wk = wid >> 1, rt = rh * 4 + (wid & 1) * 2 + wk;
        const ProjOut pr = proj_out((char*)xin, (char*)xin, rt, c5, lane);
        const LAS char *pw[3], *ph[3];
#pragma unroll
        for (int i = 0; i < 3; ++i) {
            pw[i] = opaque(lds + 3 * i * K5_STAGE + wk * 4608 + lane * 16);
            ph[i] = opaque(lds + 3 * i * K5_STAGE + wk * 4608 + 4096 + q * 128 + (r & 7) * 16);
        }
        const auto rY = RSRC(y + (size_t)(rt & ~1) * 73728 + wk * 1024);
        const unsigned vy[2] = {pin((unsigned)(wk * 73728 + lane * 16)), pin((unsigned)((1 - wk) * 73728 + lane * 16))};
        int dk[4];
#pragma unroll
        for (int i = 0; i < 4; ++i) dk[i] = pin_s((kord5(4 + i) - 4 - i) * 4096);
        auto yoff = [&](int t) { return pin_s(t == 0 || t == 17 ? kord5(t) * 4096 : t * 4096 + dk[t & 3]); };
        bf16x8 af[5][4], wf[4][5];
        auto lda = [&](int so, int i) { return __builtin_bit_cast(bf16x8, __builtin_amdgcn_raw_buffer_load_b128(rY, vy[i & 1] + (i >> 1) * 2048, so, 16)); };
        auto rdw = [&](int t, int c, int n) {
            const int sl = (t + 6) & 7, o = (sl % 3) * K5_STAGE + c * 9216;
            wf[2 * (t & 1) + c][n] = n < 4 ? LDS_FRAG(pw[sl / 3] + o + n * 1024) : LDS_FRAG(ph[sl / 3] + o);
        };
        int so[18];
#pragma unroll
        for (int i = 0; i < 5; ++i) so[i] = yoff(i);
        so[17] = yoff(17);
        LAS char* xo = (LAS char*)opaque(lds + wid * 4608 + lane * 16);
        LAS char* xo4 = (LAS char*)opaque(lds + wid * 4608 + 4096 + (lane & 31) * 16);
        const LAS char* xi = opaque(lds + (wid ^ 2) * 4608 + lane * 16);
        const LAS char* xi4 = opaque(lds + (wid ^ 2) * 4608 + 4096 + (lane & 31) * 16);
        const ProjOut po = layer ? proj_out_final(out, rt, c5, lane) : proj_out(wl + SZ_L + L_XA, wl + SZ_L + L_STA, rt, c5, lane);
        asm volatile("" :: "s"(rY));
        SCHED0();
        WAIT_V(0);
        BARX();
        STAMP(11);
        const Res rx = proj_residual_load(pr);
#pragma unroll
        for (int n = 0; n < 10; ++n) rdw(0, n / 5, n % 5);
        f32x4 v[5], u[5];
        proj_residual(v, rx);
#pragma unroll
        for (int n = 0; n < 5; ++n) v[n] = pin(v[n]);
        WAIT_L(0);
        BARX();
        STAMP(12);
#pragma unroll
        for (int i = 0; i < K5_PRE; ++i) { af[i >> 2][i & 3] = lda(so[i >> 2], i & 3); SCHED0(); if (i >= PACE0 && i + 1 < K5_PRE) PACE; SCHED0(); }
        static_for<18>([&](auto tc) {
            constexpr int t = tc.value;
            bf16x8 (&a)[4] = af[t % 5];
#pragma unroll
            for (int h = 0; h < 4; ++h) {
                const int c = t < 17 ? h >> 1 : h & 1, m = t < 17 ? h & 1 : 1 - (h >> 1);
                if (t < 17 ? m == 0 : h < 2) { if (t == 17 && h == 1) WAIT_L(0); else WAIT_L(5); }
#pragma unroll
                for (int n = 0; n < 5; ++n) {
                    f32x4& e = m ? u[n] : v[n];
                    if (t == 0 && h == 1) MFMA_0(wf[0][n], a[1], e); else MFMA_T(wf[2 * (t & 1) + c][n], a[2 * c + m], e);
                    SCHED0();
                    if (t < 17 && m == 0) rdw(t + 1, c, n);
                    if (t < 14 && m == 1 && !(n & 1)) { const int i = y_load_at(6 * t + 3 * c + n / 2); if (i >= 0) af[(i >> 2) % 5][i & 3] = lda(so[i >> 2], i & 3); }
                    if (t < 12 && h == 3 && n == 3) so[t + 5] = yoff(t + 5);
                    if (t == 17 && h * 5 + n >= 13 && h * 5 + n < 18) { const int i = h * 5 + n - 13; *(LAS f32x4*)(i < 4 ? xo + i * 1024 : xo4) = u[i]; }
                    SCHED0();
                }
            }
            if ((t & 1) && t < 17) BAR();
            SCHED0();
        });
        {
            WAIT_L0(); BARX();
#pragma unroll
            for (int n = 0; n < 5; ++n) u[n] = *(const LAS f32x4*)(n < 4 ? xi + n * 1024 : xi4);
            SCHED0();
            asm volatile("s_nop 1" : "+v"(v[0]), "+v"(v[1]), "+v"(v[2]), "+v"(v[3]), "+v"(v[4]));
#pragma unroll
            for (int n = 0; n < 5; ++n) v[n] += u[n];
        }
        STAMP(13);
        if (layer) proj_store<true>(v, po);
        else { proj_store(v, po); WAIT_V(0); BARX(); }
    }
    STAMP_END(3 + 5 * layer);
    TRACE_DUMP;
  }
  asm volatile("gm_code_end2:");
}

}

#define STOP_CHECK(n)

extern "C" void kernel_launch(void* const* d_in, const int* in_sizes, int n_in, void* d_out, int out_size, void* d_ws, size_t ws_size,
                              hipStream_t stream) {
    (void)in_sizes; (void)n_in; (void)out_size; (void)ws_size;
    const u16* x = (const u16*)d_in[0];
    const u16* wqkv = (const u16*)d_in[1];
    const u16* wo = (const u16*)d_in[2];
    const u16* w_in = (const u16*)d_in[4];
    const u16* w_sp = (const u16*)d_in[5];
    const u16* b_sp = (const u16*)d_in[6];
    const u16* w_out = (const u16*)d_in[7];
    char* ws = (char*)d_ws;
    (void)hipFuncSetAttribute((const void*)k_net, hipFuncAttributeMaxDynamicSharedMemorySize, NET_LDS);
    k_net<<<256, 512, NET_LDS, stream>>>(ws, w_sp, b_sp, (char*)d_out, (const char*)x, (const char*)wqkv, wo, w_in, w_out DBG_LAUNCH(0));
}
```

```cpp
#include <hip/hip_runtime.h>
#include <stdint.h>

namespace {

typedef unsigned short u16;
typedef short bf16x8 __attribute__((ext_vector_type(8)));
typedef float f32x4 __attribute__((ext_vector_type(4)));
typedef float f32x2 __attribute__((ext_vector_type(2)));
typedef unsigned u32x4 __attribute__((ext_vector_type(4)));
typedef unsigned u32x2 __attribute__((ext_vector_type(2)));
typedef short s16x4 __attribute__((ext_vector_type(4)));
typedef __bf16 bf16x2_t __attribute__((ext_vector_type(2)));
#define LAS __attribute__((address_space(3)))
#define DEVI __device__ __forceinline__

constexpr int S_ = 2048, D_ = 576, FH = 2304;

constexpr size_t SZ_TWQKV = (size_t)16 * 18 * 6 * 1024;
constexpr size_t SZ_TWT = (size_t)48 * 18 * 256;
constexpr size_t SZ_TWO = (size_t)8 * 18 * 4608;
constexpr size_t SZ_TWIN = (size_t)16 * 18 * 18 * 1024;
constexpr size_t SZ_TWOUT = (size_t)8 * 72 * 4608;
constexpr size_t SZ_X = (size_t)S_ * D_ * 2;
constexpr size_t SZ_Z = (size_t)S_ * FH * 2;
constexpr size_t SZ_ST = (size_t)S_ * 64;
constexpr size_t SZ_ZST = (size_t)16 * 16 * 128 * 8;
constexpr size_t OFF_TWQKV = 0;
constexpr size_t OFF_TWT = OFF_TWQKV + SZ_TWQKV;
constexpr size_t OFF_TWO = OFF_TWT + SZ_TWT;
constexpr size_t OFF_TWIN = OFF_TWO + SZ_TWO;
constexpr size_t OFF_TWOUT = OFF_TWIN + SZ_TWIN;
constexpr size_t OFF_CNT = OFF_TWOUT + SZ_TWOUT;
constexpr size_t SZ_CNT = 10 * 65536;
constexpr size_t OFF_FW = OFF_CNT + SZ_CNT;
constexpr size_t OFF_FL = OFF_FW + 262144;
constexpr size_t OFF_EP = OFF_FL + 4096;
constexpr size_t OFF_FL2 = OFF_EP + 4096;
constexpr size_t OFF_ZST = OFF_FL2 + 12288;
constexpr size_t OFF_L0 = OFF_ZST + 2 * SZ_ZST;
constexpr size_t L_XA = 0, L_STA = L_XA + SZ_X, L_O = L_STA + SZ_ST, L_XB = L_O + SZ_X, L_STB = L_XB + SZ_X, L_Z2T = L_STB + SZ_ST, L_Y = L_Z2T + SZ_Z;
constexpr size_t SZ_L = 0;
static_assert(OFF_L0 + L_Y + SZ_Z <= ((size_t)256 << 20), "workspace");
constexpr unsigned F_O = OFF_CNT, F_X1 = F_O + 65536, F_Z2 = F_O + 131072, F_Y = F_O + 196608, F_LAYER = 262144, F_X2 = F_O + 2 * F_LAYER;

DEVI unsigned pack2(float a, float b) { f32x2 v = {a, b}; bf16x2_t r = __builtin_convertvector(v, bf16x2_t); return __builtin_bit_cast(unsigned, r); }
DEVI float bflo(unsigned u) { return __uint_as_float(u << 16); }
DEVI float bfhi(unsigned u) { return __uint_as_float(u & 0xffff0000u); }
DEVI int slot2row(int sg) { return (sg & ~31) + 2 * (sg & 15) + ((sg >> 4) & 1); }
DEVI float fmax2(float a, float b) { return __builtin_amdgcn_fmed3f(a, b, __builtin_inff()); }
#define XQ4(OP, MID) \
    asm volatile("s_nop 1\n\tv_permlane32_swap_b32 %0, %1\n\tv_permlane32_swap_b32 %2, %3\n\t" OP " %0, %0, %1\n\t" OP " %2, %2, %3\n\t" \
                 "s_nop 1\n\tv_permlane16_swap_b32 %0, %2\n\t" OP " %0, %0, %2\n\t" MID \
                 "v_mov_b32 %2, %0\n\ts_nop 1\n\tv_permlane16_swap_b32 %0, %2\n\tv_mov_b32 %1, %0\n\tv_mov_b32 %3, %2\n\ts_nop 0\n\t" \
                 "v_permlane32_swap_b32 %0, %1\n\tv_permlane32_swap_b32 %2, %3" : "+v"(x[0]), "+v"(x[1]), "+v"(x[2]), "+v"(x[3]))
DEVI void xq4_max(float (&x)[4]) { XQ4("v_max_f32", ""); }
DEVI void xq4_sum(float (&x)[4]) { XQ4("v_add_f32", ""); }
DEVI void xq4_sum_rcp(float (&x)[4]) { XQ4("v_add_f32", "v_rcp_f32 %0, %0\n\ts_nop 0\n\t"); }
template <class T> DEVI T pin(T v) { asm volatile("" : "+v"(v)); return v; }
template <int V> struct IC { static constexpr int value = V; };
template <int N, int I = 0, class F> DEVI void static_for(F&& f) { if constexpr (I < N) { f(IC<I>{}); static_for<N, I + 1>(f); } }
template <class T> DEVI T* own_s(T* p) { T* r; asm volatile("s_mov_b64 %0, %1" : "=&s"(r) : "s"(p)); return r; }
DEVI int pin_s(int v) { asm volatile("" : "+s"(v)); return v; }
DEVI float xq_sum(float v) {
    auto a = __builtin_amdgcn_permlane32_swap(__float_as_uint(v), __float_as_uint(v), false, false);
    v = __uint_as_float(a[0]) + __uint_as_float(a[1]);
    auto b = __builtin_amdgcn_permlane16_swap(__float_as_uint(v), __float_as_uint(v), false, false);
    return __uint_as_float(b[0]) + __uint_as_float(b[1]);
}
#define RSRC(p) __builtin_amdgcn_make_buffer_rsrc((void*)(p), 0, 0x7fffffff, 0x00020000)
#define ST16(rs, vo, so, v, aux) __builtin_amdgcn_raw_buffer_store_b128((v), (rs), (vo) + (so), 0, aux)
#define ST8(rs, vo, so, v, aux) __builtin_amdgcn_raw_buffer_store_b64((v), (rs), (vo), (so), aux)
#define WAIT_V(n) asm volatile("s_waitcnt vmcnt(%0)" ::"i"(n) : "memory")
#define WAIT_L0() asm volatile("s_waitcnt lgkmcnt(0)" ::: "memory")
#define BAR() do { asm volatile("" ::: "memory"); __builtin_amdgcn_s_barrier(); asm volatile("" ::: "memory"); } while (0)
#define BARX() do { __builtin_amdgcn_sched_barrier(0); BAR(); __builtin_amdgcn_sched_barrier(0); } while (0)
#define LOOP_ALIGN() asm volatile(".p2align 6")
#define SCHED0() __builtin_amdgcn_sched_barrier(0)
#define MFMA(a, b, c) __builtin_amdgcn_mfma_f32_16x16x32_bf16((a), (b), (c), 0, 0, 0)
#define MFMA_T(a, b, c) asm volatile("v_mfma_f32_16x16x32_bf16 %0, %1, %2, %0" : "+v"(c) : "v"(a), "v"(b))
#define MFMA_0(a, b, c) asm volatile("v_mfma_f32_16x16x32_bf16 %0, %1, %2, 0" : "=&v"(c) : "v"(a), "v"(b))
#define WAIT_L(n) __builtin_amdgcn_s_waitcnt(0xC07F | ((n) << 8))
#define LDS_FRAG(p) (*(const LAS bf16x8*)(p))
DEVI int lane_id_fresh() { int z = 0; asm volatile("" : "+v"(z)); return __builtin_amdgcn_mbcnt_hi(~0u, __builtin_amdgcn_mbcnt_lo(~0u, z)); }
DEVI const LAS char* opaque(const LAS char* p) { asm volatile("" : "+v"(p)); return p; }
#define DBG_ARG
#define DBG_LAUNCH(n)
#define STAMP(i)
#define STAMP_DECL
#define STAMP_BEGIN
#define STAMP_END(n)
#define STAMP_PARAM
#define STAMP_FWD

#define PST_PARAM
#define PST_FWD
#define PST_DECL
#define PST(i)
#define PST_DUMP(c, k)
#define TRACE_ATTR
#define TRACE_BEGIN
#define TRACE_DUMP

DEVI float add1(float a, float b) { float r; asm("v_add_f32 %0, %1, %2" : "=v"(r) : "v"(a), "v"(b)); return r; }
DEVI float mul1(float a, float b) { float r; asm("v_mul_f32 %0, %1, %2" : "=v"(r) : "v"(a), "v"(b)); return r; }
DEVI float rstd_of(float s, float q, float inv_n, float& mean) {
    float var;
    asm("v_mul_f32 %0, %2, %3\n\tv_mul_f32 %1, %2, %4\n\tv_fma_f32 %1, -%0, %0, %1" : "=&v"(mean), "=&v"(var) : "s"(inv_n), "v"(s), "v"(q));
    return __builtin_amdgcn_rsqf(fmax2(var, 0.f) + 1e-5f);
}
DEVI float ln_rstd(const f32x4 p) {
    float s, q, mean;
    asm("v_add_f32 %0, %2, %4\n\tv_add_f32 %1, %3, %5\n\ts_nop 0\n\t"
        "v_add_f32_dpp %0, %0, %0 quad_perm:[1,0,3,2] row_mask:0xf bank_mask:0xf\n\tv_add_f32_dpp %1, %1, %1 quad_perm:[1,0,3,2] row_mask:0xf bank_mask:0xf\n\ts_nop 0\n\t"
        "v_add_f32_dpp %0, %0, %0 quad_perm:[2,3,0,1] row_mask:0xf bank_mask:0xf\n\tv_add_f32_dpp %1, %1, %1 quad_perm:[2,3,0,1] row_mask:0xf bank_mask:0xf"
        : "=&v"(s), "=&v"(q) : "v"(p[0]), "v"(p[1]), "v"(p[2]), "v"(p[3]));
    return rstd_of(s, q, 1.0f / 576.0f, mean);
}

struct Res { u32x4 a, c; u32x2 e; };
DEVI void proj_residual(f32x4 (&v)[5], const Res& x) {
    v[0] = (f32x4){bflo(x.a.x), bfhi(x.a.x), bflo(x.a.y), bfhi(x.a.y)}; v[1] = (f32x4){bflo(x.a.z), bfhi(x.a.z), bflo(x.a.w), bfhi(x.a.w)};
    v[2] = (f32x4){bflo(x.c.x), bfhi(x.c.x), bflo(x.c.y), bfhi(x.c.y)}; v[3] = (f32x4){bflo(x.c.z), bfhi(x.c.z), bflo(x.c.w), bfhi(x.c.w)};
    v[4] = (f32x4){bflo(x.e.x), bfhi(x.e.x), bflo(x.e.y), bfhi(x.e.y)};
}
struct ProjOut { __amdgpu_buffer_rsrc_t x, st; unsigned v0, v1, v2, vs; };
DEVI ProjOut proj_out(char* xout, char* stout, int rt, int cg, int lane) {
    const int r = lane & 15, q = lane >> 4;
    ProjOut p = {RSRC(xout + (size_t)rt * 18432 + cg * 2304), RSRC(stout + (size_t)rt * 1024 + cg * 8),
                 pin((unsigned)lane * 16u), pin((unsigned)lane * 16u + 1024u), pin((unsigned)(2048 + r * 16 + (q & 1) * 8)), pin((unsigned)(r * 64 + (q >> 1) * 4))};
    asm volatile("" :: "s"(p.x), "s"(p.st));
    return p;
}
DEVI ProjOut proj_out_final(char* out, int rt, int cg, int lane) {
    const int q = lane >> 4;
    const unsigned o = (unsigned)(slot2row(rt * 16 + (lane & 15)) * 1152 + cg * 144);
    ProjOut p = {RSRC(out), RSRC(out), pin(o + q * 16), pin(o + 64 + q * 16), pin(o + 128 + (q & 1) * 8), 0u};
    return p;
}
DEVI Res proj_residual_load(const ProjOut& p) {
    return {__builtin_amdgcn_raw_buffer_load_b128(p.x, p.v0, 0, 16), __builtin_amdgcn_raw_buffer_load_b128(p.x, p.v1, 0, 16), __builtin_amdgcn_raw_buffer_load_b64(p.x, p.v2, 0, 16)};
}
template <bool FINAL = false>
DEVI void proj_store(f32x4 (&v)[5], const ProjOut& p) {
    ST16(p.x, p.v0, 0, ((u32x4){pack2(v[0][0], v[0][1]), pack2(v[0][2], v[0][3]), pack2(v[1][0], v[1][1]), pack2(v[1][2], v[1][3])}), FINAL ? 0 : 16);
    SCHED0();
    asm volatile("" : "+v"(v[2]), "+v"(v[3]), "+v"(v[4]));
    ST16(p.x, p.v1, 0, ((u32x4){pack2(v[2][0], v[2][1]), pack2(v[2][2], v[2][3]), pack2(v[3][0], v[3][1]), pack2(v[3][2], v[3][3])}), FINAL ? 0 : 16);
    SCHED0();
    ST8(p.x, p.v2, 0, ((u32x2){pack2(v[4][0], v[4][1]), pack2(v[4][2], v[4][3])}), FINAL ? 0 : 16);
    SCHED0();
    if (!FINAL) {
        const f32x4 s4 = ((v[0] + v[1]) + (v[2] + v[3])) + v[4] * 0.5f;
        const f32x4 q4 = ((v[0] * v[0] + v[1] * v[1]) + (v[2] * v[2] + v[3] * v[3])) + v[4] * v[4] * 0.5f;
        auto x = __builtin_amdgcn_permlane32_swap(__float_as_uint((s4[0] + s4[2]) + (s4[1] + s4[3])), __float_as_uint((q4[0] + q4[2]) + (q4[1] + q4[3])), false, false);
        const float t = __uint_as_float(x[0]) + __uint_as_float(x[1]);
        auto y = __builtin_amdgcn_permlane16_swap(__float_as_uint(t), __float_as_uint(t), false, false);
        __builtin_amdgcn_raw_buffer_store_b32(__float_as_uint(__uint_as_float(y[0]) + __uint_as_float(y[1])), p.st, p.vs, 0, 16);
        SCHED0();
    }
}

DEVI void side_load(f32x4 (&d)[3], const char* p, const char* p2) {
    asm volatile("global_load_dwordx4 %0, %3, off sc1\n\tglobal_load_dwordx4 %1, %3, off offset:1024 sc1\n\tglobal_load_dwordx4 %2, %4, off sc1"
                 : "=&v"(d[0]), "=&v"(d[1]), "=&v"(d[2]) : "v"(p), "v"(p2) : "memory");
}
DEVI void side_fence(f32x4 (&d)[3]) { asm volatile("" : "+v"(d[0]), "+v"(d[1]), "+v"(d[2])); }
DEVI void lds_word_set(LAS char* p, unsigned v) { asm volatile("ds_write_b32 %0, %1" :: "v"((unsigned)(size_t)p), "v"(v) : "memory"); }
#define DMA_I(v, off, sc) "buffer_load_dwordx4 " v ", %1, %2 offen offset:" off sc " lds\n\t"
#define DMA5(sc) asm volatile("s_mov_b32 m0, %0\n\ts_mov_b32 exec_hi, 0\n\ts_nop 2\n\t" DMA_I("%3", "0", sc) "s_mov_b32 exec_hi, -1\n\t" \
    DMA_I("%3", "512", sc) DMA_I("%3", "1536", sc) DMA_I("%3", "2560", sc) DMA_I("%3", "3584", sc) :: "s"(la), "s"(rs), "s"(so), "v"(vo) : "memory")
#define DMA4(sc) asm volatile("s_mov_b32 m0, %0\n\ts_nop 3\n\t" DMA_I("%3", "0", sc) DMA_I("%4", "1024", sc) DMA_I("%5", "2048", sc) DMA_I("%6", "3072", sc) \
    :: "s"(la), "s"(rs), "s"(so), "v"(v0), "v"(v1), "v"(v2), "v"(v3) : "memory")
template <bool SC1> DEVI void dma5(unsigned la, __amdgpu_buffer_rsrc_t rs, int so, unsigned vo) { if (SC1) DMA5(" sc1"); else DMA5(""); }
template <bool SC1> DEVI void dma4(unsigned la, __amdgpu_buffer_rsrc_t rs, int so, unsigned v0, unsigned v1, unsigned v2, unsigned v3) { if (SC1) DMA4(" sc1"); else DMA4(""); }
#define DMA2(sc) asm volatile("s_mov_b32 m0, %0\n\ts_nop 3\n\t" DMA_I("%3", "0", sc) DMA_I("%4", "1024", sc) :: "s"(la), "s"(rs), "s"(so), "v"(v0), "v"(v1) : "memory")
template <bool SC1> DEVI void dma2(unsigned la, __amdgpu_buffer_rsrc_t rs, int so, unsigned v0, unsigned v1) { if (SC1) DMA2(" sc1"); else DMA2(""); }
DEVI void dma1(unsigned la, __amdgpu_buffer_rsrc_t rs, int so, unsigned v0) { asm volatile("s_mov_b32 m0, %0\n\ts_nop 3\n\t" DMA_I("%3", "0", "") :: "s"(la), "s"(rs), "s"(so), "v"(v0) : "memory"); }
#define WAIT_IF(c, a, b) do { if (c) WAIT_V(a); else WAIT_V(b); } while (0)
template <int T0, int T1, int P, int S0 = 0, class ISSUE>
DEVI void ring_steps(ISSUE&& issue) {
    int slot = (T0 + S0) % 3;
    LOOP_ALIGN();
#pragma unroll 1
    for (int t = T0; t < T1; ++t) {
        WAIT_V(P); BAR();
        issue(t + 3, slot);
        slot = slot == 2 ? 0 : slot + 1;
    }
}

DEVI unsigned flag_out(unsigned use, int pitch, int group, int writer, int lane) { return pin(lane < 16 ? use + (group * 16 + lane) * pitch + writer * 16 : 0x80000000u); }
DEVI unsigned flag_in(unsigned use, int pitch, int reader, int n, int lane) { return pin(use + reader * pitch + (lane < n ? lane : 0) * 16); }
DEVI void flag_raise(const char* ws, unsigned vo) { ST16(RSRC(ws), vo, 0, ((u32x4){1u, 1u, 1u, 1u}), 16); SCHED0(); }
template <int FIRST, int AGAIN = 1>
DEVI void flags_wait(const char* ws, unsigned vo) {
    unsigned v, n = 400000u;
    asm volatile("s_nop 4\n\ts_sleep %4\n"
                 "1:\n\tbuffer_load_dword %0, %2, %3, 0 offen sc1\n\ts_waitcnt vmcnt(0)\n\tv_cmp_eq_u32 vcc, 0, %0\n\ts_cbranch_vccz 2f\n\t"
                 "s_sleep %5\n\ts_sub_u32 %1, %1, 1\n\ts_cbranch_scc0 1b\n2:"
                 : "=&v"(v), "+s"(n) : "v"(vo), "s"(RSRC(ws)), "i"(FIRST), "i"(AGAIN) : "vcc", "scc", "memory");
    SCHED0();
}
template <int FIRST, int AGAIN = 1>
DEVI void epoch_wait(const char* ws, unsigned vo, unsigned long long ep) {
    unsigned long long v; unsigned n = 400000u;
    asm volatile("s_nop 4\n\ts_sleep %5\n"
                 "1:\n\tbuffer_load_dwordx2 %0, %2, %3, 0 offen sc1\n\ts_waitcnt vmcnt(0)\n\tv_cmp_ne_u64 vcc, %4, %0\n\ts_cbranch_vccz 2f\n\t"
                 "s_sleep %6\n\ts_sub_u32 %1, %1, 1\n\ts_cbranch_scc0 1b\n2:"
                 : "=&v"(v), "+s"(n) : "v"(vo), "s"(RSRC(ws)), "s"(ep), "i"(FIRST), "i"(AGAIN) : "vcc", "scc", "memory");
    SCHED0();
}
DEVI unsigned lds_arrive(LAS char* p) {
    unsigned v;
    asm volatile("s_mov_b64 exec, 1\n\tds_add_rtn_u32 %0, %1, %2\n\ts_mov_b64 exec, -1\n\ts_waitcnt lgkmcnt(0)" : "=&v"(v) : "v"((unsigned)(size_t)p), "v"(1u) : "memory");
    return __builtin_amdgcn_readfirstlane(v);
}
DEVI void lds_count_wait(LAS char* p, unsigned n) {
#pragma unroll 1
    for (unsigned spin = 0; spin < 40000000u; ++spin) {
        unsigned v;
        asm volatile("ds_read_b32 %0, %1\n\ts_waitcnt lgkmcnt(0)" : "=v"(v) : "v"((unsigned)(size_t)p) : "memory");
        if (__builtin_amdgcn_readfirstlane(v) >= n) break;
    }
}

constexpr int K1_STAGE = 32768;
constexpr int WO_OFF = 69632;
constexpr int P_IMG = 102400;
constexpr int G_OWN = 139264;
constexpr int G_PART = G_OWN + 18432;
constexpr int G_TAB = G_PART + 2048;
constexpr int K1_SCR = G_TAB + 1536;
constexpr int G_WORD = K1_SCR + 576;
constexpr int G_JUNK = G_WORD + 64;
constexpr int NET_LDS = G_JUNK + 1024;
DEVI __amdgpu_buffer_rsrc_t code_rsrc() {
    unsigned long long pc; int d0, len;
    asm volatile("s_getpc_b64 %0\n1:\n\ts_mov_b32 %1, gm_code_begin-1b\n\ts_mov_b32 %2, gm_code_end1-gm_code_begin\n\ts_max_i32 %2, %2, gm_code_end2-gm_code_begin"
                 : "=s"(pc), "=s"(d0), "=s"(len) :: "scc");
    return __builtin_amdgcn_make_buffer_rsrc((void*)((pc + d0) & ~4095ull), 0, len + ((unsigned)(pc + d0) & 4095), 0x00020000);
}

#define WO_PRE 9
#define WO_W 33
#define REL_PRIO 0
#define W_FIRST 2
#define I_SLEEP 12
#define PACE0 1
#define PACE __builtin_amdgcn_s_sleep(1)
#define POLLW 0
#define WO_EV 4
#define WIN_LOOK 20
#define W3_SLEEP 0
#define RSTD_PRIO 3

#define O_FIRST 8
#define X1_FIRST 4
#define X2_FIRST 4
#define Y_FIRST 14
#define Z2_FIRST 10
DEVI int win_strip(int b) { return ((b - 112) & 7) * 18 + ((b - 112) >> 3); }
DEVI int win_third(int b, int w) { return b & 8 ? 3 - w : w - 1; }
DEVI unsigned attn_vb(int wid, int lane) {
    const int c = lane & 15;
    return pin((unsigned)(wid < 3 ? lane * 16 : (c < 8 ? (c >> 1) & 1 : 2) * 4608 + (lane >> 4) * 64 + (c < 8 ? (c >> 2) * 2 + (c & 1) : c & 3) * 16));
}
DEVI void attn_issueB(LAS char* lds, const char* ws, int hp, int wid, unsigned vb, int s, int slot) {
    const unsigned la = (unsigned)(size_t)lds + slot * K1_STAGE + 18432 + wid * 4096;
    if (wid < 3) dma4<true>(la, RSRC(ws), (int)OFF_TWQKV + hp * (18 * 6144) + wid * 4096 + s * 12288, vb, vb, vb, vb);
    else asm volatile("s_mov_b32 m0, %0\n\ts_nop 3\n\tbuffer_load_dwordx4 %4, %1, %2 offen sc1 lds\n\tbuffer_load_dwordx4 %4, %1, %3 offen offset:1024 sc1 lds"
                      :: "s"(la), "s"(RSRC(ws)), "s"((int)OFF_TWT + hp * 13824 + s * 512), "s"((int)OFF_TWT + hp * 13824 + s * 512 + 256 - 1024), "v"(vb) : "memory");
}
struct AttnX { __amdgpu_buffer_rsrc_t rs; unsigned v[4], vh[2]; int oA, oh, step; };
DEVI AttnX attn_x(const char* x0, const char* xa, int layer, int pm, int wid, int lane) {
    const int l3 = lane >> 3, pmh = pm > 0 ? pm : 1;
    const unsigned v = layer ? lane * 16 : 2 * l3 * 1152 + ((lane & 7) ^ l3) * 16 + 4096, d1 = layer ? 0 : 16 * 1152 - 1024, d2 = layer ? 16384 : 1152 - 2048;
    const unsigned vh = layer ? (l3 & 1) * 18432 + (lane >> 4) * 256 + (8 + (lane & 7)) * 16 : v + 16 * 1152;
    return {RSRC(layer ? xa : x0 - 4096), {pin(v), pin(v + d1), pin(v + d2), pin(v + d1 + d2)}, {pin(vh), pin(vh + (layer ? 0 : 1152 - 1024))},
            (pm * 8 + 2 * wid) * 18432, (pmh * 8 - 2) * 18432, layer ? 2048 : 128};
}
DEVI void attn_issueA(LAS char* lds, const AttnX& a, int wid, int s, int slot) {
    const unsigned la = (unsigned)(size_t)lds + slot * K1_STAGE;
    if (wid == 3) dma2<true>(la, a.rs, a.oh + s * a.step, a.vh[0], a.vh[1]);
    dma4<true>(la + 2048 + wid * 4096, a.rs, a.oA + s * a.step, a.v[0], a.v[1], a.v[2], a.v[3]);
}
template <class DONE, class NEXT>
DEVI void attn_producer(LAS char* lds, const char* ws, const char* x0, const char* wl, unsigned fl, unsigned long long ep, int layer, int pm, int hp, int wid, bool side, DONE&& done, NEXT&& next) {
    const int lane = lane_id_fresh();
    const int pmh = pm > 0 ? pm : 1;
    const unsigned vo = lane * 16;
    const AttnX ax = attn_x(x0, wl + L_XA, layer, pm, wid, lane);
    const unsigned vb = attn_vb(wid, lane), fd = pin((unsigned)OFF_FL + wid * 1024 + vo);
    auto issueB = [&](int s, int slot) { attn_issueB(lds, ws, hp, wid, vb, s, slot); };
    auto issue = [&](int s, int slot) { attn_issueA(lds, ax, wid, s, slot); issueB(s, slot); };
    const int hr = lane >> 2;
    const char* ps = pin(wl + L_STA + (size_t)(pm * 128 + wid * 32) * 64 + lane * 16);
    const char* psh = pin(wl + L_STA + (size_t)((pmh * 8 - 2 + (hr >> 3)) * 16 + 8 + (hr & 7)) * 64 + (lane & 3) * 16);
    LAS char *tr = (LAS char*)opaque(lds + K1_SCR + (16 + wid * 32 + hr) * 4), *th = (LAS char*)opaque(lds + K1_SCR + hr * 4);
    if (layer == 0) {
        const unsigned fi = pin((unsigned)OFF_FW + (pm * 16 + hp) * 1024 + (lane < 6 ? lane : 0) * 128);
        done();
        next(-2);
        if (wid == 0) { epoch_wait<W_FIRST>(ws, fi, ep); issueB(0, 0); }
        BARX();
        if (wid) issueB(0, 0);
        issueB(1, 1); issue(2, 2);
        WAIT_IF(wid == 3, 10, 12);
    } else {
        issueB(2, 2);
        const unsigned fi = flag_in(F_X2, 512, pm * 16 + hp, pm > 0 ? 24 : 16, lane);
        if (wid == 0) {
            flags_wait<X2_FIRST>(ws, fi);
            AttnX a0 = ax;
#pragma unroll
            for (int i = 0; i < 4; ++i) { attn_issueA(lds, a0, i, 0, 0); a0.oA += 36864; }
        }
        BARX();
        attn_issueA(lds, ax, wid, 1, 1); attn_issueA(lds, ax, wid, 2, 2);
        WAIT_IF(wid == 3, 12, 8);
    }
    __builtin_amdgcn_s_setprio(0);
    BAR();
    if (layer) WAIT_IF(wid == 3, 6, 4);
    next(-1);
    ring_steps<0, 4, 8>(issue);
    {
        unsigned long long done;
        if (layer == 0) asm volatile("s_nop 4\n\tbuffer_load_dwordx2 %0, %1, %2, 0 offen sc1" : "+v"(done) : "v"(fd), "s"(RSRC(ws)) : "memory");
        WAIT_IF(layer, 8, 9); BAR(); issue(7, 1);
        WAIT_V(8);
        if (layer == 0) { asm volatile("" : "+v"(done)); if (__any(done != ep)) epoch_wait<0, 12>(ws, fd, ep); }
        BAR(); issue(8, 2);
    }
    const auto rW = RSRC(ws + OFF_TWO + (size_t)(hp >> 1) * 82944 + wid * 4096);
    auto issueW = [&](int i) { dma4<true>((unsigned)(size_t)lds + WO_OFF + (i * 4 + wid) * 4096, rW, i * 16384, vo, vo, vo, vo); };
    {
        f32x4 sp[3];
        side_load(sp, ps, psh);
        WAIT_V(11); BAR(); issueW(2); issueW(3);
        if (side) next(0);
        WAIT_IF(side, 14, 8); BARX();
        __builtin_amdgcn_s_setprio(RSTD_PRIO);
        side_fence(sp);
#pragma unroll
        for (int i = 0; i < 3; ++i) lds_word_set(i < 2 ? tr + i * 64 : th, __float_as_uint(ln_rstd(sp[i])));
        __builtin_amdgcn_s_setprio(0);
        issueW(4);
        if (wid == 0) dma2<true>((unsigned)(size_t)lds + WO_OFF + 80896, rW, 80896, vo, vo);
        WAIT_L0();
    }
    BAR();
    issueW(0); issueW(1);
    next(1);
    const unsigned fo = flag_out(fl + F_O, 256, pm, hp, lane), fi = flag_in(fl + F_O, 256, pm * 16 + hp, 16, lane);
    WAIT_IF(side, 3, 0);
    if (wid == POLLW) __builtin_amdgcn_s_setprio(3);
    BARX();
    if (wid == POLLW) flag_raise(ws, fo);
    next(2);
    if (wid == POLLW) flags_wait<O_FIRST>(ws, fi);
    __builtin_amdgcn_s_setprio(0);
    BARX();
}

#define STAMP_SEC 0
template <class DONE>
DEVI void attn_consumer(LAS char* lds, const char* x0, char* wl, int layer, int pm, int hp, int wid, DONE&& done STAMP_PARAM) {
    const int lane = lane_id_fresh(), r = lane & 15, q = lane >> 4;
    const char* xa = wl + L_XA;
    char* o = wl + L_O;
    const int T = wid & 1, h = wid >> 1;
    const LAS char *ph[2], *pa[2];
#pragma unroll
    for (int kk = 0; kk < 2; ++kk) {
        const int sw = ((4 * kk + q) ^ (r & 7)) * 16;
        ph[kk] = opaque(lds + (layer ? kk * 1024 + q * 256 + (r ^ (T ? 0 : 8)) * 16 : (r ^ (T ? 0 : 8)) * 128 + sw));
        pa[kk] = opaque(lds + T * 2048 + (layer ? kk * 1024 + lane * 16 : r * 128 + sw));
    }
    const LAS char* pb = lds + h * 3072 + lane * 16;
    const LAS char* pt = lds + q * 256 + (r ^ (r < 8 ? 4 * h : 2 * h)) * 16;
    f32x4 acc[5][4];
#pragma unroll
    for (int m = 0; m < 5; ++m)
#pragma unroll
        for (int n = 0; n < 4; ++n) if (m + n) acc[m][n] = pin((f32x4){0.f, 0.f, 0.f, 0.f});
    bf16x8 fa[2][5], fb[2][4];
    auto rd1 = [&](int kk, int i) {
        if (i < 3) fb[kk][i] = LDS_FRAG(pb + 18432 + kk * 6144 + i * 1024);
        else if (i == 3) fb[kk][3] = LDS_FRAG(pt + 30720 + kk * 1024);
        else if (i == 4) fa[kk][0] = LDS_FRAG(ph[kk]);
        else if (i < 9) fa[kk][i - 4] = LDS_FRAG(pa[kk] + 2048 + (i - 5) * 4096);
    };
    f32x4 mb, mb0;
#pragma unroll
    for (int j = 0; j < 4; ++j) {
        const bool ok = ((r - 4 * q - j) & 15) < 8;
        mb[j] = ok ? 0.f : -__builtin_inff();
        mb0[j] = (ok && (pm > 0 || 4 * q + j <= r)) ? 0.f : -__builtin_inff();
    }
    mb = pin(mb); mb0 = pin(mb0);
    const unsigned mp0 = pin((4 * q > r ? 0xffffu : 0u) | (4 * q + 1 > r ? 0xffff0000u : 0u)), mp1 = pin((4 * q + 2 > r ? 0xffffu : 0u) | (4 * q + 3 > r ? 0xffff0000u : 0u));
    const unsigned qtm = pin(q == 0 ? 0xffffffffu : 0u);
    const unsigned vom = pin((unsigned)(r * 16 + hp * 1024 + (2 * h + (q >> 1)) * 256 + (q & 1) * 36864));
    const unsigned vot = pin((unsigned)(r * 16 + 16384 + (hp >> 1) * 256 + (hp & 1) * 8 + h * 4 + q * 36864));
    LAS char* xw = (LAS char*)opaque(lds + (q == 2 ? wid * 640 : G_JUNK) + r * 8);
    const LAS char* xr = opaque(lds + wid * 640 + (q * 4 + (r >> 2)) * 8);
    const LAS char* tk0 = opaque(lds + K1_SCR + ((q * 4) ^ (T ? 0 : 8)) * 4);
    const LAS char* tk = opaque(lds + K1_SCR + ((1 + T) * 16 + q * 4) * 4);
    const LAS char* tq = opaque(lds + K1_SCR + ((1 + T) * 16 + r) * 4);
    pb = opaque(pb); pt = opaque(pt);
    STAMP(2);
    const int rt = pm * 8 + (hp & 1) * 4 + wid, cg = hp >> 1;
    const ProjOut pr = layer ? proj_out((char*)xa, (char*)xa, rt, cg, lane) : proj_out_final((char*)x0, rt, cg, lane);
    BAR();
    if (layer == 0) done();
    BARX();
    STAMP(3);
#pragma unroll
    for (int i = 0; i < 9; ++i) rd1(0, i);
    SCHED0();
    const Res rx = proj_residual_load(pr);
    SCHED0();
    int slot = 0, left = 8;
    LOOP_ALIGN();
#pragma unroll 1
    do {
        int d;
#pragma unroll
        for (int kk = 0; kk < 2; ++kk) {
            WAIT_L(0);
            if (kk) BAR();
            SCHED0();
#pragma unroll
            for (int i = 1; i < 20; ++i) {
                const int m = i >> 2, n = i & 3;
                const bf16x8 &A = fa[kk][m], &B = fb[kk][n];
                acc[m][n] = n != 2 ? MFMA(B, A, acc[m][n]) : MFMA(A, B, acc[m][n]);
                SCHED0();
                if (i <= 9) rd1(kk ^ 1, i - 1);
                if (kk == 0) {
                    if (i == 10) d = pin_s(slot == 2 ? -2 * K1_STAGE : K1_STAGE);
                    if (i == 11 || i == 12) ph[i & 1] = opaque(ph[i & 1] + d);
                    if (i == 13 || i == 14) pa[i & 1] = opaque(pa[i & 1] + d);
                    if (i == 15) pb = opaque(pb + d);
                    if (i == 16) pt = opaque(pt + d);
                } else {
                    if (i == 11) slot = pin_s(slot == 2 ? 0 : slot + 1);
                    if (i == 13) left = pin_s(left - 1);
                }
                SCHED0();
            }
        }
    } while (left);
    {
        typedef unsigned u32x10 __attribute__((ext_vector_type(10)));
        f32x4 rk[5], rs[4], ks[4]; float rq[4];
        u32x2 ts[5];
        u32x4 kf[5], qf[4];
        u32x10 tf, vf;
        WAIT_L(0);
        SCHED0();
        static_for<38>([&](auto ic) {
            constexpr int i = ic.value, kk = i < 14 ? 0 : i < 28 ? 1 : i >= 33, t = i % 14, n = i >= 28 ? 2 : t < 5 ? 3 : t < 10 ? 1 : 0, m = i >= 28 ? (i - 28) % 5 : t < 10 ? t % 5 : t - 9;
            if (i == 14) { WAIT_L(0); BAR(); SCHED0(); }
            acc[m][n] = n != 2 ? MFMA(fb[kk][n], fa[kk][m], acc[m][n]) : MFMA(fa[kk][m], fb[kk][n], acc[m][n]);
            SCHED0();
            if (i < 9) rd1(1, i);
            if (i == 14) { rk[0] = *(const LAS f32x4*)(tk0); rk[1] = *(const LAS f32x4*)(tk); }
            if (i == 15) { rk[2] = *(const LAS f32x4*)(tk + 128); rk[3] = *(const LAS f32x4*)(tk + 256); }
            if (i == 16) {
                rk[4] = *(const LAS f32x4*)(tk + 384);
#pragma unroll
                for (int g = 0; g < 4; ++g) rq[g] = *(const LAS float*)(tq + g * 128);
            }
            if constexpr (i >= 19 && i < 24) {
                ts[i - 19] = (u32x2){pack2(acc[i - 19][3][0], acc[i - 19][3][1]), pack2(acc[i - 19][3][2], acc[i - 19][3][3])};
                *(LAS u32x2*)(xw + (i - 19) * 128) = ts[i - 19];
            }
            if constexpr (i >= 24 && i < 29) kf[i - 24] = pin((u32x4){pack2(acc[i - 24][1][0], acc[i - 24][1][1]), pack2(acc[i - 24][1][2], acc[i - 24][1][3]), ts[i - 24].x, ts[i - 24].y});
            if constexpr (i >= 29 && i < 33) qf[i - 29] = pin((u32x4){pack2(acc[i - 28][0][0], acc[i - 28][0][1]), pack2(acc[i - 28][0][2], acc[i - 28][0][3]), pin(0u), kf[i - 28].z & qtm});
            if (i == 33) {
#pragma unroll
                for (int e = 0; e < 5; ++e) {
                    const u32x2 w = __builtin_bit_cast(u32x2, __builtin_amdgcn_ds_read_tr16_b64_v4i16((LAS s16x4*)(xr + e * 128)));
                    tf[2 * e] = w.x; tf[2 * e + 1] = w.y;
                }
            }
            if constexpr (i >= 34) {
#pragma unroll
                for (int j = 0; j < 4; ++j) rs[i - 34][j] = 4 * q + j > r ? rk[i - 34][j] : rk[i - 33][j];
                rs[i - 34] = pin(rs[i - 34]);
            }
            SCHED0();
        });
        STAMP(4);
        const f32x4 zero = {0.f, 0.f, 0.f, 0.f};
        f32x4 sp[4], sc[4];
#pragma unroll
        for (int g = 0; g < 4; ++g) {
            sp[g] = MFMA(__builtin_bit_cast(bf16x8, kf[g]), __builtin_bit_cast(bf16x8, qf[g]), zero);
            sc[g] = MFMA(__builtin_bit_cast(bf16x8, kf[g + 1]), __builtin_bit_cast(bf16x8, qf[g]), zero);
            const float c = rq[g] * (0.23570226039551584f * 1.4426950408889634f);
#pragma unroll
            for (int j = 0; j < 4; ++j) ks[g][j] = mul1(rs[g][j], c);
            SCHED0();
        }
#pragma unroll
        for (int m = 0; m < 5; ++m) { vf[2 * m] = pack2(acc[m][2][0], acc[m][2][1]); vf[2 * m + 1] = pack2(acc[m][2][2], acc[m][2][3]); }
        vf = pin(vf); tf = pin(tf);
        f32x4 t[4]; float mx[4], sm[4];
#pragma unroll
        for (int g = 0; g < 4; ++g) {
#pragma unroll
            for (int j = 0; j < 4; ++j) t[g][j] = 4 * q + j > r ? sp[g][j] : sc[g][j];
            t[g] = t[g] * ks[g] + (g == 0 ? mb0 : mb);
            mx[g] = fmaxf(fmaxf(t[g][0], t[g][1]), fmaxf(t[g][2], t[g][3]));
        }
        xq4_max(mx);
#pragma unroll
        for (int g = 0; g < 4; ++g) {
            t[g] = t[g] - mx[g];
#pragma unroll
            for (int j = 0; j < 4; ++j) t[g][j] = __builtin_amdgcn_exp2f(t[g][j]);
            sm[g] = (t[g][0] + t[g][2]) + (t[g][1] + t[g][3]);
        }
        f32x4 om[4], ot[4];
        static_for<4>([&](auto gc) {
            constexpr int g = gc.value;
            const f32x4 p = t[g] * rs[g];
            const unsigned p01 = pack2(p[0], p[1]), p23 = pack2(p[2], p[3]);
            const u32x4 pf = {p01 & mp0, p23 & mp1, p01 & ~mp0, p23 & ~mp1};
            om[g] = MFMA(__builtin_bit_cast(bf16x8, (u32x4)__builtin_shufflevector(vf, vf, 2 * g, 2 * g + 1, 2 * g + 2, 2 * g + 3)), __builtin_bit_cast(bf16x8, pf), zero);
            ot[g] = MFMA(__builtin_bit_cast(bf16x8, (u32x4)__builtin_shufflevector(tf, tf, 2 * g, 2 * g + 1, 2 * g + 2, 2 * g + 3)), __builtin_bit_cast(bf16x8, pf), zero);
        });
        xq4_sum_rcp(sm);
        const auto ro = RSRC(o + (size_t)(pm * 8 + T) * 18432);
#pragma unroll
        for (int g = 0; g < 4; g += 2) {
            const f32x4 a = om[g] * sm[g], c = om[g + 1] * sm[g + 1];
            const auto x = __builtin_amdgcn_permlane16_swap(pack2(a[0], a[1]), pack2(c[0], c[1]), false, false);
            const auto y = __builtin_amdgcn_permlane16_swap(pack2(a[2], a[3]), pack2(c[2], c[3]), false, false);
            ST16(ro, vom, g * 36864, ((u32x4){x[0], y[0], x[1], y[1]}), 16);
        }
        unsigned tw[4];
#pragma unroll
        for (int g = 0; g < 4; ++g) { const f32x2 w = ot[g].xy * sm[g]; tw[g] = pack2(w.x, w.y); }
        __builtin_amdgcn_raw_buffer_store_b32(q < 2 ? (q ? tw[1] : tw[0]) : (q == 3 ? tw[3] : tw[2]), ro, vot, 0, 16);
    }
    STAMP(5);
    {
        WAIT_V(0); BARX();
        STAMP(6);
        f32x4 v[5];
        proj_residual(v, rx);
        const LAS char* pw = opaque(lds + WO_OFF + lane * 16);
        const LAS char* pw2 = opaque(lds + WO_OFF + 41472 + lane * 16);
        const LAS char* ph2 = opaque(lds + WO_OFF + 4096 + q * 128 + (r & 7) * 16);
        const LAS char* ph3 = opaque(lds + WO_OFF + 41472 + 4096 + q * 128 + (r & 7) * 16);
        bf16x8 wf[90];
        auto rdw = [&](int i) {
            const int kc = i / 5, n = i % 5;
            wf[i] = n < 4 ? LDS_FRAG((kc < 9 ? pw : pw2) + (kc % 9) * 4608 + n * 1024) : LDS_FRAG((kc < 9 ? ph2 : ph3) + (kc % 9) * 4608);
        };
#pragma unroll
        for (int i = 0; i < WO_W; ++i) rdw(i);
#pragma unroll
        for (int n = 0; n < 5; ++n) v[n] = pin(v[n]);
        const auto rO = RSRC(o + (size_t)rt * 18432);
        const ProjOut po = proj_out(wl + L_XB, wl + L_STB, rt, cg, lane);
        asm volatile("" :: "s"(rO));
        WAIT_L(0);
        __builtin_amdgcn_s_setprio(REL_PRIO);
        BARX();
        STAMP(7);
        bf16x8 af[18];
        auto ldo = [&](int kc) { af[kc] = __builtin_bit_cast(bf16x8, __builtin_amdgcn_raw_buffer_load_b128(rO, lane * 16 + (kc & 3) * 1024, (kc >> 2) * 4096, 16)); };
#pragma unroll
        for (int kc = 0; kc < WO_PRE; ++kc) { ldo(kc); SCHED0(); if (kc >= PACE0 && kc + 1 < WO_PRE) PACE; SCHED0(); }
        static_for<90>([&](auto ic) {
            constexpr int i = ic.value, kc = i / 5, n = i % 5, j = WO_W + 2 * (i / 3) + i % 3;
            MFMA_T(wf[i], af[kc], v[n]);
            SCHED0();
            if (i % 3 < 2 && j < 90) rdw(j);
            if (i % WO_EV == 2 && i / WO_EV + WO_PRE < 18) ldo(i / WO_EV + WO_PRE);
            SCHED0();
        });
        asm volatile("s_nop 4" : "+v"(v[0]), "+v"(v[1]));
        STAMP(8);
        proj_store(v, po);
        __builtin_amdgcn_s_setprio(0);
    }
}

DEVI f32x2 gelu2(f32x2 v) {
    const f32x2 w = v * (v * v * -0.10294324f + -2.3022082f);
    const f32x2 d = (f32x2){__builtin_amdgcn_exp2f(w.x), __builtin_amdgcn_exp2f(w.y)} + 1.0f;
    return v * (f32x2){__builtin_amdgcn_rcpf(d.x), __builtin_amdgcn_rcpf(d.y)};
}
DEVI f32x4 gelu4(f32x4 v) { const f32x2 a = gelu2(v.xy), b = gelu2(v.zw); return (f32x4){a.x, a.y, b.x, b.y}; }
DEVI void rowsum16x8(const f32x4 a, const f32x4 b, float& o0, float& o1) {
    float t0, t1, t2, t3;
    asm volatile(
        "s_nop 1\n\t"
        "v_add_f32_dpp %2, %6, %6 row_ror:8 row_mask:0xf bank_mask:0x3\n\t"
        "v_add_f32_dpp %3, %7, %7 row_ror:8 row_mask:0xf bank_mask:0x3\n\t"
        "v_add_f32_dpp %4, %10, %10 row_ror:8 row_mask:0xf bank_mask:0x3\n\t"
        "v_add_f32_dpp %5, %11, %11 row_ror:8 row_mask:0xf bank_mask:0x3\n\t"
        "v_add_f32_dpp %2, %8, %8 row_ror:8 row_mask:0xf bank_mask:0xc\n\t"
        "v_add_f32_dpp %3, %9, %9 row_ror:8 row_mask:0xf bank_mask:0xc\n\t"
        "v_add_f32_dpp %4, %12, %12 row_ror:8 row_mask:0xf bank_mask:0xc\n\t"
        "v_add_f32_dpp %5, %13, %13 row_ror:8 row_mask:0xf bank_mask:0xc\n\t"
        "v_add_f32_dpp %0, %2, %2 row_half_mirror row_mask:0xf bank_mask:0x5\n\t"
        "s_nop 0\n\t"
        "v_add_f32_dpp %1, %4, %4 row_half_mirror row_mask:0xf bank_mask:0x5\n\t"
        "v_add_f32_dpp %0, %3, %3 row_half_mirror row_mask:0xf bank_mask:0xa\n\t"
        "v_add_f32_dpp %1, %5, %5 row_half_mirror row_mask:0xf bank_mask:0xa\n\t"
        "s_nop 0\n\t"
        "v_add_f32_dpp %0, %0, %0 quad_perm:[1,0,3,2] row_mask:0xf bank_mask:0xf\n\t"
        "v_add_f32_dpp %1, %1, %1 quad_perm:[1,0,3,2] row_mask:0xf bank_mask:0xf\n\t"
        "s_nop 0\n\t"
        "v_add_f32_dpp %0, %0, %0 quad_perm:[2,3,0,1] row_mask:0xf bank_mask:0xf\n\t"
        "v_add_f32_dpp %1, %1, %1 quad_perm:[2,3,0,1] row_mask:0xf bank_mask:0xf\n\t"
        : "=&v"(o0), "=&v"(o1), "=&v"(t0), "=&v"(t1), "=&v"(t2), "=&v"(t3)
        : "v"(a[0]), "v"(a[1]), "v"(a[2]), "v"(a[3]), "v"(b[0]), "v"(b[1]), "v"(b[2]), "v"(b[3]));
}

#define K5_PRE 9
#define RH_MAP 0
#define NET_LAYERS 2
constexpr int G_STAGE = 34816;
constexpr int K5_STAGE = 18432, K5_NS = 8;

constexpr int y_load_at(int slot) {
    int next = K5_PRE;
    for (int s = 0;; ++s) { const bool go = next < 18 + 2 * (s / 3); if (s == slot) return go ? next : -1; next += go; }
}

struct GTile { f32x4 a[4][4], e[2]; };
struct GFrag { bf16x8 a[2][4], b[2][5]; const LAS char *pa[2][2], *pb, *p8; int slot; };
DEVI void g_rd(GFrag& f, int kk, int i) {
    if (i < 4) f.a[kk][i] = LDS_FRAG(f.pa[kk][i >> 1] + (i & 1) * 2048);
    else if (i < 8) f.b[kk][i - 4] = LDS_FRAG(f.pb + (kk * 9 + i - 4) * 1024);
    else f.b[kk][4] = LDS_FRAG(f.p8 + kk * 9216);
}
template <int NS, int G, bool ROWS_IN_REGS>
DEVI void g_steps(GTile& c, GFrag& f, int n) {
    LOOP_ALIGN();
#pragma unroll 1
    do {
        int d;
#pragma unroll
        for (int kk = 0; kk < 2; ++kk) {
            WAIT_L(0);
            if (kk) BAR();
            SCHED0();
#pragma unroll
            for (int i = 0; i < 18; ++i) {
                const int nn = i >> 2, m = i & 3;
                f32x4& e = nn < 4 ? c.a[m][nn] : c.e[m & 1];
                e = ROWS_IN_REGS ? MFMA(f.a[kk][m], f.b[kk][nn], e) : MFMA(f.b[kk][nn], f.a[kk][m], e);
                SCHED0();
                if (i < 9) g_rd(f, kk ^ 1, i);
                if (kk == 0) {
                    if (i == 9) d = pin_s(f.slot == NS - 1 ? -(NS - 1) * G : G);
                    if (i >= 10 && i < 14) f.pa[(i - 10) >> 1][i & 1] = opaque(f.pa[(i - 10) >> 1][i & 1] + d);
                    if (i == 14) f.pb = opaque(f.pb + d);
                    if (i == 15) f.p8 = opaque(f.p8 + d);
                } else {
                    if (i == 10) f.slot = pin_s(f.slot == NS - 1 ? 0 : f.slot + 1);
                    if (i == 12) n = pin_s(n - 1);
                }
                SCHED0();
            }
        }
    } while (n);
}
DEVI void keep_s(__amdgpu_buffer_rsrc_t r) { asm volatile("" :: "s"(r)); }
DEVI void keep4(const f32x4 (&c)[4]) { asm volatile("" :: "v"(c[0]), "v"(c[1]), "v"(c[2]), "v"(c[3])); }
template <int D, int DB, bool FIRST, class HOOK>
DEVI void g_step_u(GTile& c, GFrag& f, const f32x4 (&c0)[4], HOOK&& hook) {
    static_for<36>([&](auto ic) {
        constexpr int kk = ic.value / 18, i = ic.value % 18, n = i >> 2, m = i & 3;
        if (i == 0) { WAIT_L(0); if (kk) BAR(); SCHED0(); }
        f32x4& e = n < 4 ? c.a[m][n] : c.e[m & 1];
        if (FIRST && kk == 0) asm volatile("v_mfma_f32_16x16x32_bf16 %0, %1, %2, %3" : "=&v"(e) : "v"(f.b[kk][n]), "v"(f.a[kk][m]), "v"(c0[m]));
        else MFMA_T(f.b[kk][n], f.a[kk][m], e);
        SCHED0();
        if (i < 9) g_rd(f, kk ^ 1, i);
        if (kk == 0) {
            if (i >= 10 && i < 14) f.pa[(i - 10) >> 1][i & 1] = opaque(f.pa[(i - 10) >> 1][i & 1] + D);
            if (i == 14) f.pb = opaque(f.pb + DB);
            if (i == 15) f.p8 = opaque(f.p8 + DB);
        }
        if (FIRST && ic.value == 20) keep4(c0);
        hook(ic);
        SCHED0();
    });
}
#define MF(d, a, b) "v_mfma_f32_16x16x32_bf16 " d ", " a ", " b ", " d "\n\t"
template <int D, int DB>
DEVI void g_step_gelu(GTile& c, GFrag& f, f32x4& z, float r, float c1) {
#pragma unroll
    for (int kk = 0; kk < 2; ++kk) {
        float va = z[2 * kk], vb = z[2 * kk + 1], ta, tb;
        WAIT_L(0); if (kk) BAR(); SCHED0();
#pragma unroll
        for (int i = 0; i < 9; ++i) {
            MFMA_T(f.b[kk][i >> 2], f.a[kk][i & 3], c.a[i & 3][i >> 2]);
            if (i == 5) asm volatile("v_mul_f32 %0, %0, %1" : "+v"(va) : "v"(r));
            if (i == 6) asm volatile("v_mul_f32 %0, %0, %1" : "+v"(vb) : "v"(r));
            if (i == 7) asm volatile("v_mul_f32 %0, %1, %1" : "=v"(ta) : "v"(va));
            if (i == 8) asm volatile("v_mul_f32 %0, %1, %1" : "=v"(tb) : "v"(vb));
            SCHED0();
            g_rd(f, kk ^ 1, i);
            SCHED0();
        }
        asm volatile(MF("%0", "%5", "%6") "v_fmaak_f32 %3, %3, %11, 0xc0135761\n\tv_fmaak_f32 %4, %4, %11, 0xc0135761\n\t"
                     MF("%1", "%5", "%7") "v_mul_f32 %3, %3, %9\n\tv_mul_f32 %4, %4, %10\n\t" MF("%2", "%5", "%8") "v_exp_f32 %3, %3"
                     : "+v"(c.a[1][2]), "+v"(c.a[2][2]), "+v"(c.a[3][2]), "+v"(ta), "+v"(tb)
                     : "v"(f.b[kk][2]), "v"(f.a[kk][1]), "v"(f.a[kk][2]), "v"(f.a[kk][3]), "v"(va), "v"(vb), "v"(c1));
        SCHED0(); f.pa[kk][0] = opaque(f.pa[kk][0] + D); SCHED0();
        asm volatile(MF("%0", "%6", "%7") "v_exp_f32 %5, %5\n\t" MF("%1", "%6", "%8") "v_add_f32 %4, 1.0, %4\n\tv_add_f32 %5, 1.0, %5\n\t"
                     MF("%2", "%6", "%9") "v_rcp_f32 %4, %4\n\t" MF("%3", "%6", "%10") "v_rcp_f32 %5, %5"
                     : "+v"(c.a[0][3]), "+v"(c.a[1][3]), "+v"(c.a[2][3]), "+v"(c.a[3][3]), "+v"(ta), "+v"(tb)
                     : "v"(f.b[kk][3]), "v"(f.a[kk][0]), "v"(f.a[kk][1]), "v"(f.a[kk][2]), "v"(f.a[kk][3]));
        SCHED0(); f.pa[kk][1] = opaque(f.pa[kk][1] + D); SCHED0();
        asm volatile(MF("%0", "%4", "%5") "v_mul_f32 %2, %2, %7\n\tv_mul_f32 %3, %3, %8\n\t" MF("%1", "%4", "%6")
                     : "+v"(c.e[0]), "+v"(c.e[1]), "+v"(va), "+v"(vb) : "v"(f.b[kk][4]), "v"(f.a[kk][0]), "v"(f.a[kk][1]), "v"(ta), "v"(tb));
        SCHED0();
        if (kk == 0) { f.pb = opaque(f.pb + DB); f.p8 = opaque(f.p8 + DB); }
        SCHED0();
        z[2 * kk] = va; z[2 * kk + 1] = vb;
    }
}
DEVI void g_fill(GTile& c, const float (&e)[4]) {
#pragma unroll
    for (int m = 0; m < 4; ++m) {
#pragma unroll
        for (int n = 0; n < 4; ++n) c.a[m][n] = pin((f32x4){e[m], e[m], e[m], e[m]});
        if (m < 2) c.e[m] = pin((f32x4){e[m], e[m], e[m], e[m]});
    }
}

#undef STAMP_SEC
#define STAMP_SEC 1
DEVI void gmlp_body(LAS char* lds, int wm, int wn, int pm, int cg, char* z2t, char* y, const u16* __restrict__ bsp STAMP_PARAM) {
    const int mlo = wm * 4 + wn * 2, mhi = wm * 4 + (1 - wn) * 2;
    const float zero4[4] = {0.f, 0.f, 0.f, 0.f};
    GTile z2, z1, acc;
    GFrag f;
    {
        const int lane = lane_id_fresh();
#pragma unroll
        for (int kk = 0; kk < 2; ++kk) { f.pa[kk][0] = lds + mlo * 2048 + kk * 1024 + lane * 16; f.pa[kk][1] = lds + mhi * 2048 + kk * 1024 + lane * 16; }
        f.pb = lds + 16384 + wn * 4096 + lane * 16; f.p8 = lds + 16384 + 8192 + lane * 16; f.slot = 0;
    }
    g_fill(z2, zero4); g_fill(z1, zero4);
#pragma unroll
    for (int i = 0; i < 4; ++i) f.pa[i >> 1][i & 1] = opaque(f.pa[i >> 1][i & 1]);
    f.pb = opaque(f.pb); f.p8 = opaque(f.p8);
    SCHED0();
    WAIT_V(0); BARX();
    BAR();
    BARX();
    STAMP(2);
#pragma unroll
    for (int i = 0; i < 9; ++i) g_rd(f, 0, i);
    g_steps<4, G_STAGE, true>(z2, f, 9);
    STAMP(3);
    {
        const int lane = lane_id_fresh(), r = lane & 15, q = lane >> 4;
        f32x4 ra[4];
#pragma unroll
        for (int m = 0; m < 4; ++m) ra[m] = *(const LAS f32x4*)(lds + G_TAB + ((m < 2 ? mlo : mhi) + (m & 1)) * 64 + q * 16);
        SCHED0();
#pragma unroll
        for (int m = 0; m < 4; ++m) {
            f32x4 s1, s2;
#pragma unroll
            for (int n = 0; n < 4; ++n) {
                const f32x4 g = gelu4(z2.a[m][n] * ra[m]);
                z2.a[m][n] = g;
                if (n == 0) { s1 = g; s2 = g * g; } else { s1 += g; s2 += g * g; }
            }
            if (m < 2) { const f32x4 g = gelu4(z2.e[m] * ra[m]); z2.e[m] = g; s1 += g; s2 += g * g; }
            float u0, u1;
            rowsum16x8(s1, s2, u0, u1);
            if ((r & 3) == 0) *(LAS f32x2*)(lds + G_PART + wn * 1024 + ((m < 2 ? mlo : mhi) + (m & 1)) * 128 + (q * 4 + (r >> 2)) * 8) = (f32x2){u0, u1};
            SCHED0();
        }
    }
    WAIT_L0(); BAR();
    STAMP(4);
    g_steps<4, G_STAGE, false>(z1, f, 4);
    unsigned b0, b1;
    f32x4 c0[4];
    g_step_u<G_STAGE, G_STAGE, false>(z1, f, c0, [&](auto ic) {
        if (ic.value != 18) return;
        STAMP(5);
        const int lane = lane_id_fresh(), q = lane >> 4;
        b0 = __builtin_amdgcn_raw_buffer_load_b32(RSRC(bsp), (lane & 15) * 4, pm * 256 + mlo * 32, 0);
        b1 = __builtin_amdgcn_raw_buffer_load_b32(RSRC(bsp), (lane & 15) * 4, pm * 256 + mhi * 32, 0);
#pragma unroll
        for (int g = 0; g < 2; ++g) {
            const auto rz2 = RSRC(z2t + (size_t)pm * 589824 + cg * 36864 + ((g ? mhi : mlo) >> 1) * 9216);
            const LAS char* tab = lds + G_TAB + (g ? mhi : mlo) * 64 + q * 16;
            LAS char* own = lds + (wm ? G_OWN : G_STAGE + 16384) + (g ^ wn) * 9216 + lane * 16;
            f32x4 ra[2], rb[2];
#pragma unroll
            for (int T = 0; T < 2; ++T) { ra[T] = *(const LAS f32x4*)(tab + 512 + T * 64); rb[T] = *(const LAS f32x4*)(tab + 1024 + T * 64); }
#pragma unroll
            for (int n = 0; n < (g ? 4 : 5); ++n) {
                const f32x4 v0 = (n < 4 ? z2.a[2 * g][n & 3] : z2.e[0]) * ra[0] + rb[0], v1 = (n < 4 ? z2.a[2 * g + 1][n & 3] : z2.e[1]) * ra[1] + rb[1];
                const u32x4 o = {pack2(v0[0], v1[0]), pack2(v0[1], v1[1]), pack2(v0[2], v1[2]), pack2(v0[3], v1[3])};
                ST16(rz2, lane * 16, n < 4 ? wn * 4096 + n * 1024 : 8192, o, 16);
                *(LAS u32x4*)(own + (n < 4 ? wn * 4096 + n * 1024 : 8192)) = o;
            }
        }
        STAMP(6);
    });
    static_for<3>([&](auto sc) {
        constexpr int D = sc.value == 0 ? G_STAGE : sc.value == 1 ? -3 * G_STAGE : 2 * G_STAGE;
        g_step_u<D, D, false>(z1, f, c0, [&](auto ic) { if (ic.value == 17 && sc.value == 1) WAIT_V(0); });
    });
    float ra[4], c1;
    {
        int lane, da[2];
        g_step_u<-G_STAGE, -G_STAGE, false>(z1, f, c0, [&](auto ic) {
            constexpr int i = ic.value;
            if (i == 9) lane = pin(lane_id_fresh());
            if (i == 14 || i == 15) da[i & 1] = pin((lane & 15) * 128 + (((4 * (i & 1) + (lane >> 4)) ^ (lane & 7)) * 16) - lane * 16 - (i & 1) * 1024);
            if (i == 16 || i == 17) f.pa[0][i & 1] = opaque(f.pa[0][i & 1] + da[0]);
            if (i == 27 || i == 28) f.pa[1][i & 1] = opaque(f.pa[1][i & 1] + da[1]);
            if (i >= 29 && i < 33) {
                constexpr int m = (i - 29) & 3;
                const float e = m & 1 ? bfhi(m < 2 ? b0 : b1) : bflo(m < 2 ? b0 : b1);
                c0[m] = pin((f32x4){e, e, e, e});
            }
            if (i == 33) {
#pragma unroll
                for (int m = 0; m < 4; ++m) ra[m] = *(const LAS float*)(lds + G_TAB + ((m < 2 ? mlo : mhi) + (m & 1)) * 64 + (lane & 15) * 4);
                c1 = pin(-0.10294324f);
            }
        });
    }
    STAMP(7);
    g_step_u<2 * G_STAGE, G_OWN - G_STAGE - 16384, true>(acc, f, c0, [](auto) {});
    static_for<18>([&](auto sc) {
        constexpr int st = sc.value, m = st < 16 ? st >> 2 : st & 1;
        constexpr int D = st == 0 ? -3 * G_STAGE : st % 3 == 1 ? 2 * G_STAGE : -G_STAGE;
        g_step_gelu<D, st == 0 ? 16384 - G_OWN : D>(acc, f, st < 16 ? z1.a[m][st & 3] : z1.e[m], ra[m], c1);
    });
    f.slot = 1;
    STAMP(9);
    g_steps<3, -G_STAGE, false>(acc, f, 12);
    __amdgpu_buffer_rsrc_t ry;
    unsigned vy[4], ve[2];
    int lane;
    u32x4 o = {0u, 0u, 0u, 0u};
    auto y_part = [&](auto jc) {
        constexpr int j = jc.value, m = j / 10, k = j % 10, e = (k & 1) * 2;
        const f32x4 &a = k < 8 ? acc.a[m][k >> 1] : acc.e[m & 1], &z = k < 8 ? z1.a[m][k >> 1] : z1.e[m & 1];
        float t0, t1;
        asm volatile("v_mul_f32 %1, %3, %5\n\tv_mul_f32 %2, %4, %6\n\tv_cvt_pk_bf16_f32 %0, %1, %2" : "=v"(o[k & 3]), "=&v"(t0), "=&v"(t1) : "v"(a[e]), "v"(a[e + 1]), "v"(z[e]), "v"(z[e + 1]));
        if (k == 3 || k == 7) ST16(ry, vy[m] + (k >> 2) * 1024, 0, o, 16);
        if (k == 9) ST8(ry, ve[m & 1], 0, ((u32x2){o[0], o[1]}), 16);
    };
    WAIT_L(0);
    SCHED0();
    static_for<36>([&](auto ic) {
        constexpr int i = ic.value, kk = i < 14 ? 0 : i < 28 ? 1 : i >= 32, t = i < 28 ? i % 14 : 14 + (i & 3), m = t < 10 ? t / 5 : 2 + (t - 10) / 4, n = t < 10 ? t % 5 : (t - 10) & 3;
        if (i == 14) { WAIT_L(0); BAR(); SCHED0(); }
        f32x4& e = n < 4 ? acc.a[m][n & 3] : acc.e[m & 1];
        MFMA_T(f.b[kk][n], f.a[kk][m], e);
        SCHED0();
        if (i < 9) g_rd(f, 1, i);
        if (i == 10) { ry = RSRC(own_s(y) + (size_t)pin_s(pm * 8 * 73728 + cg * 4608)); keep_s(ry); }
        if (i == 12) { lane = pin(lane_id_fresh()); vy[0] = pin((unsigned)(mlo * 73728 + wn * 2048 + lane * 16)); }
        if (i == 14) ve[0] = pin((unsigned)(mlo * 73728 + 4096 + (lane >> 5) * 256 + (lane & 15) * 16 + ((lane >> 4) & 1) * 8));
        if (i == 15) { vy[1] = pin(vy[0] + 73728); ve[1] = pin(ve[0] + 73728); }
        if (i == 16) { vy[2] = pin(vy[0] + (mhi - mlo) * 73728); vy[3] = pin(vy[2] + 73728); asm volatile("s_nop 4"); }
        if constexpr (i >= 17) y_part(IC<i - 17>{});
        SCHED0();
    });
    STAMP(10);
    y_part(IC<19>{});
    SCHED0();
#pragma unroll
    for (int i = 0; i < 4; ++i) {
        const int m = 2 + (i >> 1), p = i & 1;
        if (i == 2) asm volatile("" : "+v"(acc.a[3][0]), "+v"(acc.a[3][1]), "+v"(acc.a[3][2]), "+v"(acc.a[3][3]));
        const f32x4 a = acc.a[m][2 * p] * z1.a[m][2 * p], c = acc.a[m][2 * p + 1] * z1.a[m][2 * p + 1];
        ST16(ry, vy[m] + p * 1024, 0, ((u32x4){pack2(a[0], a[1]), pack2(a[2], a[3]), pack2(c[0], c[1]), pack2(c[2], c[3])}), 16);
        SCHED0();
    }
}

#define NET_IDS \
    int b = __builtin_amdgcn_readfirstlane(blockIdx.x), wid = wid8 & 3; \
    asm volatile("" : "+s"(b), "+s"(wid));     \
    const int xcd = b & 7, idx = b >> 3; \
    const int pa = (xcd >> 1) * 4 + (idx & 3), hp = (xcd & 1) * 8 + (idx >> 2); \
    const int pm = (xcd >> 2) * 8 + (idx & 7), cg = (xcd & 3) * 4 + (idx >> 3); \
    const int rh = RH_MAP ? xcd * 4 + (idx & 3) : (xcd >> 1) * 8 + (idx & 7), c5 = RH_MAP ? idx >> 2 : (xcd & 1) * 4 + (idx >> 3); \
    const int lead = (idx + (idx >> 3)) & 3; \
    auto kord = [&](int t, int n) { \
        const int k = t + ((int)(0x1F000000u << (((t - lead) & 3) * 4)) >> 28); \
        return (unsigned)k < (unsigned)n ? k : t; \
    }; \
    auto kord5 = [&](int t) { \
        const int k = t + ((int)(0x1F000000u << (((t - (RH_MAP ? c5 : lead)) & 3) * 4)) >> 28); \
        return (unsigned)k < 18u ? k : t; \
    };

DEVI u32x4 tr_frag(const LAS char* p, int four_rows) {
    const u32x2 a = __builtin_bit_cast(u32x2, __builtin_amdgcn_ds_read_tr16_b64_v4i16((LAS s16x4*)p));
    const u32x2 b = __builtin_bit_cast(u32x2, __builtin_amdgcn_ds_read_tr16_b64_v4i16((LAS s16x4*)(p + four_rows)));
    return (u32x4){a.x, a.y, b.x, b.y};
}
DEVI float dot2(unsigned v, unsigned w, float acc) { return __builtin_amdgcn_fdot2_f32_bf16(__builtin_bit_cast(bf16x2_t, v), __builtin_bit_cast(bf16x2_t, w), acc, false); }
DEVI float sum2(unsigned v, float acc) { return dot2(v, 0x3f803f80u, acc); }
struct Sel { unsigned lo, hi, nlo, nhi; };
DEVI Sel make_sel() { return {(unsigned)pin_s(0x00003f80), (unsigned)pin_s(0x3f800000), (unsigned)pin_s(0x0000bf80), (unsigned)pin_s((int)0xbf800000u)}; }
DEVI unsigned centre2(unsigned v, f32x2& e, float mean, const Sel& k) {
    const float a = dot2(k.lo, v, e.x), b = dot2(k.hi, v, e.y);
    const unsigned r = pack2(a, b);
    e = (f32x2){dot2(k.nlo, r, a), dot2(k.nhi, r, b)} - mean;
    return r;
}
typedef u32x4 Retile[9];
template <int PITCH>
DEVI void retile_out_make(Retile& o, const LAS char* img, int lane) {
    const int c = lane & 15, q = lane >> 4, p = lane & 3;
    const LAS char* src = img + (q * 8 + (c >> 2)) * PITCH;
#pragma unroll
    for (int i = 0; i < 8; ++i) o[i] = tr_frag(src + p * 16 + (i >> 2) * 144 + ((i >> 1) & 1) * 64 + (i & 1) * 8, 4 * PITCH);
    o[8] = tr_frag(src + 128 + (p >> 1) * 144 + (p & 1) * 8, 4 * PITCH);
}
template <int I0 = 0, int I1 = 9>
DEVI void retile_out_store(const Retile& o, char* dst, int nkc, int kc, int w, int lane) {
    const int c = lane & 15, q = lane >> 4;
    const auto rd = RSRC(dst + (size_t)(2 * w * nkc + kc) * 4608);
#pragma unroll
    for (int i = I0; i < 8 && i < I1; ++i) ST16(rd, lane * 16 + (i & 3) * 1024, (i >> 2) * nkc * 4608, o[i], 16);
    if (I1 == 9) ST16(rd, (c >> 3) * nkc * 4608 + 4096 + q * 128 + (c & 7) * 16, 0, o[8], 16);
}

DEVI float dpp_ror(float v, int n) { return n == 8 ? __builtin_amdgcn_update_dpp(0.f, v, 0x128, 0xf, 0xf, true) : n == 4 ? __builtin_amdgcn_update_dpp(0.f, v, 0x124, 0xf, 0xf, true)
                                          : n == 2 ? __builtin_amdgcn_update_dpp(0.f, v, 0x122, 0xf, 0xf, true) : __builtin_amdgcn_update_dpp(0.f, v, 0x121, 0xf, 0xf, true); }
DEVI unsigned long long epoch_of(const char* ws) {
    SCHED0();
    const unsigned long long ep = *(const __attribute__((address_space(4))) unsigned long long*)(ws + pin_s((int)OFF_EP)) + 1ull;
    asm volatile("" :: "s"(ep));
    return ep;
}
DEVI void prologue_done(LAS char* lds, char* ws, unsigned long long ep, int b, int lane) {
    if (lds_arrive(lds + G_WORD + 8) == 7) ST16(RSRC(ws), lane ? 0x80000000u : (unsigned)OFF_FL + b * 16, 0, ((u32x4){(unsigned)ep, (unsigned)(ep >> 32), 0u, 0u}), 16);
}
DEVI void centre_frag(u32x4& f, f32x2 (&er)[4], float mean, const Sel& k) {
#pragma unroll
    for (int e = 0; e < 4; ++e) f[e] = centre2(f[e], er[e], mean, k);
}
template <int N>
DEVI void column_means(LAS char* lds, LAS char* cnt, int me, int lane, float a, float b, float& ma, float& mb) {
    LAS float* red = (LAS float*)(lds + G_PART);
    auto x = __builtin_amdgcn_permlane16_swap(__float_as_uint(a), __float_as_uint(b), false, false);
    red[me * 64 + lane] = __uint_as_float(x[0]) + __uint_as_float(x[1]);
    if (lds_arrive(cnt) < N - 1) lds_count_wait(cnt, N);
    float t = 0.f;
#pragma unroll
    for (int i = 0; i < N; ++i) t += red[i * 64 + lane];
    x = __builtin_amdgcn_permlane32_swap(__float_as_uint(t), __float_as_uint(t), false, false);
    t = (__uint_as_float(x[0]) + __uint_as_float(x[1])) * (1.0f / 576.0f);
    x = __builtin_amdgcn_permlane16_swap(__float_as_uint(t), __float_as_uint(t), false, false);
    ma = __uint_as_float(x[0]); mb = __uint_as_float(x[1]);
}
template <class ARGS>
DEVI unsigned long long prologue(LAS char* lds, char*& ws, const char*& x0, const char*& wqkv, const u16*& wo, const u16*& w_in, const u16*& w_out, int b, int pa, int wid8, ARGS&& args PST_PARAM) {
    PST_DECL;
    const int lane = lane_id_fresh(), c = lane & 15, q = lane >> 4, w = wid8 & 3;
    const int jw = w - 1 + 3 * (((wid8 >> 2) ^ (b >> 3)) & 1);
    const bool wjob = b >= 16 && b < 112 && w;
    const int uo = (wid8 - 1 - (wid8 >> 2)) * 16 + b;
    const bool ojob = b < 16 && w && uo < 72;
    const unsigned l0 = (unsigned)(size_t)lds;
    LAS char* img = lds + 2 * K1_STAGE;
    const int uw = b - 16, head = (uw & 7) * 4 + ((uw >> 3) & 3), part = uw >> 5, hp = head >> 1, odd = head & 1;
    const int c0 = part * 1152 + hp * 72 + odd * 40;
    {
        const int so = pin_s(jw * 96 * 3456 + (c0 & ~15));
        const unsigned v = (lane & 3) < ((head & 3) == 3 ? 2 : 3) ? (lane >> 2) * 3456 + (lane & 3) * 16 : 0x80000000u, d = 16 * 3456 - 1024;
        const unsigned v0 = pin(v), v1 = pin(v + d), v2 = pin(v + 2 * d), v3 = pin(v + 3 * d);
        const int e = ((b - 112) >> 3) * 192 + wid8 * 64 + lane, row = (e * 10923) >> 16, r = e - row * 6;
        const unsigned vp = pin((unsigned)(row * 3456 + (r >> 1) * 1152 + (r & 1) * 128));
        const unsigned la = pin_s(l0 + 2 * K1_STAGE + jw * 6144);
        args(0);
        if (b >= 112 && wid8 < 3) dma1(l0 + G_JUNK, RSRC(wqkv), (b & 7) * 128, vp);
        if (wjob) { dma4<false>(la, RSRC(wqkv), so, v0, v1, v2, v3); dma2<false>(la + 4096, RSRC(wqkv), so + 64 * 3456, v0, v1); }
        args(1);
    }
    if (ojob) {
        const auto rs = RSRC(wo + (uo & 3) * 144);
#pragma unroll
        for (int j = 0; j < 9; ++j) {
            const int i = lane + 64 * j, rl = (i * 3641) >> 16, kp = (uo >> 2) * 32 + rl;
            const int row = kp < 512 ? 18 * (kp >> 4) + (kp & 15) + 2 * ((kp >> 4) & 1) : 36 * ((kp - 512) >> 2) + 16 + (kp & 3);
            dma1(l0 + 2 * K1_STAGE + wid8 * 9216 + j * 1024, rs, 0, (unsigned)(row * 1152 + (i - rl * 18) * 16));
        }
    }
    if (wid8 == 4) { lds_word_set(lds + G_WORD + c * 4, 0u); WAIT_L0(); }
    BAR();
    unsigned long long ep;
    if (wid8 == 4) ep = epoch_of(ws);
    if (wid8 == 4 && b >= 16 && b < 112) lds_count_wait(lds + G_WORD + 12, 12);
    if (wid8 > 4 && b >= 112) {
        __builtin_amdgcn_s_sleep(I_SLEEP);
        const unsigned v = (lane >> 2) * 9216 + (lane & 3) * 16, d = 16 * 9216 - 1024;
#pragma unroll
        for (int i = 0; i < 3; ++i) dma4<false>(l0 + P_IMG + (w - 1) * 12288 + i * 4096, RSRC(w_in + win_strip(b) * 32), (win_third(b, w) * 192 + i * 64) * 9216, v, v + d, v + 2 * d, v + 3 * d);
    }
    if (wid8 == 4) {
        AttnX ax = attn_x(x0, x0, 0, pa, 0, lane);
#pragma unroll
        for (int s = 0; s < 2; ++s)
#pragma unroll
            for (int i = 0; i < 4; ++i) { ax.oA = (pa * 8 + 2 * i) * 18432; attn_issueA(lds, ax, i, s, s); }
    }
    u32x4 xv[9];
    if (wid8 == 0) {
        const unsigned v = (lane >> 3) * 2304 + (lane & 7) * 16;
        const auto rx = RSRC(x0 + (size_t)slot2row(b * 8) * 1152);
#pragma unroll
        for (int i = 0; i < 9; ++i) asm volatile("s_nop 4\n\tbuffer_load_dwordx4 %0, %1, %2, 0 offen offset:%3" : "+v"(xv[i]) : "v"(v), "s"(rx), "i"(i * 128) : "memory");
        const auto rw = RSRC(ws);
        const u32x4 ones = {~0u, ~0u, ~0u, ~0u}, zero = {0u, 0u, 0u, 0u};
        const unsigned vo = lane * 16;
        ST16(rw, vo, (unsigned)OFF_ZST + b * 2048, ones, 16); ST16(rw, vo, (unsigned)OFF_ZST + b * 2048 + 1024, ones, 16);
        ST16(rw, vo, (unsigned)OFF_CNT + b * 2560, zero, 16); ST16(rw, vo, (unsigned)OFF_CNT + b * 2560 + 1024, zero, 16);
        ST16(rw, vo, lane < 32 ? (unsigned)OFF_CNT + b * 2560 + 2048 : (unsigned)(OFF_L0 + L_STB) + b * 512 - 512, zero, 16);
        ST16(rw, vo, (unsigned)(OFF_L0 + L_O) + (b >> 1) * 18432 + 16384 + (b & 1) * 1024, zero, 16);
        ST16(rw, b < 12 ? vo : 0x80000000u, (unsigned)OFF_FL2 + b * 1024, zero, 16);
    }
    PST(1);
    if (wjob) {
        const Sel k = make_sel();
        const LAS char* s0 = opaque(img + jw * 6144 + (q * 8 + (c >> 2)) * 64 + (c0 & 15) + (c & 3) * 8);
        const LAS char* s1 = opaque(img + jw * 6144 + (q * 8 + (c >> 2)) * 64 + (c0 & 15) + 32 + min(c & 3, 2) * 2048);
        const auto rd = RSRC(ws + OFF_TWQKV + (size_t)(hp * 18 + jw * 3) * 6144 + (3 * odd + part) * 1024), rd2 = RSRC(ws + OFF_TWT + (size_t)((hp * 3 + part) * 18 + jw * 3) * 256);
        const unsigned vd2 = pin(c < 12 && !odd ? (unsigned)((c >> 2) * 256 + q * 64 + (c & 3) * 16) : 0x80000000u);
        asm volatile("" :: "s"(rd), "s"(rd2));
        WAIT_V(0);
        PST(2);
        u32x4 f[4];
        float sm[4] = {0.f, 0.f, 0.f, 0.f}, m0, m1;
#pragma unroll
        for (int i = 0; i < 4; ++i) f[i] = tr_frag(i < 3 ? s0 + i * 2048 : s1, 256);
#pragma unroll
        for (int e = 0; e < 16; ++e) sm[(e >= 12) * 2 + (e & 1)] = sum2(f[e >> 2][e & 3], sm[(e >= 12) * 2 + (e & 1)]);
        sm[2] = c < 12 ? sm[2] + sm[3] : 0.f;
        sm[2] += dpp_ror(sm[2], 4); sm[2] += dpp_ror(sm[2], 8);
        column_means<6>(lds, lds + G_WORD + 12, jw, lane, sm[0] + sm[1], sm[2], m0, m1);
        PST(3);
        f32x2 e0[4] = {{-m0, -m0}, {-m0, -m0}, {-m0, -m0}, {-m0, -m0}}, e1[4] = {{-m1, -m1}, {-m1, -m1}, {-m1, -m1}, {-m1, -m1}};
#pragma unroll
        for (int i = 0; i < 3; ++i) { centre_frag(f[i], e0, m0, k); ST16(rd, lane * 16, i * 6144, f[i], 16); }
        centre_frag(f[3], e1, m1, k); ST16(rd2, vd2, 0, f[3], 16);
        PST(4);
        lds_arrive(lds + G_WORD + 12);
    }
    if (wid8 != 4) ep = epoch_of(ws);
    {
        if (wjob) {
            const unsigned fo = pin((unsigned)OFF_FW + ((lane >> 3) * 16 + hp) * 1024 + (3 * odd + part) * 128 + (lane & 7) * 16);
            WAIT_V(0);
            PST(5);
            if (lds_arrive(lds + G_WORD + 4) == 5) {
                const u32x4 e4 = {(unsigned)ep, (unsigned)(ep >> 32), (unsigned)ep, (unsigned)(ep >> 32)};
                ST16(RSRC(ws), fo, 0, e4, 16); ST16(RSRC(ws), fo + 131072, 0, e4, 16);
            }
            if (wid8 > 4) {
                const int oj = uw * 3 + w - 1;
                const auto rs = RSRC(w_out + (size_t)(oj >> 2) * 32 * 576 + (oj & 3) * 144);
#pragma unroll
                for (int j = 0; j < 9; ++j) { const int i = lane + 64 * j; dma1(l0 + P_IMG + (w - 1) * 9216 + j * 1024, rs, 0, (unsigned)(((i * 3641) >> 16) * (1152 - 288) + i * 16)); }
            }
        }
    }
    if (wid8 == 0) {
        WAIT_V(7);
        asm volatile("" : "+v"(xv[0]), "+v"(xv[1]), "+v"(xv[2]), "+v"(xv[3]), "+v"(xv[4]), "+v"(xv[5]), "+v"(xv[6]), "+v"(xv[7]), "+v"(xv[8]));
        float s = 0.f, q2 = 0.f;
#pragma unroll
        for (int e = 0; e < 36; ++e) { s = sum2(xv[e >> 2][e & 3], s); q2 = dot2(xv[e >> 2][e & 3], xv[e >> 2][e & 3], q2); }
        ST8(RSRC(ws + OFF_L0 + L_STA + (size_t)b * 512), lane * 8, 0, ((u32x2){__float_as_uint(s), __float_as_uint(q2)}), 16);
    }
    if (ojob) {
        WAIT_V(0);
        Retile rt;
        retile_out_make<288>(rt, img + wid8 * 9216, lane); retile_out_store(rt, ws + OFF_TWO, 18, uo >> 2, uo & 3, lane);
    }
    PST(7);
    PST_DUMP(wid8 == 0 || wid8 == 4 || wid8 == 5, wid8 ? wid8 : 2);
    return ep;
}

__global__ __launch_bounds__(512, 2) TRACE_ATTR void k_net(char* ws, const u16* wsp, const u16* bsp, char* out, const char* x0, const char* wqkv, const u16* wo,
                                                             const u16* w_in, const u16* w_out DBG_ARG) {
    extern __shared__ __attribute__((aligned(16))) char smem[];
    LAS char* lds = (LAS char*)smem;
    asm volatile("gm_code_begin:");
    if (blockIdx.x < 16) dma1((unsigned)(size_t)lds + G_JUNK, code_rsrc(), 0, (threadIdx.x * 2 + (blockIdx.x >> 3)) * 128);
    STAMP_DECL;
    const int wid8 = __builtin_amdgcn_readfirstlane(threadIdx.x >> 6);
    unsigned long long ep;
    {
        NET_IDS
        ep = prologue(lds, ws, x0, wqkv, wo, w_in, w_out, b, pa, wid8, [&](int part) {
            if (part == 0) { asm volatile("" :: "s"(wqkv), "s"(w_in), "s"(w_out)); return; }
            ws = own_s(ws); wsp = own_s(wsp); bsp = own_s(bsp); out = own_s(out); x0 = own_s(x0); wo = own_s(wo); w_out = own_s(w_out);
        } PST_FWD);
    }
    const char *twin = ws + OFF_TWIN, *twout = ws + OFF_TWOUT;
    if (wid8 >= 4) {
        u32x4 wr[5][9];
#pragma unroll 1
      for (int layer = 0; layer < NET_LAYERS; ++layer) {
        NET_IDS
        TRACE_BEGIN;
        char* wl = ws + OFF_L0 + layer * SZ_L;
        const unsigned fl = layer * F_LAYER;
        const char *xin = wl + L_XB, *st = wl + L_STB, *z2t = wl + (layer ? L_Y : L_Z2T);
        unsigned long long* zst = (unsigned long long*)(ws + OFF_ZST + layer * SZ_ZST);
        const int lane = lane_id_fresh(), l3 = lane >> 3;
        const unsigned vo = lane * 16;
        const unsigned l0 = (unsigned)(size_t)lds, lA = l0 + wid * 4096, lB = l0 + 16384 + wid * 4608;
        const auto rX = RSRC(xin + (size_t)(pm * 8 + 2 * wid) * 18432), rI = RSRC(twin + (size_t)cg * (18 * 18432) + wid * 4608);
        auto issue1B = [&](int g, int slot) { const int k = kord(g < 9 ? g : (g < 13 ? g - 4 : g - 13), 9); dma5<true>(lB + slot * G_STAGE, rI, (g < 9 ? 9 + k : k) * 18432, vo); };
        auto issue1A = [&](int g, int slot) { dma4<true>(lA + slot * G_STAGE, rX, kord(g < 9 ? g : g - 13, 9) * 2048, vo, vo, vo + 16384, vo + 16384); };
        auto issue1 = [&](int g, int slot) { issue1B(g, slot); issue1A(g, slot); };
        const auto rA = RSRC((const char*)wsp + (size_t)(pm * 128 + wid * 32) * 4096), rB = RSRC(z2t + cg * 36864 + wid * 4608);
        const unsigned voA = (unsigned)(2 * l3 * 4096 + ((lane & 7) ^ l3) * 16);
        auto issueB = [&](int k, int slot) { dma5<true>(lB + slot * G_STAGE, rB, (k >> 1) * 589824 + (k & 1) * 18432, vo); };
        const auto rW = RSRC(twout + (size_t)c5 * (72 * 4608));
        auto issue5 = [&](int t) { dma5<true>(l0 + ((t + 6) & 7) * K5_STAGE + wid * 4608, rW, kord5(t) * K5_STAGE + wid * 4608, vo); };
        auto load5 = [&](int p, int i) {
            const int t0 = p ? 2 * p + 6 : 4, ow = kord5(t0 + i) * K5_STAGE + wid * 4096;
            asm volatile("s_nop 4\n\tbuffer_load_dwordx4 %0, %4, %5, %6 offen sc1\n\tbuffer_load_dwordx4 %1, %4, %5, %6 offen offset:1024 sc1\n\t"
                         "buffer_load_dwordx4 %2, %4, %5, %6 offen offset:2048 sc1\n\tbuffer_load_dwordx4 %3, %4, %5, %6 offen offset:3072 sc1"
                         : "+v"(wr[p][4 * i]), "+v"(wr[p][4 * i + 1]), "+v"(wr[p][4 * i + 2]), "+v"(wr[p][4 * i + 3]) : "v"(vo), "s"(rW), "s"(ow) : "memory");
            if (i) {
                const int oe = kord5(t0 + (wid >> 1)) * K5_STAGE + 16384 + (wid & 1) * 1024;
                asm volatile("s_nop 4\n\tbuffer_load_dwordx4 %0, %1, %2, %3 offen sc1" : "+v"(wr[p][8]) : "v"(vo), "s"(rW), "s"(oe) : "memory");
            }
        };
        auto put5 = [&](int w0, int w1) {
#pragma unroll
            for (int w = w0; w < w1; ++w) {
                const int p = w / 9, i = w % 9 == 4 ? -1 : w % 9 - (w % 9 > 4);
                LAS char* sl = lds + (((p ? 2 * p + 6 : 4) + 6) & 7) * K5_STAGE + lane * 16;
                if (i < 0) *(LAS u32x4*)(sl + (wid >> 1) * K5_STAGE + 16384 + (wid & 1) * 1024) = wr[p][8];
                else *(LAS u32x4*)(sl + (i >> 2) * K5_STAGE + wid * 4096 + (i & 3) * 1024) = wr[p][i];
            }
            WAIT_L0();
        };
        const int row = wid * 32 + (lane & 31);
        auto step1 = [&](int t) { BAR(); issue1(t + 4, t & 3); };
        const char* ps;
        unsigned fd, fd2;
        {
            unsigned fo, fi;
            const bool oj = !layer && wid && b >= 16 && b < 112, ij = !layer && wid && b >= 112;
            const int ojob = (b - 16) * 3 + wid - 1;
            float mw[2];
            unsigned fw;
            auto win_half = [&](int h, bool make, bool store) {
                const int u = win_strip(b) * 2 + h, half = u >= 144, g = u - half * 144, ug = g / 9, j = g - ug * 9, c = lane & 15;
                const auto rd = RSRC(ws + OFF_TWIN + (size_t)ug * (18 * 18432) + (half * 18 + win_third(b, wid) * 6) * 9216);
                const unsigned vd = (lane >> 4) * 256 + (j < 8 ? ((j & ~1) + (c >> 3)) * 1024 + ((j & 1) * 8 + (c & 7)) * 16 : 8192 + c * 16);
                const Sel k = make_sel();
                const float m = mw[h];
                f32x2 er[4] = {{-m, -m}, {-m, -m}, {-m, -m}, {-m, -m}};
#pragma unroll
                for (int i = 0; i < 6; ++i) { if (make) centre_frag(wr[1 + h][i], er, m, k); if (store) ST16(rd, vd, i * 9216, wr[1 + h][i], 16); }
            };
            attn_producer(lds, ws, x0, wl, fl, ep, layer, pa, hp, wid, oj || ij, [&] { if (b < 16) WAIT_V(0); prologue_done(lds, ws, ep, b, lane); }, [&](int part) {
                if (part == -2 && ij) {
                    WAIT_V(0);
#pragma unroll
                    for (int h = 0; h < 2; ++h) {
                        const LAS char* src = lds + P_IMG + (wid - 1) * 12288 + ((lane >> 4) * 8 + ((lane & 15) >> 2)) * 64 + h * 32
                                              + ((win_strip(b) * 2 + h) % 9 < 8 ? (lane & 1) * 16 + (lane & 2) * 4 : (lane & 3) * 8);
#pragma unroll
                        for (int i = 0; i < 6; ++i) wr[1 + h][i] = tr_frag(src + i * 2048, 256);
                    }
                    float sm[4] = {0.f, 0.f, 0.f, 0.f};
#pragma unroll
                    for (int e = 0; e < 48; ++e) sm[(e >= 24) * 2 + (e & 1)] = sum2(wr[1 + (e >= 24)][(e % 24) >> 2][e & 3], sm[(e >= 24) * 2 + (e & 1)]);
                    column_means<3>(lds, lds + G_WORD, wid - 1, lane, sm[0] + sm[1], sm[2] + sm[3], mw[0], mw[1]);
                    win_half(0, true, false);
                }
                if (part == -1 && oj) { retile_out_make<288>(wr[1], lds + P_IMG + (wid - 1) * 9216, lane); WAIT_L(0); }
                if (part == 0) { if (ij) win_half(0, false, true); else retile_out_store<0, 6>(wr[1], ws + OFF_TWOUT, 72, ojob >> 2, ojob & 3, lane); }
                if (part == 1) {
                    if (layer) { issue1B(0, 0); issue1B(1, 1); }
                    if (oj) retile_out_store<6, 9>(wr[1], ws + OFF_TWOUT, 72, ojob >> 2, ojob & 3, lane);
                    if (ij) win_half(1, true, true);
                    fo = flag_out(fl + F_X1, 256, pa, hp, lane); fi = flag_in(fl + F_X1, 256, pm * 16 + cg, 16, lane);
                    ps = pin(st + (size_t)(pm * 128 + wid * 32) * 64 + lane * 16);
                    fd = pin((unsigned)OFF_FL2 + wid * 1024 + vo);
                    fd2 = pin((unsigned)OFF_FL2 + (lane < 32 ? 256 + lane : 288 + ((cg * 9) >> 1) * 3 + min(lane & 15, 14)) * 16);
                    fw = pin((unsigned)OFF_FL2 + (288 + (72 + ((cg * 9) >> 1)) * 3 + min(lane & 15, 14)) * 16);
                }
                if (part == 2 && (oj || ij)) {
                    WAIT_V(0);
                    flag_raise(ws, lane ? 0x80000000u : (unsigned)OFF_FL2 + (oj ? ojob : 288 + win_strip(b) * 3 + wid - 1) * 16);
                }
            });
            if (layer == 0) {
                __builtin_amdgcn_s_sleep(WIN_LOOK);
                flags_wait<0, 12>(ws, fw);
                issue1B(0, 0); issue1B(1, 1);
            }
            BARX();
            if (wid == 0) flag_raise(ws, fo);
            issue1B(2, 2); issue1B(3, 3);
            asm volatile("" :: "s"(rX));
            const int o0 = pin_s(kord(0, 9) * 2048);
            const unsigned v2 = pin(vo + 16384);
            if (wid == 0) {
                flags_wait<X1_FIRST>(ws, fi);
#pragma unroll
                for (int i = 0; i < 4; ++i) dma4<true>(lA + i * 4096, rX, o0 + i * 36864, vo, vo, v2, v2);
            }
            BARX();
        }
        issue1A(1, 1); issue1A(2, 2);
        WAIT_V(8); BAR();
        issue1A(3, 3);
        {
            f32x4 sp[3];
            WAIT_V(8); step1(0);
            WAIT_V(13); step1(1);
            WAIT_V(18); step1(2);
            unsigned got[2];
            if (layer == 0) asm volatile("s_nop 4\n\tbuffer_load_dword %0, %2, %4, 0 offen sc1\n\tbuffer_load_dword %1, %3, %4, 0 offen sc1" : "+v"(got[0]), "+v"(got[1]) : "v"(fd), "v"(fd2), "s"(RSRC(ws)) : "memory");
            WAIT_IF(layer, 18, 20); step1(3);
            WAIT_IF(layer, 18, 20); step1(4);
            WAIT_V(18);
            if (layer == 0) {
                asm volatile("" : "+v"(got[0]), "+v"(got[1]));
                if (__any((got[0] & got[1]) == 0u)) { flags_wait<0, 12>(ws, fd); flags_wait<0, 12>(ws, fd2); }
            }
            BAR(); issue1B(9, 1); side_load(sp, ps, ps); if (layer == 0) load5(3, 0);
            WAIT_IF(layer, 17, 21); BAR(); issue1B(10, 2); if (layer == 0) load5(3, 1);
            WAIT_IF(layer, 5, 14); BARX();
            side_fence(sp);
#pragma unroll
            for (int i = 0; i < 2; ++i) lds_word_set(lds + G_TAB + (wid * 32 + i * 16 + (lane >> 2)) * 4, __float_as_uint(ln_rstd(sp[i])));
            issue1B(11, 3);
            WAIT_L0();
        }
        BAR(); issue1B(12, 0);
        if (layer == 1) {
            const auto rd = RSRC(out + (size_t)b * 9216 + wid * 2304);
            ST16(rd, vo, 0, ((u32x4){0u, 0u, 0u, 0u}), 16); ST16(rd, vo, 1024, ((u32x4){0u, 0u, 0u, 0u}), 16);
            if (lane < 16) ST16(rd, vo, 2048, ((u32x4){0u, 0u, 0u, 0u}), 16);
        }
        if (layer == 0) {
#pragma unroll
            for (int p = 0; p < 6; ++p) load5(p >> 1, p & 1);
        }
        WAIT_V(0);
        __builtin_amdgcn_s_setprio(3);
        const char* src = pin((const char*)zst + (size_t)(pm * 16 + (lane >> 4) * 4) * 1024 + wid * 256 + (lane & 15) * 16);
        const unsigned tw = pin(l0 + G_TAB + 512 + (lane >> 5) * 512 + (wid * 32 + (lane & 15) * 2 + ((lane >> 4) & 1)) * 4);
        const unsigned fz = flag_out(fl + F_Z2, 256, cg, pm, lane), fzi = pin(fl + F_Z2 + (cg * 16 + pm) * 256 + ((lane ^ pm) & 8) * 16 + (lane & 7) * 16);
        const unsigned vA[4] = {pin(voA), pin(voA + 16 * 4096 - 1024), pin(voA + 4096 - 2048), pin(voA + 17 * 4096 - 3072)};
        auto spA = [&](int so, unsigned la) { dma4<false>(la, rA, so, vA[0], vA[1], vA[2], vA[3]); };
        auto issueA = [&](int k, int slot) { spA(k * 128, lA + slot * G_STAGE); };
        {
            const LAS char* pp = opaque(lds + G_PART + row * 8);
            const auto rs = RSRC(zst + (size_t)(pm * 16 + cg) * 128);
            const unsigned vp = pin(lane < 32 ? (unsigned)row * 8u : 0x80000000u);
            asm volatile("" :: "s"(rs));
            BARX();
            const f32x2 a = *(const LAS f32x2*)pp, c = *(const LAS f32x2*)(pp + 1024);
            ST8(rs, vp, 0, ((u32x2){__float_as_uint(add1(a.x, c.x)), min(__float_as_uint(add1(a.y, c.y)), 0xfffffffeu)}), 16);
            SCHED0();
        }
        __builtin_amdgcn_s_setprio(0);
        {
            f32x4 g[4];
            float sm[4];
            auto gather = [&] {
                asm volatile("global_load_dwordx4 %0, %4, off sc1\n\tglobal_load_dwordx4 %1, %4, off offset:1024 sc1\n\t"
                             "global_load_dwordx4 %2, %4, off offset:2048 sc1\n\tglobal_load_dwordx4 %3, %4, off offset:3072 sc1"
                             : "=&v"(g[0]), "=&v"(g[1]), "=&v"(g[2]), "=&v"(g[3]) : "v"(src) : "memory");
            };
            auto total = [&] {
                asm volatile("" : "+v"(g[0]), "+v"(g[1]), "+v"(g[2]), "+v"(g[3]));
#pragma unroll
                for (int i = 0; i < 4; ++i) sm[i] = add1(add1(g[0][i], g[1][i]), add1(g[2][i], g[3][i]));
            };
            step1(9); step1(10);
            BARX(); gather(); issue1(15, 3);
            WAIT_V(22); BARX();
            __builtin_amdgcn_s_setprio(3);
            WAIT_V(9);
            total();
            if (__any(__builtin_isunordered(sm[1], sm[3]))) {
#pragma unroll 1
                for (unsigned spin = 0; spin < 400000u; ++spin) {
                    bool ok = true;
#pragma unroll
                    for (int i = 0; i < 4; ++i) ok &= __float_as_uint(g[i][1]) != 0xffffffffu && __float_as_uint(g[i][3]) != 0xffffffffu;
                    if (__all(ok)) break;
                    gather();
                    WAIT_V(0);
                    total();
                }
            }
            xq4_sum(sm);
            const bool odd = lane & 16;
            float mean;
            const float rs = rstd_of(odd ? sm[2] : sm[0], odd ? sm[3] : sm[1], 1.0f / 2304.0f, mean);
            asm volatile("ds_write_b32 %0, %1" :: "v"(tw), "v"(lane < 32 ? rs : -mean * rs) : "memory");
            __builtin_amdgcn_s_setprio(0);
            issue1B(16, 0);
            WAIT_L0();
        }
        BARX(); issue1A(16, 0); issueA(2 * pm, 1);
        auto kord2 = [&](int t) { const int j = kord(t - 2, 30); return ((j < 2 * (pm & 7) ? j : j + 2) + 2 * (pm & 8)) & 31; };
        unsigned seen;
        auto look = [&] { if (wid == 0) asm volatile("s_nop 4\n\tbuffer_load_dword %0, %1, %2, 0 offen sc1" : "+v"(seen) : "v"(fzi), "s"(RSRC(ws)) : "memory"); };
        auto need = [&](int n) {
            if (wid == 0) {
                asm volatile("" : "+v"(seen));
                if (__ballot(seen == 0u) & ((1ull << n) - 1ull)) flags_wait<0>(ws, lane < n ? fzi : fl + F_Z2 + (cg * 16 + pm) * 256 + pm * 16);
            }
        };
        auto issue = [&](int t, int sl) { const int k = kord2(t); issueB(k, sl); issueA(k, sl); };
        auto steps2 = [&](int t0, int t1) {
            int sl = (2 * t0 + 2) % 3 * G_STAGE, k = kord2(t0 + 3), ob = (k >> 1) * 589824 + (k & 1) * 18432;
            LOOP_ALIGN();
#pragma unroll 1
            for (int t = t0; t < t1; ++t) {
                WAIT_V(9); BARX();
                dma5<true>(lB + sl, rB, ob, vo);
                const int oa = pin_s(k * 128);
                const unsigned la = pin_s(lA + sl);
                k = pin_s(kord2(t + 4)); ob = pin_s((k >> 1) * 589824 + (k & 1) * 18432);
                sl = pin_s(sl == 0 ? 2 * G_STAGE : sl - G_STAGE);
                spA(oa, la);
            }
        };
        WAIT_V(13); BAR(); issue1(17, 2);
        WAIT_V(13); BARX();
        if (wid == 0) flag_raise(ws, fz);
        issueA(2 * pm + 1, 3); if (layer == 0) load5(4, 0);
        if (wid == 0) WAIT_IF(layer, 5, 9); else WAIT_IF(layer, 4, 8);
        BAR(); issueA(kord2(2), 0); if (layer == 0) load5(4, 1);
        BAR(); look(); issueA(kord2(3), 2);
        WAIT_V(4); need(3); BARX(); issueB(kord2(2), 0); issueB(kord2(3), 2); look();
        WAIT_IF(wid == 0, 6, 5); BAR(); issue(4, 1);
        WAIT_V(9); need(8);
        steps2(2, 11); look();
        WAIT_IF(wid == 0, 10, 9); BAR(); issue(14, 0);
        WAIT_V(9); need(16);
        steps2(12, 29);
        WAIT_V(9); BAR(); issue5(0); issue5(1); issue5(2);
        WAIT_V(15); BAR(); issue5(6); issue5(7);
        BAR(); __builtin_amdgcn_s_sleep(W3_SLEEP); issue5(3); put5(0, 9);
        unsigned vb;
        {
            vb = attn_vb(wid, lane);
            const unsigned fo = flag_out(fl + F_Y, 256, pm, cg, lane), fi = flag_in(fl + F_Y, 256, rh * 8 + c5, 16, lane);
            __builtin_amdgcn_s_setprio(3);
            WAIT_V(0); BARX();
            if (wid == 0) { flag_raise(ws, fo); flags_wait<Y_FIRST>(ws, fi); }
            __builtin_amdgcn_s_setprio(0);
            BARX();
        }
        put5(9, 13);
        BAR(); put5(13, 18);
        BAR(); put5(18, 24);
        BAR(); put5(24, 30);
        BAR(); put5(30, 36);
        BAR(); issue5(16); put5(36, 41);
        BAR(); issue5(17); put5(41, 45);
        WAIT_V(5); BAR();
        WAIT_V(0); BAR();
        if (layer == 0) { attn_issueB(lds, ws, hp, wid, vb, 0, 0); attn_issueB(lds, ws, hp, wid, vb, 1, 1); }
        BAR();
        if (layer == 0) {
            __builtin_amdgcn_s_setprio(3);
            const unsigned fo = flag_out(F_X2, 512, (rh >> 1) + wid, wid ? 16 + c5 : (rh & 1) * 8 + c5, wid == 0 || (wid == 1 && (rh & 1) && rh < 31) ? lane : 16);
            BARX();
            if (wid < 2) flag_raise(ws, fo);
        }
        TRACE_DUMP;
      }
        if (blockIdx.x == 0 && wid8 == 4) ST8(RSRC(ws), lane_id_fresh() ? 0x80000000u : (unsigned)OFF_EP, 0, ((u32x2){(unsigned)ep, (unsigned)(ep >> 32)}), 16);
        asm volatile("gm_code_end1:");
        return;
    }
#pragma unroll 1
  for (int layer = 0; layer < NET_LAYERS; ++layer) {
    NET_IDS
    TRACE_BEGIN;
    char* wl = ws + OFF_L0 + layer * SZ_L;
    const unsigned fl = layer * F_LAYER;
    const char* xin = wl + L_XB;
    char* y = wl + (layer ? L_Z2T : L_Y);
#undef STAMP_SEC
#define STAMP_SEC 0
    {
    STAMP_BEGIN;
    attn_consumer(lds, x0, wl, layer, pa, hp, wid, [&] { WAIT_V(0); prologue_done(lds, ws, ep, b, lane_id_fresh()); } STAMP_FWD);
    STAMP_END(1 + 5 * layer);
    }
#undef STAMP_SEC
#define STAMP_SEC 1
    STAMP_BEGIN;
    gmlp_body(lds, wid & 1, wid >> 1, pm, cg, wl + (layer ? L_Y : L_Z2T), y, bsp STAMP_FWD);
    {
        const int lane = lane_id_fresh(), r = lane & 15, q = lane >> 4;
        const int wk = wid >> 1, rt = rh * 4 + (wid & 1) * 2 + wk;
        const ProjOut pr = proj_out((char*)xin, (char*)xin, rt, c5, lane);
        const LAS char *pw[3], *ph[3];
#pragma unroll
        for (int i = 0; i < 3; ++i) {
            pw[i] = opaque(lds + 3 * i * K5_STAGE + wk * 4608 + lane * 16);
            ph[i] = opaque(lds + 3 * i * K5_STAGE + wk * 4608 + 4096 + q * 128 + (r & 7) * 16);
        }
        const auto rY = RSRC(y + (size_t)(rt & ~1) * 73728 + wk * 1024);
        const unsigned vy[2] = {pin((unsigned)(wk * 73728 + lane * 16)), pin((unsigned)((1 - wk) * 73728 + lane * 16))};
        int dk[4];
#pragma unroll
        for (int i = 0; i < 4; ++i) dk[i] = pin_s((kord5(4 + i) - 4 - i) * 4096);
        auto yoff = [&](int t) { return pin_s(t == 0 || t == 17 ? kord5(t) * 4096 : t * 4096 + dk[t & 3]); };
        bf16x8 af[5][4], wf[4][5];
        auto lda = [&](int so, int i) { return __builtin_bit_cast(bf16x8, __builtin_amdgcn_raw_buffer_load_b128(rY, vy[i & 1] + (i >> 1) * 2048, so, 16)); };
        auto rdw = [&](int t, int c, int n) {
            const int sl = (t + 6) & 7, o = (sl % 3) * K5_STAGE + c * 9216;
            wf[2 * (t & 1) + c][n] = n < 4 ? LDS_FRAG(pw[sl / 3] + o + n * 1024) : LDS_FRAG(ph[sl / 3] + o);
        };
        int so[18];
#pragma unroll
        for (int i = 0; i < 5; ++i) so[i] = yoff(i);
        so[17] = yoff(17);
        LAS char* xo = (LAS char*)opaque(lds + wid * 4608 + lane * 16);
        LAS char* xo4 = (LAS char*)opaque(lds + wid * 4608 + 4096 + (lane & 31) * 16);
        const LAS char* xi = opaque(lds + (wid ^ 2) * 4608 + lane * 16);
        const LAS char* xi4 = opaque(lds + (wid ^ 2) * 4608 + 4096 + (lane & 31) * 16);
        const ProjOut po = layer ? proj_out_final(out, rt, c5, lane) : proj_out(wl + SZ_L + L_XA, wl + SZ_L + L_STA, rt, c5, lane);
        asm volatile("" :: "s"(rY));
        SCHED0();
        WAIT_V(0);
        BARX();
        STAMP(11);
        const Res rx = proj_residual_load(pr);
#pragma unroll
        for (int n = 0; n < 10; ++n) rdw(0, n / 5, n % 5);
        f32x4 v[5], u[5];
        proj_residual(v, rx);
#pragma unroll
        for (int n = 0; n < 5; ++n) v[n] = pin(v[n]);
        WAIT_L(0);
        BARX();
        STAMP(12);
#pragma unroll
        for (int i = 0; i < K5_PRE; ++i) { af[i >> 2][i & 3] = lda(so[i >> 2], i & 3); SCHED0(); if (i >= PACE0 && i + 1 < K5_PRE) PACE; SCHED0(); }
        static_for<18>([&](auto tc) {
            constexpr int t = tc.value;
            bf16x8 (&a)[4] = af[t % 5];
#pragma unroll
            for (int h = 0; h < 4; ++h) {
                const int c = t < 17 ? h >> 1 : h & 1, m = t < 17 ? h & 1 : 1 - (h >> 1);
                if (t < 17 ? m == 0 : h < 2) { if (t == 17 && h == 1) WAIT_L(0); else WAIT_L(5); }
#pragma unroll
                for (int n = 0; n < 5; ++n) {
                    f32x4& e = m ? u[n] : v[n];
                    if (t == 0 && h == 1) MFMA_0(wf[0][n], a[1], e); else MFMA_T(wf[2 * (t & 1) + c][n], a[2 * c + m], e);
                    SCHED0();
                    if (t < 17 && m == 0) rdw(t + 1, c, n);
                    if (t < 14 && m == 1 && !(n & 1)) { const int i = y_load_at(6 * t + 3 * c + n / 2); if (i >= 0) af[(i >> 2) % 5][i & 3] = lda(so[i >> 2], i & 3); }
                    if (t < 12 && h == 3 && n == 3) so[t + 5] = yoff(t + 5);
                    if (t == 17 && h * 5 + n >= 13 && h * 5 + n < 18) { const int i = h * 5 + n - 13; *(LAS f32x4*)(i < 4 ? xo + i * 1024 : xo4) = u[i]; }
                    SCHED0();
                }
            }
            if ((t & 1) && t < 17) BAR();
            SCHED0();
        });
        {
            WAIT_L0(); BARX();
#pragma unroll
            for (int n = 0; n < 5; ++n) u[n] = *(const LAS f32x4*)(n < 4 ? xi + n * 1024 : xi4);
            SCHED0();
            asm volatile("s_nop 1" : "+v"(v[0]), "+v"(v[1]), "+v"(v[2]), "+v"(v[3]), "+v"(v[4]));
#pragma unroll
            for (int n = 0; n < 5; ++n) v[n] += u[n];
        }
        STAMP(13);
        if (layer) proj_store<true>(v, po);
        else { proj_store(v, po); WAIT_V(0); BARX(); }
    }
    STAMP_END(3 + 5 * layer);
    TRACE_DUMP;
  }
  asm volatile("gm_code_end2:");
}

}

#define STOP_CHECK(n)

extern "C" void kernel_launch(void* const* d_in, const int* in_sizes, int n_in, void* d_out, int out_size, void* d_ws, size_t ws_size,
                              hipStream_t stream) {
    (void)in_sizes; (void)n_in; (void)out_size; (void)ws_size;
    const u16* x = (const u16*)d_in[0];
    const u16* wqkv = (const u16*)d_in[1];
    const u16* wo = (const u16*)d_in[2];
    const u16* w_in = (const u16*)d_in[4];
    const u16* w_sp = (const u16*)d_in[5];
    const u16* b_sp = (const u16*)d_in[6];
    const u16* w_out = (const u16*)d_in[7];
    char* ws = (char*)d_ws;
    (void)hipFuncSetAttribute((const void*)k_net, hipFuncAttributeMaxDynamicSharedMemorySize, NET_LDS);
    k_net<<<256, 512, NET_LDS, stream>>>(ws, w_sp, b_sp, (char*)d_out, (const char*)x, (const char*)wqkv, wo, w_in, w_out DBG_LAUNCH(0));
}
```
